```python
import torch

def _softmax(x, axis=-1):
    return torch.softmax(x, dim=axis)
def _gelu(x):
    return torch.nn.functional.gelu(x, approximate="tanh")
def _silu(x):
    return torch.nn.functional.silu(x)
def _sigmoid(x):
    return torch.sigmoid(x)
def _layernorm(x, axis=-1, eps=1e-5):
    m = x.mean(dim=axis, keepdim=True)
    v = x.var(dim=axis, keepdim=True, unbiased=False)
    return (x - m) / torch.sqrt(v + eps)
def _scatter_add(tgt, idx, src, axis=0):
    return tgt.clone().index_add_(axis, idx, src)
def _segment_sum(src, seg, n):
    out = torch.zeros((n,) + src.shape[1:], dtype=src.dtype)
    return out.index_add_(0, seg, src)
def _pad(x, pw):
    flat = tuple(v for pair in reversed(pw) for v in pair)
    return torch.nn.functional.pad(x, flat)

kernel_name = 'standalone__w8a8_smoothquant_per_tensor_per_channel_int8_activations_weights__prompt'

def setup_inputs():

    _w = (lambda a: (lambda t: t.to(torch.bfloat16) if t.is_floating_point() else t)(torch.as_tensor(a)))
    B, S, D, F = 1, 2048, 1024, 3584
    x = abs((lambda *s: torch.randn(*s, dtype=torch.bfloat16) * 0.05)(B, S, D))
    w = (lambda *s: torch.randn(*s, dtype=torch.bfloat16) * 0.05)(D, F)
    x_min, x_max = float(x.min()), float(x.max())
    sx = (x_max - x_min) / 255.0 + 1e-6
    zx = int(torch.as_tensor(-128.0 - x_min / sx).round())
    qx = torch.clamp(torch.as_tensor(x / sx).round() + zx, -128, 127).to(torch.int32)
    sw = (torch.abs(w).amax(dim=0) / 127.0 + 1e-6).to(float)
    qw = torch.clamp(torch.as_tensor(w / sw[None, :]).round(), -127, 127).to(torch.int32)
    return {
        "qx": _w(qx),
        "qw": _w(qw),
        "scale_x": _w(torch.tensor(sx, dtype=float)),
        "zero_x": _w(torch.tensor(zx, dtype=torch.int32)),
        "scale_w": _w(sw),
        "bias": _w((lambda *s: torch.randn(*s, dtype=torch.bfloat16) * 0.05)(F)),
    }

def reference(qx, qw, scale_x, zero_x, scale_w, bias):
    dt = scale_w.dtype
    acc = torch.einsum("bsd,df->bsf", (lambda x, dt: x.to(dt))(qx, dt), (lambda x, dt: x.to(dt))(qw, dt))
    w_colsum = torch.sum((lambda x, dt: x.to(dt))(qw, dt), axis=0)
    acc = acc - (lambda x, dt: x.to(dt))(zero_x, dt) * w_colsum[None, None, :]
    return acc * scale_x * scale_w[None, None, :] + bias[None, None, :]
```

```cpp
#include <hip/hip_runtime.h>
#include <stdint.h>

__global__ __launch_bounds__(512, 2) void w8a8_fused(const void* qx, const void* qw, void* ws, void* out,
                                                     const void* sw, const void* bias, const void* sx, const void* zx) {
    int tid = threadIdx.x;
    asm volatile(
        "W8_start:\n"
        "s_load_dwordx16 s[4:19], %1, 0x0\n"
        "s_mov_b32 s3, %2\n"
        "v_readfirstlane_b32 s20, v0\n"
        "s_and_b32 s24, s3, 1\n"
        "s_lshl_b32 s24, s24, 3\n"
        "s_bfe_u32 s21, s3, 0x30003\n"
        "s_or_b32 s21, s21, s24\n"
        "s_bfe_u32 s24, s3, 0x20001\n"
        "s_lshl_b32 s24, s24, 2\n"
        "s_lshr_b32 s22, s3, 6\n"
        "s_or_b32 s22, s22, s24\n"
        "v_lshlrev_b32 v2, 2, v0\n"
        "v_add_u32 v2, 135684, v2\n"
        "v_mov_b32 v3, 0\n"
        "ds_write_b32 v2, v3\n"
        "s_and_b32 s23, s22, 1\n"
        "s_lshr_b32 s20, s20, 6\n"
        "s_cbranch_scc0 W8_prod\n"
        "s_waitcnt lgkmcnt(0)\n"
        "s_barrier\n"
        "s_bfe_u32 s24, s3, 0x20003\n"
        "s_add_u32 s24, s24, 4\n"
        "s_cmp_lg_u32 s20, s24\n"
        "s_cbranch_scc1 W8_np\n"
        "s_getpc_b64 s[24:25]\n"
        "W8_pc:\n"
        "v_and_b32 v2, 255, v0\n"
        "v_lshlrev_b32 v2, 7, v2\n"
        "v_min_u32 v2, W8_end-W8_pc-4, v2\n"
        "global_load_dword v255, v2, s[24:25]\n"
        "W8_np:\n"
        "s_branch W8_other\n"
        "W8_prod:\n"
        "s_lshl_b32 s24, s21, 4\n"
        "s_add_u32 s24, s24, s22\n"
        "s_lshl_b32 s24, s24, 15\n"
        "s_add_u32 s24, s24, 4096\n"
        "v_lshl_add_u32 v4, v0, 4, s24\n"
        "s_mul_i32 s24, s22, 896\n"
        "s_lshl_b32 s25, s23, 7\n"
        "s_add_u32 s25, s25, s24\n"
        "s_mul_i32 s26, s21, 229376\n"
        "s_add_u32 s27, s26, s25\n"
        "v_lshrrev_b32 v8, 5, v0\n"
        "v_mul_u32_u24 v9, 114688, v8\n"
        "v_and_b32 v10, 31, v0\n"
        "v_lshl_add_u32 v5, v10, 4, v9\n"
        "v_add_u32 v5, s27, v5\n"
        "s_movk_i32 s28, 8192\n"
        "s_movk_i32 s29, 14336\n"
        "v_bfe_u32 v60, v0, 4, 1\n"
        "s_add_u32 s27, s22, 16\n"
        "s_sub_u32 s27, s27, s21\n"
        "v_mul_lo_u32 v60, v60, s27\n"
        "v_add_lshl_u32 v60, v60, s21, 7\n"
        "v_add_u32 v60, 6815744, v60\n"
        "s_waitcnt lgkmcnt(0)\n"
        "global_load_dwordx2 v[62:63], v60, s[8:9] sc1\n"
        "global_load_dwordx4 v[64:67], v4, s[4:5] offset:-4096 nt\n"
        "global_load_dwordx4 v[68:71], v4, s[4:5] offset:0 nt\n"
        "v_add_u32 v4, s28, v4\n"
        "global_load_dwordx4 v[72:75], v4, s[4:5] offset:-4096 nt\n"
        "global_load_dwordx4 v[76:79], v4, s[4:5] offset:0 nt\n"
        "v_add_u32 v4, s28, v4\n"
        "global_load_dwordx4 v[80:83], v4, s[4:5] offset:-4096 nt\n"
        "global_load_dwordx4 v[84:87], v4, s[4:5] offset:0 nt\n"
        "v_add_u32 v4, s28, v4\n"
        "global_load_dwordx4 v[88:91], v4, s[4:5] offset:-4096 nt\n"
        "global_load_dwordx4 v[92:95], v4, s[4:5] offset:0 nt\n"
        "v_add_u32 v4, 4294943744, v4\n"
        "global_load_dwordx4 v[96:99], v5, s[6:7] nt\n"
        "v_add_u32 v5, s29, v5\n"
        "global_load_dwordx4 v[100:103], v5, s[6:7] nt\n"
        "v_add_u32 v5, s29, v5\n"
        "global_load_dwordx4 v[104:107], v5, s[6:7] nt\n"
        "v_add_u32 v5, s29, v5\n"
        "global_load_dwordx4 v[108:111], v5, s[6:7] nt\n"
        "v_add_u32 v5, s29, v5\n"
        "global_load_dwordx4 v[112:115], v5, s[6:7] nt\n"
        "v_add_u32 v5, s29, v5\n"
        "global_load_dwordx4 v[116:119], v5, s[6:7] nt\n"
        "v_add_u32 v5, s29, v5\n"
        "global_load_dwordx4 v[120:123], v5, s[6:7] nt\n"
        "v_add_u32 v5, s29, v5\n"
        "global_load_dwordx4 v[124:127], v5, s[6:7] nt\n"
        "v_add_u32 v5, 3569664, v5\n"
        "global_load_dwordx4 v[128:131], v4, s[4:5] offset:-4096 nt\n"
        "global_load_dwordx4 v[132:135], v4, s[4:5] offset:0 nt\n"
        "v_add_u32 v4, s28, v4\n"
        "global_load_dwordx4 v[136:139], v4, s[4:5] offset:-4096 nt\n"
        "global_load_dwordx4 v[140:143], v4, s[4:5] offset:0 nt\n"
        "v_add_u32 v4, s28, v4\n"
        "v_mov_b32 v253, 0\n"
        "s_lshl_b32 s56, s23, 5\n"
        "s_mul_i32 s57, s23, -192\n"
        "v_mov_b32 v254, s56\n"
        "v_mov_b32 v255, s57\n"
        "v_cmp_gt_u32 vcc, 48, v0\n"
        "s_mul_i32 s56, s22, 224\n"
        "global_load_ushort v252, v253, s[16:17]\n"
        "v_cndmask_b32 v254, v255, v254, vcc\n"
        "v_lshl_add_u32 v254, v0, 2, v254\n"
        "v_add_lshl_u32 v254, v254, s56, 1\n"
        "s_mov_b32 exec_hi, 0x00ffffff\n"
        "global_load_dwordx2 v[248:249], v254, s[12:13]\n"
        "global_load_dwordx2 v[250:251], v254, s[14:15]\n"
        "s_mov_b32 exec_hi, -1\n"
        "s_barrier\n"
        "s_lshl_b32 s26, s26, 2\n"
        "s_add_u32 s27, s26, s25\n"
        "s_add_u32 s27, s27, 512\n"
        "v_lshrrev_b32 v11, 4, v0\n"
        "v_mul_u32_u24 v12, 229376, v11\n"
        "v_and_b32 v13, 15, v0\n"
        "v_lshl_add_u32 v7, v13, 4, v12\n"
        "v_add_u32 v7, s27, v7\n"
        "s_cmp_eq_u32 s23, 0\n"
        "s_cselect_b32 s27, 768, 0\n"
        "s_add_u32 s27, s27, s24\n"
        "s_add_u32 s27, s27, s26\n"
        "v_bfe_u32 v14, v0, 3, 2\n"
        "v_mul_u32_u24 v12, 229376, v14\n"
        "v_and_b32 v15, 7, v0\n"
        "v_lshl_add_u32 v6, v15, 4, v12\n"
        "v_add3_u32 v6, v6, v9, s27\n"
        "v_lshlrev_b32 v16, 2, v0\n"
        "v_add_u32 v25, 135684, v16\n"
        "v_add_u32 v16, 98304, v16\n"
        "v_lshlrev_b32 v19, 4, v0\n"
        "v_add_u32 v17, 98304, v19\n"
        "v_lshlrev_b32 v21, 3, v0\n"
        "v_add_u32 v21, 98304, v21\n"
        "s_lshl_b32 s26, s22, 4\n"
        "s_add_u32 s26, s26, s21\n"
        "s_mul_i32 s24, s26, 6144\n"
        "s_add_u32 s24, s24, 4194304\n"
        "s_add_u32 s44, s8, s24\n"
        "s_addc_u32 s45, s9, 0\n"
        "s_lshl_b32 s24, s26, 10\n"
        "s_add_u32 s24, s24, 5767168\n"
        "s_add_u32 s46, s8, s24\n"
        "s_addc_u32 s47, s9, 0\n"
        "v_lshlrev_b32 v22, 4, v13\n"
        "v_lshl_add_u32 v22, v11, 2, v22\n"
        "v_lshlrev_b32 v23, 4, v15\n"
        "v_lshl_add_u32 v23, v11, 2, v23\n"
        "s_mul_i32 s24, s21, 512\n"
        "s_mul_i32 s25, s22, 8\n"
        "s_add_u32 s24, s24, s25\n"
        "s_add_u32 s24, s24, 6029312\n"
        "s_mul_i32 s25, s22, 1024\n"
        "s_mul_i32 s27, s21, 8\n"
        "s_add_u32 s25, s25, s27\n"
        "s_add_u32 s25, s25, 6291456\n"
        "s_sub_u32 s25, s25, s24\n"
        "v_mul_lo_u32 v24, v0, s25\n"
        "s_mul_i32 s25, 4, 128\n"
        "s_add_u32 s24, s24, s25\n"
        "v_add_u32 v24, s24, v24\n"
        "v_lshlrev_b32 v35, 8, v14\n"
        "v_lshl_add_u32 v35, v8, 7, v35\n"
        "v_lshl_add_u32 v35, v15, 4, v35\n"
        "v_add_u32 v35, 4096, v35\n"
        "v_mov_b32 v20, 1\n"
        "s_mov_b32 s30, 0x0c0c0400\n"
        "s_mov_b32 s31, 0x01010101\n"
        "v_mov_b32 v28, 0\n"
        "v_mov_b32 v29, 0\n"
        "v_mov_b32 v30, 0\n"
        "v_mov_b32 v31, 0\n"
        "v_mov_b32 v32, 0\n"
        "v_mov_b32 v33, 0\n"
        "v_lshlrev_b32 v12, 3, v0\n"
        "v_add_u32 v12, 136968, v12\n"
        "s_waitcnt vmcnt(23)\n"
        "v_add_co_u32 v62, vcc, 1, v62\n"
        "s_nop 1\n"
        "v_addc_co_u32 v63, vcc, 0, v63, vcc\n"
        "s_mov_b32 exec_hi, 0\n"
        "ds_write_b64 v12, v[62:63]\n"
        "s_mov_b32 exec_hi, -1\n"
        "ds_write_b32 v25, v20 offset:1024\n"
        "s_waitcnt vmcnt(21)\n"
        "global_load_dwordx4 v[144:147], v4, s[4:5] offset:-4096 nt\n"
        "global_load_dwordx4 v[148:151], v4, s[4:5] offset:0 nt\n"
        "v_add_u32 v4, s28, v4\n"
        "v_perm_b32 v64, v65, v64, s30\n"
        "v_perm_b32 v66, v67, v66, s30\n"
        "v_lshl_or_b32 v64, v66, 16, v64\n"
        "ds_write_b32 v16, v64 offset:0\n"
        "v_perm_b32 v68, v69, v68, s30\n"
        "v_perm_b32 v70, v71, v70, s30\n"
        "v_lshl_or_b32 v68, v70, 16, v68\n"
        "ds_write_b32 v16, v68 offset:288\n"
        "s_waitcnt vmcnt(21)\n"
        "global_load_dwordx4 v[152:155], v4, s[4:5] offset:-4096 nt\n"
        "global_load_dwordx4 v[156:159], v4, s[4:5] offset:0 nt\n"
        "v_add_u32 v4, 4294943744, v4\n"
        "v_perm_b32 v72, v73, v72, s30\n"
        "v_perm_b32 v74, v75, v74, s30\n"
        "v_lshl_or_b32 v72, v74, 16, v72\n"
        "ds_write_b32 v16, v72 offset:576\n"
        "v_perm_b32 v76, v77, v76, s30\n"
        "v_perm_b32 v78, v79, v78, s30\n"
        "v_lshl_or_b32 v76, v78, 16, v76\n"
        "ds_write_b32 v16, v76 offset:864\n"
        "s_waitcnt vmcnt(21)\n"
        "global_load_dwordx4 v[160:163], v5, s[6:7] nt\n"
        "v_add_u32 v5, s29, v5\n"
        "global_load_dwordx4 v[164:167], v5, s[6:7] nt\n"
        "v_add_u32 v5, s29, v5\n"
        "v_perm_b32 v80, v81, v80, s30\n"
        "v_perm_b32 v82, v83, v82, s30\n"
        "v_lshl_or_b32 v80, v82, 16, v80\n"
        "ds_write_b32 v16, v80 offset:1152\n"
        "v_perm_b32 v84, v85, v84, s30\n"
        "v_perm_b32 v86, v87, v86, s30\n"
        "v_lshl_or_b32 v84, v86, 16, v84\n"
        "ds_write_b32 v16, v84 offset:1440\n"
        "s_waitcnt vmcnt(21)\n"
        "global_load_dwordx4 v[168:171], v5, s[6:7] nt\n"
        "v_add_u32 v5, s29, v5\n"
        "global_load_dwordx4 v[172:175], v5, s[6:7] nt\n"
        "v_add_u32 v5, s29, v5\n"
        "v_perm_b32 v88, v89, v88, s30\n"
        "v_perm_b32 v90, v91, v90, s30\n"
        "v_lshl_or_b32 v88, v90, 16, v88\n"
        "ds_write_b32 v16, v88 offset:1728\n"
        "v_perm_b32 v92, v93, v92, s30\n"
        "v_perm_b32 v94, v95, v94, s30\n"
        "v_lshl_or_b32 v92, v94, 16, v92\n"
        "ds_write_b32 v16, v92 offset:2016\n"
        "s_waitcnt vmcnt(21)\n"
        "global_load_dwordx4 v[176:179], v5, s[6:7] nt\n"
        "v_add_u32 v5, s29, v5\n"
        "global_load_dwordx4 v[180:183], v5, s[6:7] nt\n"
        "v_add_u32 v5, s29, v5\n"
        "s_waitcnt vmcnt(21)\n"
        "global_load_dwordx4 v[184:187], v5, s[6:7] nt\n"
        "v_add_u32 v5, s29, v5\n"
        "global_load_dwordx4 v[188:191], v5, s[6:7] nt\n"
        "v_add_u32 v5, 3569664, v5\n"
        "v_perm_b32 v96, v100, v96, s30\n"
        "v_perm_b32 v104, v108, v104, s30\n"
        "v_perm_b32 v97, v101, v97, s30\n"
        "v_perm_b32 v105, v109, v105, s30\n"
        "v_perm_b32 v98, v102, v98, s30\n"
        "v_perm_b32 v106, v110, v106, s30\n"
        "v_perm_b32 v99, v103, v99, s30\n"
        "v_perm_b32 v107, v111, v107, s30\n"
        "v_lshl_or_b32 v36, v104, 16, v96\n"
        "v_lshl_or_b32 v40, v105, 16, v97\n"
        "v_lshl_or_b32 v38, v106, 16, v98\n"
        "v_lshl_or_b32 v42, v107, 16, v99\n"
        "s_waitcnt vmcnt(21)\n"
        "global_load_dwordx4 v[192:195], v4, s[4:5] offset:-4096 nt\n"
        "global_load_dwordx4 v[196:199], v4, s[4:5] offset:0 nt\n"
        "v_add_u32 v4, s28, v4\n"
        "s_waitcnt vmcnt(21)\n"
        "global_load_dwordx4 v[200:203], v4, s[4:5] offset:-4096 nt\n"
        "global_load_dwordx4 v[204:207], v4, s[4:5] offset:0 nt\n"
        "v_add_u32 v4, s28, v4\n"
        "v_perm_b32 v112, v116, v112, s30\n"
        "v_perm_b32 v120, v124, v120, s30\n"
        "v_perm_b32 v113, v117, v113, s30\n"
        "v_perm_b32 v121, v125, v121, s30\n"
        "v_perm_b32 v114, v118, v114, s30\n"
        "v_perm_b32 v122, v126, v122, s30\n"
        "v_perm_b32 v115, v119, v115, s30\n"
        "v_perm_b32 v123, v127, v123, s30\n"
        "v_lshl_or_b32 v37, v120, 16, v112\n"
        "v_lshl_or_b32 v41, v121, 16, v113\n"
        "v_lshl_or_b32 v39, v122, 16, v114\n"
        "v_lshl_or_b32 v43, v123, 16, v115\n"
        "s_nop 0\n"
        "v_permlane32_swap_b32 v36, v38\n"
        "v_permlane32_swap_b32 v37, v39\n"
        "v_permlane32_swap_b32 v40, v42\n"
        "v_permlane32_swap_b32 v41, v43\n"
        "s_nop 0\n"
        "ds_write_b128 v17, v[36:39] offset:2304\n"
        "ds_write_b128 v17, v[40:43] offset:3328\n"
        "ds_write_b32 v25, v20 offset:1544\n"
        "v_add_u32 v20, 1, v20\n"
        "v_dot4_i32_i8 v28, v36, s31, v28\n"
        "v_dot4_i32_i8 v29, v40, s31, v29\n"
        "v_dot4_i32_i8 v28, v37, s31, v28\n"
        "v_dot4_i32_i8 v29, v41, s31, v29\n"
        "v_dot4_i32_i8 v28, v38, s31, v28\n"
        "v_dot4_i32_i8 v29, v42, s31, v29\n"
        "v_dot4_i32_i8 v28, v39, s31, v28\n"
        "v_dot4_i32_i8 v29, v43, s31, v29\n"
        "s_waitcnt vmcnt(21)\n"
        "global_load_dwordx4 v[208:211], v4, s[4:5] offset:-4096 nt\n"
        "global_load_dwordx4 v[212:215], v4, s[4:5] offset:0 nt\n"
        "v_add_u32 v4, s28, v4\n"
        "v_perm_b32 v128, v129, v128, s30\n"
        "v_perm_b32 v130, v131, v130, s30\n"
        "v_lshl_or_b32 v128, v130, 16, v128\n"
        "ds_write_b32 v16, v128 offset:5120\n"
        "v_perm_b32 v132, v133, v132, s30\n"
        "v_perm_b32 v134, v135, v134, s30\n"
        "v_lshl_or_b32 v132, v134, 16, v132\n"
        "ds_write_b32 v16, v132 offset:5408\n"
        "s_waitcnt vmcnt(21)\n"
        "global_load_dwordx4 v[216:219], v4, s[4:5] offset:-4096 nt\n"
        "global_load_dwordx4 v[220:223], v4, s[4:5] offset:0 nt\n"
        "v_add_u32 v4, 4294943744, v4\n"
        "v_perm_b32 v136, v137, v136, s30\n"
        "v_perm_b32 v138, v139, v138, s30\n"
        "v_lshl_or_b32 v136, v138, 16, v136\n"
        "ds_write_b32 v16, v136 offset:5696\n"
        "v_perm_b32 v140, v141, v140, s30\n"
        "v_perm_b32 v142, v143, v142, s30\n"
        "v_lshl_or_b32 v140, v142, 16, v140\n"
        "ds_write_b32 v16, v140 offset:5984\n"
        "s_waitcnt vmcnt(18)\n"
        "global_load_dwordx4 v[224:227], v5, s[6:7] nt\n"
        "v_add_u32 v5, s29, v5\n"
        "global_load_dwordx4 v[228:231], v5, s[6:7] nt\n"
        "v_add_u32 v5, s29, v5\n"
        "v_perm_b32 v144, v145, v144, s30\n"
        "v_perm_b32 v146, v147, v146, s30\n"
        "v_lshl_or_b32 v144, v146, 16, v144\n"
        "ds_write_b32 v16, v144 offset:6272\n"
        "v_perm_b32 v148, v149, v148, s30\n"
        "v_perm_b32 v150, v151, v150, s30\n"
        "v_lshl_or_b32 v148, v150, 16, v148\n"
        "ds_write_b32 v16, v148 offset:6560\n"
        "s_waitcnt vmcnt(18)\n"
        "global_load_dwordx4 v[232:235], v5, s[6:7] nt\n"
        "v_add_u32 v5, s29, v5\n"
        "global_load_dwordx4 v[236:239], v5, s[6:7] nt\n"
        "v_add_u32 v5, s29, v5\n"
        "v_perm_b32 v152, v153, v152, s30\n"
        "v_perm_b32 v154, v155, v154, s30\n"
        "v_lshl_or_b32 v152, v154, 16, v152\n"
        "ds_write_b32 v16, v152 offset:6848\n"
        "v_perm_b32 v156, v157, v156, s30\n"
        "v_perm_b32 v158, v159, v158, s30\n"
        "v_lshl_or_b32 v156, v158, 16, v156\n"
        "ds_write_b32 v16, v156 offset:7136\n"
        "s_waitcnt vmcnt(18)\n"
        "global_load_dwordx4 v[240:243], v5, s[6:7] nt\n"
        "v_add_u32 v5, s29, v5\n"
        "global_load_dwordx4 v[244:247], v5, s[6:7] nt\n"
        "v_add_u32 v5, s29, v5\n"
        "s_waitcnt vmcnt(18)\n"
        "global_load_dwordx4 v[64:67], v5, s[6:7] nt\n"
        "v_add_u32 v5, s29, v5\n"
        "global_load_dwordx4 v[68:71], v5, s[6:7] nt\n"
        "v_add_u32 v5, 3569664, v5\n"
        "v_perm_b32 v160, v164, v160, s30\n"
        "v_perm_b32 v168, v172, v168, s30\n"
        "v_perm_b32 v161, v165, v161, s30\n"
        "v_perm_b32 v169, v173, v169, s30\n"
        "v_perm_b32 v162, v166, v162, s30\n"
        "v_perm_b32 v170, v174, v170, s30\n"
        "v_perm_b32 v163, v167, v163, s30\n"
        "v_perm_b32 v171, v175, v171, s30\n"
        "v_lshl_or_b32 v36, v168, 16, v160\n"
        "v_lshl_or_b32 v40, v169, 16, v161\n"
        "v_lshl_or_b32 v38, v170, 16, v162\n"
        "v_lshl_or_b32 v42, v171, 16, v163\n"
        "s_waitcnt vmcnt(18)\n"
        "global_load_dwordx4 v[72:75], v4, s[4:5] offset:-4096 nt\n"
        "global_load_dwordx4 v[76:79], v4, s[4:5] offset:0 nt\n"
        "v_add_u32 v4, s28, v4\n"
        "s_waitcnt vmcnt(18)\n"
        "global_load_dwordx4 v[80:83], v4, s[4:5] offset:-4096 nt\n"
        "global_load_dwordx4 v[84:87], v4, s[4:5] offset:0 nt\n"
        "v_add_u32 v4, s28, v4\n"
        "v_perm_b32 v176, v180, v176, s30\n"
        "v_perm_b32 v184, v188, v184, s30\n"
        "v_perm_b32 v177, v181, v177, s30\n"
        "v_perm_b32 v185, v189, v185, s30\n"
        "v_perm_b32 v178, v182, v178, s30\n"
        "v_perm_b32 v186, v190, v186, s30\n"
        "v_perm_b32 v179, v183, v179, s30\n"
        "v_perm_b32 v187, v191, v187, s30\n"
        "v_lshl_or_b32 v37, v184, 16, v176\n"
        "v_lshl_or_b32 v41, v185, 16, v177\n"
        "v_lshl_or_b32 v39, v186, 16, v178\n"
        "v_lshl_or_b32 v43, v187, 16, v179\n"
        "s_nop 0\n"
        "v_permlane32_swap_b32 v36, v38\n"
        "v_permlane32_swap_b32 v37, v39\n"
        "v_permlane32_swap_b32 v40, v42\n"
        "v_permlane32_swap_b32 v41, v43\n"
        "s_nop 0\n"
        "ds_write_b128 v17, v[36:39] offset:7424\n"
        "ds_write_b128 v17, v[40:43] offset:8448\n"
        "ds_write_b32 v25, v20 offset:1544\n"
        "v_add_u32 v20, 1, v20\n"
        "v_dot4_i32_i8 v28, v36, s31, v28\n"
        "v_dot4_i32_i8 v29, v40, s31, v29\n"
        "v_dot4_i32_i8 v28, v37, s31, v28\n"
        "v_dot4_i32_i8 v29, v41, s31, v29\n"
        "v_dot4_i32_i8 v28, v38, s31, v28\n"
        "v_dot4_i32_i8 v29, v42, s31, v29\n"
        "v_dot4_i32_i8 v28, v39, s31, v28\n"
        "v_dot4_i32_i8 v29, v43, s31, v29\n"
        "s_waitcnt vmcnt(18)\n"
        "global_load_dwordx4 v[88:91], v4, s[4:5] offset:-4096 nt\n"
        "global_load_dwordx4 v[92:95], v4, s[4:5] offset:0 nt\n"
        "v_add_u32 v4, s28, v4\n"
        "v_perm_b32 v192, v193, v192, s30\n"
        "v_perm_b32 v194, v195, v194, s30\n"
        "v_lshl_or_b32 v192, v194, 16, v192\n"
        "ds_write_b32 v16, v192 offset:10240\n"
        "v_perm_b32 v196, v197, v196, s30\n"
        "v_perm_b32 v198, v199, v198, s30\n"
        "v_lshl_or_b32 v196, v198, 16, v196\n"
        "ds_write_b32 v16, v196 offset:10528\n"
        "s_waitcnt vmcnt(18)\n"
        "global_load_dwordx4 v[96:99], v4, s[4:5] offset:-4096 nt\n"
        "global_load_dwordx4 v[100:103], v4, s[4:5] offset:0 nt\n"
        "v_perm_b32 v200, v201, v200, s30\n"
        "v_perm_b32 v202, v203, v202, s30\n"
        "v_lshl_or_b32 v200, v202, 16, v200\n"
        "ds_write_b32 v16, v200 offset:10816\n"
        "v_perm_b32 v204, v205, v204, s30\n"
        "v_perm_b32 v206, v207, v206, s30\n"
        "v_lshl_or_b32 v204, v206, 16, v204\n"
        "ds_write_b32 v16, v204 offset:11104\n"
        "s_waitcnt vmcnt(18)\n"
        "global_load_dwordx4 v[104:107], v5, s[6:7] nt\n"
        "v_add_u32 v5, s29, v5\n"
        "global_load_dwordx4 v[108:111], v5, s[6:7] nt\n"
        "v_add_u32 v5, s29, v5\n"
        "v_perm_b32 v208, v209, v208, s30\n"
        "v_perm_b32 v210, v211, v210, s30\n"
        "v_lshl_or_b32 v208, v210, 16, v208\n"
        "ds_write_b32 v16, v208 offset:11392\n"
        "v_perm_b32 v212, v213, v212, s30\n"
        "v_perm_b32 v214, v215, v214, s30\n"
        "v_lshl_or_b32 v212, v214, 16, v212\n"
        "ds_write_b32 v16, v212 offset:11680\n"
        "s_waitcnt vmcnt(18)\n"
        "global_load_dwordx4 v[112:115], v5, s[6:7] nt\n"
        "v_add_u32 v5, s29, v5\n"
        "global_load_dwordx4 v[116:119], v5, s[6:7] nt\n"
        "v_add_u32 v5, s29, v5\n"
        "v_perm_b32 v216, v217, v216, s30\n"
        "v_perm_b32 v218, v219, v218, s30\n"
        "v_lshl_or_b32 v216, v218, 16, v216\n"
        "ds_write_b32 v16, v216 offset:11968\n"
        "v_perm_b32 v220, v221, v220, s30\n"
        "v_perm_b32 v222, v223, v222, s30\n"
        "v_lshl_or_b32 v220, v222, 16, v220\n"
        "ds_write_b32 v16, v220 offset:12256\n"
        "s_waitcnt vmcnt(18)\n"
        "global_load_dwordx4 v[120:123], v5, s[6:7] nt\n"
        "v_add_u32 v5, s29, v5\n"
        "global_load_dwordx4 v[124:127], v5, s[6:7] nt\n"
        "v_add_u32 v5, s29, v5\n"
        "s_waitcnt vmcnt(18)\n"
        "global_load_dwordx4 v[128:131], v5, s[6:7] nt\n"
        "v_add_u32 v5, s29, v5\n"
        "global_load_dwordx4 v[132:135], v5, s[6:7] nt\n"
        "v_perm_b32 v224, v228, v224, s30\n"
        "v_perm_b32 v232, v236, v232, s30\n"
        "v_perm_b32 v225, v229, v225, s30\n"
        "v_perm_b32 v233, v237, v233, s30\n"
        "v_perm_b32 v226, v230, v226, s30\n"
        "v_perm_b32 v234, v238, v234, s30\n"
        "v_perm_b32 v227, v231, v227, s30\n"
        "v_perm_b32 v235, v239, v235, s30\n"
        "v_lshl_or_b32 v36, v232, 16, v224\n"
        "v_lshl_or_b32 v40, v233, 16, v225\n"
        "v_lshl_or_b32 v38, v234, 16, v226\n"
        "v_lshl_or_b32 v42, v235, 16, v227\n"
        "s_waitcnt vmcnt(18)\n"
        "global_load_dwordx4 v[136:139], v6, s[6:7] nt\n"
        "v_add_u32 v6, s29, v6\n"
        "global_load_dwordx4 v[140:143], v6, s[6:7] nt\n"
        "v_add_u32 v6, s29, v6\n"
        "s_waitcnt vmcnt(18)\n"
        "global_load_dwordx4 v[144:147], v6, s[6:7] nt\n"
        "v_add_u32 v6, s29, v6\n"
        "global_load_dwordx4 v[148:151], v6, s[6:7] nt\n"
        "v_add_u32 v6, s29, v6\n"
        "v_perm_b32 v240, v244, v240, s30\n"
        "v_perm_b32 v64, v68, v64, s30\n"
        "v_perm_b32 v241, v245, v241, s30\n"
        "v_perm_b32 v65, v69, v65, s30\n"
        "v_perm_b32 v242, v246, v242, s30\n"
        "v_perm_b32 v66, v70, v66, s30\n"
        "v_perm_b32 v243, v247, v243, s30\n"
        "v_perm_b32 v67, v71, v67, s30\n"
        "v_lshl_or_b32 v37, v64, 16, v240\n"
        "v_lshl_or_b32 v41, v65, 16, v241\n"
        "v_lshl_or_b32 v39, v66, 16, v242\n"
        "v_lshl_or_b32 v43, v67, 16, v243\n"
        "s_nop 0\n"
        "v_permlane32_swap_b32 v36, v38\n"
        "v_permlane32_swap_b32 v37, v39\n"
        "v_permlane32_swap_b32 v40, v42\n"
        "v_permlane32_swap_b32 v41, v43\n"
        "s_nop 0\n"
        "ds_write_b128 v17, v[36:39] offset:12544\n"
        "ds_write_b128 v17, v[40:43] offset:13568\n"
        "ds_write_b32 v25, v20 offset:1544\n"
        "v_add_u32 v20, 1, v20\n"
        "v_dot4_i32_i8 v28, v36, s31, v28\n"
        "v_dot4_i32_i8 v29, v40, s31, v29\n"
        "v_dot4_i32_i8 v28, v37, s31, v28\n"
        "v_dot4_i32_i8 v29, v41, s31, v29\n"
        "v_dot4_i32_i8 v28, v38, s31, v28\n"
        "v_dot4_i32_i8 v29, v42, s31, v29\n"
        "v_dot4_i32_i8 v28, v39, s31, v28\n"
        "v_dot4_i32_i8 v29, v43, s31, v29\n"
        "s_waitcnt vmcnt(18)\n"
        "global_load_dwordx4 v[152:155], v6, s[6:7] nt\n"
        "v_add_u32 v6, s29, v6\n"
        "global_load_dwordx4 v[156:159], v6, s[6:7] nt\n"
        "v_add_u32 v6, s29, v6\n"
        "v_perm_b32 v72, v73, v72, s30\n"
        "v_perm_b32 v74, v75, v74, s30\n"
        "v_lshl_or_b32 v72, v74, 16, v72\n"
        "ds_write_b32 v16, v72 offset:15360\n"
        "v_perm_b32 v76, v77, v76, s30\n"
        "v_perm_b32 v78, v79, v78, s30\n"
        "v_lshl_or_b32 v76, v78, 16, v76\n"
        "ds_write_b32 v16, v76 offset:15648\n"
        "s_waitcnt vmcnt(18)\n"
        "global_load_dwordx4 v[160:163], v6, s[6:7] nt\n"
        "v_add_u32 v6, s29, v6\n"
        "global_load_dwordx4 v[164:167], v6, s[6:7] nt\n"
        "v_perm_b32 v80, v81, v80, s30\n"
        "v_perm_b32 v82, v83, v82, s30\n"
        "v_lshl_or_b32 v80, v82, 16, v80\n"
        "ds_write_b32 v16, v80 offset:15936\n"
        "v_perm_b32 v84, v85, v84, s30\n"
        "v_perm_b32 v86, v87, v86, s30\n"
        "v_lshl_or_b32 v84, v86, 16, v84\n"
        "ds_write_b32 v16, v84 offset:16224\n"
        "s_waitcnt vmcnt(18)\n"
        "global_load_dwordx4 v[168:171], v7, s[6:7] nt\n"
        "v_add_u32 v7, s29, v7\n"
        "global_load_dwordx4 v[172:175], v7, s[6:7] nt\n"
        "v_add_u32 v7, s29, v7\n"
        "v_perm_b32 v88, v89, v88, s30\n"
        "v_perm_b32 v90, v91, v90, s30\n"
        "v_lshl_or_b32 v88, v90, 16, v88\n"
        "ds_write_b32 v16, v88 offset:16512\n"
        "v_perm_b32 v92, v93, v92, s30\n"
        "v_perm_b32 v94, v95, v94, s30\n"
        "v_lshl_or_b32 v92, v94, 16, v92\n"
        "ds_write_b32 v16, v92 offset:16800\n"
        "s_waitcnt vmcnt(18)\n"
        "global_load_dwordx4 v[176:179], v7, s[6:7] nt\n"
        "v_add_u32 v7, s29, v7\n"
        "global_load_dwordx4 v[180:183], v7, s[6:7] nt\n"
        "v_add_u32 v7, s29, v7\n"
        "v_perm_b32 v96, v97, v96, s30\n"
        "v_perm_b32 v98, v99, v98, s30\n"
        "v_lshl_or_b32 v96, v98, 16, v96\n"
        "ds_write_b32 v16, v96 offset:17088\n"
        "v_perm_b32 v100, v101, v100, s30\n"
        "v_perm_b32 v102, v103, v102, s30\n"
        "v_lshl_or_b32 v100, v102, 16, v100\n"
        "ds_write_b32 v16, v100 offset:17376\n"
        "s_waitcnt vmcnt(18)\n"
        "global_load_dwordx4 v[184:187], v7, s[6:7] nt\n"
        "v_add_u32 v7, s29, v7\n"
        "global_load_dwordx4 v[188:191], v7, s[6:7] nt\n"
        "v_add_u32 v7, s29, v7\n"
        "s_waitcnt vmcnt(18)\n"
        "global_load_dwordx4 v[192:195], v7, s[6:7] nt\n"
        "v_add_u32 v7, s29, v7\n"
        "global_load_dwordx4 v[196:199], v7, s[6:7] nt\n"
        "v_add_u32 v7, s29, v7\n"
        "v_perm_b32 v104, v108, v104, s30\n"
        "v_perm_b32 v112, v116, v112, s30\n"
        "v_perm_b32 v105, v109, v105, s30\n"
        "v_perm_b32 v113, v117, v113, s30\n"
        "v_perm_b32 v106, v110, v106, s30\n"
        "v_perm_b32 v114, v118, v114, s30\n"
        "v_perm_b32 v107, v111, v107, s30\n"
        "v_perm_b32 v115, v119, v115, s30\n"
        "v_lshl_or_b32 v36, v112, 16, v104\n"
        "v_lshl_or_b32 v40, v113, 16, v105\n"
        "v_lshl_or_b32 v38, v114, 16, v106\n"
        "v_lshl_or_b32 v42, v115, 16, v107\n"
        "s_waitcnt vmcnt(18)\n"
        "global_load_dwordx4 v[200:203], v7, s[6:7] nt\n"
        "v_add_u32 v7, s29, v7\n"
        "global_load_dwordx4 v[204:207], v7, s[6:7] nt\n"
        "v_add_u32 v7, s29, v7\n"
        "s_waitcnt vmcnt(18)\n"
        "global_load_dwordx4 v[208:211], v7, s[6:7] nt\n"
        "v_add_u32 v7, s29, v7\n"
        "global_load_dwordx4 v[212:215], v7, s[6:7] nt\n"
        "v_add_u32 v7, s29, v7\n"
        "v_perm_b32 v120, v124, v120, s30\n"
        "v_perm_b32 v128, v132, v128, s30\n"
        "v_perm_b32 v121, v125, v121, s30\n"
        "v_perm_b32 v129, v133, v129, s30\n"
        "v_perm_b32 v122, v126, v122, s30\n"
        "v_perm_b32 v130, v134, v130, s30\n"
        "v_perm_b32 v123, v127, v123, s30\n"
        "v_perm_b32 v131, v135, v131, s30\n"
        "v_lshl_or_b32 v37, v128, 16, v120\n"
        "v_lshl_or_b32 v41, v129, 16, v121\n"
        "v_lshl_or_b32 v39, v130, 16, v122\n"
        "v_lshl_or_b32 v43, v131, 16, v123\n"
        "s_nop 0\n"
        "v_permlane32_swap_b32 v36, v38\n"
        "v_permlane32_swap_b32 v37, v39\n"
        "v_permlane32_swap_b32 v40, v42\n"
        "v_permlane32_swap_b32 v41, v43\n"
        "s_nop 0\n"
        "ds_write_b128 v17, v[36:39] offset:17664\n"
        "ds_write_b128 v17, v[40:43] offset:18688\n"
        "v_dot4_i32_i8 v28, v36, s31, v28\n"
        "v_dot4_i32_i8 v29, v40, s31, v29\n"
        "v_dot4_i32_i8 v28, v37, s31, v28\n"
        "v_dot4_i32_i8 v29, v41, s31, v29\n"
        "v_dot4_i32_i8 v28, v38, s31, v28\n"
        "v_dot4_i32_i8 v29, v42, s31, v29\n"
        "v_dot4_i32_i8 v28, v39, s31, v28\n"
        "v_dot4_i32_i8 v29, v43, s31, v29\n"
        "s_nop 2\n"
        "ds_write_b64 v21, v[28:29] offset:19712\n"
        "ds_write_b32 v25, v20 offset:1544\n"
        "v_add_u32 v20, 1, v20\n"
        "s_waitcnt vmcnt(18)\n"
        "global_load_dwordx4 v[216:219], v7, s[6:7] nt\n"
        "v_add_u32 v7, s29, v7\n"
        "global_load_dwordx4 v[220:223], v7, s[6:7] nt\n"
        "v_add_u32 v7, s29, v7\n"
        "s_waitcnt vmcnt(18)\n"
        "global_load_dwordx4 v[224:227], v7, s[6:7] nt\n"
        "v_add_u32 v7, s29, v7\n"
        "global_load_dwordx4 v[228:231], v7, s[6:7] nt\n"
        "v_mov_b32 v12, 1\n"
        "ds_write_b32 v25, v12 offset:512\n"
        "v_perm_b32 v136, v140, v136, s30\n"
        "v_perm_b32 v144, v148, v144, s30\n"
        "v_perm_b32 v137, v141, v137, s30\n"
        "v_perm_b32 v145, v149, v145, s30\n"
        "v_perm_b32 v138, v142, v138, s30\n"
        "v_perm_b32 v146, v150, v146, s30\n"
        "v_perm_b32 v139, v143, v139, s30\n"
        "v_perm_b32 v147, v151, v147, s30\n"
        "v_lshl_or_b32 v36, v144, 16, v136\n"
        "v_lshl_or_b32 v40, v145, 16, v137\n"
        "v_lshl_or_b32 v38, v146, 16, v138\n"
        "v_lshl_or_b32 v42, v147, 16, v139\n"
        "s_waitcnt vmcnt(18)\n"
        "s_waitcnt vmcnt(16)\n"
        "v_perm_b32 v152, v156, v152, s30\n"
        "v_perm_b32 v160, v164, v160, s30\n"
        "v_perm_b32 v153, v157, v153, s30\n"
        "v_perm_b32 v161, v165, v161, s30\n"
        "v_perm_b32 v154, v158, v154, s30\n"
        "v_perm_b32 v162, v166, v162, s30\n"
        "v_perm_b32 v155, v159, v155, s30\n"
        "v_perm_b32 v163, v167, v163, s30\n"
        "v_lshl_or_b32 v37, v160, 16, v152\n"
        "v_lshl_or_b32 v41, v161, 16, v153\n"
        "v_lshl_or_b32 v39, v162, 16, v154\n"
        "v_lshl_or_b32 v43, v163, 16, v155\n"
        "s_nop 0\n"
        "v_permlane32_swap_b32 v36, v38\n"
        "v_permlane32_swap_b32 v37, v39\n"
        "v_permlane32_swap_b32 v40, v42\n"
        "v_permlane32_swap_b32 v41, v43\n"
        "s_nop 0\n"
        "global_store_dwordx4 v35, v[36:39], s[44:45] sc1\n"
        "global_store_dwordx4 v35, v[40:43], s[44:45] offset:1024 sc1\n"
        "v_dot4_i32_i8 v30, v36, s31, v30\n"
        "v_dot4_i32_i8 v31, v40, s31, v31\n"
        "v_dot4_i32_i8 v30, v37, s31, v30\n"
        "v_dot4_i32_i8 v31, v41, s31, v31\n"
        "v_dot4_i32_i8 v30, v38, s31, v30\n"
        "v_dot4_i32_i8 v31, v42, s31, v31\n"
        "v_dot4_i32_i8 v30, v39, s31, v30\n"
        "v_dot4_i32_i8 v31, v43, s31, v31\n"
        "s_nop 2\n"
        "v_permlane16_swap_b32 v30, v31\n"
        "s_nop 0\n"
        "v_add_u32 v30, v30, v31\n"
        "v_mov_b32 v31, 0\n"
        "s_nop 0\n"
        "v_add_u32_dpp v34, v30, v30 row_ror:8 row_mask:0xf bank_mask:0xf\n"
        "v_mov_b32 v30, 0\n"
        "s_mov_b32 s38, 8388608\n"
        "W8_tg0:\n"
        "ds_read_b32 v12, v25 offset:1024\n"
        "s_sub_u32 s38, s38, 1\n"
        "s_waitcnt lgkmcnt(0)\n"
        "s_cbranch_scc1 W8_tg0_x\n"
        "v_readfirstlane_b32 s39, v12\n"
        "s_cmp_ge_u32 s39, 1\n"
        "s_cbranch_scc1 W8_tg0_x\n"
        "s_sleep 1\n"
        "s_branch W8_tg0\n"
        "W8_tg0_x:\n"
        "v_lshrrev_b32 v12, 0, v0\n"
        "v_lshlrev_b32 v12, 7, v12\n"
        "v_add_u32 v12, 136968, v12\n"
        "s_mov_b64 exec, 3\n"
        "ds_read_b64 v[26:27], v12\n"
        "s_mov_b64 exec, -1\n"
        "s_waitcnt vmcnt(16)\n"
        "s_waitcnt vmcnt(14)\n"
        "v_perm_b32 v168, v172, v168, s30\n"
        "v_perm_b32 v176, v180, v176, s30\n"
        "v_perm_b32 v169, v173, v169, s30\n"
        "v_perm_b32 v177, v181, v177, s30\n"
        "v_perm_b32 v170, v174, v170, s30\n"
        "v_perm_b32 v178, v182, v178, s30\n"
        "v_perm_b32 v171, v175, v171, s30\n"
        "v_perm_b32 v179, v183, v179, s30\n"
        "v_lshl_or_b32 v36, v176, 16, v168\n"
        "v_lshl_or_b32 v40, v177, 16, v169\n"
        "v_lshl_or_b32 v44, v178, 16, v170\n"
        "v_lshl_or_b32 v48, v179, 16, v171\n"
        "v_dot4_i32_i8 v30, v36, s31, v30\n"
        "v_dot4_i32_i8 v31, v40, s31, v31\n"
        "v_dot4_i32_i8 v32, v44, s31, v32\n"
        "v_dot4_i32_i8 v33, v48, s31, v33\n"
        "s_waitcnt vmcnt(12)\n"
        "s_waitcnt vmcnt(10)\n"
        "v_perm_b32 v184, v188, v184, s30\n"
        "v_perm_b32 v192, v196, v192, s30\n"
        "v_perm_b32 v185, v189, v185, s30\n"
        "v_perm_b32 v193, v197, v193, s30\n"
        "v_perm_b32 v186, v190, v186, s30\n"
        "v_perm_b32 v194, v198, v194, s30\n"
        "v_perm_b32 v187, v191, v187, s30\n"
        "v_perm_b32 v195, v199, v195, s30\n"
        "v_lshl_or_b32 v37, v192, 16, v184\n"
        "v_lshl_or_b32 v41, v193, 16, v185\n"
        "v_lshl_or_b32 v45, v194, 16, v186\n"
        "v_lshl_or_b32 v49, v195, 16, v187\n"
        "v_dot4_i32_i8 v30, v37, s31, v30\n"
        "v_dot4_i32_i8 v31, v41, s31, v31\n"
        "v_dot4_i32_i8 v32, v45, s31, v32\n"
        "v_dot4_i32_i8 v33, v49, s31, v33\n"
        "s_waitcnt vmcnt(8)\n"
        "s_waitcnt vmcnt(6)\n"
        "v_perm_b32 v200, v204, v200, s30\n"
        "v_perm_b32 v208, v212, v208, s30\n"
        "v_perm_b32 v201, v205, v201, s30\n"
        "v_perm_b32 v209, v213, v209, s30\n"
        "v_perm_b32 v202, v206, v202, s30\n"
        "v_perm_b32 v210, v214, v210, s30\n"
        "v_perm_b32 v203, v207, v203, s30\n"
        "v_perm_b32 v211, v215, v211, s30\n"
        "v_lshl_or_b32 v38, v208, 16, v200\n"
        "v_lshl_or_b32 v42, v209, 16, v201\n"
        "v_lshl_or_b32 v46, v210, 16, v202\n"
        "v_lshl_or_b32 v50, v211, 16, v203\n"
        "v_dot4_i32_i8 v30, v38, s31, v30\n"
        "v_dot4_i32_i8 v31, v42, s31, v31\n"
        "v_dot4_i32_i8 v32, v46, s31, v32\n"
        "v_dot4_i32_i8 v33, v50, s31, v33\n"
        "s_waitcnt vmcnt(4)\n"
        "s_waitcnt vmcnt(2)\n"
        "v_perm_b32 v216, v220, v216, s30\n"
        "v_perm_b32 v224, v228, v224, s30\n"
        "v_perm_b32 v217, v221, v217, s30\n"
        "v_perm_b32 v225, v229, v225, s30\n"
        "v_perm_b32 v218, v222, v218, s30\n"
        "v_perm_b32 v226, v230, v226, s30\n"
        "v_perm_b32 v219, v223, v219, s30\n"
        "v_perm_b32 v227, v231, v227, s30\n"
        "v_lshl_or_b32 v39, v224, 16, v216\n"
        "v_lshl_or_b32 v43, v225, 16, v217\n"
        "v_lshl_or_b32 v47, v226, 16, v218\n"
        "v_lshl_or_b32 v51, v227, 16, v219\n"
        "ds_write_b32 v25, v20 offset:1544\n"
        "global_store_dwordx4 v19, v[36:39], s[44:45] offset:0 sc1\n"
        "global_store_dwordx4 v19, v[40:43], s[44:45] offset:1024 sc1\n"
        "global_store_dwordx4 v19, v[44:47], s[44:45] offset:2048 sc1\n"
        "global_store_dwordx4 v19, v[48:51], s[44:45] offset:3072 sc1\n"
        "v_dot4_i32_i8 v30, v39, s31, v30\n"
        "v_dot4_i32_i8 v31, v43, s31, v31\n"
        "v_dot4_i32_i8 v32, v47, s31, v32\n"
        "v_dot4_i32_i8 v33, v51, s31, v33\n"
        "s_nop 2\n"
        "v_permlane32_swap_b32 v30, v32\n"
        "v_permlane32_swap_b32 v31, v33\n"
        "s_nop 0\n"
        "v_add_u32 v30, v30, v32\n"
        "v_add_u32 v31, v31, v33\n"
        "s_nop 1\n"
        "v_permlane16_swap_b32 v30, v31\n"
        "s_nop 0\n"
        "v_add_u32 v30, v30, v31\n"
        "global_store_dword v22, v30, s[46:47] offset:512 sc1\n"
        "s_mov_b32 exec_lo, 0x00ff00ff\n"
        "s_mov_b32 exec_hi, 0x00ff00ff\n"
        "global_store_dword v23, v34, s[46:47] offset:768 sc1\n"
        "s_mov_b64 exec, 2\n"
        "s_waitcnt vmcnt(0) lgkmcnt(0)\n"
        "global_store_dwordx2 v24, v[26:27], s[8:9] sc1\n"
        "s_mov_b64 exec, -1\n"
        "s_load_dword s48, s[18:19], 0x0\n"
        "s_mov_b32 exec_hi, 0x00ffffff\n"
        "v_mov_b32 v32, v252\n"
        "v_mov_b32 v34, v248\n"
        "v_mov_b32 v35, v249\n"
        "v_mov_b32 v38, v250\n"
        "v_mov_b32 v39, v251\n"
        "s_lshl_b32 s24, s22, 14\n"
        "s_add_u32 s24, s24, 5771264\n"
        "v_add_u32 v21, s24, v19\n"
        "v_add_u32 v22, 8192, v21\n"
        "v_lshlrev_b32 v23, 5, v0\n"
        "v_add_u32 v23, 133632, v23\n"
        "s_waitcnt vmcnt(0)\n"
        "v_lshlrev_b32 v32, 16, v32\n"
        "v_and_b32 v37, 0xffff0000, v35\n"
        "v_lshlrev_b32 v36, 16, v35\n"
        "v_and_b32 v35, 0xffff0000, v34\n"
        "v_lshlrev_b32 v34, 16, v34\n"
        "v_and_b32 v41, 0xffff0000, v39\n"
        "v_lshlrev_b32 v40, 16, v39\n"
        "v_and_b32 v39, 0xffff0000, v38\n"
        "v_lshlrev_b32 v38, 16, v38\n"
        "v_mul_f32 v44, v32, v34\n"
        "v_mul_f32 v46, v32, v35\n"
        "v_mul_f32 v48, v32, v36\n"
        "v_mul_f32 v50, v32, v37\n"
        "s_mov_b32 s49, 6\n"
        "s_mov_b32 s50, -1\n"
        "s_mov_b32 s51, 16777215\n"
        "v_mov_b32 v33, 3\n"
        "W8_tab:\n"
        "s_mov_b64 exec, -1\n"
        "s_mov_b32 s38, 8388608\n"
        "W8_tr:\n"
        "ds_read_b32 v12, v25 offset:1024\n"
        "s_sub_u32 s38, s38, 1\n"
        "s_waitcnt lgkmcnt(0)\n"
        "s_cbranch_scc1 W8_tr_x\n"
        "v_readfirstlane_b32 s39, v12\n"
        "s_cmp_ge_u32 s39, s49\n"
        "s_cbranch_scc1 W8_tr_x\n"
        "s_sleep 2\n"
        "s_branch W8_tr\n"
        "W8_tr_x:\n"
        "s_mov_b32 s38, 8388608\n"
        "W8_tf:\n"
        "ds_read_b32 v12, v25 offset:0\n"
        "s_sub_u32 s38, s38, 1\n"
        "s_waitcnt lgkmcnt(0)\n"
        "s_cbranch_scc1 W8_tf_x\n"
        "v_readfirstlane_b32 s39, v12\n"
        "s_cmp_ge_u32 s39, 12\n"
        "s_cbranch_scc1 W8_tf_x\n"
        "s_sleep 2\n"
        "s_branch W8_tf\n"
        "W8_tf_x:\n"
        "s_mov_b64 exec, s[50:51]\n"
        "global_load_dwordx4 v[64:67], v21, s[8:9] offset:-4096 sc1\n"
        "global_load_dwordx4 v[68:71], v21, s[8:9] offset:-3072 sc1\n"
        "global_load_dwordx4 v[72:75], v21, s[8:9] offset:-2048 sc1\n"
        "global_load_dwordx4 v[76:79], v21, s[8:9] offset:-1024 sc1\n"
        "global_load_dwordx4 v[80:83], v21, s[8:9] offset:0 sc1\n"
        "global_load_dwordx4 v[84:87], v21, s[8:9] offset:1024 sc1\n"
        "global_load_dwordx4 v[88:91], v21, s[8:9] offset:2048 sc1\n"
        "global_load_dwordx4 v[92:95], v21, s[8:9] offset:3072 sc1\n"
        "global_load_dwordx4 v[96:99], v22, s[8:9] offset:-4096 sc1\n"
        "global_load_dwordx4 v[100:103], v22, s[8:9] offset:-3072 sc1\n"
        "global_load_dwordx4 v[104:107], v22, s[8:9] offset:-2048 sc1\n"
        "global_load_dwordx4 v[108:111], v22, s[8:9] offset:-1024 sc1\n"
        "global_load_dwordx4 v[112:115], v22, s[8:9] offset:0 sc1\n"
        "global_load_dwordx4 v[116:119], v22, s[8:9] offset:1024 sc1\n"
        "global_load_dwordx4 v[120:123], v22, s[8:9] offset:2048 sc1\n"
        "global_load_dwordx4 v[124:127], v22, s[8:9] offset:3072 sc1\n"
        "s_waitcnt vmcnt(0)\n"
        "v_add3_u32 v52, v64, v68, v72\n"
        "v_add3_u32 v53, v65, v69, v73\n"
        "v_add3_u32 v54, v66, v70, v74\n"
        "v_add3_u32 v55, v67, v71, v75\n"
        "v_add3_u32 v52, v52, v76, v80\n"
        "v_add3_u32 v53, v53, v77, v81\n"
        "v_add3_u32 v54, v54, v78, v82\n"
        "v_add3_u32 v55, v55, v79, v83\n"
        "v_add3_u32 v52, v52, v84, v88\n"
        "v_add3_u32 v53, v53, v85, v89\n"
        "v_add3_u32 v54, v54, v86, v90\n"
        "v_add3_u32 v55, v55, v87, v91\n"
        "v_add3_u32 v52, v52, v92, v96\n"
        "v_add3_u32 v53, v53, v93, v97\n"
        "v_add3_u32 v54, v54, v94, v98\n"
        "v_add3_u32 v55, v55, v95, v99\n"
        "v_add3_u32 v52, v52, v100, v104\n"
        "v_add3_u32 v53, v53, v101, v105\n"
        "v_add3_u32 v54, v54, v102, v106\n"
        "v_add3_u32 v55, v55, v103, v107\n"
        "v_add3_u32 v52, v52, v108, v112\n"
        "v_add3_u32 v53, v53, v109, v113\n"
        "v_add3_u32 v54, v54, v110, v114\n"
        "v_add3_u32 v55, v55, v111, v115\n"
        "v_add3_u32 v52, v52, v116, v120\n"
        "v_add3_u32 v53, v53, v117, v121\n"
        "v_add3_u32 v54, v54, v118, v122\n"
        "v_add3_u32 v55, v55, v119, v123\n"
        "v_add_u32 v52, v52, v124\n"
        "v_add_u32 v53, v53, v125\n"
        "v_add_u32 v54, v54, v126\n"
        "v_add_u32 v55, v55, v127\n"
        "v_mul_lo_u32 v52, v52, s48\n"
        "v_mul_lo_u32 v53, v53, s48\n"
        "v_mul_lo_u32 v54, v54, s48\n"
        "v_mul_lo_u32 v55, v55, s48\n"
        "v_cvt_f32_i32 v52, v52\n"
        "v_cvt_f32_i32 v53, v53\n"
        "v_cvt_f32_i32 v54, v54\n"
        "v_cvt_f32_i32 v55, v55\n"
        "v_fma_f32 v45, -v52, v44, v38\n"
        "v_fma_f32 v47, -v53, v46, v39\n"
        "v_fma_f32 v49, -v54, v48, v40\n"
        "v_fma_f32 v51, -v55, v50, v41\n"
        "ds_write_b128 v23, v[44:47]\n"
        "ds_write_b128 v23, v[48:51] offset:16\n"
        "s_mov_b64 exec, -1\n"
        "ds_write_b32 v25, v33 offset:512\n"
        "v_add_u32 v33, 1, v33\n"
        "s_mov_b32 s50, 0\n"
        "s_mov_b32 s51, 0x00ffffff\n"
        "s_add_u32 s49, s49, 2\n"
        "s_cmp_eq_u32 s49, 6\n"
        "s_cbranch_scc1 W8_tab\n"
        "s_branch W8_fin\n"
        "W8_other:\n"
        "v_and_b32 v1, 63, v0\n"
        "s_add_u32 s33, s21, s22\n"
        "s_cmp_ge_u32 s20, 4\n"
        "s_cbranch_scc1 W8_comp\n"
        "v_lshlrev_b32 v25, 2, v1\n"
        "v_add_u32 v25, 135684, v25\n"
        "s_cmp_eq_u32 s20, 1\n"
        "s_cbranch_scc1 W8_load\n"
        "s_and_b32 s30, s20, 1\n"
        "s_lshl_b32 s24, s21, 9\n"
        "s_add_u32 s24, s24, 6029312\n"
        "s_lshl_b32 s25, s22, 10\n"
        "s_add_u32 s25, s25, 6291456\n"
        "s_add_u32 s26, s22, 16\n"
        "s_cmp_eq_u32 s30, 0\n"
        "s_cselect_b32 s24, s24, s25\n"
        "s_cselect_b32 s26, s21, s26\n"
        "s_cselect_b32 s31, s22, s21\n"
        "s_mul_i32 s27, s30, 768\n"
        "s_add_u32 s27, s27, 256\n"
        "s_lshl_b32 s26, s26, 7\n"
        "s_add_u32 s26, s26, 6815744\n"
        "v_mov_b32 v5, s26\n"
        "v_add_u32 v8, s27, v25\n"
        "s_lshl_b32 s26, s30, 7\n"
        "s_add_u32 s26, s26, 136968\n"
        "v_mov_b32 v12, s26\n"
        "s_add_u32 s24, s8, s24\n"
        "s_addc_u32 s25, s9, 0\n"
        "s_add_u32 s28, s30, 1\n"
        "v_mov_b32 v13, s28\n"
        "s_lshl_b32 s28, s30, 0\n"
        "s_add_u32 s28, s28, 4\n"
        "s_load_dword s32, s[24:25], 0x0 glc\n"
        "s_load_dword s32, s[24:25], 0x80 glc\n"
        "s_load_dword s32, s[24:25], 0x100 glc\n"
        "s_load_dword s32, s[24:25], 0x180 glc\n"
        "s_load_dword s32, s[24:25], 0x200 glc\n"
        "s_load_dword s32, s[24:25], 0x40 glc\n"
        "s_load_dword s32, s[24:25], 0xc0 glc\n"
        "s_load_dword s32, s[24:25], 0x140 glc\n"
        "s_load_dword s32, s[24:25], 0x1c0 glc\n"
        "s_load_dword s32, s[24:25], 0x240 glc\n"
        "s_mov_b32 s38, 8388608\n"
        "W8_tg2:\n"
        "ds_read_b32 v14, v25 offset:1024\n"
        "s_sub_u32 s38, s38, 1\n"
        "s_waitcnt lgkmcnt(0)\n"
        "s_cbranch_scc1 W8_tg2_x\n"
        "v_readfirstlane_b32 s39, v14\n"
        "s_cmp_ge_u32 s39, 1\n"
        "s_cbranch_scc1 W8_tg2_x\n"
        "s_sleep 2\n"
        "s_branch W8_tg2\n"
        "W8_tg2_x:\n"
        "ds_read_b64 v[6:7], v12\n"
        "s_mov_b32 s38, 8388608\n"
        "W8_pst:\n"
        "ds_read_b32 v14, v25 offset:1544\n"
        "s_sub_u32 s38, s38, 1\n"
        "s_waitcnt lgkmcnt(0)\n"
        "s_cbranch_scc1 W8_pst_x\n"
        "v_readfirstlane_b32 s39, v14\n"
        "s_cmp_ge_u32 s39, 2\n"
        "s_cbranch_scc1 W8_pst_x\n"
        "s_sleep 4\n"
        "s_branch W8_pst\n"
        "W8_pst_x:\n"
        "s_mov_b32 s29, 2097152\n"
        "s_mov_b32 s26, 1\n"
        "W8_pl:\n"
        "s_load_dwordx16 s[32:47], s[24:25], 0x0 glc\n"
        "s_load_dwordx16 s[48:63], s[24:25], 0x40 glc\n"
        "s_sub_u32 s29, s29, 1\n"
        "s_cselect_b32 s27, 1, 0\n"
        "s_waitcnt lgkmcnt(0)\n"
        "v_cmpx_eq_u64 vcc, s[32:33], v[6:7]\n"
        "v_cmpx_eq_u64 vcc, s[34:35], v[6:7]\n"
        "v_cmpx_eq_u64 vcc, s[36:37], v[6:7]\n"
        "v_cmpx_eq_u64 vcc, s[38:39], v[6:7]\n"
        "v_cmpx_eq_u64 vcc, s[40:41], v[6:7]\n"
        "v_cmpx_eq_u64 vcc, s[42:43], v[6:7]\n"
        "v_cmpx_eq_u64 vcc, s[44:45], v[6:7]\n"
        "v_cmpx_eq_u64 vcc, s[46:47], v[6:7]\n"
        "v_cmpx_eq_u64 vcc, s[48:49], v[6:7]\n"
        "v_cmpx_eq_u64 vcc, s[50:51], v[6:7]\n"
        "v_cmpx_eq_u64 vcc, s[52:53], v[6:7]\n"
        "v_cmpx_eq_u64 vcc, s[54:55], v[6:7]\n"
        "v_cmpx_eq_u64 vcc, s[56:57], v[6:7]\n"
        "v_cmpx_eq_u64 vcc, s[58:59], v[6:7]\n"
        "v_cmpx_eq_u64 vcc, s[60:61], v[6:7]\n"
        "v_cmpx_eq_u64 vcc, s[62:63], v[6:7]\n"
        "s_nop 4\n"
        "s_cbranch_execnz W8_pg\n"
        "s_mov_b64 exec, -1\n"
        "s_cmp_eq_u32 s27, 1\n"
        "s_cbranch_scc1 W8_pg\n"
        "s_sleep 2\n"
        "s_branch W8_pl\n"
        "W8_pg:\n"
        "s_max_i32 s29, s29, 0\n"
        "s_cmp_eq_u32 s26, 0\n"
        "s_cbranch_scc1 W8_pn\n"
        "s_mov_b32 s38, 8388608\n"
        "W8_pub:\n"
        "ds_read_b32 v14, v25 offset:512\n"
        "s_sub_u32 s38, s38, 1\n"
        "s_waitcnt lgkmcnt(0)\n"
        "s_cbranch_scc1 W8_pub_x\n"
        "v_readfirstlane_b32 s39, v14\n"
        "s_cmp_ge_u32 s39, 1\n"
        "s_cbranch_scc1 W8_pub_x\n"
        "s_sleep 1\n"
        "s_branch W8_pub\n"
        "W8_pub_x:\n"
        "s_mov_b32 s26, 0\n"
        "W8_pn:\n"
        "ds_write_b32 v8, v13\n"
        "v_add_u32 v13, 1, v13\n"
        "s_add_u32 s24, s24, 128\n"
        "s_addc_u32 s25, s25, 0\n"
        "s_sub_u32 s28, s28, 1\n"
        "s_cmp_lg_u32 s28, 0\n"
        "s_cbranch_scc1 W8_pl\n"
        "s_cmp_lg_u32 s31, 0\n"
        "s_cbranch_scc1 W8_fin\n"
        "s_mov_b64 exec, 1\n"
        "global_store_dwordx2 v5, v[6:7], s[8:9] sc1\n"
        "s_branch W8_fin\n"
        "W8_load:\n"
        "v_lshlrev_b32 v2, 4, v1\n"
        "s_add_u32 s24, s33, 0\n"
        "s_and_b32 s24, s24, 3\n"
        "s_lshl_b32 s25, s24, 13\n"
        "v_add_u32 v4, s25, v2\n"
        "v_add_u32 v8, 4096, v4\n"
        "s_mul_i32 s25, s24, 6144\n"
        "v_add_u32 v12, s25, v2\n"
        "s_add_u32 s24, s33, 1\n"
        "s_and_b32 s24, s24, 3\n"
        "s_lshl_b32 s25, s24, 13\n"
        "v_add_u32 v5, s25, v2\n"
        "v_add_u32 v9, 4096, v5\n"
        "s_mul_i32 s25, s24, 6144\n"
        "v_add_u32 v13, s25, v2\n"
        "s_add_u32 s24, s33, 2\n"
        "s_and_b32 s24, s24, 3\n"
        "s_lshl_b32 s25, s24, 13\n"
        "v_add_u32 v6, s25, v2\n"
        "v_add_u32 v10, 4096, v6\n"
        "s_mul_i32 s25, s24, 6144\n"
        "v_add_u32 v14, s25, v2\n"
        "s_add_u32 s24, s33, 3\n"
        "s_and_b32 s24, s24, 3\n"
        "s_lshl_b32 s25, s24, 13\n"
        "v_add_u32 v7, s25, v2\n"
        "v_add_u32 v11, 4096, v7\n"
        "s_mul_i32 s25, s24, 6144\n"
        "v_add_u32 v15, s25, v2\n"
        "s_waitcnt lgkmcnt(0)\n"
        "s_lshl_b32 s24, s22, 17\n"
        "s_add_u32 s24, s24, 2097152\n"
        "s_add_u32 s40, s8, s24\n"
        "s_addc_u32 s41, s9, 0\n"
        "s_mul_i32 s24, s22, 98304\n"
        "s_add_u32 s24, s24, 4194304\n"
        "s_add_u32 s42, s8, s24\n"
        "s_addc_u32 s43, s9, 0\n"
        "s_mov_b32 s34, 0\n"
        "s_mov_b32 s35, 2\n"
        "v_mov_b32 v3, -3\n"
        "s_mov_b32 s38, 8388608\n"
        "W8_lr00:\n"
        "ds_read_b32 v20, v25 offset:1024\n"
        "s_sub_u32 s38, s38, 1\n"
        "s_waitcnt lgkmcnt(0)\n"
        "s_cbranch_scc1 W8_lr00_x\n"
        "v_readfirstlane_b32 s39, v20\n"
        "s_cmp_ge_u32 s39, 2\n"
        "s_cbranch_scc1 W8_lr00_x\n"
        "s_sleep 1\n"
        "s_branch W8_lr00\n"
        "W8_lr00_x:\n"
        "s_mov_b32 m0, s34\n"
        "s_add_u32 s34, s34, 4096\n"
        "global_load_lds_dwordx4 v4, s[40:41] offset:0 sc1\n"
        "global_load_lds_dwordx4 v4, s[40:41] offset:1024 sc1\n"
        "global_load_lds_dwordx4 v4, s[40:41] offset:2048 sc1\n"
        "global_load_lds_dwordx4 v4, s[40:41] offset:3072 sc1\n"
        "s_mov_b32 m0, s34\n"
        "s_add_u32 s34, s34, 4096\n"
        "global_load_lds_dwordx4 v8, s[40:41] offset:0 sc1\n"
        "global_load_lds_dwordx4 v8, s[40:41] offset:1024 sc1\n"
        "global_load_lds_dwordx4 v8, s[40:41] offset:2048 sc1\n"
        "global_load_lds_dwordx4 v8, s[40:41] offset:3072 sc1\n"
        "s_mov_b32 m0, s34\n"
        "s_add_u32 s34, s34, 4096\n"
        "global_load_lds_dwordx4 v5, s[40:41] offset:0 sc1\n"
        "global_load_lds_dwordx4 v5, s[40:41] offset:1024 sc1\n"
        "global_load_lds_dwordx4 v5, s[40:41] offset:2048 sc1\n"
        "global_load_lds_dwordx4 v5, s[40:41] offset:3072 sc1\n"
        "s_mov_b32 m0, s34\n"
        "s_add_u32 s34, s34, 4096\n"
        "global_load_lds_dwordx4 v9, s[40:41] offset:0 sc1\n"
        "global_load_lds_dwordx4 v9, s[40:41] offset:1024 sc1\n"
        "global_load_lds_dwordx4 v9, s[40:41] offset:2048 sc1\n"
        "global_load_lds_dwordx4 v9, s[40:41] offset:3072 sc1\n"
        "s_mov_b32 m0, s34\n"
        "s_add_u32 s34, s34, 4096\n"
        "global_load_lds_dwordx4 v6, s[40:41] offset:0 sc1\n"
        "global_load_lds_dwordx4 v6, s[40:41] offset:1024 sc1\n"
        "global_load_lds_dwordx4 v6, s[40:41] offset:2048 sc1\n"
        "global_load_lds_dwordx4 v6, s[40:41] offset:3072 sc1\n"
        "s_mov_b32 m0, s34\n"
        "s_add_u32 s34, s34, 4096\n"
        "global_load_lds_dwordx4 v10, s[40:41] offset:0 sc1\n"
        "global_load_lds_dwordx4 v10, s[40:41] offset:1024 sc1\n"
        "global_load_lds_dwordx4 v10, s[40:41] offset:2048 sc1\n"
        "global_load_lds_dwordx4 v10, s[40:41] offset:3072 sc1\n"
        "v_mov_b32 v21, 1\n"
        "s_waitcnt vmcnt(16)\n"
        "ds_max_i32 v25, v21 offset:0\n"
        "s_mov_b32 m0, s34\n"
        "s_add_u32 s34, s34, 4096\n"
        "global_load_lds_dwordx4 v7, s[40:41] offset:0 sc1\n"
        "global_load_lds_dwordx4 v7, s[40:41] offset:1024 sc1\n"
        "global_load_lds_dwordx4 v7, s[40:41] offset:2048 sc1\n"
        "global_load_lds_dwordx4 v7, s[40:41] offset:3072 sc1\n"
        "s_mov_b32 m0, s34\n"
        "s_add_u32 s34, s34, 4096\n"
        "global_load_lds_dwordx4 v11, s[40:41] offset:0 sc1\n"
        "global_load_lds_dwordx4 v11, s[40:41] offset:1024 sc1\n"
        "global_load_lds_dwordx4 v11, s[40:41] offset:2048 sc1\n"
        "global_load_lds_dwordx4 v11, s[40:41] offset:3072 sc1\n"
        "v_mov_b32 v3, 1\n"
        "s_add_u32 s40, s40, 32768\n"
        "s_addc_u32 s41, s41, 0\n"
        "s_mov_b32 s35, 3\n"
        "W8_lq:\n"
        "ds_read_b32 v20, v25 offset:1024\n"
        "s_waitcnt lgkmcnt(0)\n"
        "v_readfirstlane_b32 s39, v20\n"
        "s_cmp_ge_u32 s39, s35\n"
        "s_cbranch_scc1 W8_lr_x\n"
        "v_add_u32 v21, 1, v3\n"
        "s_waitcnt vmcnt(16)\n"
        "ds_max_i32 v25, v21 offset:0\n"
        "v_add_u32 v21, 2, v3\n"
        "s_waitcnt vmcnt(8)\n"
        "ds_max_i32 v25, v21 offset:0\n"
        "v_add_u32 v21, 3, v3\n"
        "s_waitcnt vmcnt(0)\n"
        "ds_max_i32 v25, v21 offset:0\n"
        "s_mov_b32 s38, 8388608\n"
        "W8_lr:\n"
        "ds_read_b32 v20, v25 offset:1024\n"
        "s_sub_u32 s38, s38, 1\n"
        "s_waitcnt lgkmcnt(0)\n"
        "s_cbranch_scc1 W8_lr_x\n"
        "v_readfirstlane_b32 s39, v20\n"
        "s_cmp_ge_u32 s39, s35\n"
        "s_cbranch_scc1 W8_lr_x\n"
        "s_sleep 1\n"
        "s_branch W8_lr\n"
        "W8_lr_x:\n"
        "s_mov_b32 m0, s34\n"
        "s_add_u32 s34, s34, 4096\n"
        "global_load_lds_dwordx4 v4, s[40:41] offset:0 sc1\n"
        "global_load_lds_dwordx4 v4, s[40:41] offset:1024 sc1\n"
        "global_load_lds_dwordx4 v4, s[40:41] offset:2048 sc1\n"
        "global_load_lds_dwordx4 v4, s[40:41] offset:3072 sc1\n"
        "s_mov_b32 m0, s34\n"
        "s_add_u32 s34, s34, 4096\n"
        "global_load_lds_dwordx4 v8, s[40:41] offset:0 sc1\n"
        "global_load_lds_dwordx4 v8, s[40:41] offset:1024 sc1\n"
        "global_load_lds_dwordx4 v8, s[40:41] offset:2048 sc1\n"
        "global_load_lds_dwordx4 v8, s[40:41] offset:3072 sc1\n"
        "v_add_u32 v3, 1, v3\n"
        "s_waitcnt vmcnt(24)\n"
        "ds_max_i32 v25, v3 offset:0\n"
        "s_mov_b32 m0, s34\n"
        "s_add_u32 s34, s34, 4096\n"
        "global_load_lds_dwordx4 v5, s[40:41] offset:0 sc1\n"
        "global_load_lds_dwordx4 v5, s[40:41] offset:1024 sc1\n"
        "global_load_lds_dwordx4 v5, s[40:41] offset:2048 sc1\n"
        "global_load_lds_dwordx4 v5, s[40:41] offset:3072 sc1\n"
        "s_mov_b32 m0, s34\n"
        "s_add_u32 s34, s34, 4096\n"
        "global_load_lds_dwordx4 v9, s[40:41] offset:0 sc1\n"
        "global_load_lds_dwordx4 v9, s[40:41] offset:1024 sc1\n"
        "global_load_lds_dwordx4 v9, s[40:41] offset:2048 sc1\n"
        "global_load_lds_dwordx4 v9, s[40:41] offset:3072 sc1\n"
        "v_add_u32 v3, 1, v3\n"
        "s_waitcnt vmcnt(24)\n"
        "ds_max_i32 v25, v3 offset:0\n"
        "s_mov_b32 m0, s34\n"
        "s_add_u32 s34, s34, 4096\n"
        "global_load_lds_dwordx4 v6, s[40:41] offset:0 sc1\n"
        "global_load_lds_dwordx4 v6, s[40:41] offset:1024 sc1\n"
        "global_load_lds_dwordx4 v6, s[40:41] offset:2048 sc1\n"
        "global_load_lds_dwordx4 v6, s[40:41] offset:3072 sc1\n"
        "s_mov_b32 m0, s34\n"
        "s_add_u32 s34, s34, 4096\n"
        "global_load_lds_dwordx4 v10, s[40:41] offset:0 sc1\n"
        "global_load_lds_dwordx4 v10, s[40:41] offset:1024 sc1\n"
        "global_load_lds_dwordx4 v10, s[40:41] offset:2048 sc1\n"
        "global_load_lds_dwordx4 v10, s[40:41] offset:3072 sc1\n"
        "v_add_u32 v3, 1, v3\n"
        "s_waitcnt vmcnt(24)\n"
        "ds_max_i32 v25, v3 offset:0\n"
        "s_mov_b32 m0, s34\n"
        "s_add_u32 s34, s34, 4096\n"
        "global_load_lds_dwordx4 v7, s[40:41] offset:0 sc1\n"
        "global_load_lds_dwordx4 v7, s[40:41] offset:1024 sc1\n"
        "global_load_lds_dwordx4 v7, s[40:41] offset:2048 sc1\n"
        "global_load_lds_dwordx4 v7, s[40:41] offset:3072 sc1\n"
        "s_mov_b32 m0, s34\n"
        "s_add_u32 s34, s34, 4096\n"
        "global_load_lds_dwordx4 v11, s[40:41] offset:0 sc1\n"
        "global_load_lds_dwordx4 v11, s[40:41] offset:1024 sc1\n"
        "global_load_lds_dwordx4 v11, s[40:41] offset:2048 sc1\n"
        "global_load_lds_dwordx4 v11, s[40:41] offset:3072 sc1\n"
        "v_add_u32 v3, 1, v3\n"
        "s_waitcnt vmcnt(24)\n"
        "ds_max_i32 v25, v3 offset:0\n"
        "s_add_u32 s40, s40, 32768\n"
        "s_addc_u32 s41, s41, 0\n"
        "s_add_u32 s35, s35, 1\n"
        "s_cmp_lt_u32 s35, 6\n"
        "s_cbranch_scc1 W8_lq\n"
        "s_mov_b32 s34, 0\n"
        "ds_read_b32 v20, v25 offset:1024\n"
        "s_waitcnt lgkmcnt(0)\n"
        "v_readfirstlane_b32 s39, v20\n"
        "s_cmp_ge_u32 s39, 6\n"
        "s_cbranch_scc1 W8_lr0_x\n"
        "v_add_u32 v21, 1, v3\n"
        "s_waitcnt vmcnt(16)\n"
        "ds_max_i32 v25, v21 offset:0\n"
        "v_add_u32 v21, 2, v3\n"
        "s_waitcnt vmcnt(8)\n"
        "ds_max_i32 v25, v21 offset:0\n"
        "v_add_u32 v21, 3, v3\n"
        "s_waitcnt vmcnt(0)\n"
        "ds_max_i32 v25, v21 offset:0\n"
        "s_mov_b32 s38, 8388608\n"
        "W8_lr0:\n"
        "ds_read_b32 v20, v25 offset:1024\n"
        "s_sub_u32 s38, s38, 1\n"
        "s_waitcnt lgkmcnt(0)\n"
        "s_cbranch_scc1 W8_lr0_x\n"
        "v_readfirstlane_b32 s39, v20\n"
        "s_cmp_ge_u32 s39, 6\n"
        "s_cbranch_scc1 W8_lr0_x\n"
        "s_sleep 1\n"
        "s_branch W8_lr0\n"
        "W8_lr0_x:\n"
        "ds_read_b32 v20, v25 offset:768\n"
        "s_waitcnt lgkmcnt(0)\n"
        "v_readfirstlane_b32 s39, v20\n"
        "s_cmp_ge_u32 s39, 543744\n"
        "s_cbranch_scc1 W8_lf0_x\n"
        "v_add_u32 v21, 1, v3\n"
        "s_waitcnt vmcnt(16)\n"
        "ds_max_i32 v25, v21 offset:0\n"
        "v_add_u32 v21, 2, v3\n"
        "s_waitcnt vmcnt(8)\n"
        "ds_max_i32 v25, v21 offset:0\n"
        "v_add_u32 v21, 3, v3\n"
        "s_waitcnt vmcnt(0)\n"
        "ds_max_i32 v25, v21 offset:0\n"
        "s_mov_b32 s38, 8388608\n"
        "W8_lf0:\n"
        "ds_read_b32 v20, v25 offset:768\n"
        "s_sub_u32 s38, s38, 1\n"
        "s_waitcnt lgkmcnt(0)\n"
        "s_cbranch_scc1 W8_lf0_x\n"
        "v_readfirstlane_b32 s39, v20\n"
        "s_cmp_ge_u32 s39, 543744\n"
        "s_cbranch_scc1 W8_lf0_x\n"
        "s_sleep 1\n"
        "s_branch W8_lf0\n"
        "W8_lf0_x:\n"
        "s_add_u32 s40, s42, 4096\n"
        "s_addc_u32 s41, s43, 0\n"
        "W8_ll0:\n"
        "s_mov_b32 m0, s34\n"
        "s_add_u32 s34, s34, 2048\n"
        "global_load_lds_dwordx4 v12, s[40:41] sc1\n"
        "global_load_lds_dwordx4 v12, s[40:41] offset:1024 sc1\n"
        "s_mov_b32 m0, s34\n"
        "s_add_u32 s34, s34, 2048\n"
        "global_load_lds_dwordx4 v13, s[40:41] sc1\n"
        "global_load_lds_dwordx4 v13, s[40:41] offset:1024 sc1\n"
        "s_mov_b32 m0, s34\n"
        "s_add_u32 s34, s34, 2048\n"
        "global_load_lds_dwordx4 v14, s[40:41] sc1\n"
        "global_load_lds_dwordx4 v14, s[40:41] offset:1024 sc1\n"
        "s_mov_b32 m0, s34\n"
        "s_add_u32 s34, s34, 2048\n"
        "global_load_lds_dwordx4 v15, s[40:41] sc1\n"
        "global_load_lds_dwordx4 v15, s[40:41] offset:1024 sc1\n"
        "s_add_u32 s40, s40, 24576\n"
        "s_addc_u32 s41, s41, 0\n"
        "v_add_u32 v3, 1, v3\n"
        "s_waitcnt vmcnt(24)\n"
        "ds_max_i32 v25, v3 offset:0\n"
        "s_cmp_lt_u32 s34, 32768\n"
        "s_cbranch_scc1 W8_ll0\n"
        "v_add_u32 v3, -1, v3\n"
        "ds_read_b32 v20, v25 offset:772\n"
        "s_waitcnt lgkmcnt(0)\n"
        "v_readfirstlane_b32 s39, v20\n"
        "s_cmp_ge_u32 s39, 543744\n"
        "s_cbranch_scc1 W8_lf1_x\n"
        "v_add_u32 v21, 1, v3\n"
        "s_waitcnt vmcnt(24)\n"
        "ds_max_i32 v25, v21 offset:0\n"
        "v_add_u32 v21, 2, v3\n"
        "s_waitcnt vmcnt(16)\n"
        "ds_max_i32 v25, v21 offset:0\n"
        "v_add_u32 v21, 3, v3\n"
        "s_waitcnt vmcnt(8)\n"
        "ds_max_i32 v25, v21 offset:0\n"
        "v_add_u32 v21, 4, v3\n"
        "s_waitcnt vmcnt(0)\n"
        "ds_max_i32 v25, v21 offset:0\n"
        "s_mov_b32 s38, 8388608\n"
        "W8_lf1:\n"
        "ds_read_b32 v20, v25 offset:772\n"
        "s_sub_u32 s38, s38, 1\n"
        "s_waitcnt lgkmcnt(0)\n"
        "s_cbranch_scc1 W8_lf1_x\n"
        "v_readfirstlane_b32 s39, v20\n"
        "s_cmp_ge_u32 s39, 543744\n"
        "s_cbranch_scc1 W8_lf1_x\n"
        "s_sleep 1\n"
        "s_branch W8_lf1\n"
        "W8_lf1_x:\n"
        "s_add_u32 s40, s42, 0\n"
        "s_addc_u32 s41, s43, 0\n"
        "W8_ll1:\n"
        "s_mov_b32 m0, s34\n"
        "s_add_u32 s34, s34, 2048\n"
        "global_load_lds_dwordx4 v12, s[40:41] sc1\n"
        "global_load_lds_dwordx4 v12, s[40:41] offset:1024 sc1\n"
        "s_mov_b32 m0, s34\n"
        "s_add_u32 s34, s34, 2048\n"
        "global_load_lds_dwordx4 v13, s[40:41] sc1\n"
        "global_load_lds_dwordx4 v13, s[40:41] offset:1024 sc1\n"
        "s_mov_b32 m0, s34\n"
        "s_add_u32 s34, s34, 2048\n"
        "global_load_lds_dwordx4 v14, s[40:41] sc1\n"
        "global_load_lds_dwordx4 v14, s[40:41] offset:1024 sc1\n"
        "s_mov_b32 m0, s34\n"
        "s_add_u32 s34, s34, 2048\n"
        "global_load_lds_dwordx4 v15, s[40:41] sc1\n"
        "global_load_lds_dwordx4 v15, s[40:41] offset:1024 sc1\n"
        "s_add_u32 s40, s40, 24576\n"
        "s_addc_u32 s41, s41, 0\n"
        "v_add_u32 v3, 1, v3\n"
        "s_waitcnt vmcnt(32)\n"
        "ds_max_i32 v25, v3 offset:0\n"
        "s_cmp_lt_u32 s34, 65536\n"
        "s_cbranch_scc1 W8_ll1\n"
        "ds_read_b32 v20, v25 offset:776\n"
        "s_waitcnt lgkmcnt(0)\n"
        "v_readfirstlane_b32 s39, v20\n"
        "s_cmp_ge_u32 s39, 543744\n"
        "s_cbranch_scc1 W8_lf2_x\n"
        "v_add_u32 v21, 1, v3\n"
        "s_waitcnt vmcnt(24)\n"
        "ds_max_i32 v25, v21 offset:0\n"
        "v_add_u32 v21, 2, v3\n"
        "s_waitcnt vmcnt(16)\n"
        "ds_max_i32 v25, v21 offset:0\n"
        "v_add_u32 v21, 3, v3\n"
        "s_waitcnt vmcnt(8)\n"
        "ds_max_i32 v25, v21 offset:0\n"
        "v_add_u32 v21, 4, v3\n"
        "s_waitcnt vmcnt(0)\n"
        "ds_max_i32 v25, v21 offset:0\n"
        "s_mov_b32 s38, 8388608\n"
        "W8_lf2:\n"
        "ds_read_b32 v20, v25 offset:776\n"
        "s_sub_u32 s38, s38, 1\n"
        "s_waitcnt lgkmcnt(0)\n"
        "s_cbranch_scc1 W8_lf2_x\n"
        "v_readfirstlane_b32 s39, v20\n"
        "s_cmp_ge_u32 s39, 543744\n"
        "s_cbranch_scc1 W8_lf2_x\n"
        "s_sleep 1\n"
        "s_branch W8_lf2\n"
        "W8_lf2_x:\n"
        "s_add_u32 s40, s42, 2048\n"
        "s_addc_u32 s41, s43, 0\n"
        "W8_ll2:\n"
        "s_mov_b32 m0, s34\n"
        "s_add_u32 s34, s34, 2048\n"
        "global_load_lds_dwordx4 v12, s[40:41] sc1\n"
        "global_load_lds_dwordx4 v12, s[40:41] offset:1024 sc1\n"
        "s_mov_b32 m0, s34\n"
        "s_add_u32 s34, s34, 2048\n"
        "global_load_lds_dwordx4 v13, s[40:41] sc1\n"
        "global_load_lds_dwordx4 v13, s[40:41] offset:1024 sc1\n"
        "s_mov_b32 m0, s34\n"
        "s_add_u32 s34, s34, 2048\n"
        "global_load_lds_dwordx4 v14, s[40:41] sc1\n"
        "global_load_lds_dwordx4 v14, s[40:41] offset:1024 sc1\n"
        "s_mov_b32 m0, s34\n"
        "s_add_u32 s34, s34, 2048\n"
        "global_load_lds_dwordx4 v15, s[40:41] sc1\n"
        "global_load_lds_dwordx4 v15, s[40:41] offset:1024 sc1\n"
        "s_add_u32 s40, s40, 24576\n"
        "s_addc_u32 s41, s41, 0\n"
        "v_add_u32 v3, 1, v3\n"
        "s_waitcnt vmcnt(32)\n"
        "ds_max_i32 v25, v3 offset:0\n"
        "s_cmp_lt_u32 s34, 98304\n"
        "s_cbranch_scc1 W8_ll2\n"
        "v_add_u32 v3, 1, v3\n"
        "s_waitcnt vmcnt(24)\n"
        "ds_max_i32 v25, v3 offset:0\n"
        "v_add_u32 v3, 1, v3\n"
        "s_waitcnt vmcnt(16)\n"
        "ds_max_i32 v25, v3 offset:0\n"
        "v_add_u32 v3, 1, v3\n"
        "s_waitcnt vmcnt(8)\n"
        "ds_max_i32 v25, v3 offset:0\n"
        "v_add_u32 v3, 1, v3\n"
        "s_waitcnt vmcnt(0)\n"
        "ds_max_i32 v25, v3 offset:0\n"
        "s_branch W8_fin\n"
        "W8_comp:\n"
        "s_setprio 3\n"
        "s_lshl_b32 s26, s21, 2\n"
        "s_add_u32 s26, s26, s20\n"
        "s_sub_u32 s26, s26, 4\n"
        "v_lshrrev_b32 v2, 4, v1\n"
        "v_and_b32 v3, 15, v1\n"
        "v_lshlrev_b32 v248, 11, v2\n"
        "v_lshl_add_u32 v248, v3, 4, v248\n"
        "v_add_u32 v249, 65536, v248\n"
        "v_mov_b32 v250, 135936\n"
        "v_lshlrev_b32 v240, 4, v1\n"
        "v_mul_u32_u24 v2, 28672, v2\n"
        "v_lshl_add_u32 v251, v3, 3, v2\n"
        "s_lshl_b32 s24, s23, 6\n"
        "v_add_u32 v251, s24, v251\n"
        "v_and_b32 v4, 7, v1\n"
        "v_lshl_add_u32 v252, v4, 3, v2\n"
        "v_bfe_u32 v4, v1, 3, 1\n"
        "v_lshl_add_u32 v252, v4, 2, v252\n"
        "s_cmp_eq_u32 s23, 0\n"
        "s_cselect_b32 s24, 384, 0\n"
        "v_add_u32 v252, s24, v252\n"
        "v_lshlrev_b32 v253, 5, v3\n"
        "v_add_u32 v253, 133632, v253\n"
        "s_waitcnt lgkmcnt(0)\n"
        "s_lshl_b32 s27, s26, 15\n"
        "s_add_u32 s24, s33, 0\n"
        "s_and_b32 s24, s24, 3\n"
        "s_lshl_b32 s24, s24, 11\n"
        "s_add_u32 s24, s24, s27\n"
        "s_add_u32 s56, s8, s24\n"
        "s_addc_u32 s57, s9, 0\n"
        "s_add_u32 s24, s33, 1\n"
        "s_and_b32 s24, s24, 3\n"
        "s_lshl_b32 s24, s24, 11\n"
        "s_add_u32 s24, s24, s27\n"
        "s_add_u32 s58, s8, s24\n"
        "s_addc_u32 s59, s9, 0\n"
        "s_add_u32 s24, s33, 2\n"
        "s_and_b32 s24, s24, 3\n"
        "s_lshl_b32 s24, s24, 11\n"
        "s_add_u32 s24, s24, s27\n"
        "s_add_u32 s60, s8, s24\n"
        "s_addc_u32 s61, s9, 0\n"
        "s_add_u32 s24, s33, 3\n"
        "s_and_b32 s24, s24, 3\n"
        "s_lshl_b32 s24, s24, 11\n"
        "s_add_u32 s24, s24, s27\n"
        "s_add_u32 s62, s8, s24\n"
        "s_addc_u32 s63, s9, 0\n"
        "s_mul_i32 s24, s26, 229376\n"
        "s_mul_i32 s25, s22, 448\n"
        "s_add_u32 s24, s24, s25\n"
        "s_add_u32 s40, s10, s24\n"
        "s_addc_u32 s41, s11, 0\n"
        "s_add_u32 s42, s40, 7168\n"
        "s_addc_u32 s43, s41, 0\n"
        "s_add_u32 s44, s40, 14336\n"
        "s_addc_u32 s45, s41, 0\n"
        "s_add_u32 s46, s40, 21504\n"
        "s_addc_u32 s47, s41, 0\n"
        "s_add_u32 s48, s40, 114688\n"
        "s_addc_u32 s49, s41, 0\n"
        "s_add_u32 s50, s40, 121856\n"
        "s_addc_u32 s51, s41, 0\n"
        "s_add_u32 s52, s40, 129024\n"
        "s_addc_u32 s53, s41, 0\n"
        "s_add_u32 s54, s40, 136192\n"
        "s_addc_u32 s55, s41, 0\n"
        "s_sub_u32 s28, s20, 4\n"
        "s_mul_i32 s24, s21, 512\n"
        "s_mul_i32 s25, s22, 8\n"
        "s_add_u32 s24, s24, s25\n"
        "s_add_u32 s24, s24, 6029312\n"
        "s_mul_i32 s25, s22, 1024\n"
        "s_mul_i32 s27, s21, 8\n"
        "s_add_u32 s25, s25, s27\n"
        "s_add_u32 s25, s25, 6291456\n"
        "s_sub_u32 s25, s25, s24\n"
        "v_mul_lo_u32 v24, v1, s25\n"
        "s_mul_i32 s25, s28, 128\n"
        "s_add_u32 s24, s24, s25\n"
        "v_add_u32 v24, s24, v24\n"
        "s_mul_i32 s29, s28, 5120\n"
        "s_add_u32 s29, s29, 98304\n"
        "v_and_b32 v6, 7, v1\n"
        "v_lshrrev_b32 v7, 3, v1\n"
        "v_mul_u32_u24 v8, 288, v6\n"
        "v_lshl_add_u32 v8, v7, 4, v8\n"
        "v_add_u32 v8, s29, v8\n"
        "v_add_u32 v9, s29, v240\n"
        "s_lshl_b32 s24, s21, 2\n"
        "s_lshr_b32 s25, s22, 2\n"
        "s_add_u32 s24, s24, s25\n"
        "s_lshl_b32 s24, s24, 2\n"
        "s_add_u32 s24, s24, s28\n"
        "s_lshl_b32 s24, s24, 13\n"
        "s_and_b32 s25, s22, 2\n"
        "s_lshl_b32 s25, s25, 9\n"
        "s_add_u32 s24, s24, s25\n"
        "s_lshl_b32 s25, s23, 7\n"
        "s_add_u32 s24, s24, s25\n"
        "s_add_u32 s24, s24, 2048\n"
        "v_lshrrev_b32 v10, 5, v1\n"
        "v_and_b32 v7, 3, v7\n"
        "v_lshl_add_u32 v11, v10, 11, s24\n"
        "v_lshl_add_u32 v11, v7, 8, v11\n"
        "v_lshl_add_u32 v11, v6, 4, v11\n"
        "s_lshl_b32 s24, s22, 2\n"
        "s_add_u32 s24, s24, s28\n"
        "s_lshl_b32 s24, s24, 4\n"
        "s_add_u32 s24, s24, s21\n"
        "s_lshl_b32 s24, s24, 11\n"
        "s_add_u32 s24, s24, 2097152\n"
        "v_add_u32 v13, s24, v240\n"
        "s_lshl_b32 s24, s22, 4\n"
        "s_add_u32 s24, s24, s21\n"
        "s_lshl_b32 s24, s24, 10\n"
        "s_add_u32 s24, s24, 5767168\n"
        "v_and_b32 v14, 31, v1\n"
        "v_lshl_add_u32 v14, v14, 4, s24\n"
        "v_lshl_add_u32 v14, v10, 3, v14\n"
        "v_lshlrev_b32 v15, 3, v1\n"
        "v_add_u32 v15, 118016, v15\n"
        "s_add_u32 s28, s28, 1\n"
        "s_mov_b32 s38, 8388608\n"
        "W8_tgc:\n"
        "ds_read_b32 v12, v250 offset:772\n"
        "s_sub_u32 s38, s38, 1\n"
        "s_waitcnt lgkmcnt(0)\n"
        "s_cbranch_scc1 W8_tgc_x\n"
        "v_readfirstlane_b32 s39, v12\n"
        "s_cmp_ge_u32 s39, 1\n"
        "s_cbranch_scc1 W8_tgc_x\n"
        "s_sleep 1\n"
        "s_branch W8_tgc\n"
        "W8_tgc_x:\n"
        "v_lshrrev_b32 v12, 0, v1\n"
        "v_lshlrev_b32 v12, 7, v12\n"
        "v_add_u32 v12, 136968, v12\n"
        "s_mov_b64 exec, 3\n"
        "ds_read_b64 v[26:27], v12\n"
        "s_mov_b64 exec, -1\n"
        "s_mov_b32 s38, 8388608\n"
        "W8_pkc:\n"
        "ds_read_b32 v12, v250 offset:1292\n"
        "s_sub_u32 s38, s38, 1\n"
        "s_waitcnt lgkmcnt(0)\n"
        "s_cbranch_scc1 W8_pkc_x\n"
        "v_readfirstlane_b32 s39, v12\n"
        "s_cmp_ge_u32 s39, s28\n"
        "s_cbranch_scc1 W8_pkc_x\n"
        "s_sleep 1\n"
        "s_branch W8_pkc\n"
        "W8_pkc_x:\n"
        "ds_read_b128 v[28:31], v8\n"
        "ds_read_b128 v[32:35], v8 offset:128\n"
        "ds_read_b128 v[36:39], v9 offset:2304\n"
        "ds_read_b128 v[40:43], v9 offset:3328\n"
        "ds_read_b64 v[44:45], v15\n"
        "s_waitcnt lgkmcnt(3)\n"
        "global_store_dwordx4 v11, v[28:31], s[8:9] offset:-2048 sc1\n"
        "global_store_dwordx4 v11, v[32:35], s[8:9] offset:2048 sc1\n"
        "s_waitcnt lgkmcnt(0)\n"
        "global_store_dwordx4 v13, v[36:39], s[8:9] sc1\n"
        "global_store_dwordx4 v13, v[40:43], s[8:9] offset:1024 sc1\n"
        "s_cmp_lg_u32 s28, 4\n"
        "s_cbranch_scc1 W8_ncs\n"
        "global_store_dwordx2 v14, v[44:45], s[8:9] sc1\n"
        "W8_ncs:\n"
        "s_mov_b64 exec, 3\n"
        "s_waitcnt vmcnt(0)\n"
        "global_store_dwordx2 v24, v[26:27], s[8:9] sc1\n"
        "s_mov_b64 exec, -1\n"
        "s_mov_b32 s38, 8388608\n"
        "W8_rd0:\n"
        "ds_read_b32 v254, v250 offset:4\n"
        "s_sub_u32 s38, s38, 1\n"
        "s_waitcnt lgkmcnt(0)\n"
        "s_cbranch_scc1 W8_rd0_x\n"
        "v_readfirstlane_b32 s39, v254\n"
        "s_cmp_ge_u32 s39, 1\n"
        "s_cbranch_scc1 W8_rd0_x\n"
        "s_sleep 1\n"
        "s_branch W8_rd0\n"
        "W8_rd0_x:\n"
        "global_load_dwordx4 v[0:3], v240, s[56:57] offset:0 sc1\n"
        "global_load_dwordx4 v[4:7], v240, s[56:57] offset:1024 sc1\n"
        "global_load_dwordx4 v[8:11], v240, s[58:59] offset:0 sc1\n"
        "global_load_dwordx4 v[12:15], v240, s[58:59] offset:1024 sc1\n"
        "s_mov_b32 s34, 99\n"
        "ds_read_b32 v254, v250\n"
        "s_waitcnt lgkmcnt(0)\n"
        "v_readfirstlane_b32 s31, v254\n"
        "s_cmp_lt_u32 s31, 1\n"
        "s_cbranch_scc1 W8_s0\n"
        "W8_c0:\n"
        "ds_read_b128 v[208:211], v248 offset:0\n"
        "ds_read_b128 v[212:215], v248 offset:1024\n"
        "ds_read_b128 v[216:219], v248 offset:512\n"
        "ds_read_b128 v[220:223], v248 offset:1536\n"
        "ds_read_b128 v[224:227], v248 offset:256\n"
        "ds_read_b128 v[228:231], v248 offset:1280\n"
        "s_waitcnt vmcnt(2)\n"
        "s_waitcnt lgkmcnt(5)\n"
        "v_mfma_i32_16x16x64_i8 v[128:131], v[0:3], v[208:211], 0\n"
        "ds_read_b32 v241, v250 offset:4\n"
        "ds_read_b32 v254, v250\n"
        "s_waitcnt lgkmcnt(0)\n"
        "v_readfirstlane_b32 s31, v254\n"
        "v_mfma_i32_16x16x64_i8 v[132:135], v[4:7], v[208:211], 0\n"
        "ds_read_b128 v[232:235], v248 offset:768\n"
        "v_mfma_i32_16x16x64_i8 v[136:139], v[0:3], v[212:215], 0\n"
        "global_load_dwordx4 v[16:19], v240, s[60:61] offset:0 sc1\n"
        "v_mfma_i32_16x16x64_i8 v[140:143], v[4:7], v[212:215], 0\n"
        "ds_read_b128 v[236:239], v248 offset:1792\n"
        "v_mfma_i32_16x16x64_i8 v[144:147], v[0:3], v[216:219], 0\n"
        "v_readfirstlane_b32 s35, v241\n"
        "s_cmp_lt_u32 s31, 2\n"
        "s_cbranch_scc1 W8_s1\n"
        "W8_c1:\n"
        "v_mfma_i32_16x16x64_i8 v[148:151], v[4:7], v[216:219], 0\n"
        "ds_read_b128 v[208:211], v248 offset:8192\n"
        "v_mfma_i32_16x16x64_i8 v[152:155], v[0:3], v[220:223], 0\n"
        "s_cmp_lt_u32 s35, 2\n"
        "s_cbranch_scc1 W8_sR0\n"
        "W8_cR0:\n"
        "ds_read_b32 v254, v250\n"
        "v_mfma_i32_16x16x64_i8 v[156:159], v[4:7], v[220:223], 0\n"
        "ds_read_b128 v[212:215], v248 offset:9216\n"
        "v_mfma_i32_16x16x64_i8 v[160:163], v[0:3], v[224:227], 0\n"
        "global_load_dwordx4 v[20:23], v240, s[60:61] offset:1024 sc1\n"
        "v_mfma_i32_16x16x64_i8 v[164:167], v[4:7], v[224:227], 0\n"
        "ds_read_b128 v[216:219], v248 offset:8704\n"
        "v_mfma_i32_16x16x64_i8 v[168:171], v[0:3], v[228:231], 0\n"
        "global_load_dwordx4 v[24:27], v240, s[62:63] offset:0 sc1\n"
        "v_mfma_i32_16x16x64_i8 v[172:175], v[4:7], v[228:231], 0\n"
        "ds_read_b128 v[220:223], v248 offset:9728\n"
        "s_waitcnt lgkmcnt(6)\n"
        "v_mfma_i32_16x16x64_i8 v[176:179], v[0:3], v[232:235], 0\n"
        "global_load_dwordx4 v[28:31], v240, s[62:63] offset:1024 sc1\n"
        "v_add_u32 v240, 8192, v240\n"
        "v_mfma_i32_16x16x64_i8 v[180:183], v[4:7], v[232:235], 0\n"
        "ds_read_b128 v[224:227], v248 offset:8448\n"
        "s_waitcnt lgkmcnt(6)\n"
        "v_mfma_i32_16x16x64_i8 v[184:187], v[0:3], v[236:239], 0\n"
        "s_waitcnt lgkmcnt(4)\n"
        "v_readfirstlane_b32 s31, v254\n"
        "v_mfma_i32_16x16x64_i8 v[188:191], v[4:7], v[236:239], 0\n"
        "ds_read_b128 v[228:231], v248 offset:9472\n"
        "s_waitcnt vmcnt(4)\n"
        "v_mfma_i32_16x16x64_i8 v[128:131], v[8:11], v[208:211], v[128:131]\n"
        "v_mfma_i32_16x16x64_i8 v[132:135], v[12:15], v[208:211], v[132:135]\n"
        "ds_read_b128 v[232:235], v248 offset:8960\n"
        "s_waitcnt lgkmcnt(5)\n"
        "v_mfma_i32_16x16x64_i8 v[136:139], v[8:11], v[212:215], v[136:139]\n"
        "global_load_dwordx4 v[32:35], v240, s[56:57] offset:0 sc1\n"
        "v_mfma_i32_16x16x64_i8 v[140:143], v[12:15], v[212:215], v[140:143]\n"
        "ds_read_b128 v[236:239], v248 offset:9984\n"
        "s_waitcnt lgkmcnt(5)\n"
        "v_mfma_i32_16x16x64_i8 v[144:147], v[8:11], v[216:219], v[144:147]\n"
        "s_cmp_lt_u32 s31, 3\n"
        "s_cbranch_scc1 W8_s2\n"
        "W8_c2:\n"
        "v_mfma_i32_16x16x64_i8 v[148:151], v[12:15], v[216:219], v[148:151]\n"
        "ds_read_b128 v[208:211], v248 offset:16384\n"
        "s_waitcnt lgkmcnt(5)\n"
        "v_mfma_i32_16x16x64_i8 v[152:155], v[8:11], v[220:223], v[152:155]\n"
        "ds_read_b32 v254, v250\n"
        "v_mfma_i32_16x16x64_i8 v[156:159], v[12:15], v[220:223], v[156:159]\n"
        "ds_read_b128 v[212:215], v248 offset:17408\n"
        "s_waitcnt lgkmcnt(6)\n"
        "v_mfma_i32_16x16x64_i8 v[160:163], v[8:11], v[224:227], v[160:163]\n"
        "global_load_dwordx4 v[36:39], v240, s[56:57] offset:1024 sc1\n"
        "v_mfma_i32_16x16x64_i8 v[164:167], v[12:15], v[224:227], v[164:167]\n"
        "ds_read_b128 v[216:219], v248 offset:16896\n"
        "s_waitcnt lgkmcnt(6)\n"
        "v_mfma_i32_16x16x64_i8 v[168:171], v[8:11], v[228:231], v[168:171]\n"
        "v_mfma_i32_16x16x64_i8 v[172:175], v[12:15], v[228:231], v[172:175]\n"
        "ds_read_b128 v[220:223], v248 offset:17920\n"
        "s_waitcnt lgkmcnt(6)\n"
        "v_mfma_i32_16x16x64_i8 v[176:179], v[8:11], v[232:235], v[176:179]\n"
        "v_mfma_i32_16x16x64_i8 v[180:183], v[12:15], v[232:235], v[180:183]\n"
        "ds_read_b128 v[224:227], v248 offset:16640\n"
        "s_waitcnt lgkmcnt(6)\n"
        "v_mfma_i32_16x16x64_i8 v[184:187], v[8:11], v[236:239], v[184:187]\n"
        "s_waitcnt lgkmcnt(4)\n"
        "v_readfirstlane_b32 s31, v254\n"
        "v_mfma_i32_16x16x64_i8 v[188:191], v[12:15], v[236:239], v[188:191]\n"
        "ds_read_b128 v[228:231], v248 offset:17664\n"
        "s_waitcnt vmcnt(4)\n"
        "v_mfma_i32_16x16x64_i8 v[128:131], v[16:19], v[208:211], v[128:131]\n"
        "global_load_dwordx4 v[40:43], v240, s[58:59] offset:0 sc1\n"
        "v_mfma_i32_16x16x64_i8 v[132:135], v[20:23], v[208:211], v[132:135]\n"
        "ds_read_b128 v[232:235], v248 offset:17152\n"
        "s_waitcnt lgkmcnt(5)\n"
        "v_mfma_i32_16x16x64_i8 v[136:139], v[16:19], v[212:215], v[136:139]\n"
        "v_mfma_i32_16x16x64_i8 v[140:143], v[20:23], v[212:215], v[140:143]\n"
        "ds_read_b128 v[236:239], v248 offset:18176\n"
        "s_waitcnt lgkmcnt(5)\n"
        "v_mfma_i32_16x16x64_i8 v[144:147], v[16:19], v[216:219], v[144:147]\n"
        "s_cmp_lt_u32 s31, 4\n"
        "s_cbranch_scc1 W8_s3\n"
        "W8_c3:\n"
        "v_mfma_i32_16x16x64_i8 v[148:151], v[20:23], v[216:219], v[148:151]\n"
        "ds_read_b128 v[208:211], v248 offset:24576\n"
        "s_waitcnt lgkmcnt(5)\n"
        "v_mfma_i32_16x16x64_i8 v[152:155], v[16:19], v[220:223], v[152:155]\n"
        "ds_read_b32 v254, v250\n"
        "v_mfma_i32_16x16x64_i8 v[156:159], v[20:23], v[220:223], v[156:159]\n"
        "ds_read_b128 v[212:215], v248 offset:25600\n"
        "s_waitcnt lgkmcnt(6)\n"
        "v_mfma_i32_16x16x64_i8 v[160:163], v[16:19], v[224:227], v[160:163]\n"
        "global_load_dwordx4 v[44:47], v240, s[58:59] offset:1024 sc1\n"
        "v_mfma_i32_16x16x64_i8 v[164:167], v[20:23], v[224:227], v[164:167]\n"
        "ds_read_b128 v[216:219], v248 offset:25088\n"
        "s_waitcnt lgkmcnt(6)\n"
        "v_mfma_i32_16x16x64_i8 v[168:171], v[16:19], v[228:231], v[168:171]\n"
        "v_mfma_i32_16x16x64_i8 v[172:175], v[20:23], v[228:231], v[172:175]\n"
        "ds_read_b128 v[220:223], v248 offset:26112\n"
        "s_waitcnt lgkmcnt(6)\n"
        "v_mfma_i32_16x16x64_i8 v[176:179], v[16:19], v[232:235], v[176:179]\n"
        "v_mfma_i32_16x16x64_i8 v[180:183], v[20:23], v[232:235], v[180:183]\n"
        "ds_read_b128 v[224:227], v248 offset:24832\n"
        "s_waitcnt lgkmcnt(6)\n"
        "v_mfma_i32_16x16x64_i8 v[184:187], v[16:19], v[236:239], v[184:187]\n"
        "s_waitcnt lgkmcnt(4)\n"
        "v_readfirstlane_b32 s31, v254\n"
        "v_mfma_i32_16x16x64_i8 v[188:191], v[20:23], v[236:239], v[188:191]\n"
        "ds_read_b128 v[228:231], v248 offset:25856\n"
        "s_waitcnt vmcnt(4)\n"
        "v_mfma_i32_16x16x64_i8 v[128:131], v[24:27], v[208:211], v[128:131]\n"
        "global_load_dwordx4 v[48:51], v240, s[60:61] offset:0 sc1\n"
        "v_mfma_i32_16x16x64_i8 v[132:135], v[28:31], v[208:211], v[132:135]\n"
        "ds_read_b128 v[232:235], v248 offset:25344\n"
        "s_waitcnt lgkmcnt(5)\n"
        "v_mfma_i32_16x16x64_i8 v[136:139], v[24:27], v[212:215], v[136:139]\n"
        "v_mfma_i32_16x16x64_i8 v[140:143], v[28:31], v[212:215], v[140:143]\n"
        "ds_read_b128 v[236:239], v248 offset:26368\n"
        "s_waitcnt lgkmcnt(5)\n"
        "v_mfma_i32_16x16x64_i8 v[144:147], v[24:27], v[216:219], v[144:147]\n"
        "s_cmp_lt_u32 s31, 5\n"
        "s_cbranch_scc1 W8_s4\n"
        "W8_c4:\n"
        "v_mfma_i32_16x16x64_i8 v[148:151], v[28:31], v[216:219], v[148:151]\n"
        "ds_read_b128 v[208:211], v248 offset:32768\n"
        "s_waitcnt lgkmcnt(5)\n"
        "v_mfma_i32_16x16x64_i8 v[152:155], v[24:27], v[220:223], v[152:155]\n"
        "ds_read_b32 v254, v250\n"
        "v_mfma_i32_16x16x64_i8 v[156:159], v[28:31], v[220:223], v[156:159]\n"
        "ds_read_b128 v[212:215], v248 offset:33792\n"
        "s_waitcnt lgkmcnt(6)\n"
        "v_mfma_i32_16x16x64_i8 v[160:163], v[24:27], v[224:227], v[160:163]\n"
        "s_mov_b64 exec, 1\n"
        "ds_add_u32 v250, v250 offset:516\n"
        "s_mov_b64 exec, -1\n"
        "v_mfma_i32_16x16x64_i8 v[164:167], v[28:31], v[224:227], v[164:167]\n"
        "ds_read_b128 v[216:219], v248 offset:33280\n"
        "s_waitcnt lgkmcnt(7)\n"
        "v_mfma_i32_16x16x64_i8 v[168:171], v[24:27], v[228:231], v[168:171]\n"
        "global_load_dwordx4 v[52:55], v240, s[60:61] offset:1024 sc1\n"
        "v_mfma_i32_16x16x64_i8 v[172:175], v[28:31], v[228:231], v[172:175]\n"
        "ds_read_b128 v[220:223], v248 offset:34304\n"
        "s_waitcnt lgkmcnt(7)\n"
        "v_mfma_i32_16x16x64_i8 v[176:179], v[24:27], v[232:235], v[176:179]\n"
        "v_mfma_i32_16x16x64_i8 v[180:183], v[28:31], v[232:235], v[180:183]\n"
        "ds_read_b128 v[224:227], v248 offset:33024\n"
        "s_waitcnt lgkmcnt(7)\n"
        "v_mfma_i32_16x16x64_i8 v[184:187], v[24:27], v[236:239], v[184:187]\n"
        "s_waitcnt lgkmcnt(5)\n"
        "v_readfirstlane_b32 s31, v254\n"
        "v_mfma_i32_16x16x64_i8 v[188:191], v[28:31], v[236:239], v[188:191]\n"
        "ds_read_b128 v[228:231], v248 offset:34048\n"
        "s_waitcnt vmcnt(4)\n"
        "v_mfma_i32_16x16x64_i8 v[128:131], v[32:35], v[208:211], v[128:131]\n"
        "ds_read_b32 v241, v250 offset:4\n"
        "global_load_dwordx4 v[56:59], v240, s[62:63] offset:0 sc1\n"
        "v_mfma_i32_16x16x64_i8 v[132:135], v[36:39], v[208:211], v[132:135]\n"
        "ds_read_b128 v[232:235], v248 offset:33536\n"
        "s_waitcnt lgkmcnt(7)\n"
        "v_mfma_i32_16x16x64_i8 v[136:139], v[32:35], v[212:215], v[136:139]\n"
        "v_mfma_i32_16x16x64_i8 v[140:143], v[36:39], v[212:215], v[140:143]\n"
        "ds_read_b128 v[236:239], v248 offset:34560\n"
        "s_waitcnt lgkmcnt(6)\n"
        "v_mfma_i32_16x16x64_i8 v[144:147], v[32:35], v[216:219], v[144:147]\n"
        "s_waitcnt lgkmcnt(2)\n"
        "v_readfirstlane_b32 s35, v241\n"
        "s_cmp_lt_u32 s31, 6\n"
        "s_cbranch_scc1 W8_s5\n"
        "W8_c5:\n"
        "v_mfma_i32_16x16x64_i8 v[148:151], v[36:39], v[216:219], v[148:151]\n"
        "ds_read_b128 v[208:211], v248 offset:40960\n"
        "v_mfma_i32_16x16x64_i8 v[152:155], v[32:35], v[220:223], v[152:155]\n"
        "s_cmp_lt_u32 s35, 3\n"
        "s_cbranch_scc1 W8_sR1\n"
        "W8_cR1:\n"
        "ds_read_b32 v254, v250\n"
        "v_mfma_i32_16x16x64_i8 v[156:159], v[36:39], v[220:223], v[156:159]\n"
        "ds_read_b128 v[212:215], v248 offset:41984\n"
        "v_mfma_i32_16x16x64_i8 v[160:163], v[32:35], v[224:227], v[160:163]\n"
        "global_load_dwordx4 v[60:63], v240, s[62:63] offset:1024 sc1\n"
        "v_add_u32 v240, 8192, v240\n"
        "v_mfma_i32_16x16x64_i8 v[164:167], v[36:39], v[224:227], v[164:167]\n"
        "ds_read_b128 v[216:219], v248 offset:41472\n"
        "v_mfma_i32_16x16x64_i8 v[168:171], v[32:35], v[228:231], v[168:171]\n"
        "v_mfma_i32_16x16x64_i8 v[172:175], v[36:39], v[228:231], v[172:175]\n"
        "ds_read_b128 v[220:223], v248 offset:42496\n"
        "s_waitcnt lgkmcnt(6)\n"
        "v_mfma_i32_16x16x64_i8 v[176:179], v[32:35], v[232:235], v[176:179]\n"
        "v_mfma_i32_16x16x64_i8 v[180:183], v[36:39], v[232:235], v[180:183]\n"
        "ds_read_b128 v[224:227], v248 offset:41216\n"
        "s_waitcnt lgkmcnt(6)\n"
        "v_mfma_i32_16x16x64_i8 v[184:187], v[32:35], v[236:239], v[184:187]\n"
        "s_waitcnt lgkmcnt(4)\n"
        "v_readfirstlane_b32 s31, v254\n"
        "v_mfma_i32_16x16x64_i8 v[188:191], v[36:39], v[236:239], v[188:191]\n"
        "ds_read_b128 v[228:231], v248 offset:42240\n"
        "s_waitcnt vmcnt(4)\n"
        "v_mfma_i32_16x16x64_i8 v[128:131], v[40:43], v[208:211], v[128:131]\n"
        "global_load_dwordx4 v[64:67], v240, s[56:57] offset:0 sc1\n"
        "v_mfma_i32_16x16x64_i8 v[132:135], v[44:47], v[208:211], v[132:135]\n"
        "ds_read_b128 v[232:235], v248 offset:41728\n"
        "s_waitcnt lgkmcnt(5)\n"
        "v_mfma_i32_16x16x64_i8 v[136:139], v[40:43], v[212:215], v[136:139]\n"
        "v_mfma_i32_16x16x64_i8 v[140:143], v[44:47], v[212:215], v[140:143]\n"
        "ds_read_b128 v[236:239], v248 offset:42752\n"
        "s_waitcnt lgkmcnt(5)\n"
        "v_mfma_i32_16x16x64_i8 v[144:147], v[40:43], v[216:219], v[144:147]\n"
        "s_cmp_lt_u32 s31, 7\n"
        "s_cbranch_scc1 W8_s6\n"
        "W8_c6:\n"
        "v_mfma_i32_16x16x64_i8 v[148:151], v[44:47], v[216:219], v[148:151]\n"
        "ds_read_b128 v[208:211], v248 offset:49152\n"
        "s_waitcnt lgkmcnt(5)\n"
        "v_mfma_i32_16x16x64_i8 v[152:155], v[40:43], v[220:223], v[152:155]\n"
        "ds_read_b32 v254, v250\n"
        "v_mfma_i32_16x16x64_i8 v[156:159], v[44:47], v[220:223], v[156:159]\n"
        "ds_read_b128 v[212:215], v248 offset:50176\n"
        "s_waitcnt lgkmcnt(6)\n"
        "v_mfma_i32_16x16x64_i8 v[160:163], v[40:43], v[224:227], v[160:163]\n"
        "global_load_dwordx4 v[68:71], v240, s[56:57] offset:1024 sc1\n"
        "v_mfma_i32_16x16x64_i8 v[164:167], v[44:47], v[224:227], v[164:167]\n"
        "ds_read_b128 v[216:219], v248 offset:49664\n"
        "s_waitcnt lgkmcnt(6)\n"
        "v_mfma_i32_16x16x64_i8 v[168:171], v[40:43], v[228:231], v[168:171]\n"
        "v_mfma_i32_16x16x64_i8 v[172:175], v[44:47], v[228:231], v[172:175]\n"
        "ds_read_b128 v[220:223], v248 offset:50688\n"
        "s_waitcnt lgkmcnt(6)\n"
        "v_mfma_i32_16x16x64_i8 v[176:179], v[40:43], v[232:235], v[176:179]\n"
        "v_mfma_i32_16x16x64_i8 v[180:183], v[44:47], v[232:235], v[180:183]\n"
        "ds_read_b128 v[224:227], v248 offset:49408\n"
        "s_waitcnt lgkmcnt(6)\n"
        "v_mfma_i32_16x16x64_i8 v[184:187], v[40:43], v[236:239], v[184:187]\n"
        "s_waitcnt lgkmcnt(4)\n"
        "v_readfirstlane_b32 s31, v254\n"
        "v_mfma_i32_16x16x64_i8 v[188:191], v[44:47], v[236:239], v[188:191]\n"
        "ds_read_b128 v[228:231], v248 offset:50432\n"
        "s_waitcnt vmcnt(4)\n"
        "v_mfma_i32_16x16x64_i8 v[128:131], v[48:51], v[208:211], v[128:131]\n"
        "global_load_dwordx4 v[72:75], v240, s[58:59] offset:0 sc1\n"
        "v_mfma_i32_16x16x64_i8 v[132:135], v[52:55], v[208:211], v[132:135]\n"
        "ds_read_b128 v[232:235], v248 offset:49920\n"
        "s_waitcnt lgkmcnt(5)\n"
        "v_mfma_i32_16x16x64_i8 v[136:139], v[48:51], v[212:215], v[136:139]\n"
        "v_mfma_i32_16x16x64_i8 v[140:143], v[52:55], v[212:215], v[140:143]\n"
        "ds_read_b128 v[236:239], v248 offset:50944\n"
        "s_waitcnt lgkmcnt(5)\n"
        "v_mfma_i32_16x16x64_i8 v[144:147], v[48:51], v[216:219], v[144:147]\n"
        "s_cmp_lt_u32 s31, 8\n"
        "s_cbranch_scc1 W8_s7\n"
        "W8_c7:\n"
        "v_mfma_i32_16x16x64_i8 v[148:151], v[52:55], v[216:219], v[148:151]\n"
        "ds_read_b128 v[208:211], v248 offset:57344\n"
        "s_waitcnt lgkmcnt(5)\n"
        "v_mfma_i32_16x16x64_i8 v[152:155], v[48:51], v[220:223], v[152:155]\n"
        "ds_read_b32 v254, v250\n"
        "v_mfma_i32_16x16x64_i8 v[156:159], v[52:55], v[220:223], v[156:159]\n"
        "ds_read_b128 v[212:215], v248 offset:58368\n"
        "s_waitcnt lgkmcnt(6)\n"
        "v_mfma_i32_16x16x64_i8 v[160:163], v[48:51], v[224:227], v[160:163]\n"
        "global_load_dwordx4 v[76:79], v240, s[58:59] offset:1024 sc1\n"
        "v_mfma_i32_16x16x64_i8 v[164:167], v[52:55], v[224:227], v[164:167]\n"
        "ds_read_b128 v[216:219], v248 offset:57856\n"
        "s_waitcnt lgkmcnt(6)\n"
        "v_mfma_i32_16x16x64_i8 v[168:171], v[48:51], v[228:231], v[168:171]\n"
        "v_mfma_i32_16x16x64_i8 v[172:175], v[52:55], v[228:231], v[172:175]\n"
        "ds_read_b128 v[220:223], v248 offset:58880\n"
        "s_waitcnt lgkmcnt(6)\n"
        "v_mfma_i32_16x16x64_i8 v[176:179], v[48:51], v[232:235], v[176:179]\n"
        "v_mfma_i32_16x16x64_i8 v[180:183], v[52:55], v[232:235], v[180:183]\n"
        "ds_read_b128 v[224:227], v248 offset:57600\n"
        "s_waitcnt lgkmcnt(6)\n"
        "v_mfma_i32_16x16x64_i8 v[184:187], v[48:51], v[236:239], v[184:187]\n"
        "s_waitcnt lgkmcnt(4)\n"
        "v_readfirstlane_b32 s31, v254\n"
        "v_mfma_i32_16x16x64_i8 v[188:191], v[52:55], v[236:239], v[188:191]\n"
        "ds_read_b128 v[228:231], v248 offset:58624\n"
        "s_waitcnt vmcnt(4)\n"
        "v_mfma_i32_16x16x64_i8 v[128:131], v[56:59], v[208:211], v[128:131]\n"
        "global_load_dwordx4 v[80:83], v240, s[60:61] offset:0 sc1\n"
        "v_mfma_i32_16x16x64_i8 v[132:135], v[60:63], v[208:211], v[132:135]\n"
        "ds_read_b128 v[232:235], v248 offset:58112\n"
        "s_waitcnt lgkmcnt(5)\n"
        "v_mfma_i32_16x16x64_i8 v[136:139], v[56:59], v[212:215], v[136:139]\n"
        "v_mfma_i32_16x16x64_i8 v[140:143], v[60:63], v[212:215], v[140:143]\n"
        "ds_read_b128 v[236:239], v248 offset:59136\n"
        "s_waitcnt lgkmcnt(5)\n"
        "v_mfma_i32_16x16x64_i8 v[144:147], v[56:59], v[216:219], v[144:147]\n"
        "s_cmp_lt_u32 s31, 9\n"
        "s_cbranch_scc1 W8_s8\n"
        "W8_c8:\n"
        "v_mfma_i32_16x16x64_i8 v[148:151], v[60:63], v[216:219], v[148:151]\n"
        "ds_read_b128 v[208:211], v249 offset:0\n"
        "s_waitcnt lgkmcnt(5)\n"
        "v_mfma_i32_16x16x64_i8 v[152:155], v[56:59], v[220:223], v[152:155]\n"
        "ds_read_b32 v254, v250\n"
        "v_mfma_i32_16x16x64_i8 v[156:159], v[60:63], v[220:223], v[156:159]\n"
        "ds_read_b128 v[212:215], v249 offset:1024\n"
        "s_waitcnt lgkmcnt(6)\n"
        "v_mfma_i32_16x16x64_i8 v[160:163], v[56:59], v[224:227], v[160:163]\n"
        "s_mov_b64 exec, 1\n"
        "ds_add_u32 v250, v250 offset:520\n"
        "s_mov_b64 exec, -1\n"
        "v_mfma_i32_16x16x64_i8 v[164:167], v[60:63], v[224:227], v[164:167]\n"
        "ds_read_b128 v[216:219], v249 offset:512\n"
        "s_waitcnt lgkmcnt(7)\n"
        "v_mfma_i32_16x16x64_i8 v[168:171], v[56:59], v[228:231], v[168:171]\n"
        "global_load_dwordx4 v[84:87], v240, s[60:61] offset:1024 sc1\n"
        "v_mfma_i32_16x16x64_i8 v[172:175], v[60:63], v[228:231], v[172:175]\n"
        "ds_read_b128 v[220:223], v249 offset:1536\n"
        "s_waitcnt lgkmcnt(7)\n"
        "v_mfma_i32_16x16x64_i8 v[176:179], v[56:59], v[232:235], v[176:179]\n"
        "v_mfma_i32_16x16x64_i8 v[180:183], v[60:63], v[232:235], v[180:183]\n"
        "ds_read_b128 v[224:227], v249 offset:256\n"
        "s_waitcnt lgkmcnt(7)\n"
        "v_mfma_i32_16x16x64_i8 v[184:187], v[56:59], v[236:239], v[184:187]\n"
        "s_waitcnt lgkmcnt(5)\n"
        "v_readfirstlane_b32 s31, v254\n"
        "v_mfma_i32_16x16x64_i8 v[188:191], v[60:63], v[236:239], v[188:191]\n"
        "ds_read_b128 v[228:231], v249 offset:1280\n"
        "s_waitcnt vmcnt(4)\n"
        "v_mfma_i32_16x16x64_i8 v[128:131], v[64:67], v[208:211], v[128:131]\n"
        "ds_read_b32 v241, v250 offset:4\n"
        "global_load_dwordx4 v[88:91], v240, s[62:63] offset:0 sc1\n"
        "v_mfma_i32_16x16x64_i8 v[132:135], v[68:71], v[208:211], v[132:135]\n"
        "ds_read_b128 v[232:235], v249 offset:768\n"
        "s_waitcnt lgkmcnt(7)\n"
        "v_mfma_i32_16x16x64_i8 v[136:139], v[64:67], v[212:215], v[136:139]\n"
        "v_mfma_i32_16x16x64_i8 v[140:143], v[68:71], v[212:215], v[140:143]\n"
        "ds_read_b128 v[236:239], v249 offset:1792\n"
        "s_waitcnt lgkmcnt(6)\n"
        "v_mfma_i32_16x16x64_i8 v[144:147], v[64:67], v[216:219], v[144:147]\n"
        "s_waitcnt lgkmcnt(2)\n"
        "v_readfirstlane_b32 s35, v241\n"
        "s_cmp_lt_u32 s31, 10\n"
        "s_cbranch_scc1 W8_s9\n"
        "W8_c9:\n"
        "v_mfma_i32_16x16x64_i8 v[148:151], v[68:71], v[216:219], v[148:151]\n"
        "ds_read_b128 v[208:211], v249 offset:8192\n"
        "v_mfma_i32_16x16x64_i8 v[152:155], v[64:67], v[220:223], v[152:155]\n"
        "s_cmp_lt_u32 s35, 4\n"
        "s_cbranch_scc1 W8_sR2\n"
        "W8_cR2:\n"
        "ds_read_b32 v254, v250\n"
        "v_mfma_i32_16x16x64_i8 v[156:159], v[68:71], v[220:223], v[156:159]\n"
        "ds_read_b128 v[212:215], v249 offset:9216\n"
        "v_mfma_i32_16x16x64_i8 v[160:163], v[64:67], v[224:227], v[160:163]\n"
        "global_load_dwordx4 v[92:95], v240, s[62:63] offset:1024 sc1\n"
        "v_add_u32 v240, 8192, v240\n"
        "v_mfma_i32_16x16x64_i8 v[164:167], v[68:71], v[224:227], v[164:167]\n"
        "ds_read_b128 v[216:219], v249 offset:8704\n"
        "v_mfma_i32_16x16x64_i8 v[168:171], v[64:67], v[228:231], v[168:171]\n"
        "v_mfma_i32_16x16x64_i8 v[172:175], v[68:71], v[228:231], v[172:175]\n"
        "ds_read_b128 v[220:223], v249 offset:9728\n"
        "s_waitcnt lgkmcnt(6)\n"
        "v_mfma_i32_16x16x64_i8 v[176:179], v[64:67], v[232:235], v[176:179]\n"
        "v_mfma_i32_16x16x64_i8 v[180:183], v[68:71], v[232:235], v[180:183]\n"
        "ds_read_b128 v[224:227], v249 offset:8448\n"
        "s_waitcnt lgkmcnt(6)\n"
        "v_mfma_i32_16x16x64_i8 v[184:187], v[64:67], v[236:239], v[184:187]\n"
        "s_waitcnt lgkmcnt(4)\n"
        "v_readfirstlane_b32 s31, v254\n"
        "v_mfma_i32_16x16x64_i8 v[188:191], v[68:71], v[236:239], v[188:191]\n"
        "ds_read_b128 v[228:231], v249 offset:9472\n"
        "s_waitcnt vmcnt(4)\n"
        "v_mfma_i32_16x16x64_i8 v[128:131], v[72:75], v[208:211], v[128:131]\n"
        "global_load_dwordx4 v[96:99], v240, s[56:57] offset:0 sc1\n"
        "v_mfma_i32_16x16x64_i8 v[132:135], v[76:79], v[208:211], v[132:135]\n"
        "ds_read_b128 v[232:235], v249 offset:8960\n"
        "s_waitcnt lgkmcnt(5)\n"
        "v_mfma_i32_16x16x64_i8 v[136:139], v[72:75], v[212:215], v[136:139]\n"
        "v_mfma_i32_16x16x64_i8 v[140:143], v[76:79], v[212:215], v[140:143]\n"
        "ds_read_b128 v[236:239], v249 offset:9984\n"
        "s_waitcnt lgkmcnt(5)\n"
        "v_mfma_i32_16x16x64_i8 v[144:147], v[72:75], v[216:219], v[144:147]\n"
        "s_cmp_lt_u32 s31, 11\n"
        "s_cbranch_scc1 W8_s10\n"
        "W8_c10:\n"
        "v_mfma_i32_16x16x64_i8 v[148:151], v[76:79], v[216:219], v[148:151]\n"
        "ds_read_b128 v[208:211], v249 offset:16384\n"
        "s_waitcnt lgkmcnt(5)\n"
        "v_mfma_i32_16x16x64_i8 v[152:155], v[72:75], v[220:223], v[152:155]\n"
        "ds_read_b32 v254, v250\n"
        "v_mfma_i32_16x16x64_i8 v[156:159], v[76:79], v[220:223], v[156:159]\n"
        "ds_read_b128 v[212:215], v249 offset:17408\n"
        "s_waitcnt lgkmcnt(6)\n"
        "v_mfma_i32_16x16x64_i8 v[160:163], v[72:75], v[224:227], v[160:163]\n"
        "global_load_dwordx4 v[100:103], v240, s[56:57] offset:1024 sc1\n"
        "v_mfma_i32_16x16x64_i8 v[164:167], v[76:79], v[224:227], v[164:167]\n"
        "ds_read_b128 v[216:219], v249 offset:16896\n"
        "s_waitcnt lgkmcnt(6)\n"
        "v_mfma_i32_16x16x64_i8 v[168:171], v[72:75], v[228:231], v[168:171]\n"
        "v_mfma_i32_16x16x64_i8 v[172:175], v[76:79], v[228:231], v[172:175]\n"
        "ds_read_b128 v[220:223], v249 offset:17920\n"
        "s_waitcnt lgkmcnt(6)\n"
        "v_mfma_i32_16x16x64_i8 v[176:179], v[72:75], v[232:235], v[176:179]\n"
        "v_mfma_i32_16x16x64_i8 v[180:183], v[76:79], v[232:235], v[180:183]\n"
        "ds_read_b128 v[224:227], v249 offset:16640\n"
        "s_waitcnt lgkmcnt(6)\n"
        "v_mfma_i32_16x16x64_i8 v[184:187], v[72:75], v[236:239], v[184:187]\n"
        "s_waitcnt lgkmcnt(4)\n"
        "v_readfirstlane_b32 s31, v254\n"
        "v_mfma_i32_16x16x64_i8 v[188:191], v[76:79], v[236:239], v[188:191]\n"
        "ds_read_b128 v[228:231], v249 offset:17664\n"
        "s_waitcnt vmcnt(4)\n"
        "v_mfma_i32_16x16x64_i8 v[128:131], v[80:83], v[208:211], v[128:131]\n"
        "global_load_dwordx4 v[104:107], v240, s[58:59] offset:0 sc1\n"
        "v_mfma_i32_16x16x64_i8 v[132:135], v[84:87], v[208:211], v[132:135]\n"
        "ds_read_b128 v[232:235], v249 offset:17152\n"
        "s_waitcnt lgkmcnt(5)\n"
        "v_mfma_i32_16x16x64_i8 v[136:139], v[80:83], v[212:215], v[136:139]\n"
        "v_mfma_i32_16x16x64_i8 v[140:143], v[84:87], v[212:215], v[140:143]\n"
        "ds_read_b128 v[236:239], v249 offset:18176\n"
        "s_waitcnt lgkmcnt(5)\n"
        "v_mfma_i32_16x16x64_i8 v[144:147], v[80:83], v[216:219], v[144:147]\n"
        "s_cmp_lt_u32 s31, 12\n"
        "s_cbranch_scc1 W8_s11\n"
        "W8_c11:\n"
        "v_mfma_i32_16x16x64_i8 v[148:151], v[84:87], v[216:219], v[148:151]\n"
        "ds_read_b128 v[208:211], v249 offset:24576\n"
        "s_waitcnt lgkmcnt(5)\n"
        "v_mfma_i32_16x16x64_i8 v[152:155], v[80:83], v[220:223], v[152:155]\n"
        "ds_read_b32 v254, v250\n"
        "v_mfma_i32_16x16x64_i8 v[156:159], v[84:87], v[220:223], v[156:159]\n"
        "ds_read_b128 v[212:215], v249 offset:25600\n"
        "s_waitcnt lgkmcnt(6)\n"
        "v_mfma_i32_16x16x64_i8 v[160:163], v[80:83], v[224:227], v[160:163]\n"
        "global_load_dwordx4 v[108:111], v240, s[58:59] offset:1024 sc1\n"
        "v_mfma_i32_16x16x64_i8 v[164:167], v[84:87], v[224:227], v[164:167]\n"
        "ds_read_b128 v[216:219], v249 offset:25088\n"
        "s_waitcnt lgkmcnt(6)\n"
        "v_mfma_i32_16x16x64_i8 v[168:171], v[80:83], v[228:231], v[168:171]\n"
        "v_mfma_i32_16x16x64_i8 v[172:175], v[84:87], v[228:231], v[172:175]\n"
        "ds_read_b128 v[220:223], v249 offset:26112\n"
        "s_waitcnt lgkmcnt(6)\n"
        "v_mfma_i32_16x16x64_i8 v[176:179], v[80:83], v[232:235], v[176:179]\n"
        "v_mfma_i32_16x16x64_i8 v[180:183], v[84:87], v[232:235], v[180:183]\n"
        "ds_read_b128 v[224:227], v249 offset:24832\n"
        "s_waitcnt lgkmcnt(6)\n"
        "v_mfma_i32_16x16x64_i8 v[184:187], v[80:83], v[236:239], v[184:187]\n"
        "s_waitcnt lgkmcnt(4)\n"
        "v_readfirstlane_b32 s31, v254\n"
        "v_mfma_i32_16x16x64_i8 v[188:191], v[84:87], v[236:239], v[188:191]\n"
        "ds_read_b128 v[228:231], v249 offset:25856\n"
        "s_waitcnt vmcnt(4)\n"
        "v_mfma_i32_16x16x64_i8 v[128:131], v[88:91], v[208:211], v[128:131]\n"
        "global_load_dwordx4 v[112:115], v240, s[60:61] offset:0 sc1\n"
        "v_mfma_i32_16x16x64_i8 v[132:135], v[92:95], v[208:211], v[132:135]\n"
        "ds_read_b128 v[232:235], v249 offset:25344\n"
        "s_waitcnt lgkmcnt(5)\n"
        "v_mfma_i32_16x16x64_i8 v[136:139], v[88:91], v[212:215], v[136:139]\n"
        "v_mfma_i32_16x16x64_i8 v[140:143], v[92:95], v[212:215], v[140:143]\n"
        "ds_read_b128 v[236:239], v249 offset:26368\n"
        "s_waitcnt lgkmcnt(5)\n"
        "v_mfma_i32_16x16x64_i8 v[144:147], v[88:91], v[216:219], v[144:147]\n"
        "s_cmp_lt_u32 s31, 13\n"
        "s_cbranch_scc1 W8_s12\n"
        "W8_c12:\n"
        "v_mfma_i32_16x16x64_i8 v[148:151], v[92:95], v[216:219], v[148:151]\n"
        "ds_read_b128 v[208:211], v249 offset:32768\n"
        "s_waitcnt lgkmcnt(5)\n"
        "v_mfma_i32_16x16x64_i8 v[152:155], v[88:91], v[220:223], v[152:155]\n"
        "ds_read_b32 v254, v250\n"
        "v_mfma_i32_16x16x64_i8 v[156:159], v[92:95], v[220:223], v[156:159]\n"
        "ds_read_b128 v[212:215], v249 offset:33792\n"
        "s_waitcnt lgkmcnt(6)\n"
        "v_mfma_i32_16x16x64_i8 v[160:163], v[88:91], v[224:227], v[160:163]\n"
        "s_mov_b64 exec, 1\n"
        "ds_add_u32 v250, v250 offset:524\n"
        "s_mov_b64 exec, -1\n"
        "v_mfma_i32_16x16x64_i8 v[164:167], v[92:95], v[224:227], v[164:167]\n"
        "ds_read_b128 v[216:219], v249 offset:33280\n"
        "s_waitcnt lgkmcnt(7)\n"
        "v_mfma_i32_16x16x64_i8 v[168:171], v[88:91], v[228:231], v[168:171]\n"
        "global_load_dwordx4 v[116:119], v240, s[60:61] offset:1024 sc1\n"
        "v_mfma_i32_16x16x64_i8 v[172:175], v[92:95], v[228:231], v[172:175]\n"
        "ds_read_b128 v[220:223], v249 offset:34304\n"
        "s_waitcnt lgkmcnt(7)\n"
        "v_mfma_i32_16x16x64_i8 v[176:179], v[88:91], v[232:235], v[176:179]\n"
        "v_mfma_i32_16x16x64_i8 v[180:183], v[92:95], v[232:235], v[180:183]\n"
        "ds_read_b128 v[224:227], v249 offset:33024\n"
        "s_waitcnt lgkmcnt(7)\n"
        "v_mfma_i32_16x16x64_i8 v[184:187], v[88:91], v[236:239], v[184:187]\n"
        "s_waitcnt lgkmcnt(5)\n"
        "v_readfirstlane_b32 s31, v254\n"
        "v_mfma_i32_16x16x64_i8 v[188:191], v[92:95], v[236:239], v[188:191]\n"
        "ds_read_b128 v[228:231], v249 offset:34048\n"
        "s_waitcnt vmcnt(4)\n"
        "v_mfma_i32_16x16x64_i8 v[128:131], v[96:99], v[208:211], v[128:131]\n"
        "global_load_dwordx4 v[120:123], v240, s[62:63] offset:0 sc1\n"
        "v_mfma_i32_16x16x64_i8 v[132:135], v[100:103], v[208:211], v[132:135]\n"
        "ds_read_b128 v[232:235], v249 offset:33536\n"
        "s_waitcnt lgkmcnt(6)\n"
        "v_mfma_i32_16x16x64_i8 v[136:139], v[96:99], v[212:215], v[136:139]\n"
        "v_and_b32 v248, 0x3ff, v240\n"
        "v_mfma_i32_16x16x64_i8 v[140:143], v[100:103], v[212:215], v[140:143]\n"
        "ds_read_b128 v[236:239], v249 offset:34560\n"
        "s_waitcnt lgkmcnt(5)\n"
        "v_mfma_i32_16x16x64_i8 v[144:147], v[96:99], v[216:219], v[144:147]\n"
        "s_cmp_lt_u32 s31, 14\n"
        "s_cbranch_scc1 W8_s13\n"
        "W8_c13:\n"
        "v_mfma_i32_16x16x64_i8 v[148:151], v[100:103], v[216:219], v[148:151]\n"
        "ds_read_b128 v[208:211], v249 offset:40960\n"
        "s_waitcnt lgkmcnt(5)\n"
        "v_mfma_i32_16x16x64_i8 v[152:155], v[96:99], v[220:223], v[152:155]\n"
        "ds_read_b32 v254, v250\n"
        "v_mfma_i32_16x16x64_i8 v[156:159], v[100:103], v[220:223], v[156:159]\n"
        "ds_read_b128 v[212:215], v249 offset:41984\n"
        "s_waitcnt lgkmcnt(6)\n"
        "v_mfma_i32_16x16x64_i8 v[160:163], v[96:99], v[224:227], v[160:163]\n"
        "global_load_dwordx4 v[124:127], v240, s[62:63] offset:1024 sc1\n"
        "v_add_u32 v240, 8192, v240\n"
        "v_mfma_i32_16x16x64_i8 v[164:167], v[100:103], v[224:227], v[164:167]\n"
        "ds_read_b128 v[216:219], v249 offset:41472\n"
        "s_waitcnt lgkmcnt(6)\n"
        "v_mfma_i32_16x16x64_i8 v[168:171], v[96:99], v[228:231], v[168:171]\n"
        "v_mfma_i32_16x16x64_i8 v[172:175], v[100:103], v[228:231], v[172:175]\n"
        "ds_read_b128 v[220:223], v249 offset:42496\n"
        "s_waitcnt lgkmcnt(6)\n"
        "v_mfma_i32_16x16x64_i8 v[176:179], v[96:99], v[232:235], v[176:179]\n"
        "v_mfma_i32_16x16x64_i8 v[180:183], v[100:103], v[232:235], v[180:183]\n"
        "ds_read_b128 v[224:227], v249 offset:41216\n"
        "s_waitcnt lgkmcnt(6)\n"
        "v_mfma_i32_16x16x64_i8 v[184:187], v[96:99], v[236:239], v[184:187]\n"
        "s_waitcnt lgkmcnt(4)\n"
        "v_readfirstlane_b32 s31, v254\n"
        "v_mfma_i32_16x16x64_i8 v[188:191], v[100:103], v[236:239], v[188:191]\n"
        "ds_read_b128 v[228:231], v249 offset:42240\n"
        "s_waitcnt vmcnt(4)\n"
        "v_mfma_i32_16x16x64_i8 v[128:131], v[104:107], v[208:211], v[128:131]\n"
        "v_mfma_i32_16x16x64_i8 v[132:135], v[108:111], v[208:211], v[132:135]\n"
        "ds_read_b128 v[232:235], v249 offset:41728\n"
        "s_waitcnt lgkmcnt(5)\n"
        "v_mfma_i32_16x16x64_i8 v[136:139], v[104:107], v[212:215], v[136:139]\n"
        "v_mfma_i32_16x16x64_i8 v[140:143], v[108:111], v[212:215], v[140:143]\n"
        "ds_read_b128 v[236:239], v249 offset:42752\n"
        "s_waitcnt lgkmcnt(5)\n"
        "v_mfma_i32_16x16x64_i8 v[144:147], v[104:107], v[216:219], v[144:147]\n"
        "s_cmp_lt_u32 s31, 15\n"
        "s_cbranch_scc1 W8_s14\n"
        "W8_c14:\n"
        "v_mfma_i32_16x16x64_i8 v[148:151], v[108:111], v[216:219], v[148:151]\n"
        "ds_read_b128 v[208:211], v249 offset:49152\n"
        "s_waitcnt lgkmcnt(5)\n"
        "v_mfma_i32_16x16x64_i8 v[152:155], v[104:107], v[220:223], v[152:155]\n"
        "ds_read_b32 v254, v250\n"
        "v_mfma_i32_16x16x64_i8 v[156:159], v[108:111], v[220:223], v[156:159]\n"
        "ds_read_b128 v[212:215], v249 offset:50176\n"
        "s_waitcnt lgkmcnt(6)\n"
        "v_mfma_i32_16x16x64_i8 v[160:163], v[104:107], v[224:227], v[160:163]\n"
        "v_mfma_i32_16x16x64_i8 v[164:167], v[108:111], v[224:227], v[164:167]\n"
        "ds_read_b128 v[216:219], v249 offset:49664\n"
        "s_waitcnt lgkmcnt(6)\n"
        "v_mfma_i32_16x16x64_i8 v[168:171], v[104:107], v[228:231], v[168:171]\n"
        "v_mfma_i32_16x16x64_i8 v[172:175], v[108:111], v[228:231], v[172:175]\n"
        "ds_read_b128 v[220:223], v249 offset:50688\n"
        "s_waitcnt lgkmcnt(6)\n"
        "v_mfma_i32_16x16x64_i8 v[176:179], v[104:107], v[232:235], v[176:179]\n"
        "v_mfma_i32_16x16x64_i8 v[180:183], v[108:111], v[232:235], v[180:183]\n"
        "ds_read_b128 v[224:227], v249 offset:49408\n"
        "s_waitcnt lgkmcnt(6)\n"
        "v_mfma_i32_16x16x64_i8 v[184:187], v[104:107], v[236:239], v[184:187]\n"
        "s_waitcnt lgkmcnt(4)\n"
        "v_readfirstlane_b32 s31, v254\n"
        "v_mfma_i32_16x16x64_i8 v[188:191], v[108:111], v[236:239], v[188:191]\n"
        "ds_read_b128 v[228:231], v249 offset:50432\n"
        "s_waitcnt vmcnt(2)\n"
        "v_mfma_i32_16x16x64_i8 v[128:131], v[112:115], v[208:211], v[128:131]\n"
        "v_mfma_i32_16x16x64_i8 v[132:135], v[116:119], v[208:211], v[132:135]\n"
        "ds_read_b128 v[232:235], v249 offset:49920\n"
        "s_waitcnt lgkmcnt(5)\n"
        "v_mfma_i32_16x16x64_i8 v[136:139], v[112:115], v[212:215], v[136:139]\n"
        "v_mfma_i32_16x16x64_i8 v[140:143], v[116:119], v[212:215], v[140:143]\n"
        "ds_read_b128 v[236:239], v249 offset:50944\n"
        "s_waitcnt lgkmcnt(5)\n"
        "v_mfma_i32_16x16x64_i8 v[144:147], v[112:115], v[216:219], v[144:147]\n"
        "s_cmp_lt_u32 s31, 16\n"
        "s_cbranch_scc1 W8_s15\n"
        "W8_c15:\n"
        "v_mfma_i32_16x16x64_i8 v[148:151], v[116:119], v[216:219], v[148:151]\n"
        "ds_read_b128 v[208:211], v249 offset:57344\n"
        "s_waitcnt lgkmcnt(5)\n"
        "v_mfma_i32_16x16x64_i8 v[152:155], v[112:115], v[220:223], v[152:155]\n"
        "ds_read_b32 v254, v250\n"
        "v_mfma_i32_16x16x64_i8 v[156:159], v[116:119], v[220:223], v[156:159]\n"
        "ds_read_b128 v[212:215], v249 offset:58368\n"
        "s_waitcnt lgkmcnt(6)\n"
        "v_mfma_i32_16x16x64_i8 v[160:163], v[112:115], v[224:227], v[160:163]\n"
        "v_mfma_i32_16x16x64_i8 v[164:167], v[116:119], v[224:227], v[164:167]\n"
        "ds_read_b128 v[216:219], v249 offset:57856\n"
        "s_waitcnt lgkmcnt(6)\n"
        "v_mfma_i32_16x16x64_i8 v[168:171], v[112:115], v[228:231], v[168:171]\n"
        "v_mfma_i32_16x16x64_i8 v[172:175], v[116:119], v[228:231], v[172:175]\n"
        "ds_read_b128 v[220:223], v249 offset:58880\n"
        "s_waitcnt lgkmcnt(6)\n"
        "v_mfma_i32_16x16x64_i8 v[176:179], v[112:115], v[232:235], v[176:179]\n"
        "v_mfma_i32_16x16x64_i8 v[180:183], v[116:119], v[232:235], v[180:183]\n"
        "ds_read_b128 v[224:227], v249 offset:57600\n"
        "s_waitcnt lgkmcnt(6)\n"
        "v_mfma_i32_16x16x64_i8 v[184:187], v[112:115], v[236:239], v[184:187]\n"
        "s_waitcnt lgkmcnt(4)\n"
        "v_readfirstlane_b32 s31, v254\n"
        "ds_read_b32 v255, v250 offset:260\n"
        "v_mfma_i32_16x16x64_i8 v[188:191], v[116:119], v[236:239], v[188:191]\n"
        "ds_read_b128 v[228:231], v249 offset:58624\n"
        "s_waitcnt vmcnt(0)\n"
        "s_waitcnt lgkmcnt(5)\n"
        "v_mfma_i32_16x16x64_i8 v[128:131], v[120:123], v[208:211], v[128:131]\n"
        "v_mfma_i32_16x16x64_i8 v[132:135], v[124:127], v[208:211], v[132:135]\n"
        "ds_read_b128 v[232:235], v249 offset:58112\n"
        "v_mfma_i32_16x16x64_i8 v[136:139], v[120:123], v[212:215], v[136:139]\n"
        "v_mfma_i32_16x16x64_i8 v[140:143], v[124:127], v[212:215], v[140:143]\n"
        "ds_read_b128 v[236:239], v249 offset:59136\n"
        "s_waitcnt lgkmcnt(5)\n"
        "v_mfma_i32_16x16x64_i8 v[144:147], v[120:123], v[216:219], v[144:147]\n"
        "s_cmp_lt_u32 s31, 18\n"
        "s_cbranch_scc1 W8_s16\n"
        "W8_c16:\n"
        "v_mfma_i32_16x16x64_i8 v[148:151], v[124:127], v[216:219], v[148:151]\n"
        "ds_read_b128 v[208:211], v248 offset:0\n"
        "v_mfma_i32_16x16x64_i8 v[152:155], v[120:123], v[220:223], v[152:155]\n"
        "v_mfma_i32_16x16x64_i8 v[156:159], v[124:127], v[220:223], v[156:159]\n"
        "ds_read_b128 v[212:215], v248 offset:1024\n"
        "s_waitcnt lgkmcnt(4)\n"
        "v_mfma_i32_16x16x64_i8 v[160:163], v[120:123], v[224:227], v[160:163]\n"
        "v_readfirstlane_b32 s30, v255\n"
        "v_mfma_i32_16x16x64_i8 v[164:167], v[124:127], v[224:227], v[164:167]\n"
        "ds_read_b128 v[216:219], v248 offset:2048\n"
        "v_mfma_i32_16x16x64_i8 v[168:171], v[120:123], v[228:231], v[168:171]\n"
        "v_add_u32 v249, 65536, v248\n"
        "s_cmp_lt_u32 s30, 2\n"
        "s_cbranch_scc1 W8_sT\n"
        "W8_cT:\n"
        "v_mfma_i32_16x16x64_i8 v[172:175], v[124:127], v[228:231], v[172:175]\n"
        "ds_read_b128 v[220:223], v248 offset:3072\n"
        "s_waitcnt lgkmcnt(4)\n"
        "v_mfma_i32_16x16x64_i8 v[176:179], v[120:123], v[232:235], v[176:179]\n"
        "ds_read_b128 v[240:243], v253\n"
        "ds_read_b128 v[244:247], v253 offset:16\n"
        "v_mfma_i32_16x16x64_i8 v[180:183], v[124:127], v[232:235], v[180:183]\n"
        "ds_read_b128 v[224:227], v248 offset:4096\n"
        "v_mfma_i32_16x16x64_i8 v[184:187], v[120:123], v[236:239], v[184:187]\n"
        "v_cvt_f32_i32 v128, v128\n"
        "v_cvt_f32_i32 v129, v129\n"
        "v_cvt_f32_i32 v130, v130\n"
        "v_mfma_i32_16x16x64_i8 v[188:191], v[124:127], v[236:239], v[188:191]\n"
        "ds_read_b128 v[228:231], v248 offset:5120\n"
        "s_waitcnt lgkmcnt(6)\n"
        "v_mfma_i32_16x16x64_i8 v[192:195], v[0:3], v[208:211], 0\n"
        "v_cvt_f32_i32 v131, v131\n"
        "v_cvt_f32_i32 v136, v136\n"
        "v_cvt_f32_i32 v137, v137\n"
        "v_mfma_i32_16x16x64_i8 v[196:199], v[4:7], v[208:211], 0\n"
        "ds_read_b128 v[232:235], v248 offset:6144\n"
        "v_cvt_f32_i32 v138, v138\n"
        "v_mfma_i32_16x16x64_i8 v[200:203], v[0:3], v[212:215], 0\n"
        "v_cvt_f32_i32 v139, v139\n"
        "v_cvt_f32_i32 v144, v144\n"
        "v_mfma_i32_16x16x64_i8 v[204:207], v[4:7], v[212:215], 0\n"
        "ds_read_b128 v[236:239], v248 offset:7168\n"
        "v_cvt_f32_i32 v145, v145\n"
        "s_waitcnt lgkmcnt(6)\n"
        "v_mfma_i32_16x16x64_i8 v[192:195], v[8:11], v[216:219], v[192:195]\n"
        "v_cvt_f32_i32 v146, v146\n"
        "v_cvt_f32_i32 v147, v147\n"
        "v_mfma_i32_16x16x64_i8 v[196:199], v[12:15], v[216:219], v[196:199]\n"
        "ds_read_b128 v[208:211], v248 offset:8192\n"
        "v_cvt_f32_i32 v152, v152\n"
        "v_mfma_i32_16x16x64_i8 v[200:203], v[8:11], v[220:223], v[200:203]\n"
        "ds_read_b32 v254, v250\n"
        "v_cvt_f32_i32 v153, v153\n"
        "v_cvt_f32_i32 v154, v154\n"
        "v_mfma_i32_16x16x64_i8 v[204:207], v[12:15], v[220:223], v[204:207]\n"
        "ds_read_b128 v[212:215], v248 offset:9216\n"
        "s_waitcnt lgkmcnt(5)\n"
        "v_mfma_i32_16x16x64_i8 v[192:195], v[16:19], v[224:227], v[192:195]\n"
        "v_cvt_f32_i32 v155, v155\n"
        "v_mfma_i32_16x16x64_i8 v[196:199], v[20:23], v[224:227], v[196:199]\n"
        "ds_read_b128 v[216:219], v248 offset:10240\n"
        "v_fma_f32 v128, v128, v240, v241\n"
        "v_fma_f32 v129, v129, v240, v241\n"
        "v_mfma_i32_16x16x64_i8 v[200:203], v[16:19], v[228:231], v[200:203]\n"
        "v_fma_f32 v130, v130, v240, v241\n"
        "v_fma_f32 v131, v131, v240, v241\n"
        "v_mfma_i32_16x16x64_i8 v[204:207], v[20:23], v[228:231], v[204:207]\n"
        "ds_read_b128 v[220:223], v248 offset:11264\n"
        "s_waitcnt lgkmcnt(5)\n"
        "v_mfma_i32_16x16x64_i8 v[192:195], v[24:27], v[232:235], v[192:195]\n"
        "v_fma_f32 v136, v136, v242, v243\n"
        "v_fma_f32 v137, v137, v242, v243\n"
        "v_fma_f32 v138, v138, v242, v243\n"
        "v_mfma_i32_16x16x64_i8 v[196:199], v[28:31], v[232:235], v[196:199]\n"
        "ds_read_b128 v[224:227], v248 offset:12288\n"
        "v_fma_f32 v139, v139, v242, v243\n"
        "v_mfma_i32_16x16x64_i8 v[200:203], v[24:27], v[236:239], v[200:203]\n"
        "s_waitcnt lgkmcnt(4)\n"
        "v_readfirstlane_b32 s31, v254\n"
        "v_mfma_i32_16x16x64_i8 v[204:207], v[28:31], v[236:239], v[204:207]\n"
        "ds_read_b128 v[228:231], v248 offset:13312\n"
        "v_fma_f32 v144, v144, v244, v245\n"
        "s_waitcnt lgkmcnt(4)\n"
        "v_mfma_i32_16x16x64_i8 v[192:195], v[32:35], v[208:211], v[192:195]\n"
        "v_fma_f32 v145, v145, v244, v245\n"
        "v_fma_f32 v146, v146, v244, v245\n"
        "v_mfma_i32_16x16x64_i8 v[196:199], v[36:39], v[208:211], v[196:199]\n"
        "ds_read_b128 v[232:235], v248 offset:14336\n"
        "v_fma_f32 v147, v147, v244, v245\n"
        "v_mfma_i32_16x16x64_i8 v[200:203], v[32:35], v[212:215], v[200:203]\n"
        "v_fma_f32 v152, v152, v246, v247\n"
        "v_fma_f32 v153, v153, v246, v247\n"
        "v_fma_f32 v154, v154, v246, v247\n"
        "v_mfma_i32_16x16x64_i8 v[204:207], v[36:39], v[212:215], v[204:207]\n"
        "ds_read_b128 v[236:239], v248 offset:15360\n"
        "s_waitcnt lgkmcnt(4)\n"
        "v_mfma_i32_16x16x64_i8 v[192:195], v[40:43], v[216:219], v[192:195]\n"
        "s_cmp_lt_u32 s31, 20\n"
        "s_cbranch_scc1 W8_s18\n"
        "W8_c18:\n"
        "v_fma_f32 v155, v155, v246, v247\n"
        "v_mfma_i32_16x16x64_i8 v[196:199], v[44:47], v[216:219], v[196:199]\n"
        "ds_read_b128 v[208:211], v248 offset:16384\n"
        "v_cvt_pk_bf16_f32 v128, v128, v136\n"
        "v_mfma_i32_16x16x64_i8 v[200:203], v[40:43], v[220:223], v[200:203]\n"
        "v_cvt_pk_bf16_f32 v136, v129, v137\n"
        "v_cvt_pk_bf16_f32 v130, v130, v138\n"
        "v_mfma_i32_16x16x64_i8 v[204:207], v[44:47], v[220:223], v[204:207]\n"
        "ds_read_b128 v[212:215], v248 offset:17408\n"
        "v_cvt_pk_bf16_f32 v138, v131, v139\n"
        "s_waitcnt lgkmcnt(4)\n"
        "v_mfma_i32_16x16x64_i8 v[192:195], v[48:51], v[224:227], v[192:195]\n"
        "v_cvt_pk_bf16_f32 v129, v144, v152\n"
        "global_store_dwordx2 v251, v[128:129], s[40:41] offset:0 sc1\n"
        "v_mfma_i32_16x16x64_i8 v[196:199], v[52:55], v[224:227], v[196:199]\n"
        "ds_read_b128 v[216:219], v248 offset:18432\n"
        "v_cvt_pk_bf16_f32 v137, v145, v153\n"
        "v_mfma_i32_16x16x64_i8 v[200:203], v[48:51], v[228:231], v[200:203]\n"
        "global_store_dwordx2 v251, v[136:137], s[42:43] offset:0 sc1\n"
        "v_cvt_pk_bf16_f32 v131, v146, v154\n"
        "global_store_dwordx2 v251, v[130:131], s[44:45] offset:0 sc1\n"
        "v_mfma_i32_16x16x64_i8 v[204:207], v[52:55], v[228:231], v[204:207]\n"
        "ds_read_b128 v[220:223], v248 offset:19456\n"
        "s_waitcnt lgkmcnt(4)\n"
        "v_mfma_i32_16x16x64_i8 v[192:195], v[56:59], v[232:235], v[192:195]\n"
        "v_cvt_pk_bf16_f32 v139, v147, v155\n"
        "global_store_dwordx2 v251, v[138:139], s[46:47] offset:0 sc1\n"
        "v_mfma_i32_16x16x64_i8 v[196:199], v[60:63], v[232:235], v[196:199]\n"
        "ds_read_b128 v[224:227], v248 offset:20480\n"
        "v_cvt_f32_i32 v132, v132\n"
        "v_cvt_f32_i32 v133, v133\n"
        "v_mfma_i32_16x16x64_i8 v[200:203], v[56:59], v[236:239], v[200:203]\n"
        "v_cvt_f32_i32 v134, v134\n"
        "v_cvt_f32_i32 v135, v135\n"
        "v_mfma_i32_16x16x64_i8 v[204:207], v[60:63], v[236:239], v[204:207]\n"
        "ds_read_b128 v[228:231], v248 offset:21504\n"
        "s_waitcnt lgkmcnt(4)\n"
        "v_mfma_i32_16x16x64_i8 v[192:195], v[64:67], v[208:211], v[192:195]\n"
        "v_cvt_f32_i32 v140, v140\n"
        "v_cvt_f32_i32 v141, v141\n"
        "v_cvt_f32_i32 v142, v142\n"
        "v_mfma_i32_16x16x64_i8 v[196:199], v[68:71], v[208:211], v[196:199]\n"
        "ds_read_b128 v[232:235], v248 offset:22528\n"
        "v_cvt_f32_i32 v143, v143\n"
        "v_mfma_i32_16x16x64_i8 v[200:203], v[64:67], v[212:215], v[200:203]\n"
        "v_cvt_f32_i32 v148, v148\n"
        "v_cvt_f32_i32 v149, v149\n"
        "v_mfma_i32_16x16x64_i8 v[204:207], v[68:71], v[212:215], v[204:207]\n"
        "ds_read_b128 v[236:239], v248 offset:23552\n"
        "v_cvt_f32_i32 v150, v150\n"
        "s_waitcnt lgkmcnt(4)\n"
        "v_mfma_i32_16x16x64_i8 v[192:195], v[72:75], v[216:219], v[192:195]\n"
        "v_cvt_f32_i32 v151, v151\n"
        "v_cvt_f32_i32 v156, v156\n"
        "v_mfma_i32_16x16x64_i8 v[196:199], v[76:79], v[216:219], v[196:199]\n"
        "ds_read_b128 v[208:211], v248 offset:24576\n"
        "v_cvt_f32_i32 v157, v157\n"
        "v_mfma_i32_16x16x64_i8 v[200:203], v[72:75], v[220:223], v[200:203]\n"
        "ds_read_b32 v254, v250\n"
        "v_cvt_f32_i32 v158, v158\n"
        "v_cvt_f32_i32 v159, v159\n"
        "v_mfma_i32_16x16x64_i8 v[204:207], v[76:79], v[220:223], v[204:207]\n"
        "ds_read_b128 v[212:215], v248 offset:25600\n"
        "s_waitcnt lgkmcnt(5)\n"
        "v_mfma_i32_16x16x64_i8 v[192:195], v[80:83], v[224:227], v[192:195]\n"
        "v_fma_f32 v132, v132, v240, v241\n"
        "v_fma_f32 v133, v133, v240, v241\n"
        "v_fma_f32 v134, v134, v240, v241\n"
        "v_mfma_i32_16x16x64_i8 v[196:199], v[84:87], v[224:227], v[196:199]\n"
        "ds_read_b128 v[216:219], v248 offset:26624\n"
        "v_fma_f32 v135, v135, v240, v241\n"
        "v_mfma_i32_16x16x64_i8 v[200:203], v[80:83], v[228:231], v[200:203]\n"
        "v_fma_f32 v140, v140, v242, v243\n"
        "v_fma_f32 v141, v141, v242, v243\n"
        "v_mfma_i32_16x16x64_i8 v[204:207], v[84:87], v[228:231], v[204:207]\n"
        "ds_read_b128 v[220:223], v248 offset:27648\n"
        "v_fma_f32 v142, v142, v242, v243\n"
        "s_waitcnt lgkmcnt(5)\n"
        "v_mfma_i32_16x16x64_i8 v[192:195], v[88:91], v[232:235], v[192:195]\n"
        "v_fma_f32 v143, v143, v242, v243\n"
        "v_fma_f32 v148, v148, v244, v245\n"
        "v_mfma_i32_16x16x64_i8 v[196:199], v[92:95], v[232:235], v[196:199]\n"
        "ds_read_b128 v[224:227], v248 offset:28672\n"
        "v_fma_f32 v149, v149, v244, v245\n"
        "v_mfma_i32_16x16x64_i8 v[200:203], v[88:91], v[236:239], v[200:203]\n"
        "s_waitcnt lgkmcnt(4)\n"
        "v_readfirstlane_b32 s31, v254\n"
        "v_fma_f32 v150, v150, v244, v245\n"
        "v_mfma_i32_16x16x64_i8 v[204:207], v[92:95], v[236:239], v[204:207]\n"
        "ds_read_b128 v[228:231], v248 offset:29696\n"
        "s_waitcnt lgkmcnt(4)\n"
        "v_mfma_i32_16x16x64_i8 v[192:195], v[96:99], v[208:211], v[192:195]\n"
        "v_fma_f32 v151, v151, v244, v245\n"
        "v_fma_f32 v156, v156, v246, v247\n"
        "v_mfma_i32_16x16x64_i8 v[196:199], v[100:103], v[208:211], v[196:199]\n"
        "ds_read_b128 v[232:235], v248 offset:30720\n"
        "v_fma_f32 v157, v157, v246, v247\n"
        "v_fma_f32 v158, v158, v246, v247\n"
        "v_mfma_i32_16x16x64_i8 v[200:203], v[96:99], v[212:215], v[200:203]\n"
        "v_fma_f32 v159, v159, v246, v247\n"
        "v_cvt_pk_bf16_f32 v132, v132, v140\n"
        "v_mfma_i32_16x16x64_i8 v[204:207], v[100:103], v[212:215], v[204:207]\n"
        "ds_read_b128 v[236:239], v248 offset:31744\n"
        "s_waitcnt lgkmcnt(4)\n"
        "v_mfma_i32_16x16x64_i8 v[192:195], v[104:107], v[216:219], v[192:195]\n"
        "s_cmp_lt_u32 s31, 22\n"
        "s_cbranch_scc1 W8_s20\n"
        "W8_c20:\n"
        "v_cvt_pk_bf16_f32 v140, v133, v141\n"
        "v_mfma_i32_16x16x64_i8 v[196:199], v[108:111], v[216:219], v[196:199]\n"
        "ds_read_b128 v[208:211], v248 offset:32768\n"
        "v_cvt_pk_bf16_f32 v134, v134, v142\n"
        "v_mfma_i32_16x16x64_i8 v[200:203], v[104:107], v[220:223], v[200:203]\n"
        "v_cvt_pk_bf16_f32 v142, v135, v143\n"
        "v_cvt_pk_bf16_f32 v133, v148, v156\n"
        "v_mfma_i32_16x16x64_i8 v[204:207], v[108:111], v[220:223], v[204:207]\n"
        "ds_read_b128 v[212:215], v248 offset:33792\n"
        "global_store_dwordx2 v251, v[132:133], s[48:49] offset:0 sc1\n"
        "s_waitcnt lgkmcnt(4)\n"
        "v_mfma_i32_16x16x64_i8 v[192:195], v[112:115], v[224:227], v[192:195]\n"
        "v_cvt_pk_bf16_f32 v141, v149, v157\n"
        "global_store_dwordx2 v251, v[140:141], s[50:51] offset:0 sc1\n"
        "v_mfma_i32_16x16x64_i8 v[196:199], v[116:119], v[224:227], v[196:199]\n"
        "ds_read_b128 v[216:219], v248 offset:34816\n"
        "v_cvt_pk_bf16_f32 v135, v150, v158\n"
        "v_mfma_i32_16x16x64_i8 v[200:203], v[112:115], v[228:231], v[200:203]\n"
        "global_store_dwordx2 v251, v[134:135], s[52:53] offset:0 sc1\n"
        "v_cvt_pk_bf16_f32 v143, v151, v159\n"
        "global_store_dwordx2 v251, v[142:143], s[54:55] offset:0 sc1\n"
        "v_mfma_i32_16x16x64_i8 v[204:207], v[116:119], v[228:231], v[204:207]\n"
        "ds_read_b128 v[220:223], v248 offset:35840\n"
        "s_waitcnt lgkmcnt(4)\n"
        "v_mfma_i32_16x16x64_i8 v[192:195], v[120:123], v[232:235], v[192:195]\n"
        "ds_read_b128 v[240:243], v253 offset:512\n"
        "ds_read_b128 v[244:247], v253 offset:528\n"
        "v_mfma_i32_16x16x64_i8 v[196:199], v[124:127], v[232:235], v[196:199]\n"
        "ds_read_b128 v[224:227], v248 offset:36864\n"
        "v_cvt_f32_i32 v160, v160\n"
        "v_cvt_f32_i32 v161, v161\n"
        "v_mfma_i32_16x16x64_i8 v[200:203], v[120:123], v[236:239], v[200:203]\n"
        "v_cvt_f32_i32 v162, v162\n"
        "v_cvt_f32_i32 v163, v163\n"
        "v_mfma_i32_16x16x64_i8 v[204:207], v[124:127], v[236:239], v[204:207]\n"
        "ds_read_b128 v[228:231], v248 offset:37888\n"
        "s_waitcnt lgkmcnt(6)\n"
        "v_mfma_i32_16x16x64_i8 v[128:131], v[0:3], v[208:211], 0\n"
        "v_cvt_f32_i32 v168, v168\n"
        "v_cvt_f32_i32 v169, v169\n"
        "v_cvt_f32_i32 v170, v170\n"
        "v_mfma_i32_16x16x64_i8 v[132:135], v[4:7], v[208:211], 0\n"
        "ds_read_b128 v[232:235], v248 offset:38912\n"
        "v_cvt_f32_i32 v171, v171\n"
        "v_mfma_i32_16x16x64_i8 v[136:139], v[0:3], v[212:215], 0\n"
        "v_cvt_f32_i32 v176, v176\n"
        "v_cvt_f32_i32 v177, v177\n"
        "v_mfma_i32_16x16x64_i8 v[140:143], v[4:7], v[212:215], 0\n"
        "ds_read_b128 v[236:239], v248 offset:39936\n"
        "v_cvt_f32_i32 v178, v178\n"
        "s_waitcnt lgkmcnt(6)\n"
        "v_mfma_i32_16x16x64_i8 v[128:131], v[8:11], v[216:219], v[128:131]\n"
        "v_cvt_f32_i32 v179, v179\n"
        "v_cvt_f32_i32 v184, v184\n"
        "v_mfma_i32_16x16x64_i8 v[132:135], v[12:15], v[216:219], v[132:135]\n"
        "ds_read_b128 v[208:211], v248 offset:40960\n"
        "v_cvt_f32_i32 v185, v185\n"
        "v_mfma_i32_16x16x64_i8 v[136:139], v[8:11], v[220:223], v[136:139]\n"
        "ds_read_b32 v254, v250\n"
        "v_cvt_f32_i32 v186, v186\n"
        "v_cvt_f32_i32 v187, v187\n"
        "v_mfma_i32_16x16x64_i8 v[140:143], v[12:15], v[220:223], v[140:143]\n"
        "ds_read_b128 v[212:215], v248 offset:41984\n"
        "s_waitcnt lgkmcnt(5)\n"
        "v_mfma_i32_16x16x64_i8 v[128:131], v[16:19], v[224:227], v[128:131]\n"
        "v_fma_f32 v160, v160, v240, v241\n"
        "v_mfma_i32_16x16x64_i8 v[132:135], v[20:23], v[224:227], v[132:135]\n"
        "ds_read_b128 v[216:219], v248 offset:43008\n"
        "v_fma_f32 v161, v161, v240, v241\n"
        "v_fma_f32 v162, v162, v240, v241\n"
        "v_mfma_i32_16x16x64_i8 v[136:139], v[16:19], v[228:231], v[136:139]\n"
        "v_fma_f32 v163, v163, v240, v241\n"
        "v_fma_f32 v168, v168, v242, v243\n"
        "v_mfma_i32_16x16x64_i8 v[140:143], v[20:23], v[228:231], v[140:143]\n"
        "ds_read_b128 v[220:223], v248 offset:44032\n"
        "s_waitcnt lgkmcnt(5)\n"
        "v_mfma_i32_16x16x64_i8 v[128:131], v[24:27], v[232:235], v[128:131]\n"
        "v_fma_f32 v169, v169, v242, v243\n"
        "v_fma_f32 v170, v170, v242, v243\n"
        "v_fma_f32 v171, v171, v242, v243\n"
        "v_mfma_i32_16x16x64_i8 v[132:135], v[28:31], v[232:235], v[132:135]\n"
        "ds_read_b128 v[224:227], v248 offset:45056\n"
        "v_fma_f32 v176, v176, v244, v245\n"
        "v_mfma_i32_16x16x64_i8 v[136:139], v[24:27], v[236:239], v[136:139]\n"
        "s_waitcnt lgkmcnt(4)\n"
        "v_readfirstlane_b32 s31, v254\n"
        "v_mfma_i32_16x16x64_i8 v[140:143], v[28:31], v[236:239], v[140:143]\n"
        "ds_read_b128 v[228:231], v248 offset:46080\n"
        "v_fma_f32 v177, v177, v244, v245\n"
        "s_waitcnt lgkmcnt(4)\n"
        "v_mfma_i32_16x16x64_i8 v[128:131], v[32:35], v[208:211], v[128:131]\n"
        "v_fma_f32 v178, v178, v244, v245\n"
        "v_fma_f32 v179, v179, v244, v245\n"
        "v_mfma_i32_16x16x64_i8 v[132:135], v[36:39], v[208:211], v[132:135]\n"
        "ds_read_b128 v[232:235], v248 offset:47104\n"
        "v_fma_f32 v184, v184, v246, v247\n"
        "v_mfma_i32_16x16x64_i8 v[136:139], v[32:35], v[212:215], v[136:139]\n"
        "v_fma_f32 v185, v185, v246, v247\n"
        "v_fma_f32 v186, v186, v246, v247\n"
        "v_fma_f32 v187, v187, v246, v247\n"
        "v_mfma_i32_16x16x64_i8 v[140:143], v[36:39], v[212:215], v[140:143]\n"
        "ds_read_b128 v[236:239], v248 offset:48128\n"
        "s_waitcnt lgkmcnt(4)\n"
        "v_mfma_i32_16x16x64_i8 v[128:131], v[40:43], v[216:219], v[128:131]\n"
        "s_cmp_lt_u32 s31, 24\n"
        "s_cbranch_scc1 W8_s22\n"
        "W8_c22:\n"
        "v_mfma_i32_16x16x64_i8 v[132:135], v[44:47], v[216:219], v[132:135]\n"
        "ds_read_b128 v[208:211], v248 offset:49152\n"
        "v_cvt_pk_bf16_f32 v160, v160, v168\n"
        "v_cvt_pk_bf16_f32 v168, v161, v169\n"
        "v_mfma_i32_16x16x64_i8 v[136:139], v[40:43], v[220:223], v[136:139]\n"
        "v_cvt_pk_bf16_f32 v162, v162, v170\n"
        "v_cvt_pk_bf16_f32 v170, v163, v171\n"
        "v_mfma_i32_16x16x64_i8 v[140:143], v[44:47], v[220:223], v[140:143]\n"
        "ds_read_b128 v[212:215], v248 offset:50176\n"
        "s_waitcnt lgkmcnt(4)\n"
        "v_mfma_i32_16x16x64_i8 v[128:131], v[48:51], v[224:227], v[128:131]\n"
        "v_cvt_pk_bf16_f32 v161, v176, v184\n"
        "global_store_dwordx2 v251, v[160:161], s[40:41] offset:128 sc1\n"
        "v_cvt_pk_bf16_f32 v169, v177, v185\n"
        "v_mfma_i32_16x16x64_i8 v[132:135], v[52:55], v[224:227], v[132:135]\n"
        "ds_read_b128 v[216:219], v248 offset:51200\n"
        "global_store_dwordx2 v251, v[168:169], s[42:43] offset:128 sc1\n"
        "v_mfma_i32_16x16x64_i8 v[136:139], v[48:51], v[228:231], v[136:139]\n"
        "v_cvt_pk_bf16_f32 v163, v178, v186\n"
        "global_store_dwordx2 v251, v[162:163], s[44:45] offset:128 sc1\n"
        "v_mfma_i32_16x16x64_i8 v[140:143], v[52:55], v[228:231], v[140:143]\n"
        "ds_read_b128 v[220:223], v248 offset:52224\n"
        "v_cvt_pk_bf16_f32 v171, v179, v187\n"
        "s_waitcnt lgkmcnt(4)\n"
        "v_mfma_i32_16x16x64_i8 v[128:131], v[56:59], v[232:235], v[128:131]\n"
        "global_store_dwordx2 v251, v[170:171], s[46:47] offset:128 sc1\n"
        "v_cvt_f32_i32 v164, v164\n"
        "v_mfma_i32_16x16x64_i8 v[132:135], v[60:63], v[232:235], v[132:135]\n"
        "ds_read_b128 v[224:227], v248 offset:53248\n"
        "v_cvt_f32_i32 v165, v165\n"
        "v_mfma_i32_16x16x64_i8 v[136:139], v[56:59], v[236:239], v[136:139]\n"
        "v_cvt_f32_i32 v166, v166\n"
        "v_cvt_f32_i32 v167, v167\n"
        "v_cvt_f32_i32 v172, v172\n"
        "v_mfma_i32_16x16x64_i8 v[140:143], v[60:63], v[236:239], v[140:143]\n"
        "ds_read_b128 v[228:231], v248 offset:54272\n"
        "s_waitcnt lgkmcnt(4)\n"
        "v_mfma_i32_16x16x64_i8 v[128:131], v[64:67], v[208:211], v[128:131]\n"
        "v_cvt_f32_i32 v173, v173\n"
        "v_cvt_f32_i32 v174, v174\n"
        "v_mfma_i32_16x16x64_i8 v[132:135], v[68:71], v[208:211], v[132:135]\n"
        "ds_read_b128 v[232:235], v248 offset:55296\n"
        "v_cvt_f32_i32 v175, v175\n"
        "v_cvt_f32_i32 v180, v180\n"
        "v_mfma_i32_16x16x64_i8 v[136:139], v[64:67], v[212:215], v[136:139]\n"
        "v_cvt_f32_i32 v181, v181\n"
        "v_cvt_f32_i32 v182, v182\n"
        "v_mfma_i32_16x16x64_i8 v[140:143], v[68:71], v[212:215], v[140:143]\n"
        "ds_read_b128 v[236:239], v248 offset:56320\n"
        "s_waitcnt lgkmcnt(4)\n"
        "v_mfma_i32_16x16x64_i8 v[128:131], v[72:75], v[216:219], v[128:131]\n"
        "v_cvt_f32_i32 v183, v183\n"
        "v_cvt_f32_i32 v188, v188\n"
        "v_cvt_f32_i32 v189, v189\n"
        "v_mfma_i32_16x16x64_i8 v[132:135], v[76:79], v[216:219], v[132:135]\n"
        "ds_read_b128 v[208:211], v248 offset:57344\n"
        "v_cvt_f32_i32 v190, v190\n"
        "v_mfma_i32_16x16x64_i8 v[136:139], v[72:75], v[220:223], v[136:139]\n"
        "ds_read_b32 v254, v250\n"
        "v_cvt_f32_i32 v191, v191\n"
        "v_mfma_i32_16x16x64_i8 v[140:143], v[76:79], v[220:223], v[140:143]\n"
        "ds_read_b128 v[212:215], v248 offset:58368\n"
        "v_fma_f32 v164, v164, v240, v241\n"
        "s_waitcnt lgkmcnt(5)\n"
        "v_mfma_i32_16x16x64_i8 v[128:131], v[80:83], v[224:227], v[128:131]\n"
        "v_fma_f32 v165, v165, v240, v241\n"
        "v_fma_f32 v166, v166, v240, v241\n"
        "v_mfma_i32_16x16x64_i8 v[132:135], v[84:87], v[224:227], v[132:135]\n"
        "ds_read_b128 v[216:219], v248 offset:59392\n"
        "v_fma_f32 v167, v167, v240, v241\n"
        "v_mfma_i32_16x16x64_i8 v[136:139], v[80:83], v[228:231], v[136:139]\n"
        "v_fma_f32 v172, v172, v242, v243\n"
        "v_fma_f32 v173, v173, v242, v243\n"
        "v_fma_f32 v174, v174, v242, v243\n"
        "v_mfma_i32_16x16x64_i8 v[140:143], v[84:87], v[228:231], v[140:143]\n"
        "ds_read_b128 v[220:223], v248 offset:60416\n"
        "s_waitcnt lgkmcnt(5)\n"
        "v_mfma_i32_16x16x64_i8 v[128:131], v[88:91], v[232:235], v[128:131]\n"
        "v_fma_f32 v175, v175, v242, v243\n"
        "v_fma_f32 v180, v180, v244, v245\n"
        "v_mfma_i32_16x16x64_i8 v[132:135], v[92:95], v[232:235], v[132:135]\n"
        "ds_read_b128 v[224:227], v248 offset:61440\n"
        "v_fma_f32 v181, v181, v244, v245\n"
        "v_fma_f32 v182, v182, v244, v245\n"
        "v_mfma_i32_16x16x64_i8 v[136:139], v[88:91], v[236:239], v[136:139]\n"
        "s_waitcnt lgkmcnt(4)\n"
        "v_readfirstlane_b32 s31, v254\n"
        "v_mfma_i32_16x16x64_i8 v[140:143], v[92:95], v[236:239], v[140:143]\n"
        "ds_read_b128 v[228:231], v248 offset:62464\n"
        "s_waitcnt lgkmcnt(4)\n"
        "v_mfma_i32_16x16x64_i8 v[128:131], v[96:99], v[208:211], v[128:131]\n"
        "v_fma_f32 v183, v183, v244, v245\n"
        "v_fma_f32 v188, v188, v246, v247\n"
        "v_fma_f32 v189, v189, v246, v247\n"
        "v_mfma_i32_16x16x64_i8 v[132:135], v[100:103], v[208:211], v[132:135]\n"
        "ds_read_b128 v[232:235], v248 offset:63488\n"
        "v_fma_f32 v190, v190, v246, v247\n"
        "v_mfma_i32_16x16x64_i8 v[136:139], v[96:99], v[212:215], v[136:139]\n"
        "v_fma_f32 v191, v191, v246, v247\n"
        "v_cvt_pk_bf16_f32 v164, v164, v172\n"
        "v_mfma_i32_16x16x64_i8 v[140:143], v[100:103], v[212:215], v[140:143]\n"
        "ds_read_b128 v[236:239], v248 offset:64512\n"
        "v_cvt_pk_bf16_f32 v172, v165, v173\n"
        "s_waitcnt lgkmcnt(4)\n"
        "v_mfma_i32_16x16x64_i8 v[128:131], v[104:107], v[216:219], v[128:131]\n"
        "s_cmp_lt_u32 s31, 26\n"
        "s_cbranch_scc1 W8_s24\n"
        "W8_c24:\n"
        "v_mfma_i32_16x16x64_i8 v[132:135], v[108:111], v[216:219], v[132:135]\n"
        "ds_read_b128 v[208:211], v249 offset:0\n"
        "v_cvt_pk_bf16_f32 v166, v166, v174\n"
        "v_mfma_i32_16x16x64_i8 v[136:139], v[104:107], v[220:223], v[136:139]\n"
        "v_cvt_pk_bf16_f32 v174, v167, v175\n"
        "v_cvt_pk_bf16_f32 v165, v180, v188\n"
        "global_store_dwordx2 v251, v[164:165], s[48:49] offset:128 sc1\n"
        "v_mfma_i32_16x16x64_i8 v[140:143], v[108:111], v[220:223], v[140:143]\n"
        "ds_read_b128 v[212:215], v249 offset:1024\n"
        "s_waitcnt lgkmcnt(4)\n"
        "v_mfma_i32_16x16x64_i8 v[128:131], v[112:115], v[224:227], v[128:131]\n"
        "v_cvt_pk_bf16_f32 v173, v181, v189\n"
        "global_store_dwordx2 v251, v[172:173], s[50:51] offset:128 sc1\n"
        "v_mfma_i32_16x16x64_i8 v[132:135], v[116:119], v[224:227], v[132:135]\n"
        "ds_read_b128 v[216:219], v249 offset:2048\n"
        "v_cvt_pk_bf16_f32 v167, v182, v190\n"
        "global_store_dwordx2 v251, v[166:167], s[52:53] offset:128 sc1\n"
        "v_mfma_i32_16x16x64_i8 v[136:139], v[112:115], v[228:231], v[136:139]\n"
        "v_cvt_pk_bf16_f32 v175, v183, v191\n"
        "global_store_dwordx2 v251, v[174:175], s[54:55] offset:128 sc1\n"
        "v_mfma_i32_16x16x64_i8 v[140:143], v[116:119], v[228:231], v[140:143]\n"
        "ds_read_b128 v[220:223], v249 offset:3072\n"
        "s_waitcnt lgkmcnt(4)\n"
        "v_mfma_i32_16x16x64_i8 v[128:131], v[120:123], v[232:235], v[128:131]\n"
        "v_mfma_i32_16x16x64_i8 v[132:135], v[124:127], v[232:235], v[132:135]\n"
        "ds_read_b128 v[224:227], v249 offset:4096\n"
        "v_and_b32 v255, 0xfffffeff, v253\n"
        "v_bfe_u32 v240, v253, 8, 1\n"
        "v_mfma_i32_16x16x64_i8 v[136:139], v[120:123], v[236:239], v[136:139]\n"
        "v_cvt_f32_i32 v192, v192\n"
        "v_lshl_add_u32 v255, v240, 4, v255\n"
        "v_mfma_i32_16x16x64_i8 v[140:143], v[124:127], v[236:239], v[140:143]\n"
        "ds_read_b128 v[228:231], v249 offset:5120\n"
        "s_waitcnt lgkmcnt(4)\n"
        "v_mfma_i32_16x16x64_i8 v[144:147], v[0:3], v[208:211], 0\n"
        "ds_read_b128 v[244:247], v255 offset:1536\n"
        "ds_read_b128 v[240:243], v253 offset:1024\n"
        "v_mfma_i32_16x16x64_i8 v[148:151], v[4:7], v[208:211], 0\n"
        "ds_read_b128 v[232:235], v249 offset:6144\n"
        "v_cvt_f32_i32 v193, v193\n"
        "v_mfma_i32_16x16x64_i8 v[152:155], v[0:3], v[212:215], 0\n"
        "v_cvt_f32_i32 v194, v194\n"
        "v_cvt_f32_i32 v195, v195\n"
        "v_mfma_i32_16x16x64_i8 v[156:159], v[4:7], v[212:215], 0\n"
        "ds_read_b128 v[236:239], v249 offset:7168\n"
        "v_cvt_f32_i32 v200, v200\n"
        "s_waitcnt lgkmcnt(6)\n"
        "v_mfma_i32_16x16x64_i8 v[144:147], v[8:11], v[216:219], v[144:147]\n"
        "v_cvt_f32_i32 v201, v201\n"
        "v_cvt_f32_i32 v202, v202\n"
        "v_mfma_i32_16x16x64_i8 v[148:151], v[12:15], v[216:219], v[148:151]\n"
        "ds_read_b128 v[208:211], v249 offset:8192\n"
        "v_cvt_f32_i32 v203, v203\n"
        "v_mfma_i32_16x16x64_i8 v[152:155], v[8:11], v[220:223], v[152:155]\n"
        "ds_read_b32 v254, v250\n"
        "v_cvt_f32_i32 v196, v196\n"
        "v_mfma_i32_16x16x64_i8 v[156:159], v[12:15], v[220:223], v[156:159]\n"
        "ds_read_b128 v[212:215], v249 offset:9216\n"
        "s_waitcnt lgkmcnt(7)\n"
        "v_mfma_i32_16x16x64_i8 v[144:147], v[16:19], v[224:227], v[144:147]\n"
        "v_cvt_f32_i32 v197, v197\n"
        "v_cvt_f32_i32 v198, v198\n"
        "v_mfma_i32_16x16x64_i8 v[148:151], v[20:23], v[224:227], v[148:151]\n"
        "ds_read_b128 v[216:219], v249 offset:10240\n"
        "v_cvt_f32_i32 v199, v199\n"
        "v_mfma_i32_16x16x64_i8 v[152:155], v[16:19], v[228:231], v[152:155]\n"
        "v_cvt_f32_i32 v204, v204\n"
        "v_cvt_f32_i32 v205, v205\n"
        "v_cvt_f32_i32 v206, v206\n"
        "v_mfma_i32_16x16x64_i8 v[156:159], v[20:23], v[228:231], v[156:159]\n"
        "ds_read_b128 v[220:223], v249 offset:11264\n"
        "s_waitcnt lgkmcnt(5)\n"
        "v_mfma_i32_16x16x64_i8 v[144:147], v[24:27], v[232:235], v[144:147]\n"
        "v_cvt_f32_i32 v207, v207\n"
        "v_mfma_i32_16x16x64_i8 v[148:151], v[28:31], v[232:235], v[148:151]\n"
        "ds_read_b128 v[224:227], v249 offset:12288\n"
        "v_fma_f32 v192, v192, v244, v245\n"
        "v_mfma_i32_16x16x64_i8 v[152:155], v[24:27], v[236:239], v[152:155]\n"
        "s_waitcnt lgkmcnt(4)\n"
        "v_readfirstlane_b32 s31, v254\n"
        "v_mfma_i32_16x16x64_i8 v[156:159], v[28:31], v[236:239], v[156:159]\n"
        "ds_read_b128 v[228:231], v249 offset:13312\n"
        "s_waitcnt lgkmcnt(4)\n"
        "v_mfma_i32_16x16x64_i8 v[144:147], v[32:35], v[208:211], v[144:147]\n"
        "v_fma_f32 v193, v193, v244, v245\n"
        "v_fma_f32 v194, v194, v244, v245\n"
        "v_mfma_i32_16x16x64_i8 v[148:151], v[36:39], v[208:211], v[148:151]\n"
        "ds_read_b128 v[232:235], v249 offset:14336\n"
        "v_fma_f32 v195, v195, v244, v245\n"
        "v_fma_f32 v200, v200, v246, v247\n"
        "v_mfma_i32_16x16x64_i8 v[152:155], v[32:35], v[212:215], v[152:155]\n"
        "v_fma_f32 v201, v201, v246, v247\n"
        "v_fma_f32 v202, v202, v246, v247\n"
        "v_mfma_i32_16x16x64_i8 v[156:159], v[36:39], v[212:215], v[156:159]\n"
        "ds_read_b128 v[236:239], v249 offset:15360\n"
        "s_waitcnt lgkmcnt(4)\n"
        "v_mfma_i32_16x16x64_i8 v[144:147], v[40:43], v[216:219], v[144:147]\n"
        "s_cmp_lt_u32 s31, 28\n"
        "s_cbranch_scc1 W8_s26\n"
        "W8_c26:\n"
        "v_mfma_i32_16x16x64_i8 v[148:151], v[44:47], v[216:219], v[148:151]\n"
        "ds_read_b128 v[208:211], v249 offset:16384\n"
        "v_fma_f32 v203, v203, v246, v247\n"
        "v_mfma_i32_16x16x64_i8 v[152:155], v[40:43], v[220:223], v[152:155]\n"
        "v_cvt_pk_bf16_f32 v192, v192, v200\n"
        "v_cvt_pk_bf16_f32 v200, v193, v201\n"
        "v_mfma_i32_16x16x64_i8 v[156:159], v[44:47], v[220:223], v[156:159]\n"
        "ds_read_b128 v[212:215], v249 offset:17408\n"
        "v_cvt_pk_bf16_f32 v194, v194, v202\n"
        "s_waitcnt lgkmcnt(4)\n"
        "v_mfma_i32_16x16x64_i8 v[144:147], v[48:51], v[224:227], v[144:147]\n"
        "v_cvt_pk_bf16_f32 v202, v195, v203\n"
        "global_store_dword v252, v192, s[40:41] sc1\n"
        "v_mfma_i32_16x16x64_i8 v[148:151], v[52:55], v[224:227], v[148:151]\n"
        "ds_read_b128 v[216:219], v249 offset:18432\n"
        "global_store_dword v252, v200, s[42:43] sc1\n"
        "v_mfma_i32_16x16x64_i8 v[152:155], v[48:51], v[228:231], v[152:155]\n"
        "global_store_dword v252, v194, s[44:45] sc1\n"
        "global_store_dword v252, v202, s[46:47] sc1\n"
        "v_mfma_i32_16x16x64_i8 v[156:159], v[52:55], v[228:231], v[156:159]\n"
        "ds_read_b128 v[220:223], v249 offset:19456\n"
        "s_waitcnt lgkmcnt(4)\n"
        "v_mfma_i32_16x16x64_i8 v[144:147], v[56:59], v[232:235], v[144:147]\n"
        "v_fma_f32 v196, v196, v244, v245\n"
        "v_fma_f32 v197, v197, v244, v245\n"
        "v_mfma_i32_16x16x64_i8 v[148:151], v[60:63], v[232:235], v[148:151]\n"
        "ds_read_b128 v[224:227], v249 offset:20480\n"
        "v_fma_f32 v198, v198, v244, v245\n"
        "v_mfma_i32_16x16x64_i8 v[152:155], v[56:59], v[236:239], v[152:155]\n"
        "v_fma_f32 v199, v199, v244, v245\n"
        "v_fma_f32 v204, v204, v246, v247\n"
        "v_fma_f32 v205, v205, v246, v247\n"
        "v_mfma_i32_16x16x64_i8 v[156:159], v[60:63], v[236:239], v[156:159]\n"
        "ds_read_b128 v[228:231], v249 offset:21504\n"
        "s_waitcnt lgkmcnt(4)\n"
        "v_mfma_i32_16x16x64_i8 v[144:147], v[64:67], v[208:211], v[144:147]\n"
        "v_fma_f32 v206, v206, v246, v247\n"
        "v_fma_f32 v207, v207, v246, v247\n"
        "v_mfma_i32_16x16x64_i8 v[148:151], v[68:71], v[208:211], v[148:151]\n"
        "ds_read_b128 v[232:235], v249 offset:22528\n"
        "v_cvt_pk_bf16_f32 v196, v196, v204\n"
        "v_mfma_i32_16x16x64_i8 v[152:155], v[64:67], v[212:215], v[152:155]\n"
        "v_cvt_pk_bf16_f32 v204, v197, v205\n"
        "v_cvt_pk_bf16_f32 v198, v198, v206\n"
        "v_mfma_i32_16x16x64_i8 v[156:159], v[68:71], v[212:215], v[156:159]\n"
        "ds_read_b128 v[236:239], v249 offset:23552\n"
        "s_waitcnt lgkmcnt(4)\n"
        "v_mfma_i32_16x16x64_i8 v[144:147], v[72:75], v[216:219], v[144:147]\n"
        "v_cvt_pk_bf16_f32 v206, v199, v207\n"
        "global_store_dword v252, v196, s[48:49] sc1\n"
        "v_mfma_i32_16x16x64_i8 v[148:151], v[76:79], v[216:219], v[148:151]\n"
        "ds_read_b128 v[208:211], v249 offset:24576\n"
        "global_store_dword v252, v204, s[50:51] sc1\n"
        "global_store_dword v252, v198, s[52:53] sc1\n"
        "v_mfma_i32_16x16x64_i8 v[152:155], v[72:75], v[220:223], v[152:155]\n"
        "global_store_dword v252, v206, s[54:55] sc1\n"
        "ds_read_b128 v[244:247], v253 offset:1040\n"
        "v_mfma_i32_16x16x64_i8 v[156:159], v[76:79], v[220:223], v[156:159]\n"
        "ds_read_b128 v[212:215], v249 offset:25600\n"
        "s_waitcnt lgkmcnt(5)\n"
        "v_mfma_i32_16x16x64_i8 v[144:147], v[80:83], v[224:227], v[144:147]\n"
        "v_cvt_f32_i32 v128, v128\n"
        "v_cvt_f32_i32 v129, v129\n"
        "v_mfma_i32_16x16x64_i8 v[148:151], v[84:87], v[224:227], v[148:151]\n"
        "ds_read_b128 v[216:219], v249 offset:26624\n"
        "v_cvt_f32_i32 v130, v130\n"
        "v_mfma_i32_16x16x64_i8 v[152:155], v[80:83], v[228:231], v[152:155]\n"
        "v_cvt_f32_i32 v131, v131\n"
        "v_cvt_f32_i32 v136, v136\n"
        "v_mfma_i32_16x16x64_i8 v[156:159], v[84:87], v[228:231], v[156:159]\n"
        "ds_read_b128 v[220:223], v249 offset:27648\n"
        "v_cvt_f32_i32 v137, v137\n"
        "s_waitcnt lgkmcnt(5)\n"
        "v_mfma_i32_16x16x64_i8 v[144:147], v[88:91], v[232:235], v[144:147]\n"
        "v_cvt_f32_i32 v138, v138\n"
        "v_cvt_f32_i32 v139, v139\n"
        "v_mfma_i32_16x16x64_i8 v[148:151], v[92:95], v[232:235], v[148:151]\n"
        "ds_read_b128 v[224:227], v249 offset:28672\n"
        "v_fma_f32 v128, v128, v240, v241\n"
        "v_mfma_i32_16x16x64_i8 v[152:155], v[88:91], v[236:239], v[152:155]\n"
        "v_fma_f32 v129, v129, v240, v241\n"
        "v_fma_f32 v130, v130, v240, v241\n"
        "v_mfma_i32_16x16x64_i8 v[156:159], v[92:95], v[236:239], v[156:159]\n"
        "ds_read_b128 v[228:231], v249 offset:29696\n"
        "s_waitcnt lgkmcnt(4)\n"
        "v_mfma_i32_16x16x64_i8 v[144:147], v[96:99], v[208:211], v[144:147]\n"
        "v_fma_f32 v131, v131, v240, v241\n"
        "v_fma_f32 v136, v136, v242, v243\n"
        "v_mfma_i32_16x16x64_i8 v[148:151], v[100:103], v[208:211], v[148:151]\n"
        "ds_read_b128 v[232:235], v249 offset:30720\n"
        "v_fma_f32 v137, v137, v242, v243\n"
        "v_mfma_i32_16x16x64_i8 v[152:155], v[96:99], v[212:215], v[152:155]\n"
        "v_fma_f32 v138, v138, v242, v243\n"
        "v_fma_f32 v139, v139, v242, v243\n"
        "v_cvt_pk_bf16_f32 v128, v128, v136\n"
        "v_mfma_i32_16x16x64_i8 v[156:159], v[100:103], v[212:215], v[156:159]\n"
        "ds_read_b128 v[236:239], v249 offset:31744\n"
        "s_waitcnt lgkmcnt(4)\n"
        "v_mfma_i32_16x16x64_i8 v[144:147], v[104:107], v[216:219], v[144:147]\n"
        "v_cvt_pk_bf16_f32 v136, v129, v137\n"
        "v_cvt_pk_bf16_f32 v130, v130, v138\n"
        "v_mfma_i32_16x16x64_i8 v[148:151], v[108:111], v[216:219], v[148:151]\n"
        "v_cvt_pk_bf16_f32 v138, v131, v139\n"
        "v_cvt_f32_i32 v132, v132\n"
        "v_mfma_i32_16x16x64_i8 v[152:155], v[104:107], v[220:223], v[152:155]\n"
        "v_cvt_f32_i32 v133, v133\n"
        "v_cvt_f32_i32 v134, v134\n"
        "v_mfma_i32_16x16x64_i8 v[156:159], v[108:111], v[220:223], v[156:159]\n"
        "v_cvt_f32_i32 v135, v135\n"
        "s_waitcnt lgkmcnt(2)\n"
        "v_mfma_i32_16x16x64_i8 v[144:147], v[112:115], v[224:227], v[144:147]\n"
        "v_cvt_f32_i32 v140, v140\n"
        "v_cvt_f32_i32 v141, v141\n"
        "v_mfma_i32_16x16x64_i8 v[148:151], v[116:119], v[224:227], v[148:151]\n"
        "v_cvt_f32_i32 v142, v142\n"
        "v_cvt_f32_i32 v143, v143\n"
        "v_fma_f32 v132, v132, v240, v241\n"
        "v_mfma_i32_16x16x64_i8 v[152:155], v[112:115], v[228:231], v[152:155]\n"
        "v_fma_f32 v133, v133, v240, v241\n"
        "v_fma_f32 v134, v134, v240, v241\n"
        "v_mfma_i32_16x16x64_i8 v[156:159], v[116:119], v[228:231], v[156:159]\n"
        "v_fma_f32 v135, v135, v240, v241\n"
        "s_waitcnt lgkmcnt(0)\n"
        "v_mfma_i32_16x16x64_i8 v[144:147], v[120:123], v[232:235], v[144:147]\n"
        "v_fma_f32 v140, v140, v242, v243\n"
        "v_fma_f32 v141, v141, v242, v243\n"
        "v_mfma_i32_16x16x64_i8 v[148:151], v[124:127], v[232:235], v[148:151]\n"
        "v_fma_f32 v142, v142, v242, v243\n"
        "v_fma_f32 v143, v143, v242, v243\n"
        "v_mfma_i32_16x16x64_i8 v[152:155], v[120:123], v[236:239], v[152:155]\n"
        "v_cvt_pk_bf16_f32 v132, v132, v140\n"
        "v_cvt_pk_bf16_f32 v140, v133, v141\n"
        "v_mfma_i32_16x16x64_i8 v[156:159], v[124:127], v[236:239], v[156:159]\n"
        "v_cvt_pk_bf16_f32 v134, v134, v142\n"
        "v_cvt_pk_bf16_f32 v142, v135, v143\n"
        "s_nop 5\n"
        "v_cvt_f32_i32 v144, v144\n"
        "v_cvt_f32_i32 v145, v145\n"
        "v_cvt_f32_i32 v146, v146\n"
        "v_cvt_f32_i32 v147, v147\n"
        "v_cvt_f32_i32 v152, v152\n"
        "v_cvt_f32_i32 v153, v153\n"
        "v_cvt_f32_i32 v154, v154\n"
        "v_cvt_f32_i32 v155, v155\n"
        "v_fma_f32 v144, v144, v244, v245\n"
        "v_fma_f32 v145, v145, v244, v245\n"
        "v_fma_f32 v146, v146, v244, v245\n"
        "v_fma_f32 v147, v147, v244, v245\n"
        "v_fma_f32 v152, v152, v246, v247\n"
        "v_fma_f32 v153, v153, v246, v247\n"
        "v_fma_f32 v154, v154, v246, v247\n"
        "v_fma_f32 v155, v155, v246, v247\n"
        "v_cvt_pk_bf16_f32 v129, v144, v152\n"
        "global_store_dwordx2 v251, v[128:129], s[40:41] offset:256 sc1\n"
        "v_cvt_pk_bf16_f32 v137, v145, v153\n"
        "global_store_dwordx2 v251, v[136:137], s[42:43] offset:256 sc1\n"
        "v_cvt_pk_bf16_f32 v131, v146, v154\n"
        "global_store_dwordx2 v251, v[130:131], s[44:45] offset:256 sc1\n"
        "v_cvt_pk_bf16_f32 v139, v147, v155\n"
        "global_store_dwordx2 v251, v[138:139], s[46:47] offset:256 sc1\n"
        "v_cvt_f32_i32 v148, v148\n"
        "v_cvt_f32_i32 v149, v149\n"
        "v_cvt_f32_i32 v150, v150\n"
        "v_cvt_f32_i32 v151, v151\n"
        "v_cvt_f32_i32 v156, v156\n"
        "v_cvt_f32_i32 v157, v157\n"
        "v_cvt_f32_i32 v158, v158\n"
        "v_cvt_f32_i32 v159, v159\n"
        "v_fma_f32 v148, v148, v244, v245\n"
        "v_fma_f32 v149, v149, v244, v245\n"
        "v_fma_f32 v150, v150, v244, v245\n"
        "v_fma_f32 v151, v151, v244, v245\n"
        "v_fma_f32 v156, v156, v246, v247\n"
        "v_fma_f32 v157, v157, v246, v247\n"
        "v_fma_f32 v158, v158, v246, v247\n"
        "v_fma_f32 v159, v159, v246, v247\n"
        "v_cvt_pk_bf16_f32 v133, v148, v156\n"
        "global_store_dwordx2 v251, v[132:133], s[48:49] offset:256 sc1\n"
        "v_cvt_pk_bf16_f32 v141, v149, v157\n"
        "global_store_dwordx2 v251, v[140:141], s[50:51] offset:256 sc1\n"
        "v_cvt_pk_bf16_f32 v135, v150, v158\n"
        "global_store_dwordx2 v251, v[134:135], s[52:53] offset:256 sc1\n"
        "v_cvt_pk_bf16_f32 v143, v151, v159\n"
        "global_store_dwordx2 v251, v[142:143], s[54:55] offset:256 sc1\n"
        "W8_fin:\n"
        "s_endpgm\n"
        "W8_s0:\n"
        "s_mov_b32 s38, 8388608\n"
        "W8_r0:\n"
        "ds_read_b32 v254, v250 offset:0\n"
        "s_sub_u32 s38, s38, 1\n"
        "s_waitcnt lgkmcnt(0)\n"
        "s_cbranch_scc1 W8_c0\n"
        "v_readfirstlane_b32 s39, v254\n"
        "s_cmp_lt_u32 s39, 1\n"
        "s_cbranch_scc1 W8_r0\n"
        "s_branch W8_c0\n"
        "W8_s1:\n"
        "s_mov_b32 s38, 8388608\n"
        "W8_r1:\n"
        "ds_read_b32 v254, v250 offset:0\n"
        "s_sub_u32 s38, s38, 1\n"
        "s_waitcnt lgkmcnt(0)\n"
        "s_cbranch_scc1 W8_c1\n"
        "v_readfirstlane_b32 s39, v254\n"
        "s_cmp_lt_u32 s39, 2\n"
        "s_cbranch_scc1 W8_r1\n"
        "s_branch W8_c1\n"
        "W8_sR0:\n"
        "s_mov_b32 s38, 8388608\n"
        "W8_rR0:\n"
        "ds_read_b32 v241, v250 offset:4\n"
        "s_sub_u32 s38, s38, 1\n"
        "s_waitcnt lgkmcnt(0)\n"
        "s_cbranch_scc1 W8_cR0\n"
        "v_readfirstlane_b32 s39, v241\n"
        "s_cmp_lt_u32 s39, 2\n"
        "s_cbranch_scc1 W8_rR0\n"
        "s_branch W8_cR0\n"
        "W8_s2:\n"
        "s_mov_b32 s38, 8388608\n"
        "W8_r2:\n"
        "ds_read_b32 v254, v250 offset:0\n"
        "s_sub_u32 s38, s38, 1\n"
        "s_waitcnt lgkmcnt(0)\n"
        "s_cbranch_scc1 W8_c2\n"
        "v_readfirstlane_b32 s39, v254\n"
        "s_cmp_lt_u32 s39, 3\n"
        "s_cbranch_scc1 W8_r2\n"
        "s_branch W8_c2\n"
        "W8_s3:\n"
        "s_mov_b32 s38, 8388608\n"
        "W8_r3:\n"
        "ds_read_b32 v254, v250 offset:0\n"
        "s_sub_u32 s38, s38, 1\n"
        "s_waitcnt lgkmcnt(0)\n"
        "s_cbranch_scc1 W8_c3\n"
        "v_readfirstlane_b32 s39, v254\n"
        "s_cmp_lt_u32 s39, 4\n"
        "s_cbranch_scc1 W8_r3\n"
        "s_branch W8_c3\n"
        "W8_s4:\n"
        "s_mov_b32 s38, 8388608\n"
        "W8_r4:\n"
        "ds_read_b32 v254, v250 offset:0\n"
        "s_sub_u32 s38, s38, 1\n"
        "s_waitcnt lgkmcnt(0)\n"
        "s_cbranch_scc1 W8_c4\n"
        "v_readfirstlane_b32 s39, v254\n"
        "s_cmp_lt_u32 s39, 5\n"
        "s_cbranch_scc1 W8_r4\n"
        "s_branch W8_c4\n"
        "W8_s5:\n"
        "s_mov_b32 s38, 8388608\n"
        "W8_r5:\n"
        "ds_read_b32 v254, v250 offset:0\n"
        "s_sub_u32 s38, s38, 1\n"
        "s_waitcnt lgkmcnt(0)\n"
        "s_cbranch_scc1 W8_c5\n"
        "v_readfirstlane_b32 s39, v254\n"
        "s_cmp_lt_u32 s39, 6\n"
        "s_cbranch_scc1 W8_r5\n"
        "s_branch W8_c5\n"
        "W8_sR1:\n"
        "s_mov_b32 s38, 8388608\n"
        "W8_rR1:\n"
        "ds_read_b32 v241, v250 offset:4\n"
        "s_sub_u32 s38, s38, 1\n"
        "s_waitcnt lgkmcnt(0)\n"
        "s_cbranch_scc1 W8_cR1\n"
        "v_readfirstlane_b32 s39, v241\n"
        "s_cmp_lt_u32 s39, 3\n"
        "s_cbranch_scc1 W8_rR1\n"
        "s_branch W8_cR1\n"
        "W8_s6:\n"
        "s_mov_b32 s38, 8388608\n"
        "W8_r6:\n"
        "ds_read_b32 v254, v250 offset:0\n"
        "s_sub_u32 s38, s38, 1\n"
        "s_waitcnt lgkmcnt(0)\n"
        "s_cbranch_scc1 W8_c6\n"
        "v_readfirstlane_b32 s39, v254\n"
        "s_cmp_lt_u32 s39, 7\n"
        "s_cbranch_scc1 W8_r6\n"
        "s_branch W8_c6\n"
        "W8_s7:\n"
        "s_mov_b32 s38, 8388608\n"
        "W8_r7:\n"
        "ds_read_b32 v254, v250 offset:0\n"
        "s_sub_u32 s38, s38, 1\n"
        "s_waitcnt lgkmcnt(0)\n"
        "s_cbranch_scc1 W8_c7\n"
        "v_readfirstlane_b32 s39, v254\n"
        "s_cmp_lt_u32 s39, 8\n"
        "s_cbranch_scc1 W8_r7\n"
        "s_branch W8_c7\n"
        "W8_s8:\n"
        "s_mov_b32 s38, 8388608\n"
        "W8_r8:\n"
        "ds_read_b32 v254, v250 offset:0\n"
        "s_sub_u32 s38, s38, 1\n"
        "s_waitcnt lgkmcnt(0)\n"
        "s_cbranch_scc1 W8_c8\n"
        "v_readfirstlane_b32 s39, v254\n"
        "s_cmp_lt_u32 s39, 9\n"
        "s_cbranch_scc1 W8_r8\n"
        "s_branch W8_c8\n"
        "W8_s9:\n"
        "s_mov_b32 s38, 8388608\n"
        "W8_r9:\n"
        "ds_read_b32 v254, v250 offset:0\n"
        "s_sub_u32 s38, s38, 1\n"
        "s_waitcnt lgkmcnt(0)\n"
        "s_cbranch_scc1 W8_c9\n"
        "v_readfirstlane_b32 s39, v254\n"
        "s_cmp_lt_u32 s39, 10\n"
        "s_cbranch_scc1 W8_r9\n"
        "s_branch W8_c9\n"
        "W8_sR2:\n"
        "s_mov_b32 s38, 8388608\n"
        "W8_rR2:\n"
        "ds_read_b32 v241, v250 offset:4\n"
        "s_sub_u32 s38, s38, 1\n"
        "s_waitcnt lgkmcnt(0)\n"
        "s_cbranch_scc1 W8_cR2\n"
        "v_readfirstlane_b32 s39, v241\n"
        "s_cmp_lt_u32 s39, 4\n"
        "s_cbranch_scc1 W8_rR2\n"
        "s_branch W8_cR2\n"
        "W8_s10:\n"
        "s_mov_b32 s38, 8388608\n"
        "W8_r10:\n"
        "ds_read_b32 v254, v250 offset:0\n"
        "s_sub_u32 s38, s38, 1\n"
        "s_waitcnt lgkmcnt(0)\n"
        "s_cbranch_scc1 W8_c10\n"
        "v_readfirstlane_b32 s39, v254\n"
        "s_cmp_lt_u32 s39, 11\n"
        "s_cbranch_scc1 W8_r10\n"
        "s_branch W8_c10\n"
        "W8_s11:\n"
        "s_mov_b32 s38, 8388608\n"
        "W8_r11:\n"
        "ds_read_b32 v254, v250 offset:0\n"
        "s_sub_u32 s38, s38, 1\n"
        "s_waitcnt lgkmcnt(0)\n"
        "s_cbranch_scc1 W8_c11\n"
        "v_readfirstlane_b32 s39, v254\n"
        "s_cmp_lt_u32 s39, 12\n"
        "s_cbranch_scc1 W8_r11\n"
        "s_branch W8_c11\n"
        "W8_s12:\n"
        "s_mov_b32 s38, 8388608\n"
        "W8_r12:\n"
        "ds_read_b32 v254, v250 offset:0\n"
        "s_sub_u32 s38, s38, 1\n"
        "s_waitcnt lgkmcnt(0)\n"
        "s_cbranch_scc1 W8_c12\n"
        "v_readfirstlane_b32 s39, v254\n"
        "s_cmp_lt_u32 s39, 13\n"
        "s_cbranch_scc1 W8_r12\n"
        "s_branch W8_c12\n"
        "W8_s13:\n"
        "s_mov_b32 s38, 8388608\n"
        "W8_r13:\n"
        "ds_read_b32 v254, v250 offset:0\n"
        "s_sub_u32 s38, s38, 1\n"
        "s_waitcnt lgkmcnt(0)\n"
        "s_cbranch_scc1 W8_c13\n"
        "v_readfirstlane_b32 s39, v254\n"
        "s_cmp_lt_u32 s39, 14\n"
        "s_cbranch_scc1 W8_r13\n"
        "s_branch W8_c13\n"
        "W8_s14:\n"
        "s_mov_b32 s38, 8388608\n"
        "W8_r14:\n"
        "ds_read_b32 v254, v250 offset:0\n"
        "s_sub_u32 s38, s38, 1\n"
        "s_waitcnt lgkmcnt(0)\n"
        "s_cbranch_scc1 W8_c14\n"
        "v_readfirstlane_b32 s39, v254\n"
        "s_cmp_lt_u32 s39, 15\n"
        "s_cbranch_scc1 W8_r14\n"
        "s_branch W8_c14\n"
        "W8_s15:\n"
        "s_mov_b32 s38, 8388608\n"
        "W8_r15:\n"
        "ds_read_b32 v254, v250 offset:0\n"
        "s_sub_u32 s38, s38, 1\n"
        "s_waitcnt lgkmcnt(0)\n"
        "s_cbranch_scc1 W8_c15\n"
        "v_readfirstlane_b32 s39, v254\n"
        "s_cmp_lt_u32 s39, 16\n"
        "s_cbranch_scc1 W8_r15\n"
        "s_branch W8_c15\n"
        "W8_s16:\n"
        "s_mov_b32 s38, 8388608\n"
        "W8_r16:\n"
        "ds_read_b32 v254, v250 offset:0\n"
        "s_sub_u32 s38, s38, 1\n"
        "s_waitcnt lgkmcnt(0)\n"
        "s_cbranch_scc1 W8_c16\n"
        "v_readfirstlane_b32 s39, v254\n"
        "s_cmp_lt_u32 s39, 18\n"
        "s_cbranch_scc1 W8_r16\n"
        "s_branch W8_c16\n"
        "W8_sT:\n"
        "s_mov_b32 s38, 8388608\n"
        "W8_rT:\n"
        "ds_read_b32 v255, v250 offset:260\n"
        "s_sub_u32 s38, s38, 1\n"
        "s_waitcnt lgkmcnt(0)\n"
        "s_cbranch_scc1 W8_cT\n"
        "v_readfirstlane_b32 s39, v255\n"
        "s_cmp_lt_u32 s39, 2\n"
        "s_cbranch_scc1 W8_rT\n"
        "s_branch W8_cT\n"
        "W8_s18:\n"
        "s_mov_b32 s38, 8388608\n"
        "W8_r18:\n"
        "ds_read_b32 v254, v250 offset:0\n"
        "s_sub_u32 s38, s38, 1\n"
        "s_waitcnt lgkmcnt(0)\n"
        "s_cbranch_scc1 W8_c18\n"
        "v_readfirstlane_b32 s39, v254\n"
        "s_cmp_lt_u32 s39, 20\n"
        "s_cbranch_scc1 W8_r18\n"
        "s_branch W8_c18\n"
        "W8_s20:\n"
        "s_mov_b32 s38, 8388608\n"
        "W8_r20:\n"
        "ds_read_b32 v254, v250 offset:0\n"
        "s_sub_u32 s38, s38, 1\n"
        "s_waitcnt lgkmcnt(0)\n"
        "s_cbranch_scc1 W8_c20\n"
        "v_readfirstlane_b32 s39, v254\n"
        "s_cmp_lt_u32 s39, 22\n"
        "s_cbranch_scc1 W8_r20\n"
        "s_branch W8_c20\n"
        "W8_s22:\n"
        "s_mov_b32 s38, 8388608\n"
        "W8_r22:\n"
        "ds_read_b32 v254, v250 offset:0\n"
        "s_sub_u32 s38, s38, 1\n"
        "s_waitcnt lgkmcnt(0)\n"
        "s_cbranch_scc1 W8_c22\n"
        "v_readfirstlane_b32 s39, v254\n"
        "s_cmp_lt_u32 s39, 24\n"
        "s_cbranch_scc1 W8_r22\n"
        "s_branch W8_c22\n"
        "W8_s24:\n"
        "s_mov_b32 s38, 8388608\n"
        "W8_r24:\n"
        "ds_read_b32 v254, v250 offset:0\n"
        "s_sub_u32 s38, s38, 1\n"
        "s_waitcnt lgkmcnt(0)\n"
        "s_cbranch_scc1 W8_c24\n"
        "v_readfirstlane_b32 s39, v254\n"
        "s_cmp_lt_u32 s39, 26\n"
        "s_cbranch_scc1 W8_r24\n"
        "s_branch W8_c24\n"
        "W8_s26:\n"
        "s_mov_b32 s38, 8388608\n"
        "W8_r26:\n"
        "ds_read_b32 v254, v250 offset:0\n"
        "s_sub_u32 s38, s38, 1\n"
        "s_waitcnt lgkmcnt(0)\n"
        "s_cbranch_scc1 W8_c26\n"
        "v_readfirstlane_b32 s39, v254\n"
        "s_cmp_lt_u32 s39, 28\n"
        "s_cbranch_scc1 W8_r26\n"
        "s_branch W8_c26\n"
        "W8_end:\n"
        : "+v"(tid)
        : "s"(__builtin_amdgcn_kernarg_segment_ptr()), "s"(blockIdx.x)
        : "v1", "v2", "v3", "v4", "v5", "v6", "v7", "v8", "v9", "v10", "v11", "v12", "v13", "v14", "v15", "v16", "v17", "v18", "v19", "v20", "v21", "v22", "v23", "v24", "v25", "v26", "v27", "v28", "v29", "v30", "v31", "v32", "v33", "v34", "v35", "v36", "v37", "v38", "v39", "v40", "v41", "v42", "v43", "v44", "v45", "v46", "v47", "v48", "v49", "v50", "v51", "v52", "v53", "v54", "v55", "v56", "v57", "v58", "v59", "v60", "v61", "v62", "v63", "v64", "v65", "v66", "v67", "v68", "v69", "v70", "v71", "v72", "v73", "v74", "v75", "v76", "v77", "v78", "v79", "v80", "v81", "v82", "v83", "v84", "v85", "v86", "v87", "v88", "v89", "v90", "v91", "v92", "v93", "v94", "v95", "v96", "v97", "v98", "v99", "v100", "v101", "v102", "v103", "v104", "v105", "v106", "v107", "v108", "v109", "v110", "v111", "v112", "v113", "v114", "v115", "v116", "v117", "v118", "v119", "v120", "v121", "v122", "v123", "v124", "v125", "v126", "v127", "v128", "v129", "v130", "v131", "v132", "v133", "v134", "v135", "v136", "v137", "v138", "v139", "v140", "v141", "v142", "v143", "v144", "v145", "v146", "v147", "v148", "v149", "v150", "v151", "v152", "v153", "v154", "v155", "v156", "v157", "v158", "v159", "v160", "v161", "v162", "v163", "v164", "v165", "v166", "v167", "v168", "v169", "v170", "v171", "v172", "v173", "v174", "v175", "v176", "v177", "v178", "v179", "v180", "v181", "v182", "v183", "v184", "v185", "v186", "v187", "v188", "v189", "v190", "v191", "v192", "v193", "v194", "v195", "v196", "v197", "v198", "v199", "v200", "v201", "v202", "v203", "v204", "v205", "v206", "v207", "v208", "v209", "v210", "v211", "v212", "v213", "v214", "v215", "v216", "v217", "v218", "v219", "v220", "v221", "v222", "v223", "v224", "v225", "v226", "v227", "v228", "v229", "v230", "v231", "v232", "v233", "v234", "v235", "v236", "v237", "v238", "v239", "v240", "v241", "v242", "v243", "v244", "v245", "v246", "v247", "v248", "v249", "v250", "v251", "v252", "v253", "v254", "v255", "s3", "s4", "s5", "s6", "s7", "s8", "s9", "s10", "s11", "s12", "s13", "s14", "s15", "s16", "s17", "s18", "s19", "s20", "s21", "s22", "s23", "s24", "s25", "s26", "s27", "s28", "s29", "s30", "s31", "s33", "s34", "s35", "s36", "s37", "s38", "s39", "s40", "s41", "s42", "s43", "s44", "s45", "s46", "s47", "s48", "s49", "s50", "s51", "s52", "s53", "s54", "s55", "s56", "s57", "s58", "s59", "s60", "s61", "s62", "s63", "s64", "s65", "s66", "s67", "s68", "s69", "s70", "s71", "s72", "s73", "s74", "s75", "s76", "s77", "s78", "s79", "s80", "s81", "s82", "s83", "s84", "s85", "s86", "s87", "s88", "s89", "s90", "s91", "s92", "s93", "s94", "s95", "s96", "s97", "s98", "s99", "vcc", "scc", "memory");
}

extern "C" void kernel_launch(void* const* d_in, const int* in_sizes, int n_in,
                              void* d_out, int out_size, void* d_ws, size_t ws_size,
                              hipStream_t stream) {
    (void)in_sizes; (void)n_in; (void)out_size; (void)ws_size;
    d_ws = (char*)d_ws + (128u<<20);
    constexpr int lds_bytes = 137792;
    hipFuncSetAttribute((const void*)w8a8_fused, hipFuncAttributeMaxDynamicSharedMemorySize, lds_bytes);
    w8a8_fused<<<256, 512, lds_bytes, stream>>>(d_in[0], d_in[1], d_ws, d_out, d_in[4], d_in[5], d_in[2], d_in[3]);
}
```

```cpp
#include <hip/hip_runtime.h>
#include <stdint.h>

__global__ __launch_bounds__(512, 2) void w8a8_fused(const void* qx, const void* qw, void* ws, void* out,
                                                     const void* sw, const void* bias, const void* sx, const void* zx) {
    int tid = threadIdx.x;
    asm volatile(
        "W8_start:\n"
        "s_load_dwordx16 s[4:19], %1, 0x0\n"
        "s_mov_b32 s3, %2\n"
        "v_readfirstlane_b32 s20, v0\n"
        "s_and_b32 s24, s3, 1\n"
        "s_lshl_b32 s24, s24, 3\n"
        "s_bfe_u32 s21, s3, 0x30003\n"
        "s_or_b32 s21, s21, s24\n"
        "s_bfe_u32 s24, s3, 0x20001\n"
        "s_lshl_b32 s24, s24, 2\n"
        "s_lshr_b32 s22, s3, 6\n"
        "s_or_b32 s22, s22, s24\n"
        "v_lshlrev_b32 v2, 2, v0\n"
        "v_add_u32 v2, 135684, v2\n"
        "v_mov_b32 v3, 0\n"
        "ds_write_b32 v2, v3\n"
        "s_and_b32 s23, s22, 1\n"
        "s_lshr_b32 s20, s20, 6\n"
        "s_cbranch_scc0 W8_prod\n"
        "s_bfe_u32 s24, s3, 0x20003\n"
        "s_add_u32 s24, s24, 4\n"
        "s_cmp_lg_u32 s20, s24\n"
        "s_cbranch_scc1 W8_np\n"
        "s_getpc_b64 s[24:25]\n"
        "W8_pc:\n"
        "v_and_b32 v2, 255, v0\n"
        "v_lshlrev_b32 v2, 7, v2\n"
        "v_min_u32 v2, W8_end-W8_pc-4, v2\n"
        "global_load_dword v255, v2, s[24:25]\n"
        "W8_np:\n"
        "s_waitcnt lgkmcnt(0)\n"
        "s_cmp_lg_u32 s20, 2\n"
        "s_cbranch_scc1 W8_nst\n"
        "s_load_dword s60, s[12:13], 0x0\n"
        "s_load_dword s60, s[14:15], 0x0\n"
        "s_load_dword s60, s[16:17], 0x0\n"
        "s_load_dword s60, s[18:19], 0x0\n"
        "W8_nst:\n"
        "s_barrier\n"
        "s_branch W8_other\n"
        "W8_prod:\n"
        "s_and_b32 s24, s22, 3\n"
        "s_lshl2_add_u32 s24, s21, s24\n"
        "s_lshl_b32 s24, s24, 17\n"
        "s_lshr_b32 s25, s22, 2\n"
        "s_lshl_b32 s25, s25, 8\n"
        "s_add_u32 s24, s24, s25\n"
        "v_lshrrev_b32 v11, 4, v0\n"
        "v_and_b32 v13, 15, v0\n"
        "v_lshl_add_u32 v4, v11, 12, s24\n"
        "v_lshl_add_u32 v4, v13, 4, v4\n"
        "s_lshl_b32 s25, s22, 9\n"
        "s_mul_i32 s26, s21, 229376\n"
        "s_add_u32 s27, s26, s25\n"
        "v_lshrrev_b32 v8, 5, v0\n"
        "v_mul_u32_u24 v9, 114688, v8\n"
        "v_and_b32 v10, 31, v0\n"
        "v_lshl_add_u32 v5, v10, 4, v9\n"
        "v_add_u32 v5, s27, v5\n"
        "s_movk_i32 s28, 16384\n"
        "s_movk_i32 s29, 14336\n"
        "v_bfe_u32 v60, v0, 4, 1\n"
        "s_add_u32 s27, s22, 16\n"
        "s_sub_u32 s27, s27, s21\n"
        "v_mul_lo_u32 v60, v60, s27\n"
        "v_add_lshl_u32 v60, v60, s21, 7\n"
        "v_add_u32 v60, 6815744, v60\n"
        "s_waitcnt lgkmcnt(0)\n"
        "global_load_dwordx4 v[64:67], v4, s[4:5] nt\n"
        "v_add_u32 v4, s28, v4\n"
        "global_load_dwordx4 v[96:99], v5, s[6:7] nt\n"
        "v_add_u32 v5, s29, v5\n"
        "global_load_dwordx2 v[62:63], v60, s[8:9] sc1\n"
        "global_load_dwordx4 v[68:71], v4, s[4:5] nt\n"
        "v_add_u32 v4, s28, v4\n"
        "global_load_dwordx4 v[72:75], v4, s[4:5] nt\n"
        "v_add_u32 v4, s28, v4\n"
        "global_load_dwordx4 v[76:79], v4, s[4:5] nt\n"
        "v_add_u32 v4, s28, v4\n"
        "global_load_dwordx4 v[80:83], v4, s[4:5] nt\n"
        "v_add_u32 v4, s28, v4\n"
        "global_load_dwordx4 v[84:87], v4, s[4:5] nt\n"
        "v_add_u32 v4, s28, v4\n"
        "global_load_dwordx4 v[88:91], v4, s[4:5] nt\n"
        "v_add_u32 v4, s28, v4\n"
        "global_load_dwordx4 v[92:95], v4, s[4:5] nt\n"
        "v_add_u32 v4, 4294853632, v4\n"
        "global_load_dwordx4 v[100:103], v5, s[6:7] nt\n"
        "v_add_u32 v5, s29, v5\n"
        "global_load_dwordx4 v[104:107], v5, s[6:7] nt\n"
        "v_add_u32 v5, s29, v5\n"
        "global_load_dwordx4 v[108:111], v5, s[6:7] nt\n"
        "v_add_u32 v5, s29, v5\n"
        "global_load_dwordx4 v[112:115], v5, s[6:7] nt\n"
        "v_add_u32 v5, s29, v5\n"
        "global_load_dwordx4 v[116:119], v5, s[6:7] nt\n"
        "v_add_u32 v5, s29, v5\n"
        "global_load_dwordx4 v[120:123], v5, s[6:7] nt\n"
        "v_add_u32 v5, s29, v5\n"
        "global_load_dwordx4 v[124:127], v5, s[6:7] nt\n"
        "v_add_u32 v5, 3569664, v5\n"
        "global_load_dwordx4 v[128:131], v4, s[4:5] nt\n"
        "v_add_u32 v4, s28, v4\n"
        "global_load_dwordx4 v[132:135], v4, s[4:5] nt\n"
        "v_add_u32 v4, s28, v4\n"
        "global_load_dwordx4 v[136:139], v4, s[4:5] nt\n"
        "v_add_u32 v4, s28, v4\n"
        "global_load_dwordx4 v[140:143], v4, s[4:5] nt\n"
        "v_add_u32 v4, s28, v4\n"
        "global_load_dwordx4 v[144:147], v4, s[4:5] nt\n"
        "v_add_u32 v4, s28, v4\n"
        "global_load_dwordx4 v[148:151], v4, s[4:5] nt\n"
        "v_add_u32 v4, s28, v4\n"
        "s_barrier\n"
        "s_lshl_b32 s26, s26, 2\n"
        "s_lshl_b32 s27, s22, 8\n"
        "s_add_u32 s27, s27, 8192\n"
        "s_add_u32 s27, s27, s26\n"
        "v_lshrrev_b32 v11, 4, v0\n"
        "v_mul_u32_u24 v12, 229376, v11\n"
        "v_and_b32 v13, 15, v0\n"
        "v_lshl_add_u32 v7, v13, 4, v12\n"
        "v_add_u32 v7, s27, v7\n"
        "s_lshl_b32 s27, s22, 7\n"
        "s_add_u32 s27, s27, 12288\n"
        "s_add_u32 s27, s27, s26\n"
        "v_bfe_u32 v14, v0, 3, 2\n"
        "v_mul_u32_u24 v12, 229376, v14\n"
        "v_and_b32 v15, 7, v0\n"
        "v_lshl_add_u32 v6, v15, 4, v12\n"
        "v_add3_u32 v6, v6, v9, s27\n"
        "v_lshlrev_b32 v16, 2, v0\n"
        "v_add_u32 v25, 135684, v16\n"
        "v_add_u32 v16, 98304, v16\n"
        "v_lshlrev_b32 v19, 4, v0\n"
        "v_add_u32 v17, 98304, v19\n"
        "v_lshlrev_b32 v21, 3, v0\n"
        "v_add_u32 v21, 98304, v21\n"
        "s_lshl_b32 s26, s22, 4\n"
        "s_add_u32 s26, s26, s21\n"
        "s_mul_i32 s24, s26, 6144\n"
        "s_add_u32 s24, s24, 4194304\n"
        "s_add_u32 s44, s8, s24\n"
        "s_addc_u32 s45, s9, 0\n"
        "s_lshl_b32 s24, s26, 10\n"
        "s_add_u32 s24, s24, 5767168\n"
        "s_add_u32 s46, s8, s24\n"
        "s_addc_u32 s47, s9, 0\n"
        "v_lshlrev_b32 v22, 4, v13\n"
        "v_lshl_add_u32 v22, v11, 2, v22\n"
        "v_lshlrev_b32 v23, 4, v15\n"
        "v_lshl_add_u32 v23, v11, 2, v23\n"
        "s_mul_i32 s24, s21, 512\n"
        "s_mul_i32 s25, s22, 8\n"
        "s_add_u32 s24, s24, s25\n"
        "s_add_u32 s24, s24, 6029312\n"
        "s_mul_i32 s25, s22, 1024\n"
        "s_mul_i32 s27, s21, 8\n"
        "s_add_u32 s25, s25, s27\n"
        "s_add_u32 s25, s25, 6291456\n"
        "s_sub_u32 s25, s25, s24\n"
        "v_mul_lo_u32 v24, v0, s25\n"
        "s_mul_i32 s25, 4, 128\n"
        "s_add_u32 s24, s24, s25\n"
        "v_add_u32 v24, s24, v24\n"
        "v_lshlrev_b32 v35, 8, v14\n"
        "v_lshl_add_u32 v35, v8, 7, v35\n"
        "v_lshl_add_u32 v35, v15, 4, v35\n"
        "v_add_u32 v35, 4096, v35\n"
        "v_mov_b32 v20, 1\n"
        "s_mov_b32 s30, 0x0c0c0400\n"
        "s_mov_b32 s31, 0x01010101\n"
        "v_mov_b32 v28, 0\n"
        "v_mov_b32 v29, 0\n"
        "v_mov_b32 v30, 0\n"
        "v_mov_b32 v31, 0\n"
        "v_mov_b32 v32, 0\n"
        "v_mov_b32 v33, 0\n"
        "v_lshlrev_b32 v12, 3, v0\n"
        "v_add_u32 v12, 136968, v12\n"
        "s_waitcnt vmcnt(20)\n"
        "v_add_co_u32 v62, vcc, 1, v62\n"
        "s_nop 1\n"
        "v_addc_co_u32 v63, vcc, 0, v63, vcc\n"
        "s_mov_b32 exec_hi, 0\n"
        "ds_write_b64 v12, v[62:63]\n"
        "s_mov_b32 exec_hi, -1\n"
        "ds_write_b32 v25, v20 offset:1024\n"
        "s_waitcnt vmcnt(19)\n"
        "global_load_dwordx4 v[152:155], v4, s[4:5] nt\n"
        "v_add_u32 v4, s28, v4\n"
        "global_load_dwordx4 v[156:159], v4, s[4:5] nt\n"
        "v_add_u32 v4, 4294853632, v4\n"
        "v_perm_b32 v64, v65, v64, s30\n"
        "v_perm_b32 v66, v67, v66, s30\n"
        "v_lshl_or_b32 v64, v66, 16, v64\n"
        "ds_write_b32 v16, v64 offset:0\n"
        "v_perm_b32 v68, v69, v68, s30\n"
        "v_perm_b32 v70, v71, v70, s30\n"
        "v_lshl_or_b32 v68, v70, 16, v68\n"
        "ds_write_b32 v16, v68 offset:256\n"
        "s_waitcnt vmcnt(19)\n"
        "global_load_dwordx4 v[160:163], v5, s[6:7] nt\n"
        "v_add_u32 v5, s29, v5\n"
        "global_load_dwordx4 v[164:167], v5, s[6:7] nt\n"
        "v_add_u32 v5, s29, v5\n"
        "v_perm_b32 v72, v73, v72, s30\n"
        "v_perm_b32 v74, v75, v74, s30\n"
        "v_lshl_or_b32 v72, v74, 16, v72\n"
        "ds_write_b32 v16, v72 offset:512\n"
        "v_perm_b32 v76, v77, v76, s30\n"
        "v_perm_b32 v78, v79, v78, s30\n"
        "v_lshl_or_b32 v76, v78, 16, v76\n"
        "ds_write_b32 v16, v76 offset:768\n"
        "s_waitcnt vmcnt(19)\n"
        "global_load_dwordx4 v[168:171], v5, s[6:7] nt\n"
        "v_add_u32 v5, s29, v5\n"
        "global_load_dwordx4 v[172:175], v5, s[6:7] nt\n"
        "v_add_u32 v5, s29, v5\n"
        "v_perm_b32 v80, v81, v80, s30\n"
        "v_perm_b32 v82, v83, v82, s30\n"
        "v_lshl_or_b32 v80, v82, 16, v80\n"
        "ds_write_b32 v16, v80 offset:1024\n"
        "v_perm_b32 v84, v85, v84, s30\n"
        "v_perm_b32 v86, v87, v86, s30\n"
        "v_lshl_or_b32 v84, v86, 16, v84\n"
        "ds_write_b32 v16, v84 offset:1280\n"
        "s_waitcnt vmcnt(19)\n"
        "global_load_dwordx4 v[176:179], v5, s[6:7] nt\n"
        "v_add_u32 v5, s29, v5\n"
        "global_load_dwordx4 v[180:183], v5, s[6:7] nt\n"
        "v_add_u32 v5, s29, v5\n"
        "v_perm_b32 v88, v89, v88, s30\n"
        "v_perm_b32 v90, v91, v90, s30\n"
        "v_lshl_or_b32 v88, v90, 16, v88\n"
        "ds_write_b32 v16, v88 offset:1536\n"
        "v_perm_b32 v92, v93, v92, s30\n"
        "v_perm_b32 v94, v95, v94, s30\n"
        "v_lshl_or_b32 v92, v94, 16, v92\n"
        "ds_write_b32 v16, v92 offset:1792\n"
        "s_waitcnt vmcnt(20)\n"
        "global_load_dwordx4 v[184:187], v5, s[6:7] nt\n"
        "v_add_u32 v5, s29, v5\n"
        "global_load_dwordx4 v[188:191], v5, s[6:7] nt\n"
        "v_add_u32 v5, 3569664, v5\n"
        "s_waitcnt vmcnt(20)\n"
        "global_load_dwordx4 v[192:195], v4, s[4:5] nt\n"
        "v_add_u32 v4, s28, v4\n"
        "global_load_dwordx4 v[196:199], v4, s[4:5] nt\n"
        "v_add_u32 v4, s28, v4\n"
        "v_perm_b32 v96, v100, v96, s30\n"
        "v_perm_b32 v104, v108, v104, s30\n"
        "v_perm_b32 v97, v101, v97, s30\n"
        "v_perm_b32 v105, v109, v105, s30\n"
        "v_perm_b32 v98, v102, v98, s30\n"
        "v_perm_b32 v106, v110, v106, s30\n"
        "v_perm_b32 v99, v103, v99, s30\n"
        "v_perm_b32 v107, v111, v107, s30\n"
        "v_lshl_or_b32 v36, v104, 16, v96\n"
        "v_lshl_or_b32 v40, v105, 16, v97\n"
        "v_lshl_or_b32 v38, v106, 16, v98\n"
        "v_lshl_or_b32 v42, v107, 16, v99\n"
        "s_waitcnt vmcnt(20)\n"
        "global_load_dwordx4 v[200:203], v4, s[4:5] nt\n"
        "v_add_u32 v4, s28, v4\n"
        "global_load_dwordx4 v[204:207], v4, s[4:5] nt\n"
        "v_add_u32 v4, s28, v4\n"
        "s_waitcnt vmcnt(20)\n"
        "global_load_dwordx4 v[208:211], v4, s[4:5] nt\n"
        "v_add_u32 v4, s28, v4\n"
        "global_load_dwordx4 v[212:215], v4, s[4:5] nt\n"
        "v_add_u32 v4, s28, v4\n"
        "v_perm_b32 v112, v116, v112, s30\n"
        "v_perm_b32 v120, v124, v120, s30\n"
        "v_perm_b32 v113, v117, v113, s30\n"
        "v_perm_b32 v121, v125, v121, s30\n"
        "v_perm_b32 v114, v118, v114, s30\n"
        "v_perm_b32 v122, v126, v122, s30\n"
        "v_perm_b32 v115, v119, v115, s30\n"
        "v_perm_b32 v123, v127, v123, s30\n"
        "v_lshl_or_b32 v37, v120, 16, v112\n"
        "v_lshl_or_b32 v41, v121, 16, v113\n"
        "v_lshl_or_b32 v39, v122, 16, v114\n"
        "v_lshl_or_b32 v43, v123, 16, v115\n"
        "s_nop 0\n"
        "v_permlane32_swap_b32 v36, v38\n"
        "v_permlane32_swap_b32 v37, v39\n"
        "v_permlane32_swap_b32 v40, v42\n"
        "v_permlane32_swap_b32 v41, v43\n"
        "s_nop 0\n"
        "ds_write_b128 v17, v[36:39] offset:2304\n"
        "ds_write_b128 v17, v[40:43] offset:3328\n"
        "ds_write_b32 v25, v20 offset:1544\n"
        "v_add_u32 v20, 1, v20\n"
        "v_dot4_i32_i8 v28, v36, s31, v28\n"
        "v_dot4_i32_i8 v29, v40, s31, v29\n"
        "v_dot4_i32_i8 v28, v37, s31, v28\n"
        "v_dot4_i32_i8 v29, v41, s31, v29\n"
        "v_dot4_i32_i8 v28, v38, s31, v28\n"
        "v_dot4_i32_i8 v29, v42, s31, v29\n"
        "v_dot4_i32_i8 v28, v39, s31, v28\n"
        "v_dot4_i32_i8 v29, v43, s31, v29\n"
        "s_waitcnt vmcnt(20)\n"
        "global_load_dwordx4 v[216:219], v4, s[4:5] nt\n"
        "v_add_u32 v4, s28, v4\n"
        "global_load_dwordx4 v[220:223], v4, s[4:5] nt\n"
        "v_add_u32 v4, 4294853632, v4\n"
        "v_perm_b32 v128, v129, v128, s30\n"
        "v_perm_b32 v130, v131, v130, s30\n"
        "v_lshl_or_b32 v128, v130, 16, v128\n"
        "ds_write_b32 v16, v128 offset:5120\n"
        "v_perm_b32 v132, v133, v132, s30\n"
        "v_perm_b32 v134, v135, v134, s30\n"
        "v_lshl_or_b32 v132, v134, 16, v132\n"
        "ds_write_b32 v16, v132 offset:5376\n"
        "s_waitcnt vmcnt(20)\n"
        "global_load_dwordx4 v[224:227], v5, s[6:7] nt\n"
        "v_add_u32 v5, s29, v5\n"
        "global_load_dwordx4 v[228:231], v5, s[6:7] nt\n"
        "v_add_u32 v5, s29, v5\n"
        "v_perm_b32 v136, v137, v136, s30\n"
        "v_perm_b32 v138, v139, v138, s30\n"
        "v_lshl_or_b32 v136, v138, 16, v136\n"
        "ds_write_b32 v16, v136 offset:5632\n"
        "v_perm_b32 v140, v141, v140, s30\n"
        "v_perm_b32 v142, v143, v142, s30\n"
        "v_lshl_or_b32 v140, v142, 16, v140\n"
        "ds_write_b32 v16, v140 offset:5888\n"
        "s_waitcnt vmcnt(20)\n"
        "global_load_dwordx4 v[232:235], v5, s[6:7] nt\n"
        "v_add_u32 v5, s29, v5\n"
        "global_load_dwordx4 v[236:239], v5, s[6:7] nt\n"
        "v_add_u32 v5, s29, v5\n"
        "v_perm_b32 v144, v145, v144, s30\n"
        "v_perm_b32 v146, v147, v146, s30\n"
        "v_lshl_or_b32 v144, v146, 16, v144\n"
        "ds_write_b32 v16, v144 offset:6144\n"
        "v_perm_b32 v148, v149, v148, s30\n"
        "v_perm_b32 v150, v151, v150, s30\n"
        "v_lshl_or_b32 v148, v150, 16, v148\n"
        "ds_write_b32 v16, v148 offset:6400\n"
        "s_waitcnt vmcnt(20)\n"
        "global_load_dwordx4 v[240:243], v5, s[6:7] nt\n"
        "v_add_u32 v5, s29, v5\n"
        "global_load_dwordx4 v[244:247], v5, s[6:7] nt\n"
        "v_add_u32 v5, s29, v5\n"
        "v_perm_b32 v152, v153, v152, s30\n"
        "v_perm_b32 v154, v155, v154, s30\n"
        "v_lshl_or_b32 v152, v154, 16, v152\n"
        "ds_write_b32 v16, v152 offset:6656\n"
        "v_perm_b32 v156, v157, v156, s30\n"
        "v_perm_b32 v158, v159, v158, s30\n"
        "v_lshl_or_b32 v156, v158, 16, v156\n"
        "ds_write_b32 v16, v156 offset:6912\n"
        "s_waitcnt vmcnt(20)\n"
        "global_load_dwordx4 v[64:67], v5, s[6:7] nt\n"
        "v_add_u32 v5, s29, v5\n"
        "global_load_dwordx4 v[68:71], v5, s[6:7] nt\n"
        "v_add_u32 v5, 3569664, v5\n"
        "s_waitcnt vmcnt(20)\n"
        "global_load_dwordx4 v[72:75], v4, s[4:5] nt\n"
        "v_add_u32 v4, s28, v4\n"
        "global_load_dwordx4 v[76:79], v4, s[4:5] nt\n"
        "v_add_u32 v4, s28, v4\n"
        "v_perm_b32 v160, v164, v160, s30\n"
        "v_perm_b32 v168, v172, v168, s30\n"
        "v_perm_b32 v161, v165, v161, s30\n"
        "v_perm_b32 v169, v173, v169, s30\n"
        "v_perm_b32 v162, v166, v162, s30\n"
        "v_perm_b32 v170, v174, v170, s30\n"
        "v_perm_b32 v163, v167, v163, s30\n"
        "v_perm_b32 v171, v175, v171, s30\n"
        "v_lshl_or_b32 v36, v168, 16, v160\n"
        "v_lshl_or_b32 v40, v169, 16, v161\n"
        "v_lshl_or_b32 v38, v170, 16, v162\n"
        "v_lshl_or_b32 v42, v171, 16, v163\n"
        "s_waitcnt vmcnt(20)\n"
        "global_load_dwordx4 v[80:83], v4, s[4:5] nt\n"
        "v_add_u32 v4, s28, v4\n"
        "global_load_dwordx4 v[84:87], v4, s[4:5] nt\n"
        "v_add_u32 v4, s28, v4\n"
        "s_waitcnt vmcnt(20)\n"
        "global_load_dwordx4 v[88:91], v4, s[4:5] nt\n"
        "v_add_u32 v4, s28, v4\n"
        "global_load_dwordx4 v[92:95], v4, s[4:5] nt\n"
        "v_add_u32 v4, s28, v4\n"
        "v_perm_b32 v176, v180, v176, s30\n"
        "v_perm_b32 v184, v188, v184, s30\n"
        "v_perm_b32 v177, v181, v177, s30\n"
        "v_perm_b32 v185, v189, v185, s30\n"
        "v_perm_b32 v178, v182, v178, s30\n"
        "v_perm_b32 v186, v190, v186, s30\n"
        "v_perm_b32 v179, v183, v179, s30\n"
        "v_perm_b32 v187, v191, v187, s30\n"
        "v_lshl_or_b32 v37, v184, 16, v176\n"
        "v_lshl_or_b32 v41, v185, 16, v177\n"
        "v_lshl_or_b32 v39, v186, 16, v178\n"
        "v_lshl_or_b32 v43, v187, 16, v179\n"
        "s_nop 0\n"
        "v_permlane32_swap_b32 v36, v38\n"
        "v_permlane32_swap_b32 v37, v39\n"
        "v_permlane32_swap_b32 v40, v42\n"
        "v_permlane32_swap_b32 v41, v43\n"
        "s_nop 0\n"
        "ds_write_b128 v17, v[36:39] offset:7424\n"
        "ds_write_b128 v17, v[40:43] offset:8448\n"
        "ds_write_b32 v25, v20 offset:1544\n"
        "v_add_u32 v20, 1, v20\n"
        "v_dot4_i32_i8 v28, v36, s31, v28\n"
        "v_dot4_i32_i8 v29, v40, s31, v29\n"
        "v_dot4_i32_i8 v28, v37, s31, v28\n"
        "v_dot4_i32_i8 v29, v41, s31, v29\n"
        "v_dot4_i32_i8 v28, v38, s31, v28\n"
        "v_dot4_i32_i8 v29, v42, s31, v29\n"
        "v_dot4_i32_i8 v28, v39, s31, v28\n"
        "v_dot4_i32_i8 v29, v43, s31, v29\n"
        "s_waitcnt vmcnt(20)\n"
        "global_load_dwordx4 v[96:99], v4, s[4:5] nt\n"
        "v_add_u32 v4, s28, v4\n"
        "global_load_dwordx4 v[100:103], v4, s[4:5] nt\n"
        "v_perm_b32 v192, v193, v192, s30\n"
        "v_perm_b32 v194, v195, v194, s30\n"
        "v_lshl_or_b32 v192, v194, 16, v192\n"
        "ds_write_b32 v16, v192 offset:10240\n"
        "v_perm_b32 v196, v197, v196, s30\n"
        "v_perm_b32 v198, v199, v198, s30\n"
        "v_lshl_or_b32 v196, v198, 16, v196\n"
        "ds_write_b32 v16, v196 offset:10496\n"
        "s_waitcnt vmcnt(20)\n"
        "global_load_dwordx4 v[104:107], v5, s[6:7] nt\n"
        "v_add_u32 v5, s29, v5\n"
        "global_load_dwordx4 v[108:111], v5, s[6:7] nt\n"
        "v_add_u32 v5, s29, v5\n"
        "v_perm_b32 v200, v201, v200, s30\n"
        "v_perm_b32 v202, v203, v202, s30\n"
        "v_lshl_or_b32 v200, v202, 16, v200\n"
        "ds_write_b32 v16, v200 offset:10752\n"
        "v_perm_b32 v204, v205, v204, s30\n"
        "v_perm_b32 v206, v207, v206, s30\n"
        "v_lshl_or_b32 v204, v206, 16, v204\n"
        "ds_write_b32 v16, v204 offset:11008\n"
        "s_waitcnt vmcnt(20)\n"
        "global_load_dwordx4 v[112:115], v5, s[6:7] nt\n"
        "v_add_u32 v5, s29, v5\n"
        "global_load_dwordx4 v[116:119], v5, s[6:7] nt\n"
        "v_add_u32 v5, s29, v5\n"
        "v_perm_b32 v208, v209, v208, s30\n"
        "v_perm_b32 v210, v211, v210, s30\n"
        "v_lshl_or_b32 v208, v210, 16, v208\n"
        "ds_write_b32 v16, v208 offset:11264\n"
        "v_perm_b32 v212, v213, v212, s30\n"
        "v_perm_b32 v214, v215, v214, s30\n"
        "v_lshl_or_b32 v212, v214, 16, v212\n"
        "ds_write_b32 v16, v212 offset:11520\n"
        "s_waitcnt vmcnt(20)\n"
        "global_load_dwordx4 v[120:123], v5, s[6:7] nt\n"
        "v_add_u32 v5, s29, v5\n"
        "global_load_dwordx4 v[124:127], v5, s[6:7] nt\n"
        "v_add_u32 v5, s29, v5\n"
        "v_perm_b32 v216, v217, v216, s30\n"
        "v_perm_b32 v218, v219, v218, s30\n"
        "v_lshl_or_b32 v216, v218, 16, v216\n"
        "ds_write_b32 v16, v216 offset:11776\n"
        "v_perm_b32 v220, v221, v220, s30\n"
        "v_perm_b32 v222, v223, v222, s30\n"
        "v_lshl_or_b32 v220, v222, 16, v220\n"
        "ds_write_b32 v16, v220 offset:12032\n"
        "s_waitcnt vmcnt(20)\n"
        "global_load_dwordx4 v[128:131], v5, s[6:7] nt\n"
        "v_add_u32 v5, s29, v5\n"
        "global_load_dwordx4 v[132:135], v5, s[6:7] nt\n"
        "s_waitcnt vmcnt(20)\n"
        "global_load_dwordx4 v[136:139], v6, s[6:7] nt\n"
        "v_add_u32 v6, s29, v6\n"
        "global_load_dwordx4 v[140:143], v6, s[6:7] nt\n"
        "v_add_u32 v6, s29, v6\n"
        "v_perm_b32 v224, v228, v224, s30\n"
        "v_perm_b32 v232, v236, v232, s30\n"
        "v_perm_b32 v225, v229, v225, s30\n"
        "v_perm_b32 v233, v237, v233, s30\n"
        "v_perm_b32 v226, v230, v226, s30\n"
        "v_perm_b32 v234, v238, v234, s30\n"
        "v_perm_b32 v227, v231, v227, s30\n"
        "v_perm_b32 v235, v239, v235, s30\n"
        "v_lshl_or_b32 v36, v232, 16, v224\n"
        "v_lshl_or_b32 v40, v233, 16, v225\n"
        "v_lshl_or_b32 v38, v234, 16, v226\n"
        "v_lshl_or_b32 v42, v235, 16, v227\n"
        "s_waitcnt vmcnt(20)\n"
        "global_load_dwordx4 v[144:147], v6, s[6:7] nt\n"
        "v_add_u32 v6, s29, v6\n"
        "global_load_dwordx4 v[148:151], v6, s[6:7] nt\n"
        "v_add_u32 v6, s29, v6\n"
        "s_waitcnt vmcnt(20)\n"
        "global_load_dwordx4 v[152:155], v6, s[6:7] nt\n"
        "v_add_u32 v6, s29, v6\n"
        "global_load_dwordx4 v[156:159], v6, s[6:7] nt\n"
        "v_add_u32 v6, s29, v6\n"
        "v_mov_b32 v253, 0\n"
        "s_lshl_b32 s56, s22, 7\n"
        "s_lshl_b32 s57, s22, 6\n"
        "s_add_u32 s57, s57, 1920\n"
        "v_mov_b32 v254, s56\n"
        "v_mov_b32 v255, s57\n"
        "v_cmp_gt_u32 vcc, 32, v0\n"
        "s_lshl_b32 s56, s22, 5\n"
        "global_load_ushort v252, v253, s[16:17]\n"
        "v_cndmask_b32 v254, v255, v254, vcc\n"
        "s_add_u32 s56, s56, 2880\n"
        "v_mov_b32 v255, s56\n"
        "v_cmp_gt_u32 vcc, 48, v0\n"
        "s_nop 1\n"
        "v_cndmask_b32 v254, v255, v254, vcc\n"
        "v_lshl_add_u32 v254, v0, 2, v254\n"
        "v_lshlrev_b32 v254, 1, v254\n"
        "s_mov_b32 exec_hi, 0x00ffffff\n"
        "global_load_dwordx2 v[248:249], v254, s[12:13]\n"
        "global_load_dwordx2 v[250:251], v254, s[14:15]\n"
        "s_mov_b32 exec_hi, -1\n"
        "v_perm_b32 v240, v244, v240, s30\n"
        "v_perm_b32 v64, v68, v64, s30\n"
        "v_perm_b32 v241, v245, v241, s30\n"
        "v_perm_b32 v65, v69, v65, s30\n"
        "v_perm_b32 v242, v246, v242, s30\n"
        "v_perm_b32 v66, v70, v66, s30\n"
        "v_perm_b32 v243, v247, v243, s30\n"
        "v_perm_b32 v67, v71, v67, s30\n"
        "v_lshl_or_b32 v37, v64, 16, v240\n"
        "v_lshl_or_b32 v41, v65, 16, v241\n"
        "v_lshl_or_b32 v39, v66, 16, v242\n"
        "v_lshl_or_b32 v43, v67, 16, v243\n"
        "s_nop 0\n"
        "v_permlane32_swap_b32 v36, v38\n"
        "v_permlane32_swap_b32 v37, v39\n"
        "v_permlane32_swap_b32 v40, v42\n"
        "v_permlane32_swap_b32 v41, v43\n"
        "s_nop 0\n"
        "ds_write_b128 v17, v[36:39] offset:12544\n"
        "ds_write_b128 v17, v[40:43] offset:13568\n"
        "ds_write_b32 v25, v20 offset:1544\n"
        "v_add_u32 v20, 1, v20\n"
        "v_dot4_i32_i8 v28, v36, s31, v28\n"
        "v_dot4_i32_i8 v29, v40, s31, v29\n"
        "v_dot4_i32_i8 v28, v37, s31, v28\n"
        "v_dot4_i32_i8 v29, v41, s31, v29\n"
        "v_dot4_i32_i8 v28, v38, s31, v28\n"
        "v_dot4_i32_i8 v29, v42, s31, v29\n"
        "v_dot4_i32_i8 v28, v39, s31, v28\n"
        "v_dot4_i32_i8 v29, v43, s31, v29\n"
        "s_waitcnt vmcnt(23)\n"
        "global_load_dwordx4 v[160:163], v6, s[6:7] nt\n"
        "v_add_u32 v6, s29, v6\n"
        "global_load_dwordx4 v[164:167], v6, s[6:7] nt\n"
        "v_perm_b32 v72, v73, v72, s30\n"
        "v_perm_b32 v74, v75, v74, s30\n"
        "v_lshl_or_b32 v72, v74, 16, v72\n"
        "ds_write_b32 v16, v72 offset:15360\n"
        "v_perm_b32 v76, v77, v76, s30\n"
        "v_perm_b32 v78, v79, v78, s30\n"
        "v_lshl_or_b32 v76, v78, 16, v76\n"
        "ds_write_b32 v16, v76 offset:15616\n"
        "s_waitcnt vmcnt(23)\n"
        "global_load_dwordx4 v[168:171], v7, s[6:7] nt\n"
        "v_add_u32 v7, s29, v7\n"
        "global_load_dwordx4 v[172:175], v7, s[6:7] nt\n"
        "v_add_u32 v7, s29, v7\n"
        "v_perm_b32 v80, v81, v80, s30\n"
        "v_perm_b32 v82, v83, v82, s30\n"
        "v_lshl_or_b32 v80, v82, 16, v80\n"
        "ds_write_b32 v16, v80 offset:15872\n"
        "v_perm_b32 v84, v85, v84, s30\n"
        "v_perm_b32 v86, v87, v86, s30\n"
        "v_lshl_or_b32 v84, v86, 16, v84\n"
        "ds_write_b32 v16, v84 offset:16128\n"
        "s_waitcnt vmcnt(23)\n"
        "global_load_dwordx4 v[176:179], v7, s[6:7] nt\n"
        "v_add_u32 v7, s29, v7\n"
        "global_load_dwordx4 v[180:183], v7, s[6:7] nt\n"
        "v_add_u32 v7, s29, v7\n"
        "v_perm_b32 v88, v89, v88, s30\n"
        "v_perm_b32 v90, v91, v90, s30\n"
        "v_lshl_or_b32 v88, v90, 16, v88\n"
        "ds_write_b32 v16, v88 offset:16384\n"
        "v_perm_b32 v92, v93, v92, s30\n"
        "v_perm_b32 v94, v95, v94, s30\n"
        "v_lshl_or_b32 v92, v94, 16, v92\n"
        "ds_write_b32 v16, v92 offset:16640\n"
        "s_waitcnt vmcnt(23)\n"
        "global_load_dwordx4 v[184:187], v7, s[6:7] nt\n"
        "v_add_u32 v7, s29, v7\n"
        "global_load_dwordx4 v[188:191], v7, s[6:7] nt\n"
        "v_add_u32 v7, s29, v7\n"
        "v_perm_b32 v96, v97, v96, s30\n"
        "v_perm_b32 v98, v99, v98, s30\n"
        "v_lshl_or_b32 v96, v98, 16, v96\n"
        "ds_write_b32 v16, v96 offset:16896\n"
        "v_perm_b32 v100, v101, v100, s30\n"
        "v_perm_b32 v102, v103, v102, s30\n"
        "v_lshl_or_b32 v100, v102, 16, v100\n"
        "ds_write_b32 v16, v100 offset:17152\n"
        "s_waitcnt vmcnt(23)\n"
        "global_load_dwordx4 v[192:195], v7, s[6:7] nt\n"
        "v_add_u32 v7, s29, v7\n"
        "global_load_dwordx4 v[196:199], v7, s[6:7] nt\n"
        "v_add_u32 v7, s29, v7\n"
        "s_waitcnt vmcnt(23)\n"
        "global_load_dwordx4 v[200:203], v7, s[6:7] nt\n"
        "v_add_u32 v7, s29, v7\n"
        "global_load_dwordx4 v[204:207], v7, s[6:7] nt\n"
        "v_add_u32 v7, s29, v7\n"
        "v_perm_b32 v104, v108, v104, s30\n"
        "v_perm_b32 v112, v116, v112, s30\n"
        "v_perm_b32 v105, v109, v105, s30\n"
        "v_perm_b32 v113, v117, v113, s30\n"
        "v_perm_b32 v106, v110, v106, s30\n"
        "v_perm_b32 v114, v118, v114, s30\n"
        "v_perm_b32 v107, v111, v107, s30\n"
        "v_perm_b32 v115, v119, v115, s30\n"
        "v_lshl_or_b32 v36, v112, 16, v104\n"
        "v_lshl_or_b32 v40, v113, 16, v105\n"
        "v_lshl_or_b32 v38, v114, 16, v106\n"
        "v_lshl_or_b32 v42, v115, 16, v107\n"
        "s_waitcnt vmcnt(23)\n"
        "global_load_dwordx4 v[208:211], v7, s[6:7] nt\n"
        "v_add_u32 v7, s29, v7\n"
        "global_load_dwordx4 v[212:215], v7, s[6:7] nt\n"
        "v_add_u32 v7, s29, v7\n"
        "s_waitcnt vmcnt(23)\n"
        "global_load_dwordx4 v[216:219], v7, s[6:7] nt\n"
        "v_add_u32 v7, s29, v7\n"
        "global_load_dwordx4 v[220:223], v7, s[6:7] nt\n"
        "v_add_u32 v7, s29, v7\n"
        "v_perm_b32 v120, v124, v120, s30\n"
        "v_perm_b32 v128, v132, v128, s30\n"
        "v_perm_b32 v121, v125, v121, s30\n"
        "v_perm_b32 v129, v133, v129, s30\n"
        "v_perm_b32 v122, v126, v122, s30\n"
        "v_perm_b32 v130, v134, v130, s30\n"
        "v_perm_b32 v123, v127, v123, s30\n"
        "v_perm_b32 v131, v135, v131, s30\n"
        "v_lshl_or_b32 v37, v128, 16, v120\n"
        "v_lshl_or_b32 v41, v129, 16, v121\n"
        "v_lshl_or_b32 v39, v130, 16, v122\n"
        "v_lshl_or_b32 v43, v131, 16, v123\n"
        "s_nop 0\n"
        "v_permlane32_swap_b32 v36, v38\n"
        "v_permlane32_swap_b32 v37, v39\n"
        "v_permlane32_swap_b32 v40, v42\n"
        "v_permlane32_swap_b32 v41, v43\n"
        "s_nop 0\n"
        "ds_write_b128 v17, v[36:39] offset:17664\n"
        "ds_write_b128 v17, v[40:43] offset:18688\n"
        "v_dot4_i32_i8 v28, v36, s31, v28\n"
        "v_dot4_i32_i8 v29, v40, s31, v29\n"
        "v_dot4_i32_i8 v28, v37, s31, v28\n"
        "v_dot4_i32_i8 v29, v41, s31, v29\n"
        "v_dot4_i32_i8 v28, v38, s31, v28\n"
        "v_dot4_i32_i8 v29, v42, s31, v29\n"
        "v_dot4_i32_i8 v28, v39, s31, v28\n"
        "v_dot4_i32_i8 v29, v43, s31, v29\n"
        "s_nop 2\n"
        "ds_write_b64 v21, v[28:29] offset:19712\n"
        "ds_write_b32 v25, v20 offset:1544\n"
        "v_add_u32 v20, 1, v20\n"
        "s_waitcnt vmcnt(23)\n"
        "global_load_dwordx4 v[224:227], v7, s[6:7] nt\n"
        "v_add_u32 v7, s29, v7\n"
        "global_load_dwordx4 v[228:231], v7, s[6:7] nt\n"
        "v_mov_b32 v12, 1\n"
        "ds_write_b32 v25, v12 offset:512\n"
        "s_waitcnt vmcnt(23)\n"
        "v_perm_b32 v136, v140, v136, s30\n"
        "v_perm_b32 v144, v148, v144, s30\n"
        "v_perm_b32 v137, v141, v137, s30\n"
        "v_perm_b32 v145, v149, v145, s30\n"
        "v_perm_b32 v138, v142, v138, s30\n"
        "v_perm_b32 v146, v150, v146, s30\n"
        "v_perm_b32 v139, v143, v139, s30\n"
        "v_perm_b32 v147, v151, v147, s30\n"
        "v_lshl_or_b32 v36, v144, 16, v136\n"
        "v_lshl_or_b32 v40, v145, 16, v137\n"
        "v_lshl_or_b32 v38, v146, 16, v138\n"
        "v_lshl_or_b32 v42, v147, 16, v139\n"
        "s_waitcnt vmcnt(21)\n"
        "s_waitcnt vmcnt(16)\n"
        "v_perm_b32 v152, v156, v152, s30\n"
        "v_perm_b32 v160, v164, v160, s30\n"
        "v_perm_b32 v153, v157, v153, s30\n"
        "v_perm_b32 v161, v165, v161, s30\n"
        "v_perm_b32 v154, v158, v154, s30\n"
        "v_perm_b32 v162, v166, v162, s30\n"
        "v_perm_b32 v155, v159, v155, s30\n"
        "v_perm_b32 v163, v167, v163, s30\n"
        "v_lshl_or_b32 v37, v160, 16, v152\n"
        "v_lshl_or_b32 v41, v161, 16, v153\n"
        "v_lshl_or_b32 v39, v162, 16, v154\n"
        "v_lshl_or_b32 v43, v163, 16, v155\n"
        "s_nop 0\n"
        "v_permlane32_swap_b32 v36, v38\n"
        "v_permlane32_swap_b32 v37, v39\n"
        "v_permlane32_swap_b32 v40, v42\n"
        "v_permlane32_swap_b32 v41, v43\n"
        "s_nop 0\n"
        "global_store_dwordx4 v35, v[36:39], s[44:45] sc1\n"
        "global_store_dwordx4 v35, v[40:43], s[44:45] offset:1024 sc1\n"
        "v_dot4_i32_i8 v30, v36, s31, v30\n"
        "v_dot4_i32_i8 v31, v40, s31, v31\n"
        "v_dot4_i32_i8 v30, v37, s31, v30\n"
        "v_dot4_i32_i8 v31, v41, s31, v31\n"
        "v_dot4_i32_i8 v30, v38, s31, v30\n"
        "v_dot4_i32_i8 v31, v42, s31, v31\n"
        "v_dot4_i32_i8 v30, v39, s31, v30\n"
        "v_dot4_i32_i8 v31, v43, s31, v31\n"
        "s_nop 2\n"
        "v_permlane16_swap_b32 v30, v31\n"
        "s_nop 0\n"
        "v_add_u32 v30, v30, v31\n"
        "v_mov_b32 v31, 0\n"
        "s_nop 0\n"
        "v_add_u32_dpp v34, v30, v30 row_ror:8 row_mask:0xf bank_mask:0xf\n"
        "v_mov_b32 v30, 0\n"
        "s_mov_b32 s38, 8388608\n"
        "W8_tg0:\n"
        "ds_read_b32 v12, v25 offset:1024\n"
        "s_sub_u32 s38, s38, 1\n"
        "s_waitcnt lgkmcnt(0)\n"
        "s_cbranch_scc1 W8_tg0_x\n"
        "v_readfirstlane_b32 s39, v12\n"
        "s_cmp_ge_u32 s39, 1\n"
        "s_cbranch_scc1 W8_tg0_x\n"
        "s_sleep 1\n"
        "s_branch W8_tg0\n"
        "W8_tg0_x:\n"
        "v_lshrrev_b32 v12, 0, v0\n"
        "v_lshlrev_b32 v12, 7, v12\n"
        "v_add_u32 v12, 136968, v12\n"
        "s_mov_b64 exec, 3\n"
        "ds_read_b64 v[26:27], v12\n"
        "s_mov_b64 exec, -1\n"
        "s_waitcnt vmcnt(16)\n"
        "s_waitcnt vmcnt(14)\n"
        "v_perm_b32 v168, v172, v168, s30\n"
        "v_perm_b32 v176, v180, v176, s30\n"
        "v_perm_b32 v169, v173, v169, s30\n"
        "v_perm_b32 v177, v181, v177, s30\n"
        "v_perm_b32 v170, v174, v170, s30\n"
        "v_perm_b32 v178, v182, v178, s30\n"
        "v_perm_b32 v171, v175, v171, s30\n"
        "v_perm_b32 v179, v183, v179, s30\n"
        "v_lshl_or_b32 v36, v176, 16, v168\n"
        "v_lshl_or_b32 v40, v177, 16, v169\n"
        "v_lshl_or_b32 v44, v178, 16, v170\n"
        "v_lshl_or_b32 v48, v179, 16, v171\n"
        "v_dot4_i32_i8 v30, v36, s31, v30\n"
        "v_dot4_i32_i8 v31, v40, s31, v31\n"
        "v_dot4_i32_i8 v32, v44, s31, v32\n"
        "v_dot4_i32_i8 v33, v48, s31, v33\n"
        "s_waitcnt vmcnt(12)\n"
        "s_waitcnt vmcnt(10)\n"
        "v_perm_b32 v184, v188, v184, s30\n"
        "v_perm_b32 v192, v196, v192, s30\n"
        "v_perm_b32 v185, v189, v185, s30\n"
        "v_perm_b32 v193, v197, v193, s30\n"
        "v_perm_b32 v186, v190, v186, s30\n"
        "v_perm_b32 v194, v198, v194, s30\n"
        "v_perm_b32 v187, v191, v187, s30\n"
        "v_perm_b32 v195, v199, v195, s30\n"
        "v_lshl_or_b32 v37, v192, 16, v184\n"
        "v_lshl_or_b32 v41, v193, 16, v185\n"
        "v_lshl_or_b32 v45, v194, 16, v186\n"
        "v_lshl_or_b32 v49, v195, 16, v187\n"
        "v_dot4_i32_i8 v30, v37, s31, v30\n"
        "v_dot4_i32_i8 v31, v41, s31, v31\n"
        "v_dot4_i32_i8 v32, v45, s31, v32\n"
        "v_dot4_i32_i8 v33, v49, s31, v33\n"
        "s_waitcnt vmcnt(8)\n"
        "s_waitcnt vmcnt(6)\n"
        "v_perm_b32 v200, v204, v200, s30\n"
        "v_perm_b32 v208, v212, v208, s30\n"
        "v_perm_b32 v201, v205, v201, s30\n"
        "v_perm_b32 v209, v213, v209, s30\n"
        "v_perm_b32 v202, v206, v202, s30\n"
        "v_perm_b32 v210, v214, v210, s30\n"
        "v_perm_b32 v203, v207, v203, s30\n"
        "v_perm_b32 v211, v215, v211, s30\n"
        "v_lshl_or_b32 v38, v208, 16, v200\n"
        "v_lshl_or_b32 v42, v209, 16, v201\n"
        "v_lshl_or_b32 v46, v210, 16, v202\n"
        "v_lshl_or_b32 v50, v211, 16, v203\n"
        "v_dot4_i32_i8 v30, v38, s31, v30\n"
        "v_dot4_i32_i8 v31, v42, s31, v31\n"
        "v_dot4_i32_i8 v32, v46, s31, v32\n"
        "v_dot4_i32_i8 v33, v50, s31, v33\n"
        "s_waitcnt vmcnt(4)\n"
        "s_waitcnt vmcnt(2)\n"
        "v_perm_b32 v216, v220, v216, s30\n"
        "v_perm_b32 v224, v228, v224, s30\n"
        "v_perm_b32 v217, v221, v217, s30\n"
        "v_perm_b32 v225, v229, v225, s30\n"
        "v_perm_b32 v218, v222, v218, s30\n"
        "v_perm_b32 v226, v230, v226, s30\n"
        "v_perm_b32 v219, v223, v219, s30\n"
        "v_perm_b32 v227, v231, v227, s30\n"
        "v_lshl_or_b32 v39, v224, 16, v216\n"
        "v_lshl_or_b32 v43, v225, 16, v217\n"
        "v_lshl_or_b32 v47, v226, 16, v218\n"
        "v_lshl_or_b32 v51, v227, 16, v219\n"
        "ds_write_b32 v25, v20 offset:1544\n"
        "global_store_dwordx4 v19, v[36:39], s[44:45] offset:0 sc1\n"
        "global_store_dwordx4 v19, v[40:43], s[44:45] offset:1024 sc1\n"
        "global_store_dwordx4 v19, v[44:47], s[44:45] offset:2048 sc1\n"
        "global_store_dwordx4 v19, v[48:51], s[44:45] offset:3072 sc1\n"
        "v_dot4_i32_i8 v30, v39, s31, v30\n"
        "v_dot4_i32_i8 v31, v43, s31, v31\n"
        "v_dot4_i32_i8 v32, v47, s31, v32\n"
        "v_dot4_i32_i8 v33, v51, s31, v33\n"
        "s_nop 2\n"
        "v_permlane32_swap_b32 v30, v32\n"
        "v_permlane32_swap_b32 v31, v33\n"
        "s_nop 0\n"
        "v_add_u32 v30, v30, v32\n"
        "v_add_u32 v31, v31, v33\n"
        "s_nop 1\n"
        "v_permlane16_swap_b32 v30, v31\n"
        "s_nop 0\n"
        "v_add_u32 v30, v30, v31\n"
        "global_store_dword v22, v30, s[46:47] offset:512 sc1\n"
        "s_mov_b32 exec_lo, 0x00ff00ff\n"
        "s_mov_b32 exec_hi, 0x00ff00ff\n"
        "global_store_dword v23, v34, s[46:47] offset:768 sc1\n"
        "s_mov_b64 exec, 2\n"
        "s_waitcnt vmcnt(0) lgkmcnt(0)\n"
        "global_atomic_swap_x2 v24, v[26:27], s[8:9] sc1\n"
        "s_mov_b64 exec, -1\n"
        "s_load_dword s48, s[18:19], 0x0\n"
        "s_mov_b32 exec_hi, 0x00ffffff\n"
        "v_mov_b32 v32, v252\n"
        "v_mov_b32 v34, v248\n"
        "v_mov_b32 v35, v249\n"
        "v_mov_b32 v38, v250\n"
        "v_mov_b32 v39, v251\n"
        "s_lshl_b32 s24, s22, 14\n"
        "s_add_u32 s24, s24, 5771264\n"
        "v_add_u32 v21, s24, v19\n"
        "v_add_u32 v22, 8192, v21\n"
        "v_lshlrev_b32 v23, 5, v0\n"
        "v_add_u32 v23, 133632, v23\n"
        "s_waitcnt vmcnt(0)\n"
        "v_lshlrev_b32 v32, 16, v32\n"
        "v_and_b32 v37, 0xffff0000, v35\n"
        "v_lshlrev_b32 v36, 16, v35\n"
        "v_and_b32 v35, 0xffff0000, v34\n"
        "v_lshlrev_b32 v34, 16, v34\n"
        "v_and_b32 v41, 0xffff0000, v39\n"
        "v_lshlrev_b32 v40, 16, v39\n"
        "v_and_b32 v39, 0xffff0000, v38\n"
        "v_lshlrev_b32 v38, 16, v38\n"
        "v_mul_f32 v44, v32, v34\n"
        "v_mul_f32 v46, v32, v35\n"
        "v_mul_f32 v48, v32, v36\n"
        "v_mul_f32 v50, v32, v37\n"
        "s_mov_b32 s49, 6\n"
        "s_mov_b32 s50, -1\n"
        "s_mov_b32 s51, 16777215\n"
        "v_mov_b32 v33, 3\n"
        "W8_tab:\n"
        "s_mov_b64 exec, -1\n"
        "s_mov_b32 s38, 8388608\n"
        "W8_tr:\n"
        "ds_read_b32 v12, v25 offset:1024\n"
        "s_sub_u32 s38, s38, 1\n"
        "s_waitcnt lgkmcnt(0)\n"
        "s_cbranch_scc1 W8_tr_x\n"
        "v_readfirstlane_b32 s39, v12\n"
        "s_cmp_ge_u32 s39, 21\n"
        "s_cbranch_scc1 W8_tr_x\n"
        "s_sleep 2\n"
        "s_branch W8_tr\n"
        "W8_tr_x:\n"
        "s_mov_b32 s38, 8388608\n"
        "W8_tf:\n"
        "ds_read_b32 v12, v25 offset:0\n"
        "s_sub_u32 s38, s38, 1\n"
        "s_waitcnt lgkmcnt(0)\n"
        "s_cbranch_scc1 W8_tf_x\n"
        "v_readfirstlane_b32 s39, v12\n"
        "s_cmp_ge_u32 s39, 12\n"
        "s_cbranch_scc1 W8_tf_x\n"
        "s_sleep 2\n"
        "s_branch W8_tf\n"
        "W8_tf_x:\n"
        "s_mov_b64 exec, s[50:51]\n"
        "global_load_dwordx4 v[64:67], v21, s[8:9] offset:-4096 sc1\n"
        "global_load_dwordx4 v[68:71], v21, s[8:9] offset:-3072 sc1\n"
        "global_load_dwordx4 v[72:75], v21, s[8:9] offset:-2048 sc1\n"
        "global_load_dwordx4 v[76:79], v21, s[8:9] offset:-1024 sc1\n"
        "global_load_dwordx4 v[80:83], v21, s[8:9] offset:0 sc1\n"
        "global_load_dwordx4 v[84:87], v21, s[8:9] offset:1024 sc1\n"
        "global_load_dwordx4 v[88:91], v21, s[8:9] offset:2048 sc1\n"
        "global_load_dwordx4 v[92:95], v21, s[8:9] offset:3072 sc1\n"
        "global_load_dwordx4 v[96:99], v22, s[8:9] offset:-4096 sc1\n"
        "global_load_dwordx4 v[100:103], v22, s[8:9] offset:-3072 sc1\n"
        "global_load_dwordx4 v[104:107], v22, s[8:9] offset:-2048 sc1\n"
        "global_load_dwordx4 v[108:111], v22, s[8:9] offset:-1024 sc1\n"
        "global_load_dwordx4 v[112:115], v22, s[8:9] offset:0 sc1\n"
        "global_load_dwordx4 v[116:119], v22, s[8:9] offset:1024 sc1\n"
        "global_load_dwordx4 v[120:123], v22, s[8:9] offset:2048 sc1\n"
        "global_load_dwordx4 v[124:127], v22, s[8:9] offset:3072 sc1\n"
        "s_waitcnt vmcnt(0)\n"
        "v_add3_u32 v52, v64, v68, v72\n"
        "v_add3_u32 v53, v65, v69, v73\n"
        "v_add3_u32 v54, v66, v70, v74\n"
        "v_add3_u32 v55, v67, v71, v75\n"
        "v_add3_u32 v52, v52, v76, v80\n"
        "v_add3_u32 v53, v53, v77, v81\n"
        "v_add3_u32 v54, v54, v78, v82\n"
        "v_add3_u32 v55, v55, v79, v83\n"
        "v_add3_u32 v52, v52, v84, v88\n"
        "v_add3_u32 v53, v53, v85, v89\n"
        "v_add3_u32 v54, v54, v86, v90\n"
        "v_add3_u32 v55, v55, v87, v91\n"
        "v_add3_u32 v52, v52, v92, v96\n"
        "v_add3_u32 v53, v53, v93, v97\n"
        "v_add3_u32 v54, v54, v94, v98\n"
        "v_add3_u32 v55, v55, v95, v99\n"
        "v_add3_u32 v52, v52, v100, v104\n"
        "v_add3_u32 v53, v53, v101, v105\n"
        "v_add3_u32 v54, v54, v102, v106\n"
        "v_add3_u32 v55, v55, v103, v107\n"
        "v_add3_u32 v52, v52, v108, v112\n"
        "v_add3_u32 v53, v53, v109, v113\n"
        "v_add3_u32 v54, v54, v110, v114\n"
        "v_add3_u32 v55, v55, v111, v115\n"
        "v_add3_u32 v52, v52, v116, v120\n"
        "v_add3_u32 v53, v53, v117, v121\n"
        "v_add3_u32 v54, v54, v118, v122\n"
        "v_add3_u32 v55, v55, v119, v123\n"
        "v_add_u32 v52, v52, v124\n"
        "v_add_u32 v53, v53, v125\n"
        "v_add_u32 v54, v54, v126\n"
        "v_add_u32 v55, v55, v127\n"
        "v_mul_lo_u32 v52, v52, s48\n"
        "v_mul_lo_u32 v53, v53, s48\n"
        "v_mul_lo_u32 v54, v54, s48\n"
        "v_mul_lo_u32 v55, v55, s48\n"
        "v_cvt_f32_i32 v52, v52\n"
        "v_cvt_f32_i32 v53, v53\n"
        "v_cvt_f32_i32 v54, v54\n"
        "v_cvt_f32_i32 v55, v55\n"
        "v_fma_f32 v45, -v52, v44, v38\n"
        "v_fma_f32 v47, -v53, v46, v39\n"
        "v_fma_f32 v49, -v54, v48, v40\n"
        "v_fma_f32 v51, -v55, v50, v41\n"
        "ds_write_b128 v23, v[44:47]\n"
        "ds_write_b128 v23, v[48:51] offset:16\n"
        "s_mov_b64 exec, -1\n"
        "ds_write_b32 v25, v33 offset:512\n"
        "v_add_u32 v33, 1, v33\n"
        "s_mov_b32 s50, 0\n"
        "s_mov_b32 s51, 0x00ffffff\n"
        "s_add_u32 s49, s49, 2\n"
        "s_cmp_eq_u32 s49, 6\n"
        "s_cbranch_scc1 W8_tab\n"
        "s_branch W8_fin\n"
        "W8_other:\n"
        "v_and_b32 v1, 63, v0\n"
        "s_add_u32 s33, s21, s22\n"
        "s_cmp_ge_u32 s20, 4\n"
        "s_cbranch_scc1 W8_comp\n"
        "v_lshlrev_b32 v25, 2, v1\n"
        "v_add_u32 v25, 135684, v25\n"
        "s_cmp_eq_u32 s20, 1\n"
        "s_cbranch_scc1 W8_load\n"
        "s_and_b32 s30, s20, 1\n"
        "s_lshl_b32 s24, s21, 9\n"
        "s_add_u32 s24, s24, 6029312\n"
        "s_lshl_b32 s25, s22, 10\n"
        "s_add_u32 s25, s25, 6291456\n"
        "s_add_u32 s26, s22, 16\n"
        "s_cmp_eq_u32 s30, 0\n"
        "s_cselect_b32 s24, s24, s25\n"
        "s_cselect_b32 s26, s21, s26\n"
        "s_cselect_b32 s31, s22, s21\n"
        "s_mul_i32 s27, s30, 768\n"
        "s_add_u32 s27, s27, 256\n"
        "s_lshl_b32 s26, s26, 7\n"
        "s_add_u32 s26, s26, 6815744\n"
        "v_mov_b32 v5, s26\n"
        "v_add_u32 v8, s27, v25\n"
        "s_lshl_b32 s26, s30, 7\n"
        "s_add_u32 s26, s26, 136968\n"
        "v_mov_b32 v12, s26\n"
        "s_add_u32 s24, s8, s24\n"
        "s_addc_u32 s25, s9, 0\n"
        "s_add_u32 s28, s30, 1\n"
        "v_mov_b32 v13, s28\n"
        "s_lshl_b32 s28, s30, 0\n"
        "s_add_u32 s28, s28, 4\n"
        "s_mov_b32 s38, 8388608\n"
        "W8_tg2:\n"
        "ds_read_b32 v14, v25 offset:1024\n"
        "s_sub_u32 s38, s38, 1\n"
        "s_waitcnt lgkmcnt(0)\n"
        "s_cbranch_scc1 W8_tg2_x\n"
        "v_readfirstlane_b32 s39, v14\n"
        "s_cmp_ge_u32 s39, 1\n"
        "s_cbranch_scc1 W8_tg2_x\n"
        "s_sleep 2\n"
        "s_branch W8_tg2\n"
        "W8_tg2_x:\n"
        "ds_read_b64 v[6:7], v12\n"
        "s_mov_b32 s38, 8388608\n"
        "W8_pst:\n"
        "ds_read_b32 v14, v25 offset:1544\n"
        "s_sub_u32 s38, s38, 1\n"
        "s_waitcnt lgkmcnt(0)\n"
        "s_cbranch_scc1 W8_pst_x\n"
        "v_readfirstlane_b32 s39, v14\n"
        "s_cmp_ge_u32 s39, 3\n"
        "s_cbranch_scc1 W8_pst_x\n"
        "s_sleep 4\n"
        "s_branch W8_pst\n"
        "W8_pst_x:\n"
        "s_add_u32 s14, s21, s22\n"
        "s_and_b32 s12, s14, 2\n"
        "s_lshl_b32 s12, s12, 5\n"
        "s_xor_b32 s13, s12, 64\n"
        "s_and_b32 s14, s14, 1\n"
        "v_add_u32 v16, 1, v13\n"
        "v_add_u32 v17, 2, v13\n"
        "v_add_u32 v18, 3, v13\n"
        "v_mov_b32 v15, v13\n"
        "s_mov_b32 s29, 2097152\n"
        "W8_pl:\n"
        "s_load_dwordx16 s[32:47], s[24:25], s12 glc\n"
        "s_load_dwordx16 s[48:63], s[24:25], s13 glc\n"
        "s_sub_u32 s29, s29, 1\n"
        "s_cselect_b32 s27, 1, 0\n"
        "s_cmp_eq_u32 s14, 1\n"
        "s_waitcnt lgkmcnt(0)\n"
        "s_cbranch_scc1 W8_plb\n"
        "v_cmpx_eq_u64 vcc, s[32:33], v[6:7]\n"
        "v_cmpx_eq_u64 vcc, s[34:35], v[6:7]\n"
        "v_cmpx_eq_u64 vcc, s[36:37], v[6:7]\n"
        "v_cmpx_eq_u64 vcc, s[38:39], v[6:7]\n"
        "ds_write_b32 v8, v15\n"
        "v_cmpx_eq_u64 vcc, s[40:41], v[6:7]\n"
        "v_cmpx_eq_u64 vcc, s[42:43], v[6:7]\n"
        "v_cmpx_eq_u64 vcc, s[44:45], v[6:7]\n"
        "v_cmpx_eq_u64 vcc, s[46:47], v[6:7]\n"
        "ds_write_b32 v8, v16\n"
        "v_cmpx_eq_u64 vcc, s[48:49], v[6:7]\n"
        "v_cmpx_eq_u64 vcc, s[50:51], v[6:7]\n"
        "v_cmpx_eq_u64 vcc, s[52:53], v[6:7]\n"
        "v_cmpx_eq_u64 vcc, s[54:55], v[6:7]\n"
        "ds_write_b32 v8, v17\n"
        "v_cmpx_eq_u64 vcc, s[56:57], v[6:7]\n"
        "v_cmpx_eq_u64 vcc, s[58:59], v[6:7]\n"
        "v_cmpx_eq_u64 vcc, s[60:61], v[6:7]\n"
        "v_cmpx_eq_u64 vcc, s[62:63], v[6:7]\n"
        "s_branch W8_plc\n"
        "W8_plb:\n"
        "v_cmpx_eq_u64 vcc, s[40:41], v[6:7]\n"
        "v_cmpx_eq_u64 vcc, s[42:43], v[6:7]\n"
        "v_cmpx_eq_u64 vcc, s[44:45], v[6:7]\n"
        "v_cmpx_eq_u64 vcc, s[46:47], v[6:7]\n"
        "ds_write_b32 v8, v15\n"
        "v_cmpx_eq_u64 vcc, s[32:33], v[6:7]\n"
        "v_cmpx_eq_u64 vcc, s[34:35], v[6:7]\n"
        "v_cmpx_eq_u64 vcc, s[36:37], v[6:7]\n"
        "v_cmpx_eq_u64 vcc, s[38:39], v[6:7]\n"
        "ds_write_b32 v8, v16\n"
        "v_cmpx_eq_u64 vcc, s[56:57], v[6:7]\n"
        "v_cmpx_eq_u64 vcc, s[58:59], v[6:7]\n"
        "v_cmpx_eq_u64 vcc, s[60:61], v[6:7]\n"
        "v_cmpx_eq_u64 vcc, s[62:63], v[6:7]\n"
        "ds_write_b32 v8, v17\n"
        "v_cmpx_eq_u64 vcc, s[48:49], v[6:7]\n"
        "v_cmpx_eq_u64 vcc, s[50:51], v[6:7]\n"
        "v_cmpx_eq_u64 vcc, s[52:53], v[6:7]\n"
        "v_cmpx_eq_u64 vcc, s[54:55], v[6:7]\n"
        "W8_plc:\n"
        "s_nop 4\n"
        "s_cbranch_execnz W8_pg\n"
        "s_mov_b64 exec, -1\n"
        "s_cmp_eq_u32 s27, 1\n"
        "s_cbranch_scc1 W8_pg\n"
        "s_sleep 1\n"
        "s_branch W8_pl\n"
        "W8_pg:\n"
        "ds_write_b32 v8, v18\n"
        "s_max_i32 s29, s29, 0\n"
        "v_add_u32 v15, 4, v15\n"
        "v_add_u32 v16, 4, v16\n"
        "v_add_u32 v17, 4, v17\n"
        "v_add_u32 v18, 4, v18\n"
        "s_add_u32 s24, s24, 128\n"
        "s_addc_u32 s25, s25, 0\n"
        "s_sub_u32 s28, s28, 1\n"
        "s_cmp_lg_u32 s28, 0\n"
        "s_cbranch_scc1 W8_pl\n"
        "s_cmp_lg_u32 s31, 0\n"
        "s_cbranch_scc1 W8_fin\n"
        "s_mov_b64 exec, 1\n"
        "global_store_dwordx2 v5, v[6:7], s[8:9] sc1\n"
        "s_branch W8_fin\n"
        "W8_load:\n"
        "v_lshlrev_b32 v2, 4, v1\n"
        "s_xor_b32 s24, s33, 0\n"
        "s_and_b32 s24, s24, 3\n"
        "s_lshl_b32 s25, s24, 13\n"
        "v_add_u32 v4, s25, v2\n"
        "v_add_u32 v8, 4096, v4\n"
        "s_mul_i32 s25, s24, 6144\n"
        "v_add_u32 v12, s25, v2\n"
        "s_xor_b32 s24, s33, 1\n"
        "s_and_b32 s24, s24, 3\n"
        "s_lshl_b32 s25, s24, 13\n"
        "v_add_u32 v5, s25, v2\n"
        "v_add_u32 v9, 4096, v5\n"
        "s_mul_i32 s25, s24, 6144\n"
        "v_add_u32 v13, s25, v2\n"
        "s_xor_b32 s24, s33, 2\n"
        "s_and_b32 s24, s24, 3\n"
        "s_lshl_b32 s25, s24, 13\n"
        "v_add_u32 v6, s25, v2\n"
        "v_add_u32 v10, 4096, v6\n"
        "s_mul_i32 s25, s24, 6144\n"
        "v_add_u32 v14, s25, v2\n"
        "s_xor_b32 s24, s33, 3\n"
        "s_and_b32 s24, s24, 3\n"
        "s_lshl_b32 s25, s24, 13\n"
        "v_add_u32 v7, s25, v2\n"
        "v_add_u32 v11, 4096, v7\n"
        "s_mul_i32 s25, s24, 6144\n"
        "v_add_u32 v15, s25, v2\n"
        "s_waitcnt lgkmcnt(0)\n"
        "s_lshl_b32 s24, s22, 17\n"
        "s_add_u32 s24, s24, 2097152\n"
        "s_add_u32 s40, s8, s24\n"
        "s_addc_u32 s41, s9, 0\n"
        "s_mul_i32 s24, s22, 98304\n"
        "s_add_u32 s24, s24, 4194304\n"
        "s_add_u32 s42, s8, s24\n"
        "s_addc_u32 s43, s9, 0\n"
        "s_mov_b32 s34, 0\n"
        "s_mov_b32 s35, 2\n"
        "v_mov_b32 v3, -2\n"
        "s_mov_b32 s38, 8388608\n"
        "W8_lgo:\n"
        "ds_read_b32 v20, v25 offset:512\n"
        "s_sub_u32 s38, s38, 1\n"
        "s_waitcnt lgkmcnt(0)\n"
        "s_cbranch_scc1 W8_lgo_x\n"
        "v_readfirstlane_b32 s39, v20\n"
        "s_cmp_ge_u32 s39, 1\n"
        "s_cbranch_scc1 W8_lgo_x\n"
        "s_sleep 2\n"
        "s_branch W8_lgo\n"
        "W8_lgo_x:\n"
        "s_mov_b32 s38, 8388608\n"
        "W8_lr00:\n"
        "ds_read_b32 v20, v25 offset:1024\n"
        "s_sub_u32 s38, s38, 1\n"
        "s_waitcnt lgkmcnt(0)\n"
        "s_cbranch_scc1 W8_lr00_x\n"
        "v_readfirstlane_b32 s39, v20\n"
        "s_cmp_ge_u32 s39, 2\n"
        "s_cbranch_scc1 W8_lr00_x\n"
        "s_sleep 1\n"
        "s_branch W8_lr00\n"
        "W8_lr00_x:\n"
        "s_mov_b32 m0, s34\n"
        "s_add_u32 s34, s34, 4096\n"
        "global_load_lds_dwordx4 v4, s[40:41] offset:0 sc1\n"
        "global_load_lds_dwordx4 v4, s[40:41] offset:1024 sc1\n"
        "global_load_lds_dwordx4 v4, s[40:41] offset:2048 sc1\n"
        "global_load_lds_dwordx4 v4, s[40:41] offset:3072 sc1\n"
        "s_mov_b32 m0, s34\n"
        "s_add_u32 s34, s34, 4096\n"
        "global_load_lds_dwordx4 v8, s[40:41] offset:0 sc1\n"
        "global_load_lds_dwordx4 v8, s[40:41] offset:1024 sc1\n"
        "global_load_lds_dwordx4 v8, s[40:41] offset:2048 sc1\n"
        "global_load_lds_dwordx4 v8, s[40:41] offset:3072 sc1\n"
        "s_cmp_ge_u32 s39, 3\n"
        "s_cbranch_scc1 W8_lg1_x\n"
        "ds_read_b32 v20, v25 offset:1024\n"
        "s_waitcnt lgkmcnt(0)\n"
        "v_readfirstlane_b32 s39, v20\n"
        "s_cmp_ge_u32 s39, 3\n"
        "s_cbranch_scc1 W8_lg1_x\n"
        "v_mov_b32 v21, 1\n"
        "s_waitcnt vmcnt(0)\n"
        "ds_max_i32 v25, v21 offset:0\n"
        "s_mov_b32 s38, 8388608\n"
        "W8_lg1:\n"
        "ds_read_b32 v20, v25 offset:1024\n"
        "s_sub_u32 s38, s38, 1\n"
        "s_waitcnt lgkmcnt(0)\n"
        "s_cbranch_scc1 W8_lg1_x\n"
        "v_readfirstlane_b32 s39, v20\n"
        "s_cmp_ge_u32 s39, 3\n"
        "s_cbranch_scc1 W8_lg1_x\n"
        "s_sleep 1\n"
        "s_branch W8_lg1\n"
        "W8_lg1_x:\n"
        "s_mov_b32 m0, s34\n"
        "s_add_u32 s34, s34, 4096\n"
        "global_load_lds_dwordx4 v5, s[40:41] offset:0 sc1\n"
        "global_load_lds_dwordx4 v5, s[40:41] offset:1024 sc1\n"
        "global_load_lds_dwordx4 v5, s[40:41] offset:2048 sc1\n"
        "global_load_lds_dwordx4 v5, s[40:41] offset:3072 sc1\n"
        "s_mov_b32 m0, s34\n"
        "s_add_u32 s34, s34, 4096\n"
        "global_load_lds_dwordx4 v9, s[40:41] offset:0 sc1\n"
        "global_load_lds_dwordx4 v9, s[40:41] offset:1024 sc1\n"
        "global_load_lds_dwordx4 v9, s[40:41] offset:2048 sc1\n"
        "global_load_lds_dwordx4 v9, s[40:41] offset:3072 sc1\n"
        "v_mov_b32 v21, 1\n"
        "s_waitcnt vmcnt(8)\n"
        "ds_max_i32 v25, v21 offset:0\n"
        "s_cmp_ge_u32 s39, 4\n"
        "s_cbranch_scc1 W8_lg2_x\n"
        "ds_read_b32 v20, v25 offset:1024\n"
        "s_waitcnt lgkmcnt(0)\n"
        "v_readfirstlane_b32 s39, v20\n"
        "s_cmp_ge_u32 s39, 4\n"
        "s_cbranch_scc1 W8_lg2_x\n"
        "v_mov_b32 v21, 2\n"
        "s_waitcnt vmcnt(0)\n"
        "ds_max_i32 v25, v21 offset:0\n"
        "s_mov_b32 s38, 8388608\n"
        "W8_lg2:\n"
        "ds_read_b32 v20, v25 offset:1024\n"
        "s_sub_u32 s38, s38, 1\n"
        "s_waitcnt lgkmcnt(0)\n"
        "s_cbranch_scc1 W8_lg2_x\n"
        "v_readfirstlane_b32 s39, v20\n"
        "s_cmp_ge_u32 s39, 4\n"
        "s_cbranch_scc1 W8_lg2_x\n"
        "s_sleep 1\n"
        "s_branch W8_lg2\n"
        "W8_lg2_x:\n"
        "s_mov_b32 m0, s34\n"
        "s_add_u32 s34, s34, 4096\n"
        "global_load_lds_dwordx4 v6, s[40:41] offset:0 sc1\n"
        "global_load_lds_dwordx4 v6, s[40:41] offset:1024 sc1\n"
        "global_load_lds_dwordx4 v6, s[40:41] offset:2048 sc1\n"
        "global_load_lds_dwordx4 v6, s[40:41] offset:3072 sc1\n"
        "s_mov_b32 m0, s34\n"
        "s_add_u32 s34, s34, 4096\n"
        "global_load_lds_dwordx4 v10, s[40:41] offset:0 sc1\n"
        "global_load_lds_dwordx4 v10, s[40:41] offset:1024 sc1\n"
        "global_load_lds_dwordx4 v10, s[40:41] offset:2048 sc1\n"
        "global_load_lds_dwordx4 v10, s[40:41] offset:3072 sc1\n"
        "s_cmp_ge_u32 s39, 5\n"
        "s_cbranch_scc1 W8_lg3_x\n"
        "ds_read_b32 v20, v25 offset:1024\n"
        "s_waitcnt lgkmcnt(0)\n"
        "v_readfirstlane_b32 s39, v20\n"
        "s_cmp_ge_u32 s39, 5\n"
        "s_cbranch_scc1 W8_lg3_x\n"
        "v_mov_b32 v21, 2\n"
        "s_waitcnt vmcnt(8)\n"
        "ds_max_i32 v25, v21 offset:0\n"
        "v_mov_b32 v21, 3\n"
        "s_waitcnt vmcnt(0)\n"
        "ds_max_i32 v25, v21 offset:0\n"
        "s_mov_b32 s38, 8388608\n"
        "W8_lg3:\n"
        "ds_read_b32 v20, v25 offset:1024\n"
        "s_sub_u32 s38, s38, 1\n"
        "s_waitcnt lgkmcnt(0)\n"
        "s_cbranch_scc1 W8_lg3_x\n"
        "v_readfirstlane_b32 s39, v20\n"
        "s_cmp_ge_u32 s39, 5\n"
        "s_cbranch_scc1 W8_lg3_x\n"
        "s_sleep 1\n"
        "s_branch W8_lg3\n"
        "W8_lg3_x:\n"
        "s_mov_b32 m0, s34\n"
        "s_add_u32 s34, s34, 4096\n"
        "global_load_lds_dwordx4 v7, s[40:41] offset:0 sc1\n"
        "global_load_lds_dwordx4 v7, s[40:41] offset:1024 sc1\n"
        "global_load_lds_dwordx4 v7, s[40:41] offset:2048 sc1\n"
        "global_load_lds_dwordx4 v7, s[40:41] offset:3072 sc1\n"
        "s_mov_b32 m0, s34\n"
        "s_add_u32 s34, s34, 4096\n"
        "global_load_lds_dwordx4 v11, s[40:41] offset:0 sc1\n"
        "global_load_lds_dwordx4 v11, s[40:41] offset:1024 sc1\n"
        "global_load_lds_dwordx4 v11, s[40:41] offset:2048 sc1\n"
        "global_load_lds_dwordx4 v11, s[40:41] offset:3072 sc1\n"
        "v_mov_b32 v21, 2\n"
        "s_waitcnt vmcnt(16)\n"
        "ds_max_i32 v25, v21 offset:0\n"
        "v_mov_b32 v3, 2\n"
        "s_add_u32 s40, s40, 32768\n"
        "s_addc_u32 s41, s41, 0\n"
        "s_mov_b32 s35, 3\n"
        "s_mov_b32 s36, 9\n"
        "W8_lq:\n"
        "ds_read_b32 v20, v25 offset:1024\n"
        "s_waitcnt lgkmcnt(0)\n"
        "v_readfirstlane_b32 s39, v20\n"
        "s_cmp_ge_u32 s39, s36\n"
        "s_cbranch_scc1 W8_lr_x\n"
        "v_add_u32 v21, 1, v3\n"
        "s_waitcnt vmcnt(8)\n"
        "ds_max_i32 v25, v21 offset:0\n"
        "v_add_u32 v21, 2, v3\n"
        "s_waitcnt vmcnt(0)\n"
        "ds_max_i32 v25, v21 offset:0\n"
        "s_mov_b32 s38, 8388608\n"
        "W8_lr:\n"
        "ds_read_b32 v20, v25 offset:1024\n"
        "s_sub_u32 s38, s38, 1\n"
        "s_waitcnt lgkmcnt(0)\n"
        "s_cbranch_scc1 W8_lr_x\n"
        "v_readfirstlane_b32 s39, v20\n"
        "s_cmp_ge_u32 s39, s36\n"
        "s_cbranch_scc1 W8_lr_x\n"
        "s_sleep 1\n"
        "s_branch W8_lr\n"
        "W8_lr_x:\n"
        "s_mov_b32 m0, s34\n"
        "s_add_u32 s34, s34, 4096\n"
        "global_load_lds_dwordx4 v4, s[40:41] offset:0 sc1\n"
        "global_load_lds_dwordx4 v4, s[40:41] offset:1024 sc1\n"
        "global_load_lds_dwordx4 v4, s[40:41] offset:2048 sc1\n"
        "global_load_lds_dwordx4 v4, s[40:41] offset:3072 sc1\n"
        "s_mov_b32 m0, s34\n"
        "s_add_u32 s34, s34, 4096\n"
        "global_load_lds_dwordx4 v8, s[40:41] offset:0 sc1\n"
        "global_load_lds_dwordx4 v8, s[40:41] offset:1024 sc1\n"
        "global_load_lds_dwordx4 v8, s[40:41] offset:2048 sc1\n"
        "global_load_lds_dwordx4 v8, s[40:41] offset:3072 sc1\n"
        "v_add_u32 v3, 1, v3\n"
        "s_waitcnt vmcnt(16)\n"
        "ds_max_i32 v25, v3 offset:0\n"
        "s_mov_b32 m0, s34\n"
        "s_add_u32 s34, s34, 4096\n"
        "global_load_lds_dwordx4 v5, s[40:41] offset:0 sc1\n"
        "global_load_lds_dwordx4 v5, s[40:41] offset:1024 sc1\n"
        "global_load_lds_dwordx4 v5, s[40:41] offset:2048 sc1\n"
        "global_load_lds_dwordx4 v5, s[40:41] offset:3072 sc1\n"
        "s_mov_b32 m0, s34\n"
        "s_add_u32 s34, s34, 4096\n"
        "global_load_lds_dwordx4 v9, s[40:41] offset:0 sc1\n"
        "global_load_lds_dwordx4 v9, s[40:41] offset:1024 sc1\n"
        "global_load_lds_dwordx4 v9, s[40:41] offset:2048 sc1\n"
        "global_load_lds_dwordx4 v9, s[40:41] offset:3072 sc1\n"
        "v_add_u32 v3, 1, v3\n"
        "s_waitcnt vmcnt(16)\n"
        "ds_max_i32 v25, v3 offset:0\n"
        "s_mov_b32 m0, s34\n"
        "s_add_u32 s34, s34, 4096\n"
        "global_load_lds_dwordx4 v6, s[40:41] offset:0 sc1\n"
        "global_load_lds_dwordx4 v6, s[40:41] offset:1024 sc1\n"
        "global_load_lds_dwordx4 v6, s[40:41] offset:2048 sc1\n"
        "global_load_lds_dwordx4 v6, s[40:41] offset:3072 sc1\n"
        "s_mov_b32 m0, s34\n"
        "s_add_u32 s34, s34, 4096\n"
        "global_load_lds_dwordx4 v10, s[40:41] offset:0 sc1\n"
        "global_load_lds_dwordx4 v10, s[40:41] offset:1024 sc1\n"
        "global_load_lds_dwordx4 v10, s[40:41] offset:2048 sc1\n"
        "global_load_lds_dwordx4 v10, s[40:41] offset:3072 sc1\n"
        "v_add_u32 v3, 1, v3\n"
        "s_waitcnt vmcnt(16)\n"
        "ds_max_i32 v25, v3 offset:0\n"
        "s_mov_b32 m0, s34\n"
        "s_add_u32 s34, s34, 4096\n"
        "global_load_lds_dwordx4 v7, s[40:41] offset:0 sc1\n"
        "global_load_lds_dwordx4 v7, s[40:41] offset:1024 sc1\n"
        "global_load_lds_dwordx4 v7, s[40:41] offset:2048 sc1\n"
        "global_load_lds_dwordx4 v7, s[40:41] offset:3072 sc1\n"
        "s_mov_b32 m0, s34\n"
        "s_add_u32 s34, s34, 4096\n"
        "global_load_lds_dwordx4 v11, s[40:41] offset:0 sc1\n"
        "global_load_lds_dwordx4 v11, s[40:41] offset:1024 sc1\n"
        "global_load_lds_dwordx4 v11, s[40:41] offset:2048 sc1\n"
        "global_load_lds_dwordx4 v11, s[40:41] offset:3072 sc1\n"
        "v_add_u32 v3, 1, v3\n"
        "s_waitcnt vmcnt(16)\n"
        "ds_max_i32 v25, v3 offset:0\n"
        "s_add_u32 s40, s40, 32768\n"
        "s_addc_u32 s41, s41, 0\n"
        "s_add_u32 s36, s36, 4\n"
        "s_add_u32 s35, s35, 1\n"
        "s_cmp_lt_u32 s35, 6\n"
        "s_cbranch_scc1 W8_lq\n"
        "s_mov_b32 s34, 0\n"
        "ds_read_b32 v20, v25 offset:1024\n"
        "s_waitcnt lgkmcnt(0)\n"
        "v_readfirstlane_b32 s39, v20\n"
        "s_cmp_ge_u32 s39, 21\n"
        "s_cbranch_scc1 W8_lr0_x\n"
        "v_add_u32 v21, 1, v3\n"
        "s_waitcnt vmcnt(8)\n"
        "ds_max_i32 v25, v21 offset:0\n"
        "v_add_u32 v21, 2, v3\n"
        "s_waitcnt vmcnt(0)\n"
        "ds_max_i32 v25, v21 offset:0\n"
        "s_mov_b32 s38, 8388608\n"
        "W8_lr0:\n"
        "ds_read_b32 v20, v25 offset:1024\n"
        "s_sub_u32 s38, s38, 1\n"
        "s_waitcnt lgkmcnt(0)\n"
        "s_cbranch_scc1 W8_lr0_x\n"
        "v_readfirstlane_b32 s39, v20\n"
        "s_cmp_ge_u32 s39, 21\n"
        "s_cbranch_scc1 W8_lr0_x\n"
        "s_sleep 1\n"
        "s_branch W8_lr0\n"
        "W8_lr0_x:\n"
        "ds_read_b32 v20, v25 offset:768\n"
        "s_waitcnt lgkmcnt(0)\n"
        "v_readfirstlane_b32 s39, v20\n"
        "s_cmp_ge_u32 s39, 543744\n"
        "s_cbranch_scc1 W8_lf0_x\n"
        "v_add_u32 v21, 1, v3\n"
        "s_waitcnt vmcnt(8)\n"
        "ds_max_i32 v25, v21 offset:0\n"
        "v_add_u32 v21, 2, v3\n"
        "s_waitcnt vmcnt(0)\n"
        "ds_max_i32 v25, v21 offset:0\n"
        "s_mov_b32 s38, 8388608\n"
        "W8_lf0:\n"
        "ds_read_b32 v20, v25 offset:768\n"
        "s_sub_u32 s38, s38, 1\n"
        "s_waitcnt lgkmcnt(0)\n"
        "s_cbranch_scc1 W8_lf0_x\n"
        "v_readfirstlane_b32 s39, v20\n"
        "s_cmp_ge_u32 s39, 543744\n"
        "s_cbranch_scc1 W8_lf0_x\n"
        "s_sleep 1\n"
        "s_branch W8_lf0\n"
        "W8_lf0_x:\n"
        "s_add_u32 s40, s42, 4096\n"
        "s_addc_u32 s41, s43, 0\n"
        "W8_ll0:\n"
        "s_mov_b32 m0, s34\n"
        "s_add_u32 s34, s34, 2048\n"
        "global_load_lds_dwordx4 v12, s[40:41] sc1\n"
        "global_load_lds_dwordx4 v12, s[40:41] offset:1024 sc1\n"
        "s_mov_b32 m0, s34\n"
        "s_add_u32 s34, s34, 2048\n"
        "global_load_lds_dwordx4 v13, s[40:41] sc1\n"
        "global_load_lds_dwordx4 v13, s[40:41] offset:1024 sc1\n"
        "s_mov_b32 m0, s34\n"
        "s_add_u32 s34, s34, 2048\n"
        "global_load_lds_dwordx4 v14, s[40:41] sc1\n"
        "global_load_lds_dwordx4 v14, s[40:41] offset:1024 sc1\n"
        "s_mov_b32 m0, s34\n"
        "s_add_u32 s34, s34, 2048\n"
        "global_load_lds_dwordx4 v15, s[40:41] sc1\n"
        "global_load_lds_dwordx4 v15, s[40:41] offset:1024 sc1\n"
        "s_add_u32 s40, s40, 24576\n"
        "s_addc_u32 s41, s41, 0\n"
        "v_add_u32 v3, 1, v3\n"
        "s_waitcnt vmcnt(16)\n"
        "ds_max_i32 v25, v3 offset:0\n"
        "s_cmp_lt_u32 s34, 32768\n"
        "s_cbranch_scc1 W8_ll0\n"
        "v_add_u32 v3, -1, v3\n"
        "ds_read_b32 v20, v25 offset:772\n"
        "s_waitcnt lgkmcnt(0)\n"
        "v_readfirstlane_b32 s39, v20\n"
        "s_cmp_ge_u32 s39, 543744\n"
        "s_cbranch_scc1 W8_lf1_x\n"
        "v_add_u32 v21, 1, v3\n"
        "s_waitcnt vmcnt(16)\n"
        "ds_max_i32 v25, v21 offset:0\n"
        "v_add_u32 v21, 2, v3\n"
        "s_waitcnt vmcnt(8)\n"
        "ds_max_i32 v25, v21 offset:0\n"
        "v_add_u32 v21, 3, v3\n"
        "s_waitcnt vmcnt(0)\n"
        "ds_max_i32 v25, v21 offset:0\n"
        "s_mov_b32 s38, 8388608\n"
        "W8_lf1:\n"
        "ds_read_b32 v20, v25 offset:772\n"
        "s_sub_u32 s38, s38, 1\n"
        "s_waitcnt lgkmcnt(0)\n"
        "s_cbranch_scc1 W8_lf1_x\n"
        "v_readfirstlane_b32 s39, v20\n"
        "s_cmp_ge_u32 s39, 543744\n"
        "s_cbranch_scc1 W8_lf1_x\n"
        "s_sleep 1\n"
        "s_branch W8_lf1\n"
        "W8_lf1_x:\n"
        "s_add_u32 s40, s42, 0\n"
        "s_addc_u32 s41, s43, 0\n"
        "W8_ll1:\n"
        "s_mov_b32 m0, s34\n"
        "s_add_u32 s34, s34, 2048\n"
        "global_load_lds_dwordx4 v12, s[40:41] sc1\n"
        "global_load_lds_dwordx4 v12, s[40:41] offset:1024 sc1\n"
        "s_mov_b32 m0, s34\n"
        "s_add_u32 s34, s34, 2048\n"
        "global_load_lds_dwordx4 v13, s[40:41] sc1\n"
        "global_load_lds_dwordx4 v13, s[40:41] offset:1024 sc1\n"
        "s_mov_b32 m0, s34\n"
        "s_add_u32 s34, s34, 2048\n"
        "global_load_lds_dwordx4 v14, s[40:41] sc1\n"
        "global_load_lds_dwordx4 v14, s[40:41] offset:1024 sc1\n"
        "s_mov_b32 m0, s34\n"
        "s_add_u32 s34, s34, 2048\n"
        "global_load_lds_dwordx4 v15, s[40:41] sc1\n"
        "global_load_lds_dwordx4 v15, s[40:41] offset:1024 sc1\n"
        "s_add_u32 s40, s40, 24576\n"
        "s_addc_u32 s41, s41, 0\n"
        "v_add_u32 v3, 1, v3\n"
        "s_waitcnt vmcnt(24)\n"
        "ds_max_i32 v25, v3 offset:0\n"
        "s_cmp_lt_u32 s34, 65536\n"
        "s_cbranch_scc1 W8_ll1\n"
        "v_add_u32 v3, -1, v3\n"
        "ds_read_b32 v20, v25 offset:776\n"
        "s_waitcnt lgkmcnt(0)\n"
        "v_readfirstlane_b32 s39, v20\n"
        "s_cmp_ge_u32 s39, 543744\n"
        "s_cbranch_scc1 W8_lf2_x\n"
        "v_add_u32 v21, 1, v3\n"
        "s_waitcnt vmcnt(24)\n"
        "ds_max_i32 v25, v21 offset:0\n"
        "v_add_u32 v21, 2, v3\n"
        "s_waitcnt vmcnt(16)\n"
        "ds_max_i32 v25, v21 offset:0\n"
        "v_add_u32 v21, 3, v3\n"
        "s_waitcnt vmcnt(8)\n"
        "ds_max_i32 v25, v21 offset:0\n"
        "v_add_u32 v21, 4, v3\n"
        "s_waitcnt vmcnt(0)\n"
        "ds_max_i32 v25, v21 offset:0\n"
        "s_mov_b32 s38, 8388608\n"
        "W8_lf2:\n"
        "ds_read_b32 v20, v25 offset:776\n"
        "s_sub_u32 s38, s38, 1\n"
        "s_waitcnt lgkmcnt(0)\n"
        "s_cbranch_scc1 W8_lf2_x\n"
        "v_readfirstlane_b32 s39, v20\n"
        "s_cmp_ge_u32 s39, 543744\n"
        "s_cbranch_scc1 W8_lf2_x\n"
        "s_sleep 1\n"
        "s_branch W8_lf2\n"
        "W8_lf2_x:\n"
        "s_add_u32 s40, s42, 2048\n"
        "s_addc_u32 s41, s43, 0\n"
        "W8_ll2:\n"
        "s_mov_b32 m0, s34\n"
        "s_add_u32 s34, s34, 2048\n"
        "global_load_lds_dwordx4 v12, s[40:41] sc1\n"
        "global_load_lds_dwordx4 v12, s[40:41] offset:1024 sc1\n"
        "s_mov_b32 m0, s34\n"
        "s_add_u32 s34, s34, 2048\n"
        "global_load_lds_dwordx4 v13, s[40:41] sc1\n"
        "global_load_lds_dwordx4 v13, s[40:41] offset:1024 sc1\n"
        "s_mov_b32 m0, s34\n"
        "s_add_u32 s34, s34, 2048\n"
        "global_load_lds_dwordx4 v14, s[40:41] sc1\n"
        "global_load_lds_dwordx4 v14, s[40:41] offset:1024 sc1\n"
        "s_mov_b32 m0, s34\n"
        "s_add_u32 s34, s34, 2048\n"
        "global_load_lds_dwordx4 v15, s[40:41] sc1\n"
        "global_load_lds_dwordx4 v15, s[40:41] offset:1024 sc1\n"
        "s_add_u32 s40, s40, 24576\n"
        "s_addc_u32 s41, s41, 0\n"
        "v_add_u32 v3, 1, v3\n"
        "s_waitcnt vmcnt(32)\n"
        "ds_max_i32 v25, v3 offset:0\n"
        "s_cmp_lt_u32 s34, 98304\n"
        "s_cbranch_scc1 W8_ll2\n"
        "v_add_u32 v3, 1, v3\n"
        "s_waitcnt vmcnt(24)\n"
        "ds_max_i32 v25, v3 offset:0\n"
        "v_add_u32 v3, 1, v3\n"
        "s_waitcnt vmcnt(16)\n"
        "ds_max_i32 v25, v3 offset:0\n"
        "v_add_u32 v3, 1, v3\n"
        "s_waitcnt vmcnt(8)\n"
        "ds_max_i32 v25, v3 offset:0\n"
        "v_add_u32 v3, 1, v3\n"
        "s_waitcnt vmcnt(0)\n"
        "ds_max_i32 v25, v3 offset:0\n"
        "s_branch W8_fin\n"
        "W8_comp:\n"
        "s_setprio 3\n"
        "s_lshl_b32 s26, s21, 2\n"
        "s_add_u32 s26, s26, s20\n"
        "s_sub_u32 s26, s26, 4\n"
        "v_lshrrev_b32 v2, 4, v1\n"
        "v_and_b32 v3, 15, v1\n"
        "v_lshlrev_b32 v248, 11, v2\n"
        "v_lshl_add_u32 v248, v3, 4, v248\n"
        "v_add_u32 v249, 65536, v248\n"
        "v_mov_b32 v250, 135936\n"
        "v_lshlrev_b32 v240, 4, v1\n"
        "v_mul_u32_u24 v2, 28672, v2\n"
        "v_lshl_add_u32 v251, v3, 3, v2\n"
        "v_and_b32 v4, 7, v1\n"
        "v_lshl_add_u32 v252, v4, 3, v2\n"
        "v_bfe_u32 v4, v1, 3, 1\n"
        "v_lshl_add_u32 v252, v4, 2, v252\n"
        "s_mul_i32 s24, s22, 192\n"
        "s_sub_u32 s24, 6144, s24\n"
        "s_lshl_b32 s25, s22, 7\n"
        "s_sub_u32 s37, 3840, s25\n"
        "v_add_u32 v252, s24, v252\n"
        "v_lshlrev_b32 v253, 5, v3\n"
        "v_add_u32 v253, 133632, v253\n"
        "s_waitcnt lgkmcnt(0)\n"
        "s_lshl_b32 s27, s26, 15\n"
        "s_xor_b32 s24, s33, 0\n"
        "s_and_b32 s24, s24, 3\n"
        "s_lshl_b32 s24, s24, 11\n"
        "s_add_u32 s24, s24, s27\n"
        "s_add_u32 s56, s8, s24\n"
        "s_addc_u32 s57, s9, 0\n"
        "s_xor_b32 s24, s33, 1\n"
        "s_and_b32 s24, s24, 3\n"
        "s_lshl_b32 s24, s24, 11\n"
        "s_add_u32 s24, s24, s27\n"
        "s_add_u32 s58, s8, s24\n"
        "s_addc_u32 s59, s9, 0\n"
        "s_xor_b32 s24, s33, 2\n"
        "s_and_b32 s24, s24, 3\n"
        "s_lshl_b32 s24, s24, 11\n"
        "s_add_u32 s24, s24, s27\n"
        "s_add_u32 s60, s8, s24\n"
        "s_addc_u32 s61, s9, 0\n"
        "s_xor_b32 s24, s33, 3\n"
        "s_and_b32 s24, s24, 3\n"
        "s_lshl_b32 s24, s24, 11\n"
        "s_add_u32 s24, s24, s27\n"
        "s_add_u32 s62, s8, s24\n"
        "s_addc_u32 s63, s9, 0\n"
        "s_mul_i32 s24, s26, 229376\n"
        "s_mul_i32 s25, s22, 256\n"
        "s_add_u32 s24, s24, s25\n"
        "s_add_u32 s40, s10, s24\n"
        "s_addc_u32 s41, s11, 0\n"
        "s_add_u32 s42, s40, 7168\n"
        "s_addc_u32 s43, s41, 0\n"
        "s_add_u32 s44, s40, 14336\n"
        "s_addc_u32 s45, s41, 0\n"
        "s_add_u32 s46, s40, 21504\n"
        "s_addc_u32 s47, s41, 0\n"
        "s_add_u32 s48, s40, 114688\n"
        "s_addc_u32 s49, s41, 0\n"
        "s_add_u32 s50, s40, 121856\n"
        "s_addc_u32 s51, s41, 0\n"
        "s_add_u32 s52, s40, 129024\n"
        "s_addc_u32 s53, s41, 0\n"
        "s_add_u32 s54, s40, 136192\n"
        "s_addc_u32 s55, s41, 0\n"
        "s_sub_u32 s28, s20, 4\n"
        "s_mul_i32 s24, s21, 512\n"
        "s_mul_i32 s25, s22, 8\n"
        "s_add_u32 s24, s24, s25\n"
        "s_add_u32 s24, s24, 6029312\n"
        "s_mul_i32 s25, s22, 1024\n"
        "s_mul_i32 s27, s21, 8\n"
        "s_add_u32 s25, s25, s27\n"
        "s_add_u32 s25, s25, 6291456\n"
        "s_sub_u32 s25, s25, s24\n"
        "v_mul_lo_u32 v24, v1, s25\n"
        "s_mul_i32 s25, s28, 128\n"
        "s_add_u32 s24, s24, s25\n"
        "v_add_u32 v24, s24, v24\n"
        "s_mul_i32 s29, s28, 5120\n"
        "s_add_u32 s29, s29, 98304\n"
        "v_and_b32 v6, 7, v1\n"
        "v_lshrrev_b32 v7, 3, v1\n"
        "v_lshrrev_b32 v8, 4, v1\n"
        "v_lshl_add_u32 v8, v8, 4, s29\n"
        "v_lshl_add_u32 v8, v3, 6, v8\n"
        "v_add_u32 v9, s29, v240\n"
        "s_and_b32 s24, s22, 3\n"
        "s_lshl2_add_u32 s24, s21, s24\n"
        "s_lshl2_add_u32 s24, s24, s28\n"
        "s_lshr_b32 s25, s22, 2\n"
        "s_lshl2_add_u32 s24, s24, s25\n"
        "s_lshl_b32 s24, s24, 11\n"
        "v_add_u32 v11, s24, v240\n"
        "v_lshrrev_b32 v10, 5, v1\n"
        "s_lshl_b32 s24, s22, 2\n"
        "s_add_u32 s24, s24, s28\n"
        "s_lshl_b32 s24, s24, 4\n"
        "s_add_u32 s24, s24, s21\n"
        "s_lshl_b32 s24, s24, 11\n"
        "s_add_u32 s24, s24, 2097152\n"
        "v_add_u32 v13, s24, v240\n"
        "s_lshl_b32 s24, s22, 4\n"
        "s_add_u32 s24, s24, s21\n"
        "s_lshl_b32 s24, s24, 10\n"
        "s_add_u32 s24, s24, 5767168\n"
        "v_and_b32 v14, 31, v1\n"
        "v_lshl_add_u32 v14, v14, 4, s24\n"
        "v_lshl_add_u32 v14, v10, 3, v14\n"
        "v_lshlrev_b32 v15, 3, v1\n"
        "v_add_u32 v15, 118016, v15\n"
        "s_add_u32 s28, s28, 1\n"
        "s_mov_b32 s38, 8388608\n"
        "W8_tgc:\n"
        "ds_read_b32 v12, v250 offset:772\n"
        "s_sub_u32 s38, s38, 1\n"
        "s_waitcnt lgkmcnt(0)\n"
        "s_cbranch_scc1 W8_tgc_x\n"
        "v_readfirstlane_b32 s39, v12\n"
        "s_cmp_ge_u32 s39, 1\n"
        "s_cbranch_scc1 W8_tgc_x\n"
        "s_sleep 1\n"
        "s_branch W8_tgc\n"
        "W8_tgc_x:\n"
        "v_lshrrev_b32 v12, 0, v1\n"
        "v_lshlrev_b32 v12, 7, v12\n"
        "v_add_u32 v12, 136968, v12\n"
        "s_mov_b64 exec, 3\n"
        "ds_read_b64 v[26:27], v12\n"
        "s_mov_b64 exec, -1\n"
        "s_mov_b32 s38, 8388608\n"
        "W8_pkc:\n"
        "ds_read_b32 v12, v250 offset:1292\n"
        "s_sub_u32 s38, s38, 1\n"
        "s_waitcnt lgkmcnt(0)\n"
        "s_cbranch_scc1 W8_pkc_x\n"
        "v_readfirstlane_b32 s39, v12\n"
        "s_cmp_ge_u32 s39, s28\n"
        "s_cbranch_scc1 W8_pkc_x\n"
        "s_sleep 1\n"
        "s_branch W8_pkc\n"
        "W8_pkc_x:\n"
        "ds_read_b128 v[28:31], v8\n"
        "ds_read_b128 v[32:35], v8 offset:1024\n"
        "ds_read_b128 v[36:39], v9 offset:2304\n"
        "ds_read_b128 v[40:43], v9 offset:3328\n"
        "ds_read_b64 v[44:45], v15\n"
        "s_waitcnt lgkmcnt(3)\n"
        "global_store_dwordx4 v11, v[28:31], s[8:9] offset:0 sc1\n"
        "global_store_dwordx4 v11, v[32:35], s[8:9] offset:1024 sc1\n"
        "s_waitcnt lgkmcnt(0)\n"
        "global_store_dwordx4 v13, v[36:39], s[8:9] sc1\n"
        "global_store_dwordx4 v13, v[40:43], s[8:9] offset:1024 sc1\n"
        "s_cmp_lg_u32 s28, 4\n"
        "s_cbranch_scc1 W8_ncs\n"
        "global_store_dwordx2 v14, v[44:45], s[8:9] sc1\n"
        "W8_ncs:\n"
        "s_mov_b64 exec, 3\n"
        "s_waitcnt vmcnt(0)\n"
        "global_atomic_swap_x2 v24, v[26:27], s[8:9] sc1\n"
        "s_mov_b64 exec, -1\n"
        "s_mov_b32 s38, 8388608\n"
        "W8_cgo:\n"
        "ds_read_b32 v254, v250 offset:260\n"
        "s_sub_u32 s38, s38, 1\n"
        "s_waitcnt lgkmcnt(0)\n"
        "s_cbranch_scc1 W8_cgo_x\n"
        "v_readfirstlane_b32 s39, v254\n"
        "s_cmp_ge_u32 s39, 1\n"
        "s_cbranch_scc1 W8_cgo_x\n"
        "s_sleep 2\n"
        "s_branch W8_cgo\n"
        "W8_cgo_x:\n"
        "s_mov_b32 s38, 8388608\n"
        "W8_rd0:\n"
        "ds_read_b32 v254, v250 offset:4\n"
        "s_sub_u32 s38, s38, 1\n"
        "s_waitcnt lgkmcnt(0)\n"
        "s_cbranch_scc1 W8_rd0_x\n"
        "v_readfirstlane_b32 s39, v254\n"
        "s_cmp_ge_u32 s39, 1\n"
        "s_cbranch_scc1 W8_rd0_x\n"
        "s_sleep 1\n"
        "s_branch W8_rd0\n"
        "W8_rd0_x:\n"
        "global_load_dwordx4 v[0:3], v240, s[56:57] offset:0 sc1\n"
        "global_load_dwordx4 v[4:7], v240, s[56:57] offset:1024 sc1\n"
        "s_sleep 2\n"
        "s_cmp_ge_u32 s39, 2\n"
        "s_cbranch_scc1 W8_rd1_x\n"
        "s_mov_b32 s38, 8388608\n"
        "W8_rd1:\n"
        "ds_read_b32 v254, v250 offset:4\n"
        "s_sub_u32 s38, s38, 1\n"
        "s_waitcnt lgkmcnt(0)\n"
        "s_cbranch_scc1 W8_rd1_x\n"
        "v_readfirstlane_b32 s39, v254\n"
        "s_cmp_ge_u32 s39, 2\n"
        "s_cbranch_scc1 W8_rd1_x\n"
        "s_sleep 1\n"
        "s_branch W8_rd1\n"
        "W8_rd1_x:\n"
        "global_load_dwordx4 v[8:11], v240, s[58:59] offset:0 sc1\n"
        "global_load_dwordx4 v[12:15], v240, s[58:59] offset:1024 sc1\n"
        "W8_dwx:\n"
        "ds_read_b32 v254, v250 offset:0\n"
        "s_waitcnt lgkmcnt(0)\n"
        "v_cmp_gt_u32 vcc, 1, v254\n"
        "s_cbranch_vccnz W8_s0\n"
        "W8_c0:\n"
        "ds_read_b32 v241, v250 offset:4\n"
        "ds_read_b128 v[208:211], v248 offset:0\n"
        "ds_read_b128 v[212:215], v248 offset:1024\n"
        "ds_read_b128 v[216:219], v248 offset:512\n"
        "ds_read_b128 v[220:223], v248 offset:1536\n"
        "ds_read_b128 v[224:227], v248 offset:256\n"
        "ds_read_b128 v[228:231], v248 offset:1280\n"
        "s_waitcnt vmcnt(2)\n"
        "s_waitcnt lgkmcnt(5)\n"
        "v_mfma_i32_16x16x64_i8 v[128:131], v[0:3], v[208:211], 0\n"
        "v_cmp_gt_u32 vcc, 4, v241\n"
        "s_cbranch_vccnz W8_sZ\n"
        "W8_cZ:\n"
        "ds_read_b32 v254, v250 offset:0\n"
        "v_mfma_i32_16x16x64_i8 v[132:135], v[4:7], v[208:211], 0\n"
        "ds_read_b128 v[232:235], v248 offset:768\n"
        "s_waitcnt lgkmcnt(6)\n"
        "v_mfma_i32_16x16x64_i8 v[136:139], v[0:3], v[212:215], 0\n"
        "global_load_dwordx4 v[16:19], v240, s[60:61] offset:0 sc1\n"
        "v_mfma_i32_16x16x64_i8 v[140:143], v[4:7], v[212:215], 0\n"
        "ds_read_b128 v[236:239], v248 offset:1792\n"
        "s_waitcnt lgkmcnt(2)\n"
        "v_cmp_gt_u32 vcc, 2, v254\n"
        "v_mfma_i32_16x16x64_i8 v[144:147], v[0:3], v[216:219], 0\n"
        "s_cbranch_vccnz W8_s1\n"
        "W8_c1:\n"
        "v_mfma_i32_16x16x64_i8 v[148:151], v[4:7], v[216:219], 0\n"
        "v_cmp_gt_u32 vcc, 8, v241\n"
        "ds_read_b128 v[208:211], v248 offset:8192\n"
        "v_mfma_i32_16x16x64_i8 v[152:155], v[0:3], v[220:223], 0\n"
        "s_cbranch_vccnz W8_sR0\n"
        "W8_cR0:\n"
        "ds_read_b32 v254, v250 offset:0\n"
        "v_mfma_i32_16x16x64_i8 v[156:159], v[4:7], v[220:223], 0\n"
        "ds_read_b128 v[212:215], v248 offset:9216\n"
        "v_mfma_i32_16x16x64_i8 v[160:163], v[0:3], v[224:227], 0\n"
        "global_load_dwordx4 v[20:23], v240, s[60:61] offset:1024 sc1\n"
        "v_mfma_i32_16x16x64_i8 v[164:167], v[4:7], v[224:227], 0\n"
        "ds_read_b128 v[216:219], v248 offset:8704\n"
        "v_mfma_i32_16x16x64_i8 v[168:171], v[0:3], v[228:231], 0\n"
        "global_load_dwordx4 v[24:27], v240, s[62:63] offset:0 sc1\n"
        "v_mfma_i32_16x16x64_i8 v[172:175], v[4:7], v[228:231], 0\n"
        "ds_read_b128 v[220:223], v248 offset:9728\n"
        "s_waitcnt lgkmcnt(6)\n"
        "v_mfma_i32_16x16x64_i8 v[176:179], v[0:3], v[232:235], 0\n"
        "global_load_dwordx4 v[28:31], v240, s[62:63] offset:1024 sc1\n"
        "v_add_u32 v240, 8192, v240\n"
        "v_mfma_i32_16x16x64_i8 v[180:183], v[4:7], v[232:235], 0\n"
        "ds_read_b128 v[224:227], v248 offset:8448\n"
        "s_waitcnt lgkmcnt(6)\n"
        "v_mfma_i32_16x16x64_i8 v[184:187], v[0:3], v[236:239], 0\n"
        "v_mfma_i32_16x16x64_i8 v[188:191], v[4:7], v[236:239], 0\n"
        "ds_read_b128 v[228:231], v248 offset:9472\n"
        "s_waitcnt vmcnt(4)\n"
        "s_waitcnt lgkmcnt(6)\n"
        "v_mfma_i32_16x16x64_i8 v[128:131], v[8:11], v[208:211], v[128:131]\n"
        "v_mfma_i32_16x16x64_i8 v[132:135], v[12:15], v[208:211], v[132:135]\n"
        "ds_read_b128 v[232:235], v248 offset:8960\n"
        "s_waitcnt lgkmcnt(5)\n"
        "v_mfma_i32_16x16x64_i8 v[136:139], v[8:11], v[212:215], v[136:139]\n"
        "global_load_dwordx4 v[32:35], v240, s[56:57] offset:0 sc1\n"
        "v_mfma_i32_16x16x64_i8 v[140:143], v[12:15], v[212:215], v[140:143]\n"
        "ds_read_b128 v[236:239], v248 offset:9984\n"
        "v_cmp_gt_u32 vcc, 3, v254\n"
        "s_waitcnt lgkmcnt(5)\n"
        "v_mfma_i32_16x16x64_i8 v[144:147], v[8:11], v[216:219], v[144:147]\n"
        "s_cbranch_vccnz W8_s2\n"
        "W8_c2:\n"
        "v_mfma_i32_16x16x64_i8 v[148:151], v[12:15], v[216:219], v[148:151]\n"
        "ds_read_b128 v[208:211], v248 offset:16384\n"
        "s_waitcnt lgkmcnt(5)\n"
        "v_mfma_i32_16x16x64_i8 v[152:155], v[8:11], v[220:223], v[152:155]\n"
        "ds_read_b32 v254, v250 offset:0\n"
        "v_mfma_i32_16x16x64_i8 v[156:159], v[12:15], v[220:223], v[156:159]\n"
        "ds_read_b128 v[212:215], v248 offset:17408\n"
        "s_waitcnt lgkmcnt(6)\n"
        "v_mfma_i32_16x16x64_i8 v[160:163], v[8:11], v[224:227], v[160:163]\n"
        "global_load_dwordx4 v[36:39], v240, s[56:57] offset:1024 sc1\n"
        "v_mfma_i32_16x16x64_i8 v[164:167], v[12:15], v[224:227], v[164:167]\n"
        "ds_read_b128 v[216:219], v248 offset:16896\n"
        "s_waitcnt lgkmcnt(6)\n"
        "v_mfma_i32_16x16x64_i8 v[168:171], v[8:11], v[228:231], v[168:171]\n"
        "v_mfma_i32_16x16x64_i8 v[172:175], v[12:15], v[228:231], v[172:175]\n"
        "ds_read_b128 v[220:223], v248 offset:17920\n"
        "s_waitcnt lgkmcnt(6)\n"
        "v_mfma_i32_16x16x64_i8 v[176:179], v[8:11], v[232:235], v[176:179]\n"
        "v_mfma_i32_16x16x64_i8 v[180:183], v[12:15], v[232:235], v[180:183]\n"
        "ds_read_b128 v[224:227], v248 offset:16640\n"
        "s_waitcnt lgkmcnt(6)\n"
        "v_mfma_i32_16x16x64_i8 v[184:187], v[8:11], v[236:239], v[184:187]\n"
        "global_load_dwordx4 v[40:43], v240, s[58:59] offset:0 sc1\n"
        "v_mfma_i32_16x16x64_i8 v[188:191], v[12:15], v[236:239], v[188:191]\n"
        "ds_read_b128 v[228:231], v248 offset:17664\n"
        "s_waitcnt vmcnt(5)\n"
        "s_waitcnt lgkmcnt(6)\n"
        "v_mfma_i32_16x16x64_i8 v[128:131], v[16:19], v[208:211], v[128:131]\n"
        "v_mfma_i32_16x16x64_i8 v[132:135], v[20:23], v[208:211], v[132:135]\n"
        "ds_read_b128 v[232:235], v248 offset:17152\n"
        "s_waitcnt lgkmcnt(5)\n"
        "v_mfma_i32_16x16x64_i8 v[136:139], v[16:19], v[212:215], v[136:139]\n"
        "v_mfma_i32_16x16x64_i8 v[140:143], v[20:23], v[212:215], v[140:143]\n"
        "ds_read_b128 v[236:239], v248 offset:18176\n"
        "v_cmp_gt_u32 vcc, 4, v254\n"
        "s_waitcnt lgkmcnt(5)\n"
        "v_mfma_i32_16x16x64_i8 v[144:147], v[16:19], v[216:219], v[144:147]\n"
        "s_cbranch_vccnz W8_s3\n"
        "W8_c3:\n"
        "v_mfma_i32_16x16x64_i8 v[148:151], v[20:23], v[216:219], v[148:151]\n"
        "ds_read_b128 v[208:211], v248 offset:24576\n"
        "s_waitcnt lgkmcnt(5)\n"
        "v_mfma_i32_16x16x64_i8 v[152:155], v[16:19], v[220:223], v[152:155]\n"
        "ds_read_b32 v254, v250 offset:0\n"
        "v_mfma_i32_16x16x64_i8 v[156:159], v[20:23], v[220:223], v[156:159]\n"
        "ds_read_b128 v[212:215], v248 offset:25600\n"
        "s_waitcnt lgkmcnt(6)\n"
        "v_mfma_i32_16x16x64_i8 v[160:163], v[16:19], v[224:227], v[160:163]\n"
        "global_load_dwordx4 v[44:47], v240, s[58:59] offset:1024 sc1\n"
        "v_mfma_i32_16x16x64_i8 v[164:167], v[20:23], v[224:227], v[164:167]\n"
        "ds_read_b128 v[216:219], v248 offset:25088\n"
        "s_waitcnt lgkmcnt(6)\n"
        "v_mfma_i32_16x16x64_i8 v[168:171], v[16:19], v[228:231], v[168:171]\n"
        "v_mfma_i32_16x16x64_i8 v[172:175], v[20:23], v[228:231], v[172:175]\n"
        "ds_read_b128 v[220:223], v248 offset:26112\n"
        "s_waitcnt lgkmcnt(6)\n"
        "v_mfma_i32_16x16x64_i8 v[176:179], v[16:19], v[232:235], v[176:179]\n"
        "v_mfma_i32_16x16x64_i8 v[180:183], v[20:23], v[232:235], v[180:183]\n"
        "ds_read_b128 v[224:227], v248 offset:24832\n"
        "s_waitcnt lgkmcnt(6)\n"
        "v_mfma_i32_16x16x64_i8 v[184:187], v[16:19], v[236:239], v[184:187]\n"
        "global_load_dwordx4 v[48:51], v240, s[60:61] offset:0 sc1\n"
        "v_mfma_i32_16x16x64_i8 v[188:191], v[20:23], v[236:239], v[188:191]\n"
        "ds_read_b128 v[228:231], v248 offset:25856\n"
        "s_waitcnt vmcnt(5)\n"
        "s_waitcnt lgkmcnt(6)\n"
        "v_mfma_i32_16x16x64_i8 v[128:131], v[24:27], v[208:211], v[128:131]\n"
        "v_mfma_i32_16x16x64_i8 v[132:135], v[28:31], v[208:211], v[132:135]\n"
        "ds_read_b128 v[232:235], v248 offset:25344\n"
        "s_waitcnt lgkmcnt(5)\n"
        "v_mfma_i32_16x16x64_i8 v[136:139], v[24:27], v[212:215], v[136:139]\n"
        "v_mfma_i32_16x16x64_i8 v[140:143], v[28:31], v[212:215], v[140:143]\n"
        "ds_read_b128 v[236:239], v248 offset:26368\n"
        "v_cmp_gt_u32 vcc, 5, v254\n"
        "s_waitcnt lgkmcnt(5)\n"
        "v_mfma_i32_16x16x64_i8 v[144:147], v[24:27], v[216:219], v[144:147]\n"
        "s_cbranch_vccnz W8_s4\n"
        "W8_c4:\n"
        "v_mfma_i32_16x16x64_i8 v[148:151], v[28:31], v[216:219], v[148:151]\n"
        "ds_read_b128 v[208:211], v248 offset:32768\n"
        "s_waitcnt lgkmcnt(5)\n"
        "v_mfma_i32_16x16x64_i8 v[152:155], v[24:27], v[220:223], v[152:155]\n"
        "ds_read_b32 v254, v250 offset:0\n"
        "v_mfma_i32_16x16x64_i8 v[156:159], v[28:31], v[220:223], v[156:159]\n"
        "ds_read_b128 v[212:215], v248 offset:33792\n"
        "s_waitcnt lgkmcnt(6)\n"
        "v_mfma_i32_16x16x64_i8 v[160:163], v[24:27], v[224:227], v[160:163]\n"
        "ds_read_b32 v241, v250 offset:4\n"
        "s_mov_b64 exec, 1\n"
        "ds_add_u32 v250, v250 offset:516\n"
        "s_mov_b64 exec, -1\n"
        "v_mfma_i32_16x16x64_i8 v[164:167], v[28:31], v[224:227], v[164:167]\n"
        "ds_read_b128 v[216:219], v248 offset:33280\n"
        "s_waitcnt lgkmcnt(8)\n"
        "v_mfma_i32_16x16x64_i8 v[168:171], v[24:27], v[228:231], v[168:171]\n"
        "global_load_dwordx4 v[52:55], v240, s[60:61] offset:1024 sc1\n"
        "v_mfma_i32_16x16x64_i8 v[172:175], v[28:31], v[228:231], v[172:175]\n"
        "ds_read_b128 v[220:223], v248 offset:34304\n"
        "s_waitcnt lgkmcnt(8)\n"
        "v_mfma_i32_16x16x64_i8 v[176:179], v[24:27], v[232:235], v[176:179]\n"
        "v_mfma_i32_16x16x64_i8 v[180:183], v[28:31], v[232:235], v[180:183]\n"
        "ds_read_b128 v[224:227], v248 offset:33024\n"
        "s_waitcnt lgkmcnt(8)\n"
        "v_mfma_i32_16x16x64_i8 v[184:187], v[24:27], v[236:239], v[184:187]\n"
        "v_mfma_i32_16x16x64_i8 v[188:191], v[28:31], v[236:239], v[188:191]\n"
        "ds_read_b128 v[228:231], v248 offset:34048\n"
        "s_waitcnt vmcnt(4)\n"
        "s_waitcnt lgkmcnt(8)\n"
        "v_mfma_i32_16x16x64_i8 v[128:131], v[32:35], v[208:211], v[128:131]\n"
        "global_load_dwordx4 v[56:59], v240, s[62:63] offset:0 sc1\n"
        "v_mfma_i32_16x16x64_i8 v[132:135], v[36:39], v[208:211], v[132:135]\n"
        "ds_read_b128 v[232:235], v248 offset:33536\n"
        "s_waitcnt lgkmcnt(7)\n"
        "v_mfma_i32_16x16x64_i8 v[136:139], v[32:35], v[212:215], v[136:139]\n"
        "v_mfma_i32_16x16x64_i8 v[140:143], v[36:39], v[212:215], v[140:143]\n"
        "ds_read_b128 v[236:239], v248 offset:34560\n"
        "v_cmp_gt_u32 vcc, 6, v254\n"
        "s_waitcnt lgkmcnt(5)\n"
        "v_mfma_i32_16x16x64_i8 v[144:147], v[32:35], v[216:219], v[144:147]\n"
        "s_cbranch_vccnz W8_s5\n"
        "W8_c5:\n"
        "v_mfma_i32_16x16x64_i8 v[148:151], v[36:39], v[216:219], v[148:151]\n"
        "v_cmp_gt_u32 vcc, 12, v241\n"
        "ds_read_b128 v[208:211], v248 offset:40960\n"
        "s_waitcnt lgkmcnt(5)\n"
        "v_mfma_i32_16x16x64_i8 v[152:155], v[32:35], v[220:223], v[152:155]\n"
        "s_cbranch_vccnz W8_sR1\n"
        "W8_cR1:\n"
        "ds_read_b32 v254, v250 offset:0\n"
        "v_mfma_i32_16x16x64_i8 v[156:159], v[36:39], v[220:223], v[156:159]\n"
        "ds_read_b128 v[212:215], v248 offset:41984\n"
        "s_waitcnt lgkmcnt(6)\n"
        "v_mfma_i32_16x16x64_i8 v[160:163], v[32:35], v[224:227], v[160:163]\n"
        "global_load_dwordx4 v[60:63], v240, s[62:63] offset:1024 sc1\n"
        "v_add_u32 v240, 8192, v240\n"
        "v_mfma_i32_16x16x64_i8 v[164:167], v[36:39], v[224:227], v[164:167]\n"
        "ds_read_b128 v[216:219], v248 offset:41472\n"
        "s_waitcnt lgkmcnt(6)\n"
        "v_mfma_i32_16x16x64_i8 v[168:171], v[32:35], v[228:231], v[168:171]\n"
        "v_mfma_i32_16x16x64_i8 v[172:175], v[36:39], v[228:231], v[172:175]\n"
        "ds_read_b128 v[220:223], v248 offset:42496\n"
        "s_waitcnt lgkmcnt(6)\n"
        "v_mfma_i32_16x16x64_i8 v[176:179], v[32:35], v[232:235], v[176:179]\n"
        "v_mfma_i32_16x16x64_i8 v[180:183], v[36:39], v[232:235], v[180:183]\n"
        "ds_read_b128 v[224:227], v248 offset:41216\n"
        "s_waitcnt lgkmcnt(6)\n"
        "v_mfma_i32_16x16x64_i8 v[184:187], v[32:35], v[236:239], v[184:187]\n"
        "global_load_dwordx4 v[64:67], v240, s[56:57] offset:0 sc1\n"
        "v_mfma_i32_16x16x64_i8 v[188:191], v[36:39], v[236:239], v[188:191]\n"
        "ds_read_b128 v[228:231], v248 offset:42240\n"
        "s_waitcnt vmcnt(5)\n"
        "s_waitcnt lgkmcnt(6)\n"
        "v_mfma_i32_16x16x64_i8 v[128:131], v[40:43], v[208:211], v[128:131]\n"
        "v_mfma_i32_16x16x64_i8 v[132:135], v[44:47], v[208:211], v[132:135]\n"
        "ds_read_b128 v[232:235], v248 offset:41728\n"
        "s_waitcnt lgkmcnt(5)\n"
        "v_mfma_i32_16x16x64_i8 v[136:139], v[40:43], v[212:215], v[136:139]\n"
        "v_mfma_i32_16x16x64_i8 v[140:143], v[44:47], v[212:215], v[140:143]\n"
        "ds_read_b128 v[236:239], v248 offset:42752\n"
        "v_cmp_gt_u32 vcc, 7, v254\n"
        "s_waitcnt lgkmcnt(5)\n"
        "v_mfma_i32_16x16x64_i8 v[144:147], v[40:43], v[216:219], v[144:147]\n"
        "s_cbranch_vccnz W8_s6\n"
        "W8_c6:\n"
        "v_mfma_i32_16x16x64_i8 v[148:151], v[44:47], v[216:219], v[148:151]\n"
        "ds_read_b128 v[208:211], v248 offset:49152\n"
        "s_waitcnt lgkmcnt(5)\n"
        "v_mfma_i32_16x16x64_i8 v[152:155], v[40:43], v[220:223], v[152:155]\n"
        "ds_read_b32 v254, v250 offset:0\n"
        "v_mfma_i32_16x16x64_i8 v[156:159], v[44:47], v[220:223], v[156:159]\n"
        "ds_read_b128 v[212:215], v248 offset:50176\n"
        "s_waitcnt lgkmcnt(6)\n"
        "v_mfma_i32_16x16x64_i8 v[160:163], v[40:43], v[224:227], v[160:163]\n"
        "global_load_dwordx4 v[68:71], v240, s[56:57] offset:1024 sc1\n"
        "v_mfma_i32_16x16x64_i8 v[164:167], v[44:47], v[224:227], v[164:167]\n"
        "ds_read_b128 v[216:219], v248 offset:49664\n"
        "s_waitcnt lgkmcnt(6)\n"
        "v_mfma_i32_16x16x64_i8 v[168:171], v[40:43], v[228:231], v[168:171]\n"
        "v_mfma_i32_16x16x64_i8 v[172:175], v[44:47], v[228:231], v[172:175]\n"
        "ds_read_b128 v[220:223], v248 offset:50688\n"
        "s_waitcnt lgkmcnt(6)\n"
        "v_mfma_i32_16x16x64_i8 v[176:179], v[40:43], v[232:235], v[176:179]\n"
        "v_mfma_i32_16x16x64_i8 v[180:183], v[44:47], v[232:235], v[180:183]\n"
        "ds_read_b128 v[224:227], v248 offset:49408\n"
        "s_waitcnt lgkmcnt(6)\n"
        "v_mfma_i32_16x16x64_i8 v[184:187], v[40:43], v[236:239], v[184:187]\n"
        "global_load_dwordx4 v[72:75], v240, s[58:59] offset:0 sc1\n"
        "v_mfma_i32_16x16x64_i8 v[188:191], v[44:47], v[236:239], v[188:191]\n"
        "ds_read_b128 v[228:231], v248 offset:50432\n"
        "s_waitcnt vmcnt(5)\n"
        "s_waitcnt lgkmcnt(6)\n"
        "v_mfma_i32_16x16x64_i8 v[128:131], v[48:51], v[208:211], v[128:131]\n"
        "v_mfma_i32_16x16x64_i8 v[132:135], v[52:55], v[208:211], v[132:135]\n"
        "ds_read_b128 v[232:235], v248 offset:49920\n"
        "s_waitcnt lgkmcnt(5)\n"
        "v_mfma_i32_16x16x64_i8 v[136:139], v[48:51], v[212:215], v[136:139]\n"
        "v_mfma_i32_16x16x64_i8 v[140:143], v[52:55], v[212:215], v[140:143]\n"
        "ds_read_b128 v[236:239], v248 offset:50944\n"
        "v_cmp_gt_u32 vcc, 8, v254\n"
        "s_waitcnt lgkmcnt(5)\n"
        "v_mfma_i32_16x16x64_i8 v[144:147], v[48:51], v[216:219], v[144:147]\n"
        "s_cbranch_vccnz W8_s7\n"
        "W8_c7:\n"
        "v_mfma_i32_16x16x64_i8 v[148:151], v[52:55], v[216:219], v[148:151]\n"
        "ds_read_b128 v[208:211], v248 offset:57344\n"
        "s_waitcnt lgkmcnt(5)\n"
        "v_mfma_i32_16x16x64_i8 v[152:155], v[48:51], v[220:223], v[152:155]\n"
        "ds_read_b32 v254, v250 offset:0\n"
        "v_mfma_i32_16x16x64_i8 v[156:159], v[52:55], v[220:223], v[156:159]\n"
        "ds_read_b128 v[212:215], v248 offset:58368\n"
        "s_waitcnt lgkmcnt(6)\n"
        "v_mfma_i32_16x16x64_i8 v[160:163], v[48:51], v[224:227], v[160:163]\n"
        "global_load_dwordx4 v[76:79], v240, s[58:59] offset:1024 sc1\n"
        "v_mfma_i32_16x16x64_i8 v[164:167], v[52:55], v[224:227], v[164:167]\n"
        "ds_read_b128 v[216:219], v248 offset:57856\n"
        "s_waitcnt lgkmcnt(6)\n"
        "v_mfma_i32_16x16x64_i8 v[168:171], v[48:51], v[228:231], v[168:171]\n"
        "v_mfma_i32_16x16x64_i8 v[172:175], v[52:55], v[228:231], v[172:175]\n"
        "ds_read_b128 v[220:223], v248 offset:58880\n"
        "s_waitcnt lgkmcnt(6)\n"
        "v_mfma_i32_16x16x64_i8 v[176:179], v[48:51], v[232:235], v[176:179]\n"
        "v_mfma_i32_16x16x64_i8 v[180:183], v[52:55], v[232:235], v[180:183]\n"
        "ds_read_b128 v[224:227], v248 offset:57600\n"
        "s_waitcnt lgkmcnt(6)\n"
        "v_mfma_i32_16x16x64_i8 v[184:187], v[48:51], v[236:239], v[184:187]\n"
        "global_load_dwordx4 v[80:83], v240, s[60:61] offset:0 sc1\n"
        "v_mfma_i32_16x16x64_i8 v[188:191], v[52:55], v[236:239], v[188:191]\n"
        "ds_read_b128 v[228:231], v248 offset:58624\n"
        "s_waitcnt vmcnt(5)\n"
        "s_waitcnt lgkmcnt(6)\n"
        "v_mfma_i32_16x16x64_i8 v[128:131], v[56:59], v[208:211], v[128:131]\n"
        "v_mfma_i32_16x16x64_i8 v[132:135], v[60:63], v[208:211], v[132:135]\n"
        "ds_read_b128 v[232:235], v248 offset:58112\n"
        "s_waitcnt lgkmcnt(5)\n"
        "v_mfma_i32_16x16x64_i8 v[136:139], v[56:59], v[212:215], v[136:139]\n"
        "v_mfma_i32_16x16x64_i8 v[140:143], v[60:63], v[212:215], v[140:143]\n"
        "ds_read_b128 v[236:239], v248 offset:59136\n"
        "v_cmp_gt_u32 vcc, 9, v254\n"
        "s_waitcnt lgkmcnt(5)\n"
        "v_mfma_i32_16x16x64_i8 v[144:147], v[56:59], v[216:219], v[144:147]\n"
        "s_cbranch_vccnz W8_s8\n"
        "W8_c8:\n"
        "v_mfma_i32_16x16x64_i8 v[148:151], v[60:63], v[216:219], v[148:151]\n"
        "ds_read_b128 v[208:211], v249 offset:0\n"
        "s_waitcnt lgkmcnt(5)\n"
        "v_mfma_i32_16x16x64_i8 v[152:155], v[56:59], v[220:223], v[152:155]\n"
        "ds_read_b32 v254, v250 offset:0\n"
        "v_mfma_i32_16x16x64_i8 v[156:159], v[60:63], v[220:223], v[156:159]\n"
        "ds_read_b128 v[212:215], v249 offset:1024\n"
        "s_waitcnt lgkmcnt(6)\n"
        "v_mfma_i32_16x16x64_i8 v[160:163], v[56:59], v[224:227], v[160:163]\n"
        "ds_read_b32 v241, v250 offset:4\n"
        "s_mov_b64 exec, 1\n"
        "ds_add_u32 v250, v250 offset:520\n"
        "s_mov_b64 exec, -1\n"
        "v_mfma_i32_16x16x64_i8 v[164:167], v[60:63], v[224:227], v[164:167]\n"
        "ds_read_b128 v[216:219], v249 offset:512\n"
        "s_waitcnt lgkmcnt(8)\n"
        "v_mfma_i32_16x16x64_i8 v[168:171], v[56:59], v[228:231], v[168:171]\n"
        "global_load_dwordx4 v[84:87], v240, s[60:61] offset:1024 sc1\n"
        "v_mfma_i32_16x16x64_i8 v[172:175], v[60:63], v[228:231], v[172:175]\n"
        "ds_read_b128 v[220:223], v249 offset:1536\n"
        "s_waitcnt lgkmcnt(8)\n"
        "v_mfma_i32_16x16x64_i8 v[176:179], v[56:59], v[232:235], v[176:179]\n"
        "v_mfma_i32_16x16x64_i8 v[180:183], v[60:63], v[232:235], v[180:183]\n"
        "ds_read_b128 v[224:227], v249 offset:256\n"
        "s_waitcnt lgkmcnt(8)\n"
        "v_mfma_i32_16x16x64_i8 v[184:187], v[56:59], v[236:239], v[184:187]\n"
        "v_mfma_i32_16x16x64_i8 v[188:191], v[60:63], v[236:239], v[188:191]\n"
        "ds_read_b128 v[228:231], v249 offset:1280\n"
        "s_waitcnt vmcnt(4)\n"
        "s_waitcnt lgkmcnt(8)\n"
        "v_mfma_i32_16x16x64_i8 v[128:131], v[64:67], v[208:211], v[128:131]\n"
        "global_load_dwordx4 v[88:91], v240, s[62:63] offset:0 sc1\n"
        "v_mfma_i32_16x16x64_i8 v[132:135], v[68:71], v[208:211], v[132:135]\n"
        "ds_read_b128 v[232:235], v249 offset:768\n"
        "s_waitcnt lgkmcnt(7)\n"
        "v_mfma_i32_16x16x64_i8 v[136:139], v[64:67], v[212:215], v[136:139]\n"
        "v_mfma_i32_16x16x64_i8 v[140:143], v[68:71], v[212:215], v[140:143]\n"
        "ds_read_b128 v[236:239], v249 offset:1792\n"
        "v_cmp_gt_u32 vcc, 10, v254\n"
        "s_waitcnt lgkmcnt(5)\n"
        "v_mfma_i32_16x16x64_i8 v[144:147], v[64:67], v[216:219], v[144:147]\n"
        "s_cbranch_vccnz W8_s9\n"
        "W8_c9:\n"
        "v_mfma_i32_16x16x64_i8 v[148:151], v[68:71], v[216:219], v[148:151]\n"
        "v_cmp_gt_u32 vcc, 16, v241\n"
        "ds_read_b128 v[208:211], v249 offset:8192\n"
        "s_waitcnt lgkmcnt(5)\n"
        "v_mfma_i32_16x16x64_i8 v[152:155], v[64:67], v[220:223], v[152:155]\n"
        "s_cbranch_vccnz W8_sR2\n"
        "W8_cR2:\n"
        "ds_read_b32 v254, v250 offset:0\n"
        "v_mfma_i32_16x16x64_i8 v[156:159], v[68:71], v[220:223], v[156:159]\n"
        "ds_read_b128 v[212:215], v249 offset:9216\n"
        "s_waitcnt lgkmcnt(6)\n"
        "v_mfma_i32_16x16x64_i8 v[160:163], v[64:67], v[224:227], v[160:163]\n"
        "global_load_dwordx4 v[92:95], v240, s[62:63] offset:1024 sc1\n"
        "v_add_u32 v240, 8192, v240\n"
        "v_mfma_i32_16x16x64_i8 v[164:167], v[68:71], v[224:227], v[164:167]\n"
        "ds_read_b128 v[216:219], v249 offset:8704\n"
        "s_waitcnt lgkmcnt(6)\n"
        "v_mfma_i32_16x16x64_i8 v[168:171], v[64:67], v[228:231], v[168:171]\n"
        "v_mfma_i32_16x16x64_i8 v[172:175], v[68:71], v[228:231], v[172:175]\n"
        "ds_read_b128 v[220:223], v249 offset:9728\n"
        "s_waitcnt lgkmcnt(6)\n"
        "v_mfma_i32_16x16x64_i8 v[176:179], v[64:67], v[232:235], v[176:179]\n"
        "v_mfma_i32_16x16x64_i8 v[180:183], v[68:71], v[232:235], v[180:183]\n"
        "ds_read_b128 v[224:227], v249 offset:8448\n"
        "s_waitcnt lgkmcnt(6)\n"
        "v_mfma_i32_16x16x64_i8 v[184:187], v[64:67], v[236:239], v[184:187]\n"
        "global_load_dwordx4 v[96:99], v240, s[56:57] offset:0 sc1\n"
        "v_mfma_i32_16x16x64_i8 v[188:191], v[68:71], v[236:239], v[188:191]\n"
        "ds_read_b128 v[228:231], v249 offset:9472\n"
        "s_waitcnt vmcnt(5)\n"
        "s_waitcnt lgkmcnt(6)\n"
        "v_mfma_i32_16x16x64_i8 v[128:131], v[72:75], v[208:211], v[128:131]\n"
        "v_mfma_i32_16x16x64_i8 v[132:135], v[76:79], v[208:211], v[132:135]\n"
        "ds_read_b128 v[232:235], v249 offset:8960\n"
        "s_waitcnt lgkmcnt(5)\n"
        "v_mfma_i32_16x16x64_i8 v[136:139], v[72:75], v[212:215], v[136:139]\n"
        "v_mfma_i32_16x16x64_i8 v[140:143], v[76:79], v[212:215], v[140:143]\n"
        "ds_read_b128 v[236:239], v249 offset:9984\n"
        "v_cmp_gt_u32 vcc, 11, v254\n"
        "s_waitcnt lgkmcnt(5)\n"
        "v_mfma_i32_16x16x64_i8 v[144:147], v[72:75], v[216:219], v[144:147]\n"
        "s_cbranch_vccnz W8_s10\n"
        "W8_c10:\n"
        "v_mfma_i32_16x16x64_i8 v[148:151], v[76:79], v[216:219], v[148:151]\n"
        "ds_read_b128 v[208:211], v249 offset:16384\n"
        "s_waitcnt lgkmcnt(5)\n"
        "v_mfma_i32_16x16x64_i8 v[152:155], v[72:75], v[220:223], v[152:155]\n"
        "ds_read_b32 v254, v250 offset:0\n"
        "v_mfma_i32_16x16x64_i8 v[156:159], v[76:79], v[220:223], v[156:159]\n"
        "ds_read_b128 v[212:215], v249 offset:17408\n"
        "s_waitcnt lgkmcnt(6)\n"
        "v_mfma_i32_16x16x64_i8 v[160:163], v[72:75], v[224:227], v[160:163]\n"
        "global_load_dwordx4 v[100:103], v240, s[56:57] offset:1024 sc1\n"
        "v_mfma_i32_16x16x64_i8 v[164:167], v[76:79], v[224:227], v[164:167]\n"
        "ds_read_b128 v[216:219], v249 offset:16896\n"
        "s_waitcnt lgkmcnt(6)\n"
        "v_mfma_i32_16x16x64_i8 v[168:171], v[72:75], v[228:231], v[168:171]\n"
        "v_mfma_i32_16x16x64_i8 v[172:175], v[76:79], v[228:231], v[172:175]\n"
        "ds_read_b128 v[220:223], v249 offset:17920\n"
        "s_waitcnt lgkmcnt(6)\n"
        "v_mfma_i32_16x16x64_i8 v[176:179], v[72:75], v[232:235], v[176:179]\n"
        "v_mfma_i32_16x16x64_i8 v[180:183], v[76:79], v[232:235], v[180:183]\n"
        "ds_read_b128 v[224:227], v249 offset:16640\n"
        "s_waitcnt lgkmcnt(6)\n"
        "v_mfma_i32_16x16x64_i8 v[184:187], v[72:75], v[236:239], v[184:187]\n"
        "global_load_dwordx4 v[104:107], v240, s[58:59] offset:0 sc1\n"
        "v_mfma_i32_16x16x64_i8 v[188:191], v[76:79], v[236:239], v[188:191]\n"
        "ds_read_b128 v[228:231], v249 offset:17664\n"
        "s_waitcnt vmcnt(5)\n"
        "s_waitcnt lgkmcnt(6)\n"
        "v_mfma_i32_16x16x64_i8 v[128:131], v[80:83], v[208:211], v[128:131]\n"
        "v_mfma_i32_16x16x64_i8 v[132:135], v[84:87], v[208:211], v[132:135]\n"
        "ds_read_b128 v[232:235], v249 offset:17152\n"
        "s_waitcnt lgkmcnt(5)\n"
        "v_mfma_i32_16x16x64_i8 v[136:139], v[80:83], v[212:215], v[136:139]\n"
        "v_mfma_i32_16x16x64_i8 v[140:143], v[84:87], v[212:215], v[140:143]\n"
        "ds_read_b128 v[236:239], v249 offset:18176\n"
        "v_cmp_gt_u32 vcc, 12, v254\n"
        "s_waitcnt lgkmcnt(5)\n"
        "v_mfma_i32_16x16x64_i8 v[144:147], v[80:83], v[216:219], v[144:147]\n"
        "s_cbranch_vccnz W8_s11\n"
        "W8_c11:\n"
        "v_mfma_i32_16x16x64_i8 v[148:151], v[84:87], v[216:219], v[148:151]\n"
        "ds_read_b128 v[208:211], v249 offset:24576\n"
        "s_waitcnt lgkmcnt(5)\n"
        "v_mfma_i32_16x16x64_i8 v[152:155], v[80:83], v[220:223], v[152:155]\n"
        "ds_read_b32 v254, v250 offset:0\n"
        "v_mfma_i32_16x16x64_i8 v[156:159], v[84:87], v[220:223], v[156:159]\n"
        "ds_read_b128 v[212:215], v249 offset:25600\n"
        "s_waitcnt lgkmcnt(6)\n"
        "v_mfma_i32_16x16x64_i8 v[160:163], v[80:83], v[224:227], v[160:163]\n"
        "global_load_dwordx4 v[108:111], v240, s[58:59] offset:1024 sc1\n"
        "v_mfma_i32_16x16x64_i8 v[164:167], v[84:87], v[224:227], v[164:167]\n"
        "ds_read_b128 v[216:219], v249 offset:25088\n"
        "s_waitcnt lgkmcnt(6)\n"
        "v_mfma_i32_16x16x64_i8 v[168:171], v[80:83], v[228:231], v[168:171]\n"
        "v_mfma_i32_16x16x64_i8 v[172:175], v[84:87], v[228:231], v[172:175]\n"
        "ds_read_b128 v[220:223], v249 offset:26112\n"
        "s_waitcnt lgkmcnt(6)\n"
        "v_mfma_i32_16x16x64_i8 v[176:179], v[80:83], v[232:235], v[176:179]\n"
        "v_mfma_i32_16x16x64_i8 v[180:183], v[84:87], v[232:235], v[180:183]\n"
        "ds_read_b128 v[224:227], v249 offset:24832\n"
        "s_waitcnt lgkmcnt(6)\n"
        "v_mfma_i32_16x16x64_i8 v[184:187], v[80:83], v[236:239], v[184:187]\n"
        "global_load_dwordx4 v[112:115], v240, s[60:61] offset:0 sc1\n"
        "v_mfma_i32_16x16x64_i8 v[188:191], v[84:87], v[236:239], v[188:191]\n"
        "ds_read_b128 v[228:231], v249 offset:25856\n"
        "s_waitcnt vmcnt(5)\n"
        "s_waitcnt lgkmcnt(6)\n"
        "v_mfma_i32_16x16x64_i8 v[128:131], v[88:91], v[208:211], v[128:131]\n"
        "v_mfma_i32_16x16x64_i8 v[132:135], v[92:95], v[208:211], v[132:135]\n"
        "ds_read_b128 v[232:235], v249 offset:25344\n"
        "s_waitcnt lgkmcnt(5)\n"
        "v_mfma_i32_16x16x64_i8 v[136:139], v[88:91], v[212:215], v[136:139]\n"
        "v_mfma_i32_16x16x64_i8 v[140:143], v[92:95], v[212:215], v[140:143]\n"
        "ds_read_b128 v[236:239], v249 offset:26368\n"
        "v_cmp_gt_u32 vcc, 13, v254\n"
        "s_waitcnt lgkmcnt(5)\n"
        "v_mfma_i32_16x16x64_i8 v[144:147], v[88:91], v[216:219], v[144:147]\n"
        "s_cbranch_vccnz W8_s12\n"
        "W8_c12:\n"
        "v_mfma_i32_16x16x64_i8 v[148:151], v[92:95], v[216:219], v[148:151]\n"
        "ds_read_b128 v[208:211], v249 offset:32768\n"
        "s_waitcnt lgkmcnt(5)\n"
        "v_mfma_i32_16x16x64_i8 v[152:155], v[88:91], v[220:223], v[152:155]\n"
        "ds_read_b32 v254, v250 offset:0\n"
        "v_mfma_i32_16x16x64_i8 v[156:159], v[92:95], v[220:223], v[156:159]\n"
        "ds_read_b128 v[212:215], v249 offset:33792\n"
        "s_waitcnt lgkmcnt(6)\n"
        "v_mfma_i32_16x16x64_i8 v[160:163], v[88:91], v[224:227], v[160:163]\n"
        "s_mov_b64 exec, 1\n"
        "ds_add_u32 v250, v250 offset:524\n"
        "s_mov_b64 exec, -1\n"
        "v_mfma_i32_16x16x64_i8 v[164:167], v[92:95], v[224:227], v[164:167]\n"
        "ds_read_b128 v[216:219], v249 offset:33280\n"
        "s_waitcnt lgkmcnt(7)\n"
        "v_mfma_i32_16x16x64_i8 v[168:171], v[88:91], v[228:231], v[168:171]\n"
        "global_load_dwordx4 v[116:119], v240, s[60:61] offset:1024 sc1\n"
        "v_mfma_i32_16x16x64_i8 v[172:175], v[92:95], v[228:231], v[172:175]\n"
        "ds_read_b128 v[220:223], v249 offset:34304\n"
        "s_waitcnt lgkmcnt(7)\n"
        "v_mfma_i32_16x16x64_i8 v[176:179], v[88:91], v[232:235], v[176:179]\n"
        "v_mfma_i32_16x16x64_i8 v[180:183], v[92:95], v[232:235], v[180:183]\n"
        "ds_read_b128 v[224:227], v249 offset:33024\n"
        "s_waitcnt lgkmcnt(7)\n"
        "v_mfma_i32_16x16x64_i8 v[184:187], v[88:91], v[236:239], v[184:187]\n"
        "v_mfma_i32_16x16x64_i8 v[188:191], v[92:95], v[236:239], v[188:191]\n"
        "ds_read_b128 v[228:231], v249 offset:34048\n"
        "s_waitcnt vmcnt(4)\n"
        "s_waitcnt lgkmcnt(7)\n"
        "v_mfma_i32_16x16x64_i8 v[128:131], v[96:99], v[208:211], v[128:131]\n"
        "global_load_dwordx4 v[120:123], v240, s[62:63] offset:0 sc1\n"
        "v_mfma_i32_16x16x64_i8 v[132:135], v[100:103], v[208:211], v[132:135]\n"
        "ds_read_b128 v[232:235], v249 offset:33536\n"
        "s_waitcnt lgkmcnt(6)\n"
        "v_mfma_i32_16x16x64_i8 v[136:139], v[96:99], v[212:215], v[136:139]\n"
        "v_and_b32 v248, 0x3ff, v240\n"
        "v_mfma_i32_16x16x64_i8 v[140:143], v[100:103], v[212:215], v[140:143]\n"
        "ds_read_b128 v[236:239], v249 offset:34560\n"
        "v_cmp_gt_u32 vcc, 14, v254\n"
        "s_waitcnt lgkmcnt(5)\n"
        "v_mfma_i32_16x16x64_i8 v[144:147], v[96:99], v[216:219], v[144:147]\n"
        "s_cbranch_vccnz W8_s13\n"
        "W8_c13:\n"
        "v_mfma_i32_16x16x64_i8 v[148:151], v[100:103], v[216:219], v[148:151]\n"
        "ds_read_b128 v[208:211], v249 offset:40960\n"
        "s_waitcnt lgkmcnt(5)\n"
        "v_mfma_i32_16x16x64_i8 v[152:155], v[96:99], v[220:223], v[152:155]\n"
        "ds_read_b32 v254, v250 offset:0\n"
        "v_mfma_i32_16x16x64_i8 v[156:159], v[100:103], v[220:223], v[156:159]\n"
        "ds_read_b128 v[212:215], v249 offset:41984\n"
        "s_waitcnt lgkmcnt(6)\n"
        "v_mfma_i32_16x16x64_i8 v[160:163], v[96:99], v[224:227], v[160:163]\n"
        "global_load_dwordx4 v[124:127], v240, s[62:63] offset:1024 sc1\n"
        "v_add_u32 v240, 8192, v240\n"
        "v_mfma_i32_16x16x64_i8 v[164:167], v[100:103], v[224:227], v[164:167]\n"
        "ds_read_b128 v[216:219], v249 offset:41472\n"
        "s_waitcnt lgkmcnt(6)\n"
        "v_mfma_i32_16x16x64_i8 v[168:171], v[96:99], v[228:231], v[168:171]\n"
        "v_mfma_i32_16x16x64_i8 v[172:175], v[100:103], v[228:231], v[172:175]\n"
        "ds_read_b128 v[220:223], v249 offset:42496\n"
        "s_waitcnt lgkmcnt(6)\n"
        "v_mfma_i32_16x16x64_i8 v[176:179], v[96:99], v[232:235], v[176:179]\n"
        "v_mfma_i32_16x16x64_i8 v[180:183], v[100:103], v[232:235], v[180:183]\n"
        "ds_read_b128 v[224:227], v249 offset:41216\n"
        "s_waitcnt lgkmcnt(6)\n"
        "v_mfma_i32_16x16x64_i8 v[184:187], v[96:99], v[236:239], v[184:187]\n"
        "v_mfma_i32_16x16x64_i8 v[188:191], v[100:103], v[236:239], v[188:191]\n"
        "ds_read_b128 v[228:231], v249 offset:42240\n"
        "s_waitcnt vmcnt(4)\n"
        "s_waitcnt lgkmcnt(6)\n"
        "v_mfma_i32_16x16x64_i8 v[128:131], v[104:107], v[208:211], v[128:131]\n"
        "v_mfma_i32_16x16x64_i8 v[132:135], v[108:111], v[208:211], v[132:135]\n"
        "ds_read_b128 v[232:235], v249 offset:41728\n"
        "s_waitcnt lgkmcnt(5)\n"
        "v_mfma_i32_16x16x64_i8 v[136:139], v[104:107], v[212:215], v[136:139]\n"
        "v_mfma_i32_16x16x64_i8 v[140:143], v[108:111], v[212:215], v[140:143]\n"
        "ds_read_b128 v[236:239], v249 offset:42752\n"
        "v_cmp_gt_u32 vcc, 15, v254\n"
        "s_waitcnt lgkmcnt(5)\n"
        "v_mfma_i32_16x16x64_i8 v[144:147], v[104:107], v[216:219], v[144:147]\n"
        "s_cbranch_vccnz W8_s14\n"
        "W8_c14:\n"
        "v_mfma_i32_16x16x64_i8 v[148:151], v[108:111], v[216:219], v[148:151]\n"
        "ds_read_b128 v[208:211], v249 offset:49152\n"
        "s_waitcnt lgkmcnt(5)\n"
        "v_mfma_i32_16x16x64_i8 v[152:155], v[104:107], v[220:223], v[152:155]\n"
        "ds_read_b32 v254, v250 offset:0\n"
        "v_mfma_i32_16x16x64_i8 v[156:159], v[108:111], v[220:223], v[156:159]\n"
        "ds_read_b128 v[212:215], v249 offset:50176\n"
        "s_waitcnt lgkmcnt(6)\n"
        "v_mfma_i32_16x16x64_i8 v[160:163], v[104:107], v[224:227], v[160:163]\n"
        "v_mfma_i32_16x16x64_i8 v[164:167], v[108:111], v[224:227], v[164:167]\n"
        "ds_read_b128 v[216:219], v249 offset:49664\n"
        "s_waitcnt lgkmcnt(6)\n"
        "v_mfma_i32_16x16x64_i8 v[168:171], v[104:107], v[228:231], v[168:171]\n"
        "v_mfma_i32_16x16x64_i8 v[172:175], v[108:111], v[228:231], v[172:175]\n"
        "ds_read_b128 v[220:223], v249 offset:50688\n"
        "s_waitcnt lgkmcnt(6)\n"
        "v_mfma_i32_16x16x64_i8 v[176:179], v[104:107], v[232:235], v[176:179]\n"
        "v_mfma_i32_16x16x64_i8 v[180:183], v[108:111], v[232:235], v[180:183]\n"
        "ds_read_b128 v[224:227], v249 offset:49408\n"
        "s_waitcnt lgkmcnt(6)\n"
        "v_mfma_i32_16x16x64_i8 v[184:187], v[104:107], v[236:239], v[184:187]\n"
        "v_mfma_i32_16x16x64_i8 v[188:191], v[108:111], v[236:239], v[188:191]\n"
        "ds_read_b128 v[228:231], v249 offset:50432\n"
        "s_waitcnt vmcnt(2)\n"
        "s_waitcnt lgkmcnt(6)\n"
        "v_mfma_i32_16x16x64_i8 v[128:131], v[112:115], v[208:211], v[128:131]\n"
        "v_mfma_i32_16x16x64_i8 v[132:135], v[116:119], v[208:211], v[132:135]\n"
        "ds_read_b128 v[232:235], v249 offset:49920\n"
        "s_waitcnt lgkmcnt(5)\n"
        "v_mfma_i32_16x16x64_i8 v[136:139], v[112:115], v[212:215], v[136:139]\n"
        "v_mfma_i32_16x16x64_i8 v[140:143], v[116:119], v[212:215], v[140:143]\n"
        "ds_read_b128 v[236:239], v249 offset:50944\n"
        "v_cmp_gt_u32 vcc, 16, v254\n"
        "s_waitcnt lgkmcnt(5)\n"
        "v_mfma_i32_16x16x64_i8 v[144:147], v[112:115], v[216:219], v[144:147]\n"
        "s_cbranch_vccnz W8_s15\n"
        "W8_c15:\n"
        "v_mfma_i32_16x16x64_i8 v[148:151], v[116:119], v[216:219], v[148:151]\n"
        "ds_read_b128 v[208:211], v249 offset:57344\n"
        "s_waitcnt lgkmcnt(5)\n"
        "v_mfma_i32_16x16x64_i8 v[152:155], v[112:115], v[220:223], v[152:155]\n"
        "ds_read_b32 v254, v250 offset:0\n"
        "v_mfma_i32_16x16x64_i8 v[156:159], v[116:119], v[220:223], v[156:159]\n"
        "ds_read_b128 v[212:215], v249 offset:58368\n"
        "s_waitcnt lgkmcnt(6)\n"
        "v_mfma_i32_16x16x64_i8 v[160:163], v[112:115], v[224:227], v[160:163]\n"
        "v_mfma_i32_16x16x64_i8 v[164:167], v[116:119], v[224:227], v[164:167]\n"
        "ds_read_b128 v[216:219], v249 offset:57856\n"
        "s_waitcnt lgkmcnt(6)\n"
        "v_mfma_i32_16x16x64_i8 v[168:171], v[112:115], v[228:231], v[168:171]\n"
        "ds_read_b32 v255, v250 offset:260\n"
        "v_mfma_i32_16x16x64_i8 v[172:175], v[116:119], v[228:231], v[172:175]\n"
        "ds_read_b128 v[220:223], v249 offset:58880\n"
        "s_waitcnt lgkmcnt(7)\n"
        "v_mfma_i32_16x16x64_i8 v[176:179], v[112:115], v[232:235], v[176:179]\n"
        "v_mfma_i32_16x16x64_i8 v[180:183], v[116:119], v[232:235], v[180:183]\n"
        "ds_read_b128 v[224:227], v249 offset:57600\n"
        "s_waitcnt lgkmcnt(7)\n"
        "v_mfma_i32_16x16x64_i8 v[184:187], v[112:115], v[236:239], v[184:187]\n"
        "v_mfma_i32_16x16x64_i8 v[188:191], v[116:119], v[236:239], v[188:191]\n"
        "ds_read_b128 v[228:231], v249 offset:58624\n"
        "s_waitcnt vmcnt(0)\n"
        "s_waitcnt lgkmcnt(5)\n"
        "v_mfma_i32_16x16x64_i8 v[128:131], v[120:123], v[208:211], v[128:131]\n"
        "v_mfma_i32_16x16x64_i8 v[132:135], v[124:127], v[208:211], v[132:135]\n"
        "ds_read_b128 v[232:235], v249 offset:58112\n"
        "v_mfma_i32_16x16x64_i8 v[136:139], v[120:123], v[212:215], v[136:139]\n"
        "v_mfma_i32_16x16x64_i8 v[140:143], v[124:127], v[212:215], v[140:143]\n"
        "ds_read_b128 v[236:239], v249 offset:59136\n"
        "v_cmp_gt_u32 vcc, 18, v254\n"
        "s_waitcnt lgkmcnt(4)\n"
        "v_mfma_i32_16x16x64_i8 v[144:147], v[120:123], v[216:219], v[144:147]\n"
        "s_cbranch_vccnz W8_s16\n"
        "W8_c16:\n"
        "v_mfma_i32_16x16x64_i8 v[148:151], v[124:127], v[216:219], v[148:151]\n"
        "ds_read_b128 v[208:211], v248 offset:0\n"
        "v_mfma_i32_16x16x64_i8 v[152:155], v[120:123], v[220:223], v[152:155]\n"
        "v_mfma_i32_16x16x64_i8 v[156:159], v[124:127], v[220:223], v[156:159]\n"
        "ds_read_b128 v[212:215], v248 offset:1024\n"
        "s_waitcnt lgkmcnt(4)\n"
        "v_mfma_i32_16x16x64_i8 v[160:163], v[120:123], v[224:227], v[160:163]\n"
        "v_mfma_i32_16x16x64_i8 v[164:167], v[124:127], v[224:227], v[164:167]\n"
        "ds_read_b128 v[216:219], v248 offset:2048\n"
        "v_cmp_gt_u32 vcc, 2, v255\n"
        "v_mfma_i32_16x16x64_i8 v[168:171], v[120:123], v[228:231], v[168:171]\n"
        "v_add_u32 v249, 65536, v248\n"
        "s_cbranch_vccnz W8_sT\n"
        "W8_cT:\n"
        "v_mfma_i32_16x16x64_i8 v[172:175], v[124:127], v[228:231], v[172:175]\n"
        "ds_read_b128 v[220:223], v248 offset:3072\n"
        "s_waitcnt lgkmcnt(4)\n"
        "v_mfma_i32_16x16x64_i8 v[176:179], v[120:123], v[232:235], v[176:179]\n"
        "ds_read_b128 v[240:243], v253\n"
        "ds_read_b128 v[244:247], v253 offset:16\n"
        "v_mfma_i32_16x16x64_i8 v[180:183], v[124:127], v[232:235], v[180:183]\n"
        "ds_read_b128 v[224:227], v248 offset:4096\n"
        "v_mfma_i32_16x16x64_i8 v[184:187], v[120:123], v[236:239], v[184:187]\n"
        "v_cvt_f32_i32 v128, v128\n"
        "v_cvt_f32_i32 v129, v129\n"
        "v_cvt_f32_i32 v130, v130\n"
        "v_mfma_i32_16x16x64_i8 v[188:191], v[124:127], v[236:239], v[188:191]\n"
        "ds_read_b128 v[228:231], v248 offset:5120\n"
        "s_waitcnt lgkmcnt(6)\n"
        "v_mfma_i32_16x16x64_i8 v[192:195], v[0:3], v[208:211], 0\n"
        "v_cvt_f32_i32 v131, v131\n"
        "v_cvt_f32_i32 v136, v136\n"
        "v_mfma_i32_16x16x64_i8 v[196:199], v[4:7], v[208:211], 0\n"
        "ds_read_b128 v[232:235], v248 offset:6144\n"
        "v_cvt_f32_i32 v137, v137\n"
        "v_cvt_f32_i32 v138, v138\n"
        "v_mfma_i32_16x16x64_i8 v[200:203], v[0:3], v[212:215], 0\n"
        "v_cvt_f32_i32 v139, v139\n"
        "v_cvt_f32_i32 v144, v144\n"
        "v_mfma_i32_16x16x64_i8 v[204:207], v[4:7], v[212:215], 0\n"
        "ds_read_b128 v[236:239], v248 offset:7168\n"
        "s_waitcnt lgkmcnt(6)\n"
        "v_mfma_i32_16x16x64_i8 v[192:195], v[8:11], v[216:219], v[192:195]\n"
        "v_cvt_f32_i32 v145, v145\n"
        "v_cvt_f32_i32 v146, v146\n"
        "v_mfma_i32_16x16x64_i8 v[196:199], v[12:15], v[216:219], v[196:199]\n"
        "ds_read_b128 v[208:211], v248 offset:8192\n"
        "v_cvt_f32_i32 v147, v147\n"
        "v_cvt_f32_i32 v152, v152\n"
        "v_mfma_i32_16x16x64_i8 v[200:203], v[8:11], v[220:223], v[200:203]\n"
        "ds_read_b32 v254, v250 offset:0\n"
        "v_cvt_f32_i32 v153, v153\n"
        "v_mfma_i32_16x16x64_i8 v[204:207], v[12:15], v[220:223], v[204:207]\n"
        "ds_read_b128 v[212:215], v248 offset:9216\n"
        "s_waitcnt lgkmcnt(5)\n"
        "v_mfma_i32_16x16x64_i8 v[192:195], v[16:19], v[224:227], v[192:195]\n"
        "v_cvt_f32_i32 v154, v154\n"
        "v_cvt_f32_i32 v155, v155\n"
        "v_mfma_i32_16x16x64_i8 v[196:199], v[20:23], v[224:227], v[196:199]\n"
        "ds_read_b128 v[216:219], v248 offset:10240\n"
        "v_fma_f32 v128, v128, v240, v241\n"
        "v_mfma_i32_16x16x64_i8 v[200:203], v[16:19], v[228:231], v[200:203]\n"
        "v_fma_f32 v129, v129, v240, v241\n"
        "v_fma_f32 v130, v130, v240, v241\n"
        "v_mfma_i32_16x16x64_i8 v[204:207], v[20:23], v[228:231], v[204:207]\n"
        "ds_read_b128 v[220:223], v248 offset:11264\n"
        "s_waitcnt lgkmcnt(5)\n"
        "v_mfma_i32_16x16x64_i8 v[192:195], v[24:27], v[232:235], v[192:195]\n"
        "v_fma_f32 v131, v131, v240, v241\n"
        "v_fma_f32 v136, v136, v242, v243\n"
        "v_mfma_i32_16x16x64_i8 v[196:199], v[28:31], v[232:235], v[196:199]\n"
        "ds_read_b128 v[224:227], v248 offset:12288\n"
        "v_fma_f32 v137, v137, v242, v243\n"
        "v_fma_f32 v138, v138, v242, v243\n"
        "v_mfma_i32_16x16x64_i8 v[200:203], v[24:27], v[236:239], v[200:203]\n"
        "v_fma_f32 v139, v139, v242, v243\n"
        "v_fma_f32 v144, v144, v244, v245\n"
        "v_mfma_i32_16x16x64_i8 v[204:207], v[28:31], v[236:239], v[204:207]\n"
        "ds_read_b128 v[228:231], v248 offset:13312\n"
        "s_waitcnt lgkmcnt(4)\n"
        "v_mfma_i32_16x16x64_i8 v[192:195], v[32:35], v[208:211], v[192:195]\n"
        "v_fma_f32 v145, v145, v244, v245\n"
        "v_fma_f32 v146, v146, v244, v245\n"
        "v_mfma_i32_16x16x64_i8 v[196:199], v[36:39], v[208:211], v[196:199]\n"
        "ds_read_b128 v[232:235], v248 offset:14336\n"
        "v_fma_f32 v147, v147, v244, v245\n"
        "v_fma_f32 v152, v152, v246, v247\n"
        "v_mfma_i32_16x16x64_i8 v[200:203], v[32:35], v[212:215], v[200:203]\n"
        "v_fma_f32 v153, v153, v246, v247\n"
        "v_fma_f32 v154, v154, v246, v247\n"
        "v_mfma_i32_16x16x64_i8 v[204:207], v[36:39], v[212:215], v[204:207]\n"
        "ds_read_b128 v[236:239], v248 offset:15360\n"
        "v_cmp_gt_u32 vcc, 20, v254\n"
        "s_waitcnt lgkmcnt(4)\n"
        "v_mfma_i32_16x16x64_i8 v[192:195], v[40:43], v[216:219], v[192:195]\n"
        "s_cbranch_vccnz W8_s18\n"
        "W8_c18:\n"
        "v_fma_f32 v155, v155, v246, v247\n"
        "v_mfma_i32_16x16x64_i8 v[196:199], v[44:47], v[216:219], v[196:199]\n"
        "ds_read_b128 v[208:211], v248 offset:16384\n"
        "v_cvt_pk_bf16_f32 v128, v128, v136\n"
        "v_cvt_pk_bf16_f32 v136, v129, v137\n"
        "v_mfma_i32_16x16x64_i8 v[200:203], v[40:43], v[220:223], v[200:203]\n"
        "v_cvt_pk_bf16_f32 v130, v130, v138\n"
        "v_cvt_pk_bf16_f32 v138, v131, v139\n"
        "v_mfma_i32_16x16x64_i8 v[204:207], v[44:47], v[220:223], v[204:207]\n"
        "ds_read_b128 v[212:215], v248 offset:17408\n"
        "s_waitcnt lgkmcnt(4)\n"
        "v_mfma_i32_16x16x64_i8 v[192:195], v[48:51], v[224:227], v[192:195]\n"
        "v_cvt_pk_bf16_f32 v129, v144, v152\n"
        "global_store_dwordx2 v251, v[128:129], s[40:41] offset:0 sc1\n"
        "v_mfma_i32_16x16x64_i8 v[196:199], v[52:55], v[224:227], v[196:199]\n"
        "ds_read_b128 v[216:219], v248 offset:18432\n"
        "v_cvt_pk_bf16_f32 v137, v145, v153\n"
        "global_store_dwordx2 v251, v[136:137], s[42:43] offset:0 sc1\n"
        "v_mfma_i32_16x16x64_i8 v[200:203], v[48:51], v[228:231], v[200:203]\n"
        "v_cvt_pk_bf16_f32 v131, v146, v154\n"
        "global_store_dwordx2 v251, v[130:131], s[44:45] offset:0 sc1\n"
        "v_mfma_i32_16x16x64_i8 v[204:207], v[52:55], v[228:231], v[204:207]\n"
        "ds_read_b128 v[220:223], v248 offset:19456\n"
        "s_waitcnt lgkmcnt(4)\n"
        "v_mfma_i32_16x16x64_i8 v[192:195], v[56:59], v[232:235], v[192:195]\n"
        "v_cvt_pk_bf16_f32 v139, v147, v155\n"
        "global_store_dwordx2 v251, v[138:139], s[46:47] offset:0 sc1\n"
        "v_mfma_i32_16x16x64_i8 v[196:199], v[60:63], v[232:235], v[196:199]\n"
        "ds_read_b128 v[224:227], v248 offset:20480\n"
        "v_cvt_f32_i32 v132, v132\n"
        "v_cvt_f32_i32 v133, v133\n"
        "v_mfma_i32_16x16x64_i8 v[200:203], v[56:59], v[236:239], v[200:203]\n"
        "v_cvt_f32_i32 v134, v134\n"
        "v_cvt_f32_i32 v135, v135\n"
        "v_mfma_i32_16x16x64_i8 v[204:207], v[60:63], v[236:239], v[204:207]\n"
        "ds_read_b128 v[228:231], v248 offset:21504\n"
        "s_waitcnt lgkmcnt(4)\n"
        "v_mfma_i32_16x16x64_i8 v[192:195], v[64:67], v[208:211], v[192:195]\n"
        "v_cvt_f32_i32 v140, v140\n"
        "v_cvt_f32_i32 v141, v141\n"
        "v_mfma_i32_16x16x64_i8 v[196:199], v[68:71], v[208:211], v[196:199]\n"
        "ds_read_b128 v[232:235], v248 offset:22528\n"
        "v_cvt_f32_i32 v142, v142\n"
        "v_cvt_f32_i32 v143, v143\n"
        "v_mfma_i32_16x16x64_i8 v[200:203], v[64:67], v[212:215], v[200:203]\n"
        "v_cvt_f32_i32 v148, v148\n"
        "v_cvt_f32_i32 v149, v149\n"
        "v_mfma_i32_16x16x64_i8 v[204:207], v[68:71], v[212:215], v[204:207]\n"
        "ds_read_b128 v[236:239], v248 offset:23552\n"
        "s_waitcnt lgkmcnt(4)\n"
        "v_mfma_i32_16x16x64_i8 v[192:195], v[72:75], v[216:219], v[192:195]\n"
        "v_cvt_f32_i32 v150, v150\n"
        "v_cvt_f32_i32 v151, v151\n"
        "v_mfma_i32_16x16x64_i8 v[196:199], v[76:79], v[216:219], v[196:199]\n"
        "ds_read_b128 v[208:211], v248 offset:24576\n"
        "v_cvt_f32_i32 v156, v156\n"
        "v_cvt_f32_i32 v157, v157\n"
        "v_mfma_i32_16x16x64_i8 v[200:203], v[72:75], v[220:223], v[200:203]\n"
        "ds_read_b32 v254, v250 offset:0\n"
        "v_cvt_f32_i32 v158, v158\n"
        "v_mfma_i32_16x16x64_i8 v[204:207], v[76:79], v[220:223], v[204:207]\n"
        "ds_read_b128 v[212:215], v248 offset:25600\n"
        "s_waitcnt lgkmcnt(5)\n"
        "v_mfma_i32_16x16x64_i8 v[192:195], v[80:83], v[224:227], v[192:195]\n"
        "v_cvt_f32_i32 v159, v159\n"
        "v_fma_f32 v132, v132, v240, v241\n"
        "v_mfma_i32_16x16x64_i8 v[196:199], v[84:87], v[224:227], v[196:199]\n"
        "ds_read_b128 v[216:219], v248 offset:26624\n"
        "v_fma_f32 v133, v133, v240, v241\n"
        "v_fma_f32 v134, v134, v240, v241\n"
        "v_mfma_i32_16x16x64_i8 v[200:203], v[80:83], v[228:231], v[200:203]\n"
        "v_fma_f32 v135, v135, v240, v241\n"
        "v_fma_f32 v140, v140, v242, v243\n"
        "v_mfma_i32_16x16x64_i8 v[204:207], v[84:87], v[228:231], v[204:207]\n"
        "ds_read_b128 v[220:223], v248 offset:27648\n"
        "s_waitcnt lgkmcnt(5)\n"
        "v_mfma_i32_16x16x64_i8 v[192:195], v[88:91], v[232:235], v[192:195]\n"
        "v_fma_f32 v141, v141, v242, v243\n"
        "v_fma_f32 v142, v142, v242, v243\n"
        "v_mfma_i32_16x16x64_i8 v[196:199], v[92:95], v[232:235], v[196:199]\n"
        "ds_read_b128 v[224:227], v248 offset:28672\n"
        "v_fma_f32 v143, v143, v242, v243\n"
        "v_fma_f32 v148, v148, v244, v245\n"
        "v_mfma_i32_16x16x64_i8 v[200:203], v[88:91], v[236:239], v[200:203]\n"
        "v_fma_f32 v149, v149, v244, v245\n"
        "v_fma_f32 v150, v150, v244, v245\n"
        "v_mfma_i32_16x16x64_i8 v[204:207], v[92:95], v[236:239], v[204:207]\n"
        "ds_read_b128 v[228:231], v248 offset:29696\n"
        "s_waitcnt lgkmcnt(4)\n"
        "v_mfma_i32_16x16x64_i8 v[192:195], v[96:99], v[208:211], v[192:195]\n"
        "v_fma_f32 v151, v151, v244, v245\n"
        "v_fma_f32 v156, v156, v246, v247\n"
        "v_mfma_i32_16x16x64_i8 v[196:199], v[100:103], v[208:211], v[196:199]\n"
        "ds_read_b128 v[232:235], v248 offset:30720\n"
        "v_fma_f32 v157, v157, v246, v247\n"
        "v_fma_f32 v158, v158, v246, v247\n"
        "v_mfma_i32_16x16x64_i8 v[200:203], v[96:99], v[212:215], v[200:203]\n"
        "v_fma_f32 v159, v159, v246, v247\n"
        "v_cvt_pk_bf16_f32 v132, v132, v140\n"
        "v_mfma_i32_16x16x64_i8 v[204:207], v[100:103], v[212:215], v[204:207]\n"
        "ds_read_b128 v[236:239], v248 offset:31744\n"
        "v_cmp_gt_u32 vcc, 22, v254\n"
        "s_waitcnt lgkmcnt(4)\n"
        "v_mfma_i32_16x16x64_i8 v[192:195], v[104:107], v[216:219], v[192:195]\n"
        "s_cbranch_vccnz W8_s20\n"
        "W8_c20:\n"
        "v_cvt_pk_bf16_f32 v140, v133, v141\n"
        "v_mfma_i32_16x16x64_i8 v[196:199], v[108:111], v[216:219], v[196:199]\n"
        "ds_read_b128 v[208:211], v248 offset:32768\n"
        "v_cvt_pk_bf16_f32 v134, v134, v142\n"
        "v_cvt_pk_bf16_f32 v142, v135, v143\n"
        "v_mfma_i32_16x16x64_i8 v[200:203], v[104:107], v[220:223], v[200:203]\n"
        "v_cvt_pk_bf16_f32 v133, v148, v156\n"
        "global_store_dwordx2 v251, v[132:133], s[48:49] offset:0 sc1\n"
        "v_mfma_i32_16x16x64_i8 v[204:207], v[108:111], v[220:223], v[204:207]\n"
        "ds_read_b128 v[212:215], v248 offset:33792\n"
        "s_waitcnt lgkmcnt(4)\n"
        "v_mfma_i32_16x16x64_i8 v[192:195], v[112:115], v[224:227], v[192:195]\n"
        "v_cvt_pk_bf16_f32 v141, v149, v157\n"
        "global_store_dwordx2 v251, v[140:141], s[50:51] offset:0 sc1\n"
        "v_mfma_i32_16x16x64_i8 v[196:199], v[116:119], v[224:227], v[196:199]\n"
        "ds_read_b128 v[216:219], v248 offset:34816\n"
        "v_cvt_pk_bf16_f32 v135, v150, v158\n"
        "global_store_dwordx2 v251, v[134:135], s[52:53] offset:0 sc1\n"
        "v_mfma_i32_16x16x64_i8 v[200:203], v[112:115], v[228:231], v[200:203]\n"
        "v_cvt_pk_bf16_f32 v143, v151, v159\n"
        "global_store_dwordx2 v251, v[142:143], s[54:55] offset:0 sc1\n"
        "v_mfma_i32_16x16x64_i8 v[204:207], v[116:119], v[228:231], v[204:207]\n"
        "ds_read_b128 v[220:223], v248 offset:35840\n"
        "s_waitcnt lgkmcnt(4)\n"
        "v_mfma_i32_16x16x64_i8 v[192:195], v[120:123], v[232:235], v[192:195]\n"
        "ds_read_b128 v[240:243], v253 offset:512\n"
        "ds_read_b128 v[244:247], v253 offset:528\n"
        "v_mfma_i32_16x16x64_i8 v[196:199], v[124:127], v[232:235], v[196:199]\n"
        "ds_read_b128 v[224:227], v248 offset:36864\n"
        "v_cvt_f32_i32 v160, v160\n"
        "v_cvt_f32_i32 v161, v161\n"
        "v_mfma_i32_16x16x64_i8 v[200:203], v[120:123], v[236:239], v[200:203]\n"
        "v_cvt_f32_i32 v162, v162\n"
        "v_cvt_f32_i32 v163, v163\n"
        "v_mfma_i32_16x16x64_i8 v[204:207], v[124:127], v[236:239], v[204:207]\n"
        "ds_read_b128 v[228:231], v248 offset:37888\n"
        "s_waitcnt lgkmcnt(6)\n"
        "v_mfma_i32_16x16x64_i8 v[128:131], v[0:3], v[208:211], 0\n"
        "v_cvt_f32_i32 v168, v168\n"
        "v_cvt_f32_i32 v169, v169\n"
        "v_cvt_f32_i32 v170, v170\n"
        "v_mfma_i32_16x16x64_i8 v[132:135], v[4:7], v[208:211], 0\n"
        "ds_read_b128 v[232:235], v248 offset:38912\n"
        "v_cvt_f32_i32 v171, v171\n"
        "v_mfma_i32_16x16x64_i8 v[136:139], v[0:3], v[212:215], 0\n"
        "v_cvt_f32_i32 v176, v176\n"
        "v_cvt_f32_i32 v177, v177\n"
        "v_mfma_i32_16x16x64_i8 v[140:143], v[4:7], v[212:215], 0\n"
        "ds_read_b128 v[236:239], v248 offset:39936\n"
        "s_waitcnt lgkmcnt(6)\n"
        "v_mfma_i32_16x16x64_i8 v[128:131], v[8:11], v[216:219], v[128:131]\n"
        "v_cvt_f32_i32 v178, v178\n"
        "v_cvt_f32_i32 v179, v179\n"
        "v_cvt_f32_i32 v184, v184\n"
        "v_mfma_i32_16x16x64_i8 v[132:135], v[12:15], v[216:219], v[132:135]\n"
        "ds_read_b128 v[208:211], v248 offset:40960\n"
        "v_cvt_f32_i32 v185, v185\n"
        "v_mfma_i32_16x16x64_i8 v[136:139], v[8:11], v[220:223], v[136:139]\n"
        "ds_read_b32 v254, v250 offset:0\n"
        "v_cvt_f32_i32 v186, v186\n"
        "v_mfma_i32_16x16x64_i8 v[140:143], v[12:15], v[220:223], v[140:143]\n"
        "ds_read_b128 v[212:215], v248 offset:41984\n"
        "s_waitcnt lgkmcnt(5)\n"
        "v_mfma_i32_16x16x64_i8 v[128:131], v[16:19], v[224:227], v[128:131]\n"
        "v_cvt_f32_i32 v187, v187\n"
        "v_fma_f32 v160, v160, v240, v241\n"
        "v_mfma_i32_16x16x64_i8 v[132:135], v[20:23], v[224:227], v[132:135]\n"
        "ds_read_b128 v[216:219], v248 offset:43008\n"
        "v_fma_f32 v161, v161, v240, v241\n"
        "v_mfma_i32_16x16x64_i8 v[136:139], v[16:19], v[228:231], v[136:139]\n"
        "v_fma_f32 v162, v162, v240, v241\n"
        "v_fma_f32 v163, v163, v240, v241\n"
        "v_mfma_i32_16x16x64_i8 v[140:143], v[20:23], v[228:231], v[140:143]\n"
        "ds_read_b128 v[220:223], v248 offset:44032\n"
        "s_waitcnt lgkmcnt(5)\n"
        "v_mfma_i32_16x16x64_i8 v[128:131], v[24:27], v[232:235], v[128:131]\n"
        "v_fma_f32 v168, v168, v242, v243\n"
        "v_fma_f32 v169, v169, v242, v243\n"
        "v_fma_f32 v170, v170, v242, v243\n"
        "v_mfma_i32_16x16x64_i8 v[132:135], v[28:31], v[232:235], v[132:135]\n"
        "ds_read_b128 v[224:227], v248 offset:45056\n"
        "v_fma_f32 v171, v171, v242, v243\n"
        "v_mfma_i32_16x16x64_i8 v[136:139], v[24:27], v[236:239], v[136:139]\n"
        "v_fma_f32 v176, v176, v244, v245\n"
        "v_fma_f32 v177, v177, v244, v245\n"
        "v_mfma_i32_16x16x64_i8 v[140:143], v[28:31], v[236:239], v[140:143]\n"
        "ds_read_b128 v[228:231], v248 offset:46080\n"
        "s_waitcnt lgkmcnt(4)\n"
        "v_mfma_i32_16x16x64_i8 v[128:131], v[32:35], v[208:211], v[128:131]\n"
        "v_fma_f32 v178, v178, v244, v245\n"
        "v_fma_f32 v179, v179, v244, v245\n"
        "v_fma_f32 v184, v184, v246, v247\n"
        "v_mfma_i32_16x16x64_i8 v[132:135], v[36:39], v[208:211], v[132:135]\n"
        "ds_read_b128 v[232:235], v248 offset:47104\n"
        "v_fma_f32 v185, v185, v246, v247\n"
        "v_mfma_i32_16x16x64_i8 v[136:139], v[32:35], v[212:215], v[136:139]\n"
        "v_fma_f32 v186, v186, v246, v247\n"
        "v_fma_f32 v187, v187, v246, v247\n"
        "v_mfma_i32_16x16x64_i8 v[140:143], v[36:39], v[212:215], v[140:143]\n"
        "ds_read_b128 v[236:239], v248 offset:48128\n"
        "v_cmp_gt_u32 vcc, 24, v254\n"
        "s_waitcnt lgkmcnt(4)\n"
        "v_mfma_i32_16x16x64_i8 v[128:131], v[40:43], v[216:219], v[128:131]\n"
        "s_cbranch_vccnz W8_s22\n"
        "W8_c22:\n"
        "v_cvt_pk_bf16_f32 v160, v160, v168\n"
        "v_cvt_pk_bf16_f32 v168, v161, v169\n"
        "v_mfma_i32_16x16x64_i8 v[132:135], v[44:47], v[216:219], v[132:135]\n"
        "ds_read_b128 v[208:211], v248 offset:49152\n"
        "v_cvt_pk_bf16_f32 v162, v162, v170\n"
        "v_mfma_i32_16x16x64_i8 v[136:139], v[40:43], v[220:223], v[136:139]\n"
        "v_cvt_pk_bf16_f32 v170, v163, v171\n"
        "v_cvt_pk_bf16_f32 v161, v176, v184\n"
        "v_mfma_i32_16x16x64_i8 v[140:143], v[44:47], v[220:223], v[140:143]\n"
        "ds_read_b128 v[212:215], v248 offset:50176\n"
        "s_waitcnt lgkmcnt(4)\n"
        "v_mfma_i32_16x16x64_i8 v[128:131], v[48:51], v[224:227], v[128:131]\n"
        "global_store_dwordx2 v251, v[160:161], s[40:41] offset:128 sc1\n"
        "v_cvt_pk_bf16_f32 v169, v177, v185\n"
        "global_store_dwordx2 v251, v[168:169], s[42:43] offset:128 sc1\n"
        "v_mfma_i32_16x16x64_i8 v[132:135], v[52:55], v[224:227], v[132:135]\n"
        "ds_read_b128 v[216:219], v248 offset:51200\n"
        "v_cvt_pk_bf16_f32 v163, v178, v186\n"
        "v_mfma_i32_16x16x64_i8 v[136:139], v[48:51], v[228:231], v[136:139]\n"
        "global_store_dwordx2 v251, v[162:163], s[44:45] offset:128 sc1\n"
        "v_cvt_pk_bf16_f32 v171, v179, v187\n"
        "v_mfma_i32_16x16x64_i8 v[140:143], v[52:55], v[228:231], v[140:143]\n"
        "ds_read_b128 v[220:223], v248 offset:52224\n"
        "s_waitcnt lgkmcnt(4)\n"
        "v_mfma_i32_16x16x64_i8 v[128:131], v[56:59], v[232:235], v[128:131]\n"
        "global_store_dwordx2 v251, v[170:171], s[46:47] offset:128 sc1\n"
        "v_cvt_f32_i32 v164, v164\n"
        "v_cvt_f32_i32 v165, v165\n"
        "v_mfma_i32_16x16x64_i8 v[132:135], v[60:63], v[232:235], v[132:135]\n"
        "ds_read_b128 v[224:227], v248 offset:53248\n"
        "v_cvt_f32_i32 v166, v166\n"
        "v_mfma_i32_16x16x64_i8 v[136:139], v[56:59], v[236:239], v[136:139]\n"
        "v_cvt_f32_i32 v167, v167\n"
        "v_cvt_f32_i32 v172, v172\n"
        "v_mfma_i32_16x16x64_i8 v[140:143], v[60:63], v[236:239], v[140:143]\n"
        "ds_read_b128 v[228:231], v248 offset:54272\n"
        "s_waitcnt lgkmcnt(4)\n"
        "v_mfma_i32_16x16x64_i8 v[128:131], v[64:67], v[208:211], v[128:131]\n"
        "v_cvt_f32_i32 v173, v173\n"
        "v_cvt_f32_i32 v174, v174\n"
        "v_cvt_f32_i32 v175, v175\n"
        "v_mfma_i32_16x16x64_i8 v[132:135], v[68:71], v[208:211], v[132:135]\n"
        "ds_read_b128 v[232:235], v248 offset:55296\n"
        "v_cvt_f32_i32 v180, v180\n"
        "v_mfma_i32_16x16x64_i8 v[136:139], v[64:67], v[212:215], v[136:139]\n"
        "v_cvt_f32_i32 v181, v181\n"
        "v_cvt_f32_i32 v182, v182\n"
        "v_mfma_i32_16x16x64_i8 v[140:143], v[68:71], v[212:215], v[140:143]\n"
        "ds_read_b128 v[236:239], v248 offset:56320\n"
        "s_waitcnt lgkmcnt(4)\n"
        "v_mfma_i32_16x16x64_i8 v[128:131], v[72:75], v[216:219], v[128:131]\n"
        "v_cvt_f32_i32 v183, v183\n"
        "v_cvt_f32_i32 v188, v188\n"
        "v_cvt_f32_i32 v189, v189\n"
        "v_mfma_i32_16x16x64_i8 v[132:135], v[76:79], v[216:219], v[132:135]\n"
        "ds_read_b128 v[208:211], v248 offset:57344\n"
        "v_cvt_f32_i32 v190, v190\n"
        "v_mfma_i32_16x16x64_i8 v[136:139], v[72:75], v[220:223], v[136:139]\n"
        "ds_read_b32 v254, v250 offset:0\n"
        "v_cvt_f32_i32 v191, v191\n"
        "v_mfma_i32_16x16x64_i8 v[140:143], v[76:79], v[220:223], v[140:143]\n"
        "ds_read_b128 v[212:215], v248 offset:58368\n"
        "s_waitcnt lgkmcnt(5)\n"
        "v_mfma_i32_16x16x64_i8 v[128:131], v[80:83], v[224:227], v[128:131]\n"
        "v_fma_f32 v164, v164, v240, v241\n"
        "v_fma_f32 v165, v165, v240, v241\n"
        "v_fma_f32 v166, v166, v240, v241\n"
        "v_mfma_i32_16x16x64_i8 v[132:135], v[84:87], v[224:227], v[132:135]\n"
        "ds_read_b128 v[216:219], v248 offset:59392\n"
        "v_fma_f32 v167, v167, v240, v241\n"
        "v_mfma_i32_16x16x64_i8 v[136:139], v[80:83], v[228:231], v[136:139]\n"
        "v_fma_f32 v172, v172, v242, v243\n"
        "v_fma_f32 v173, v173, v242, v243\n"
        "v_mfma_i32_16x16x64_i8 v[140:143], v[84:87], v[228:231], v[140:143]\n"
        "ds_read_b128 v[220:223], v248 offset:60416\n"
        "s_waitcnt lgkmcnt(5)\n"
        "v_mfma_i32_16x16x64_i8 v[128:131], v[88:91], v[232:235], v[128:131]\n"
        "v_fma_f32 v174, v174, v242, v243\n"
        "v_fma_f32 v175, v175, v242, v243\n"
        "v_fma_f32 v180, v180, v244, v245\n"
        "v_mfma_i32_16x16x64_i8 v[132:135], v[92:95], v[232:235], v[132:135]\n"
        "ds_read_b128 v[224:227], v248 offset:61440\n"
        "v_fma_f32 v181, v181, v244, v245\n"
        "v_mfma_i32_16x16x64_i8 v[136:139], v[88:91], v[236:239], v[136:139]\n"
        "v_fma_f32 v182, v182, v244, v245\n"
        "v_fma_f32 v183, v183, v244, v245\n"
        "v_mfma_i32_16x16x64_i8 v[140:143], v[92:95], v[236:239], v[140:143]\n"
        "ds_read_b128 v[228:231], v248 offset:62464\n"
        "s_waitcnt lgkmcnt(4)\n"
        "v_mfma_i32_16x16x64_i8 v[128:131], v[96:99], v[208:211], v[128:131]\n"
        "v_fma_f32 v188, v188, v246, v247\n"
        "v_fma_f32 v189, v189, v246, v247\n"
        "v_fma_f32 v190, v190, v246, v247\n"
        "v_mfma_i32_16x16x64_i8 v[132:135], v[100:103], v[208:211], v[132:135]\n"
        "ds_read_b128 v[232:235], v248 offset:63488\n"
        "v_fma_f32 v191, v191, v246, v247\n"
        "v_mfma_i32_16x16x64_i8 v[136:139], v[96:99], v[212:215], v[136:139]\n"
        "v_cvt_pk_bf16_f32 v164, v164, v172\n"
        "v_cvt_pk_bf16_f32 v172, v165, v173\n"
        "v_mfma_i32_16x16x64_i8 v[140:143], v[100:103], v[212:215], v[140:143]\n"
        "ds_read_b128 v[236:239], v248 offset:64512\n"
        "v_cmp_gt_u32 vcc, 26, v254\n"
        "s_waitcnt lgkmcnt(4)\n"
        "v_mfma_i32_16x16x64_i8 v[128:131], v[104:107], v[216:219], v[128:131]\n"
        "s_cbranch_vccnz W8_s24\n"
        "W8_c24:\n"
        "v_cvt_pk_bf16_f32 v166, v166, v174\n"
        "v_cvt_pk_bf16_f32 v174, v167, v175\n"
        "v_mfma_i32_16x16x64_i8 v[132:135], v[108:111], v[216:219], v[132:135]\n"
        "ds_read_b128 v[208:211], v249 offset:0\n"
        "v_cvt_pk_bf16_f32 v165, v180, v188\n"
        "v_mfma_i32_16x16x64_i8 v[136:139], v[104:107], v[220:223], v[136:139]\n"
        "global_store_dwordx2 v251, v[164:165], s[48:49] offset:128 sc1\n"
        "v_cvt_pk_bf16_f32 v173, v181, v189\n"
        "v_mfma_i32_16x16x64_i8 v[140:143], v[108:111], v[220:223], v[140:143]\n"
        "ds_read_b128 v[212:215], v249 offset:1024\n"
        "s_waitcnt lgkmcnt(4)\n"
        "v_mfma_i32_16x16x64_i8 v[128:131], v[112:115], v[224:227], v[128:131]\n"
        "global_store_dwordx2 v251, v[172:173], s[50:51] offset:128 sc1\n"
        "v_cvt_pk_bf16_f32 v167, v182, v190\n"
        "global_store_dwordx2 v251, v[166:167], s[52:53] offset:128 sc1\n"
        "v_mfma_i32_16x16x64_i8 v[132:135], v[116:119], v[224:227], v[132:135]\n"
        "ds_read_b128 v[216:219], v249 offset:2048\n"
        "v_cvt_pk_bf16_f32 v175, v183, v191\n"
        "v_mfma_i32_16x16x64_i8 v[136:139], v[112:115], v[228:231], v[136:139]\n"
        "global_store_dwordx2 v251, v[174:175], s[54:55] offset:128 sc1\n"
        "v_add_u32 v251, s37, v251\n"
        "v_mfma_i32_16x16x64_i8 v[140:143], v[116:119], v[228:231], v[140:143]\n"
        "ds_read_b128 v[220:223], v249 offset:3072\n"
        "s_waitcnt lgkmcnt(4)\n"
        "v_mfma_i32_16x16x64_i8 v[128:131], v[120:123], v[232:235], v[128:131]\n"
        "v_mfma_i32_16x16x64_i8 v[132:135], v[124:127], v[232:235], v[132:135]\n"
        "ds_read_b128 v[224:227], v249 offset:4096\n"
        "v_and_b32 v255, 0xfffffeff, v253\n"
        "v_bfe_u32 v240, v253, 8, 1\n"
        "v_mfma_i32_16x16x64_i8 v[136:139], v[120:123], v[236:239], v[136:139]\n"
        "v_cvt_f32_i32 v192, v192\n"
        "v_lshl_add_u32 v255, v240, 4, v255\n"
        "v_mfma_i32_16x16x64_i8 v[140:143], v[124:127], v[236:239], v[140:143]\n"
        "ds_read_b128 v[228:231], v249 offset:5120\n"
        "s_waitcnt lgkmcnt(4)\n"
        "v_mfma_i32_16x16x64_i8 v[144:147], v[0:3], v[208:211], 0\n"
        "ds_read_b128 v[244:247], v255 offset:1536\n"
        "ds_read_b128 v[240:243], v253 offset:1024\n"
        "v_mfma_i32_16x16x64_i8 v[148:151], v[4:7], v[208:211], 0\n"
        "ds_read_b128 v[232:235], v249 offset:6144\n"
        "v_cvt_f32_i32 v193, v193\n"
        "v_mfma_i32_16x16x64_i8 v[152:155], v[0:3], v[212:215], 0\n"
        "v_cvt_f32_i32 v194, v194\n"
        "v_cvt_f32_i32 v195, v195\n"
        "v_mfma_i32_16x16x64_i8 v[156:159], v[4:7], v[212:215], 0\n"
        "ds_read_b128 v[236:239], v249 offset:7168\n"
        "s_waitcnt lgkmcnt(6)\n"
        "v_mfma_i32_16x16x64_i8 v[144:147], v[8:11], v[216:219], v[144:147]\n"
        "v_cvt_f32_i32 v200, v200\n"
        "v_cvt_f32_i32 v201, v201\n"
        "v_mfma_i32_16x16x64_i8 v[148:151], v[12:15], v[216:219], v[148:151]\n"
        "ds_read_b128 v[208:211], v249 offset:8192\n"
        "v_cvt_f32_i32 v202, v202\n"
        "v_mfma_i32_16x16x64_i8 v[152:155], v[8:11], v[220:223], v[152:155]\n"
        "ds_read_b32 v254, v250 offset:0\n"
        "v_cvt_f32_i32 v203, v203\n"
        "v_cvt_f32_i32 v196, v196\n"
        "v_mfma_i32_16x16x64_i8 v[156:159], v[12:15], v[220:223], v[156:159]\n"
        "ds_read_b128 v[212:215], v249 offset:9216\n"
        "s_waitcnt lgkmcnt(7)\n"
        "v_mfma_i32_16x16x64_i8 v[144:147], v[16:19], v[224:227], v[144:147]\n"
        "v_cvt_f32_i32 v197, v197\n"
        "v_cvt_f32_i32 v198, v198\n"
        "v_mfma_i32_16x16x64_i8 v[148:151], v[20:23], v[224:227], v[148:151]\n"
        "ds_read_b128 v[216:219], v249 offset:10240\n"
        "v_cvt_f32_i32 v199, v199\n"
        "v_mfma_i32_16x16x64_i8 v[152:155], v[16:19], v[228:231], v[152:155]\n"
        "v_cvt_f32_i32 v204, v204\n"
        "v_cvt_f32_i32 v205, v205\n"
        "v_mfma_i32_16x16x64_i8 v[156:159], v[20:23], v[228:231], v[156:159]\n"
        "ds_read_b128 v[220:223], v249 offset:11264\n"
        "s_waitcnt lgkmcnt(5)\n"
        "v_mfma_i32_16x16x64_i8 v[144:147], v[24:27], v[232:235], v[144:147]\n"
        "v_cvt_f32_i32 v206, v206\n"
        "v_cvt_f32_i32 v207, v207\n"
        "v_mfma_i32_16x16x64_i8 v[148:151], v[28:31], v[232:235], v[148:151]\n"
        "ds_read_b128 v[224:227], v249 offset:12288\n"
        "v_mfma_i32_16x16x64_i8 v[152:155], v[24:27], v[236:239], v[152:155]\n"
        "v_fma_f32 v192, v192, v244, v245\n"
        "v_fma_f32 v193, v193, v244, v245\n"
        "v_mfma_i32_16x16x64_i8 v[156:159], v[28:31], v[236:239], v[156:159]\n"
        "ds_read_b128 v[228:231], v249 offset:13312\n"
        "s_waitcnt lgkmcnt(4)\n"
        "v_mfma_i32_16x16x64_i8 v[144:147], v[32:35], v[208:211], v[144:147]\n"
        "v_fma_f32 v194, v194, v244, v245\n"
        "v_fma_f32 v195, v195, v244, v245\n"
        "v_fma_f32 v200, v200, v246, v247\n"
        "v_mfma_i32_16x16x64_i8 v[148:151], v[36:39], v[208:211], v[148:151]\n"
        "ds_read_b128 v[232:235], v249 offset:14336\n"
        "v_fma_f32 v201, v201, v246, v247\n"
        "v_mfma_i32_16x16x64_i8 v[152:155], v[32:35], v[212:215], v[152:155]\n"
        "v_fma_f32 v202, v202, v246, v247\n"
        "v_fma_f32 v203, v203, v246, v247\n"
        "v_mfma_i32_16x16x64_i8 v[156:159], v[36:39], v[212:215], v[156:159]\n"
        "ds_read_b128 v[236:239], v249 offset:15360\n"
        "v_cmp_gt_u32 vcc, 28, v254\n"
        "s_waitcnt lgkmcnt(4)\n"
        "v_mfma_i32_16x16x64_i8 v[144:147], v[40:43], v[216:219], v[144:147]\n"
        "s_cbranch_vccnz W8_s26\n"
        "W8_c26:\n"
        "v_cvt_pk_bf16_f32 v192, v192, v200\n"
        "v_mfma_i32_16x16x64_i8 v[148:151], v[44:47], v[216:219], v[148:151]\n"
        "ds_read_b128 v[208:211], v249 offset:16384\n"
        "v_cvt_pk_bf16_f32 v200, v193, v201\n"
        "v_mfma_i32_16x16x64_i8 v[152:155], v[40:43], v[220:223], v[152:155]\n"
        "v_cvt_pk_bf16_f32 v194, v194, v202\n"
        "v_cvt_pk_bf16_f32 v202, v195, v203\n"
        "v_mfma_i32_16x16x64_i8 v[156:159], v[44:47], v[220:223], v[156:159]\n"
        "ds_read_b128 v[212:215], v249 offset:17408\n"
        "s_waitcnt lgkmcnt(4)\n"
        "v_mfma_i32_16x16x64_i8 v[144:147], v[48:51], v[224:227], v[144:147]\n"
        "global_store_dword v252, v192, s[40:41] sc1\n"
        "global_store_dword v252, v200, s[42:43] sc1\n"
        "v_mfma_i32_16x16x64_i8 v[148:151], v[52:55], v[224:227], v[148:151]\n"
        "ds_read_b128 v[216:219], v249 offset:18432\n"
        "global_store_dword v252, v194, s[44:45] sc1\n"
        "global_store_dword v252, v202, s[46:47] sc1\n"
        "v_mfma_i32_16x16x64_i8 v[152:155], v[48:51], v[228:231], v[152:155]\n"
        "v_fma_f32 v196, v196, v244, v245\n"
        "v_fma_f32 v197, v197, v244, v245\n"
        "v_mfma_i32_16x16x64_i8 v[156:159], v[52:55], v[228:231], v[156:159]\n"
        "ds_read_b128 v[220:223], v249 offset:19456\n"
        "s_waitcnt lgkmcnt(4)\n"
        "v_mfma_i32_16x16x64_i8 v[144:147], v[56:59], v[232:235], v[144:147]\n"
        "v_fma_f32 v198, v198, v244, v245\n"
        "v_fma_f32 v199, v199, v244, v245\n"
        "v_mfma_i32_16x16x64_i8 v[148:151], v[60:63], v[232:235], v[148:151]\n"
        "ds_read_b128 v[224:227], v249 offset:20480\n"
        "v_fma_f32 v204, v204, v246, v247\n"
        "v_mfma_i32_16x16x64_i8 v[152:155], v[56:59], v[236:239], v[152:155]\n"
        "v_fma_f32 v205, v205, v246, v247\n"
        "v_fma_f32 v206, v206, v246, v247\n"
        "v_mfma_i32_16x16x64_i8 v[156:159], v[60:63], v[236:239], v[156:159]\n"
        "ds_read_b128 v[228:231], v249 offset:21504\n"
        "s_waitcnt lgkmcnt(4)\n"
        "v_mfma_i32_16x16x64_i8 v[144:147], v[64:67], v[208:211], v[144:147]\n"
        "v_fma_f32 v207, v207, v246, v247\n"
        "v_cvt_pk_bf16_f32 v196, v196, v204\n"
        "v_mfma_i32_16x16x64_i8 v[148:151], v[68:71], v[208:211], v[148:151]\n"
        "ds_read_b128 v[232:235], v249 offset:22528\n"
        "v_cvt_pk_bf16_f32 v204, v197, v205\n"
        "v_mfma_i32_16x16x64_i8 v[152:155], v[64:67], v[212:215], v[152:155]\n"
        "v_cvt_pk_bf16_f32 v198, v198, v206\n"
        "v_cvt_pk_bf16_f32 v206, v199, v207\n"
        "v_mfma_i32_16x16x64_i8 v[156:159], v[68:71], v[212:215], v[156:159]\n"
        "ds_read_b128 v[236:239], v249 offset:23552\n"
        "global_store_dword v252, v196, s[48:49] sc1\n"
        "s_waitcnt lgkmcnt(4)\n"
        "v_mfma_i32_16x16x64_i8 v[144:147], v[72:75], v[216:219], v[144:147]\n"
        "global_store_dword v252, v204, s[50:51] sc1\n"
        "global_store_dword v252, v198, s[52:53] sc1\n"
        "v_mfma_i32_16x16x64_i8 v[148:151], v[76:79], v[216:219], v[148:151]\n"
        "ds_read_b128 v[208:211], v249 offset:24576\n"
        "global_store_dword v252, v206, s[54:55] sc1\n"
        "v_mfma_i32_16x16x64_i8 v[152:155], v[72:75], v[220:223], v[152:155]\n"
        "ds_read_b128 v[244:247], v253 offset:1040\n"
        "v_cvt_f32_i32 v128, v128\n"
        "v_mfma_i32_16x16x64_i8 v[156:159], v[76:79], v[220:223], v[156:159]\n"
        "ds_read_b128 v[212:215], v249 offset:25600\n"
        "s_waitcnt lgkmcnt(5)\n"
        "v_mfma_i32_16x16x64_i8 v[144:147], v[80:83], v[224:227], v[144:147]\n"
        "v_cvt_f32_i32 v129, v129\n"
        "v_cvt_f32_i32 v130, v130\n"
        "v_mfma_i32_16x16x64_i8 v[148:151], v[84:87], v[224:227], v[148:151]\n"
        "ds_read_b128 v[216:219], v249 offset:26624\n"
        "v_cvt_f32_i32 v131, v131\n"
        "v_mfma_i32_16x16x64_i8 v[152:155], v[80:83], v[228:231], v[152:155]\n"
        "v_cvt_f32_i32 v136, v136\n"
        "v_cvt_f32_i32 v137, v137\n"
        "v_mfma_i32_16x16x64_i8 v[156:159], v[84:87], v[228:231], v[156:159]\n"
        "ds_read_b128 v[220:223], v249 offset:27648\n"
        "s_waitcnt lgkmcnt(5)\n"
        "v_mfma_i32_16x16x64_i8 v[144:147], v[88:91], v[232:235], v[144:147]\n"
        "v_cvt_f32_i32 v138, v138\n"
        "v_cvt_f32_i32 v139, v139\n"
        "v_fma_f32 v128, v128, v240, v241\n"
        "v_mfma_i32_16x16x64_i8 v[148:151], v[92:95], v[232:235], v[148:151]\n"
        "ds_read_b128 v[224:227], v249 offset:28672\n"
        "v_fma_f32 v129, v129, v240, v241\n"
        "v_mfma_i32_16x16x64_i8 v[152:155], v[88:91], v[236:239], v[152:155]\n"
        "v_fma_f32 v130, v130, v240, v241\n"
        "v_fma_f32 v131, v131, v240, v241\n"
        "v_mfma_i32_16x16x64_i8 v[156:159], v[92:95], v[236:239], v[156:159]\n"
        "ds_read_b128 v[228:231], v249 offset:29696\n"
        "s_waitcnt lgkmcnt(4)\n"
        "v_mfma_i32_16x16x64_i8 v[144:147], v[96:99], v[208:211], v[144:147]\n"
        "v_fma_f32 v136, v136, v242, v243\n"
        "v_fma_f32 v137, v137, v242, v243\n"
        "v_mfma_i32_16x16x64_i8 v[148:151], v[100:103], v[208:211], v[148:151]\n"
        "ds_read_b128 v[232:235], v249 offset:30720\n"
        "v_fma_f32 v138, v138, v242, v243\n"
        "v_mfma_i32_16x16x64_i8 v[152:155], v[96:99], v[212:215], v[152:155]\n"
        "v_fma_f32 v139, v139, v242, v243\n"
        "v_cvt_pk_bf16_f32 v128, v128, v136\n"
        "v_mfma_i32_16x16x64_i8 v[156:159], v[100:103], v[212:215], v[156:159]\n"
        "ds_read_b128 v[236:239], v249 offset:31744\n"
        "s_waitcnt lgkmcnt(4)\n"
        "v_mfma_i32_16x16x64_i8 v[144:147], v[104:107], v[216:219], v[144:147]\n"
        "v_cvt_pk_bf16_f32 v136, v129, v137\n"
        "v_cvt_pk_bf16_f32 v130, v130, v138\n"
        "v_mfma_i32_16x16x64_i8 v[148:151], v[108:111], v[216:219], v[148:151]\n"
        "v_cvt_pk_bf16_f32 v138, v131, v139\n"
        "v_cvt_f32_i32 v132, v132\n"
        "v_mfma_i32_16x16x64_i8 v[152:155], v[104:107], v[220:223], v[152:155]\n"
        "v_cvt_f32_i32 v133, v133\n"
        "v_cvt_f32_i32 v134, v134\n"
        "v_cvt_f32_i32 v135, v135\n"
        "v_mfma_i32_16x16x64_i8 v[156:159], v[108:111], v[220:223], v[156:159]\n"
        "v_cvt_f32_i32 v140, v140\n"
        "s_waitcnt lgkmcnt(2)\n"
        "v_mfma_i32_16x16x64_i8 v[144:147], v[112:115], v[224:227], v[144:147]\n"
        "v_cvt_f32_i32 v141, v141\n"
        "v_cvt_f32_i32 v142, v142\n"
        "v_mfma_i32_16x16x64_i8 v[148:151], v[116:119], v[224:227], v[148:151]\n"
        "v_cvt_f32_i32 v143, v143\n"
        "v_fma_f32 v132, v132, v240, v241\n"
        "v_mfma_i32_16x16x64_i8 v[152:155], v[112:115], v[228:231], v[152:155]\n"
        "v_fma_f32 v133, v133, v240, v241\n"
        "v_fma_f32 v134, v134, v240, v241\n"
        "v_mfma_i32_16x16x64_i8 v[156:159], v[116:119], v[228:231], v[156:159]\n"
        "v_fma_f32 v135, v135, v240, v241\n"
        "s_waitcnt lgkmcnt(0)\n"
        "v_mfma_i32_16x16x64_i8 v[144:147], v[120:123], v[232:235], v[144:147]\n"
        "v_fma_f32 v140, v140, v242, v243\n"
        "v_fma_f32 v141, v141, v242, v243\n"
        "v_mfma_i32_16x16x64_i8 v[148:151], v[124:127], v[232:235], v[148:151]\n"
        "v_fma_f32 v142, v142, v242, v243\n"
        "v_fma_f32 v143, v143, v242, v243\n"
        "v_mfma_i32_16x16x64_i8 v[152:155], v[120:123], v[236:239], v[152:155]\n"
        "v_cvt_pk_bf16_f32 v132, v132, v140\n"
        "v_cvt_pk_bf16_f32 v140, v133, v141\n"
        "v_mfma_i32_16x16x64_i8 v[156:159], v[124:127], v[236:239], v[156:159]\n"
        "v_cvt_pk_bf16_f32 v134, v134, v142\n"
        "v_cvt_pk_bf16_f32 v142, v135, v143\n"
        "s_nop 5\n"
        "v_cvt_f32_i32 v144, v144\n"
        "v_cvt_f32_i32 v145, v145\n"
        "v_cvt_f32_i32 v146, v146\n"
        "v_cvt_f32_i32 v147, v147\n"
        "v_cvt_f32_i32 v152, v152\n"
        "v_cvt_f32_i32 v153, v153\n"
        "v_cvt_f32_i32 v154, v154\n"
        "v_cvt_f32_i32 v155, v155\n"
        "v_fma_f32 v144, v144, v244, v245\n"
        "v_fma_f32 v145, v145, v244, v245\n"
        "v_fma_f32 v146, v146, v244, v245\n"
        "v_fma_f32 v147, v147, v244, v245\n"
        "v_fma_f32 v152, v152, v246, v247\n"
        "v_fma_f32 v153, v153, v246, v247\n"
        "v_fma_f32 v154, v154, v246, v247\n"
        "v_fma_f32 v155, v155, v246, v247\n"
        "v_cvt_pk_bf16_f32 v129, v144, v152\n"
        "global_store_dwordx2 v251, v[128:129], s[40:41] offset:256 sc1\n"
        "v_cvt_pk_bf16_f32 v137, v145, v153\n"
        "global_store_dwordx2 v251, v[136:137], s[42:43] offset:256 sc1\n"
        "v_cvt_pk_bf16_f32 v131, v146, v154\n"
        "global_store_dwordx2 v251, v[130:131], s[44:45] offset:256 sc1\n"
        "v_cvt_pk_bf16_f32 v139, v147, v155\n"
        "global_store_dwordx2 v251, v[138:139], s[46:47] offset:256 sc1\n"
        "v_cvt_f32_i32 v148, v148\n"
        "v_cvt_f32_i32 v149, v149\n"
        "v_cvt_f32_i32 v150, v150\n"
        "v_cvt_f32_i32 v151, v151\n"
        "v_cvt_f32_i32 v156, v156\n"
        "v_cvt_f32_i32 v157, v157\n"
        "v_cvt_f32_i32 v158, v158\n"
        "v_cvt_f32_i32 v159, v159\n"
        "v_fma_f32 v148, v148, v244, v245\n"
        "v_fma_f32 v149, v149, v244, v245\n"
        "v_fma_f32 v150, v150, v244, v245\n"
        "v_fma_f32 v151, v151, v244, v245\n"
        "v_fma_f32 v156, v156, v246, v247\n"
        "v_fma_f32 v157, v157, v246, v247\n"
        "v_fma_f32 v158, v158, v246, v247\n"
        "v_fma_f32 v159, v159, v246, v247\n"
        "v_cvt_pk_bf16_f32 v133, v148, v156\n"
        "global_store_dwordx2 v251, v[132:133], s[48:49] offset:256 sc1\n"
        "v_cvt_pk_bf16_f32 v141, v149, v157\n"
        "global_store_dwordx2 v251, v[140:141], s[50:51] offset:256 sc1\n"
        "v_cvt_pk_bf16_f32 v135, v150, v158\n"
        "global_store_dwordx2 v251, v[134:135], s[52:53] offset:256 sc1\n"
        "v_cvt_pk_bf16_f32 v143, v151, v159\n"
        "global_store_dwordx2 v251, v[142:143], s[54:55] offset:256 sc1\n"
        "W8_fin:\n"
        "s_endpgm\n"
        "W8_s0:\n"
        "s_mov_b32 s38, 8388608\n"
        "W8_r0:\n"
        "ds_read_b32 v254, v250 offset:0\n"
        "s_sub_u32 s38, s38, 1\n"
        "s_waitcnt lgkmcnt(0)\n"
        "s_cbranch_scc1 W8_c0\n"
        "v_readfirstlane_b32 s39, v254\n"
        "s_cmp_lt_u32 s39, 1\n"
        "s_cbranch_scc1 W8_r0\n"
        "s_branch W8_c0\n"
        "W8_sZ:\n"
        "s_mov_b32 s38, 8388608\n"
        "W8_rZ:\n"
        "ds_read_b32 v241, v250 offset:4\n"
        "s_sub_u32 s38, s38, 1\n"
        "s_waitcnt lgkmcnt(0)\n"
        "s_cbranch_scc1 W8_cZ\n"
        "v_readfirstlane_b32 s39, v241\n"
        "s_cmp_lt_u32 s39, 4\n"
        "s_cbranch_scc1 W8_rZ\n"
        "s_branch W8_cZ\n"
        "W8_s1:\n"
        "s_mov_b32 s38, 8388608\n"
        "W8_r1:\n"
        "ds_read_b32 v254, v250 offset:0\n"
        "s_sub_u32 s38, s38, 1\n"
        "s_waitcnt lgkmcnt(0)\n"
        "s_cbranch_scc1 W8_c1\n"
        "v_readfirstlane_b32 s39, v254\n"
        "s_cmp_lt_u32 s39, 2\n"
        "s_cbranch_scc1 W8_r1\n"
        "s_branch W8_c1\n"
        "W8_sR0:\n"
        "s_mov_b32 s38, 8388608\n"
        "W8_rR0:\n"
        "ds_read_b32 v241, v250 offset:4\n"
        "s_sub_u32 s38, s38, 1\n"
        "s_waitcnt lgkmcnt(0)\n"
        "s_cbranch_scc1 W8_cR0\n"
        "v_readfirstlane_b32 s39, v241\n"
        "s_cmp_lt_u32 s39, 8\n"
        "s_cbranch_scc1 W8_rR0\n"
        "s_branch W8_cR0\n"
        "W8_s2:\n"
        "s_mov_b32 s38, 8388608\n"
        "W8_r2:\n"
        "ds_read_b32 v254, v250 offset:0\n"
        "s_sub_u32 s38, s38, 1\n"
        "s_waitcnt lgkmcnt(0)\n"
        "s_cbranch_scc1 W8_c2\n"
        "v_readfirstlane_b32 s39, v254\n"
        "s_cmp_lt_u32 s39, 3\n"
        "s_cbranch_scc1 W8_r2\n"
        "s_branch W8_c2\n"
        "W8_s3:\n"
        "s_mov_b32 s38, 8388608\n"
        "W8_r3:\n"
        "ds_read_b32 v254, v250 offset:0\n"
        "s_sub_u32 s38, s38, 1\n"
        "s_waitcnt lgkmcnt(0)\n"
        "s_cbranch_scc1 W8_c3\n"
        "v_readfirstlane_b32 s39, v254\n"
        "s_cmp_lt_u32 s39, 4\n"
        "s_cbranch_scc1 W8_r3\n"
        "s_branch W8_c3\n"
        "W8_s4:\n"
        "s_mov_b32 s38, 8388608\n"
        "W8_r4:\n"
        "ds_read_b32 v254, v250 offset:0\n"
        "s_sub_u32 s38, s38, 1\n"
        "s_waitcnt lgkmcnt(0)\n"
        "s_cbranch_scc1 W8_c4\n"
        "v_readfirstlane_b32 s39, v254\n"
        "s_cmp_lt_u32 s39, 5\n"
        "s_cbranch_scc1 W8_r4\n"
        "s_branch W8_c4\n"
        "W8_s5:\n"
        "s_mov_b32 s38, 8388608\n"
        "W8_r5:\n"
        "ds_read_b32 v254, v250 offset:0\n"
        "s_sub_u32 s38, s38, 1\n"
        "s_waitcnt lgkmcnt(0)\n"
        "s_cbranch_scc1 W8_c5\n"
        "v_readfirstlane_b32 s39, v254\n"
        "s_cmp_lt_u32 s39, 6\n"
        "s_cbranch_scc1 W8_r5\n"
        "s_branch W8_c5\n"
        "W8_sR1:\n"
        "s_mov_b32 s38, 8388608\n"
        "W8_rR1:\n"
        "ds_read_b32 v241, v250 offset:4\n"
        "s_sub_u32 s38, s38, 1\n"
        "s_waitcnt lgkmcnt(0)\n"
        "s_cbranch_scc1 W8_cR1\n"
        "v_readfirstlane_b32 s39, v241\n"
        "s_cmp_lt_u32 s39, 12\n"
        "s_cbranch_scc1 W8_rR1\n"
        "s_branch W8_cR1\n"
        "W8_s6:\n"
        "s_mov_b32 s38, 8388608\n"
        "W8_r6:\n"
        "ds_read_b32 v254, v250 offset:0\n"
        "s_sub_u32 s38, s38, 1\n"
        "s_waitcnt lgkmcnt(0)\n"
        "s_cbranch_scc1 W8_c6\n"
        "v_readfirstlane_b32 s39, v254\n"
        "s_cmp_lt_u32 s39, 7\n"
        "s_cbranch_scc1 W8_r6\n"
        "s_branch W8_c6\n"
        "W8_s7:\n"
        "s_mov_b32 s38, 8388608\n"
        "W8_r7:\n"
        "ds_read_b32 v254, v250 offset:0\n"
        "s_sub_u32 s38, s38, 1\n"
        "s_waitcnt lgkmcnt(0)\n"
        "s_cbranch_scc1 W8_c7\n"
        "v_readfirstlane_b32 s39, v254\n"
        "s_cmp_lt_u32 s39, 8\n"
        "s_cbranch_scc1 W8_r7\n"
        "s_branch W8_c7\n"
        "W8_s8:\n"
        "s_mov_b32 s38, 8388608\n"
        "W8_r8:\n"
        "ds_read_b32 v254, v250 offset:0\n"
        "s_sub_u32 s38, s38, 1\n"
        "s_waitcnt lgkmcnt(0)\n"
        "s_cbranch_scc1 W8_c8\n"
        "v_readfirstlane_b32 s39, v254\n"
        "s_cmp_lt_u32 s39, 9\n"
        "s_cbranch_scc1 W8_r8\n"
        "s_branch W8_c8\n"
        "W8_s9:\n"
        "s_mov_b32 s38, 8388608\n"
        "W8_r9:\n"
        "ds_read_b32 v254, v250 offset:0\n"
        "s_sub_u32 s38, s38, 1\n"
        "s_waitcnt lgkmcnt(0)\n"
        "s_cbranch_scc1 W8_c9\n"
        "v_readfirstlane_b32 s39, v254\n"
        "s_cmp_lt_u32 s39, 10\n"
        "s_cbranch_scc1 W8_r9\n"
        "s_branch W8_c9\n"
        "W8_sR2:\n"
        "s_mov_b32 s38, 8388608\n"
        "W8_rR2:\n"
        "ds_read_b32 v241, v250 offset:4\n"
        "s_sub_u32 s38, s38, 1\n"
        "s_waitcnt lgkmcnt(0)\n"
        "s_cbranch_scc1 W8_cR2\n"
        "v_readfirstlane_b32 s39, v241\n"
        "s_cmp_lt_u32 s39, 16\n"
        "s_cbranch_scc1 W8_rR2\n"
        "s_branch W8_cR2\n"
        "W8_s10:\n"
        "s_mov_b32 s38, 8388608\n"
        "W8_r10:\n"
        "ds_read_b32 v254, v250 offset:0\n"
        "s_sub_u32 s38, s38, 1\n"
        "s_waitcnt lgkmcnt(0)\n"
        "s_cbranch_scc1 W8_c10\n"
        "v_readfirstlane_b32 s39, v254\n"
        "s_cmp_lt_u32 s39, 11\n"
        "s_cbranch_scc1 W8_r10\n"
        "s_branch W8_c10\n"
        "W8_s11:\n"
        "s_mov_b32 s38, 8388608\n"
        "W8_r11:\n"
        "ds_read_b32 v254, v250 offset:0\n"
        "s_sub_u32 s38, s38, 1\n"
        "s_waitcnt lgkmcnt(0)\n"
        "s_cbranch_scc1 W8_c11\n"
        "v_readfirstlane_b32 s39, v254\n"
        "s_cmp_lt_u32 s39, 12\n"
        "s_cbranch_scc1 W8_r11\n"
        "s_branch W8_c11\n"
        "W8_s12:\n"
        "s_mov_b32 s38, 8388608\n"
        "W8_r12:\n"
        "ds_read_b32 v254, v250 offset:0\n"
        "s_sub_u32 s38, s38, 1\n"
        "s_waitcnt lgkmcnt(0)\n"
        "s_cbranch_scc1 W8_c12\n"
        "v_readfirstlane_b32 s39, v254\n"
        "s_cmp_lt_u32 s39, 13\n"
        "s_cbranch_scc1 W8_r12\n"
        "s_branch W8_c12\n"
        "W8_s13:\n"
        "s_mov_b32 s38, 8388608\n"
        "W8_r13:\n"
        "ds_read_b32 v254, v250 offset:0\n"
        "s_sub_u32 s38, s38, 1\n"
        "s_waitcnt lgkmcnt(0)\n"
        "s_cbranch_scc1 W8_c13\n"
        "v_readfirstlane_b32 s39, v254\n"
        "s_cmp_lt_u32 s39, 14\n"
        "s_cbranch_scc1 W8_r13\n"
        "s_branch W8_c13\n"
        "W8_s14:\n"
        "s_mov_b32 s38, 8388608\n"
        "W8_r14:\n"
        "ds_read_b32 v254, v250 offset:0\n"
        "s_sub_u32 s38, s38, 1\n"
        "s_waitcnt lgkmcnt(0)\n"
        "s_cbranch_scc1 W8_c14\n"
        "v_readfirstlane_b32 s39, v254\n"
        "s_cmp_lt_u32 s39, 15\n"
        "s_cbranch_scc1 W8_r14\n"
        "s_branch W8_c14\n"
        "W8_s15:\n"
        "s_mov_b32 s38, 8388608\n"
        "W8_r15:\n"
        "ds_read_b32 v254, v250 offset:0\n"
        "s_sub_u32 s38, s38, 1\n"
        "s_waitcnt lgkmcnt(0)\n"
        "s_cbranch_scc1 W8_c15\n"
        "v_readfirstlane_b32 s39, v254\n"
        "s_cmp_lt_u32 s39, 16\n"
        "s_cbranch_scc1 W8_r15\n"
        "s_branch W8_c15\n"
        "W8_s16:\n"
        "s_mov_b32 s38, 8388608\n"
        "W8_r16:\n"
        "ds_read_b32 v254, v250 offset:0\n"
        "s_sub_u32 s38, s38, 1\n"
        "s_waitcnt lgkmcnt(0)\n"
        "s_cbranch_scc1 W8_c16\n"
        "v_readfirstlane_b32 s39, v254\n"
        "s_cmp_lt_u32 s39, 18\n"
        "s_cbranch_scc1 W8_r16\n"
        "s_branch W8_c16\n"
        "W8_sT:\n"
        "s_mov_b32 s38, 8388608\n"
        "W8_rT:\n"
        "ds_read_b32 v255, v250 offset:260\n"
        "s_sub_u32 s38, s38, 1\n"
        "s_waitcnt lgkmcnt(0)\n"
        "s_cbranch_scc1 W8_cT\n"
        "v_readfirstlane_b32 s39, v255\n"
        "s_cmp_lt_u32 s39, 2\n"
        "s_cbranch_scc1 W8_rT\n"
        "s_branch W8_cT\n"
        "W8_s18:\n"
        "s_mov_b32 s38, 8388608\n"
        "W8_r18:\n"
        "ds_read_b32 v254, v250 offset:0\n"
        "s_sub_u32 s38, s38, 1\n"
        "s_waitcnt lgkmcnt(0)\n"
        "s_cbranch_scc1 W8_c18\n"
        "v_readfirstlane_b32 s39, v254\n"
        "s_cmp_lt_u32 s39, 20\n"
        "s_cbranch_scc1 W8_r18\n"
        "s_branch W8_c18\n"
        "W8_s20:\n"
        "s_mov_b32 s38, 8388608\n"
        "W8_r20:\n"
        "ds_read_b32 v254, v250 offset:0\n"
        "s_sub_u32 s38, s38, 1\n"
        "s_waitcnt lgkmcnt(0)\n"
        "s_cbranch_scc1 W8_c20\n"
        "v_readfirstlane_b32 s39, v254\n"
        "s_cmp_lt_u32 s39, 22\n"
        "s_cbranch_scc1 W8_r20\n"
        "s_branch W8_c20\n"
        "W8_s22:\n"
        "s_mov_b32 s38, 8388608\n"
        "W8_r22:\n"
        "ds_read_b32 v254, v250 offset:0\n"
        "s_sub_u32 s38, s38, 1\n"
        "s_waitcnt lgkmcnt(0)\n"
        "s_cbranch_scc1 W8_c22\n"
        "v_readfirstlane_b32 s39, v254\n"
        "s_cmp_lt_u32 s39, 24\n"
        "s_cbranch_scc1 W8_r22\n"
        "s_branch W8_c22\n"
        "W8_s24:\n"
        "s_mov_b32 s38, 8388608\n"
        "W8_r24:\n"
        "ds_read_b32 v254, v250 offset:0\n"
        "s_sub_u32 s38, s38, 1\n"
        "s_waitcnt lgkmcnt(0)\n"
        "s_cbranch_scc1 W8_c24\n"
        "v_readfirstlane_b32 s39, v254\n"
        "s_cmp_lt_u32 s39, 26\n"
        "s_cbranch_scc1 W8_r24\n"
        "s_branch W8_c24\n"
        "W8_s26:\n"
        "s_mov_b32 s38, 8388608\n"
        "W8_r26:\n"
        "ds_read_b32 v254, v250 offset:0\n"
        "s_sub_u32 s38, s38, 1\n"
        "s_waitcnt lgkmcnt(0)\n"
        "s_cbranch_scc1 W8_c26\n"
        "v_readfirstlane_b32 s39, v254\n"
        "s_cmp_lt_u32 s39, 28\n"
        "s_cbranch_scc1 W8_r26\n"
        "s_branch W8_c26\n"
        "W8_end:\n"
        : "+v"(tid)
        : "s"(__builtin_amdgcn_kernarg_segment_ptr()), "s"(blockIdx.x)
        : "v1", "v2", "v3", "v4", "v5", "v6", "v7", "v8", "v9", "v10", "v11", "v12", "v13", "v14", "v15", "v16", "v17", "v18", "v19", "v20", "v21", "v22", "v23", "v24", "v25", "v26", "v27", "v28", "v29", "v30", "v31", "v32", "v33", "v34", "v35", "v36", "v37", "v38", "v39", "v40", "v41", "v42", "v43", "v44", "v45", "v46", "v47", "v48", "v49", "v50", "v51", "v52", "v53", "v54", "v55", "v56", "v57", "v58", "v59", "v60", "v61", "v62", "v63", "v64", "v65", "v66", "v67", "v68", "v69", "v70", "v71", "v72", "v73", "v74", "v75", "v76", "v77", "v78", "v79", "v80", "v81", "v82", "v83", "v84", "v85", "v86", "v87", "v88", "v89", "v90", "v91", "v92", "v93", "v94", "v95", "v96", "v97", "v98", "v99", "v100", "v101", "v102", "v103", "v104", "v105", "v106", "v107", "v108", "v109", "v110", "v111", "v112", "v113", "v114", "v115", "v116", "v117", "v118", "v119", "v120", "v121", "v122", "v123", "v124", "v125", "v126", "v127", "v128", "v129", "v130", "v131", "v132", "v133", "v134", "v135", "v136", "v137", "v138", "v139", "v140", "v141", "v142", "v143", "v144", "v145", "v146", "v147", "v148", "v149", "v150", "v151", "v152", "v153", "v154", "v155", "v156", "v157", "v158", "v159", "v160", "v161", "v162", "v163", "v164", "v165", "v166", "v167", "v168", "v169", "v170", "v171", "v172", "v173", "v174", "v175", "v176", "v177", "v178", "v179", "v180", "v181", "v182", "v183", "v184", "v185", "v186", "v187", "v188", "v189", "v190", "v191", "v192", "v193", "v194", "v195", "v196", "v197", "v198", "v199", "v200", "v201", "v202", "v203", "v204", "v205", "v206", "v207", "v208", "v209", "v210", "v211", "v212", "v213", "v214", "v215", "v216", "v217", "v218", "v219", "v220", "v221", "v222", "v223", "v224", "v225", "v226", "v227", "v228", "v229", "v230", "v231", "v232", "v233", "v234", "v235", "v236", "v237", "v238", "v239", "v240", "v241", "v242", "v243", "v244", "v245", "v246", "v247", "v248", "v249", "v250", "v251", "v252", "v253", "v254", "v255", "s3", "s4", "s5", "s6", "s7", "s8", "s9", "s10", "s11", "s12", "s13", "s14", "s15", "s16", "s17", "s18", "s19", "s20", "s21", "s22", "s23", "s24", "s25", "s26", "s27", "s28", "s29", "s30", "s31", "s33", "s34", "s35", "s36", "s37", "s38", "s39", "s40", "s41", "s42", "s43", "s44", "s45", "s46", "s47", "s48", "s49", "s50", "s51", "s52", "s53", "s54", "s55", "s56", "s57", "s58", "s59", "s60", "s61", "s62", "s63", "s64", "s65", "s66", "s67", "s68", "s69", "s70", "s71", "s72", "s73", "s74", "s75", "s76", "s77", "s78", "s79", "s80", "s81", "s82", "s83", "s84", "s85", "s86", "s87", "s88", "s89", "s90", "s91", "s92", "s93", "s94", "s95", "s96", "s97", "s98", "s99", "vcc", "scc", "memory");
}

extern "C" void kernel_launch(void* const* d_in, const int* in_sizes, int n_in,
                              void* d_out, int out_size, void* d_ws, size_t ws_size,
                              hipStream_t stream) {
    (void)in_sizes; (void)n_in; (void)out_size; (void)ws_size;
    d_ws = (char*)d_ws + (128u<<20);
    constexpr int lds_bytes = 137792;
    hipFuncSetAttribute((const void*)w8a8_fused, hipFuncAttributeMaxDynamicSharedMemorySize, lds_bytes);
    w8a8_fused<<<256, 512, lds_bytes, stream>>>(d_in[0], d_in[1], d_ws, d_out, d_in[4], d_in[5], d_in[2], d_in[3]);
}
```

```cpp
#include <hip/hip_runtime.h>
#include <stdint.h>

__global__ __launch_bounds__(512, 2) void w8a8_fused(const void* qx, const void* qw, void* ws, void* out,
                                                     const void* sw, const void* bias, const void* sx, const void* zx) {
    int tid = threadIdx.x;
    asm volatile(
        "W8_start:\n"
        "s_load_dwordx16 s[4:19], %1, 0x0\n"
        "s_mov_b32 s3, %2\n"
        "v_readfirstlane_b32 s20, v0\n"
        "s_and_b32 s24, s3, 1\n"
        "s_lshl_b32 s24, s24, 3\n"
        "s_bfe_u32 s21, s3, 0x30003\n"
        "s_or_b32 s21, s21, s24\n"
        "s_bfe_u32 s24, s3, 0x20001\n"
        "s_lshl_b32 s24, s24, 2\n"
        "s_lshr_b32 s22, s3, 6\n"
        "s_or_b32 s22, s22, s24\n"
        "v_lshlrev_b32 v2, 2, v0\n"
        "v_add_u32 v2, 135684, v2\n"
        "v_mov_b32 v3, 0\n"
        "ds_write_b32 v2, v3\n"
        "s_and_b32 s23, s22, 1\n"
        "s_lshr_b32 s20, s20, 6\n"
        "s_cbranch_scc0 W8_prod\n"
        "s_bfe_u32 s24, s3, 0x20003\n"
        "s_add_u32 s24, s24, 4\n"
        "s_cmp_lg_u32 s20, s24\n"
        "s_cbranch_scc1 W8_np\n"
        "s_getpc_b64 s[24:25]\n"
        "W8_pc:\n"
        "v_and_b32 v2, 255, v0\n"
        "v_lshlrev_b32 v2, 7, v2\n"
        "v_min_u32 v2, W8_end-W8_pc-4, v2\n"
        "global_load_dword v255, v2, s[24:25]\n"
        "W8_np:\n"
        "s_waitcnt lgkmcnt(0)\n"
        "s_barrier\n"
        "s_branch W8_other\n"
        "W8_prod:\n"
        "s_and_b32 s24, s22, 3\n"
        "s_lshl2_add_u32 s24, s21, s24\n"
        "s_lshl_b32 s24, s24, 17\n"
        "s_lshr_b32 s25, s22, 2\n"
        "s_lshl_b32 s25, s25, 8\n"
        "s_add_u32 s24, s24, s25\n"
        "v_lshrrev_b32 v11, 4, v0\n"
        "v_and_b32 v13, 15, v0\n"
        "v_lshl_add_u32 v4, v11, 12, s24\n"
        "v_lshl_add_u32 v4, v13, 4, v4\n"
        "s_lshl_b32 s25, s22, 9\n"
        "s_mul_i32 s26, s21, 229376\n"
        "s_add_u32 s27, s26, s25\n"
        "v_lshrrev_b32 v8, 5, v0\n"
        "v_mul_u32_u24 v9, 114688, v8\n"
        "v_and_b32 v10, 31, v0\n"
        "v_lshl_add_u32 v5, v10, 4, v9\n"
        "v_add_u32 v5, s27, v5\n"
        "s_movk_i32 s28, 16384\n"
        "s_movk_i32 s29, 14336\n"
        "v_bfe_u32 v60, v0, 4, 1\n"
        "s_add_u32 s27, s22, 16\n"
        "s_sub_u32 s27, s27, s21\n"
        "v_mul_lo_u32 v60, v60, s27\n"
        "v_add_lshl_u32 v60, v60, s21, 7\n"
        "v_add_u32 v60, 6815744, v60\n"
        "s_waitcnt lgkmcnt(0)\n"
        "global_load_dwordx4 v[64:67], v4, s[4:5] nt\n"
        "v_add_u32 v4, s28, v4\n"
        "global_load_dwordx4 v[96:99], v5, s[6:7] nt\n"
        "v_add_u32 v5, s29, v5\n"
        "global_load_dwordx2 v[62:63], v60, s[8:9] sc1\n"
        "global_load_dwordx4 v[68:71], v4, s[4:5] nt\n"
        "v_add_u32 v4, s28, v4\n"
        "global_load_dwordx4 v[72:75], v4, s[4:5] nt\n"
        "v_add_u32 v4, s28, v4\n"
        "global_load_dwordx4 v[76:79], v4, s[4:5] nt\n"
        "v_add_u32 v4, s28, v4\n"
        "global_load_dwordx4 v[80:83], v4, s[4:5] nt\n"
        "v_add_u32 v4, s28, v4\n"
        "global_load_dwordx4 v[84:87], v4, s[4:5] nt\n"
        "v_add_u32 v4, s28, v4\n"
        "global_load_dwordx4 v[88:91], v4, s[4:5] nt\n"
        "v_add_u32 v4, s28, v4\n"
        "global_load_dwordx4 v[92:95], v4, s[4:5] nt\n"
        "v_add_u32 v4, 4294853632, v4\n"
        "global_load_dwordx4 v[100:103], v5, s[6:7] nt\n"
        "v_add_u32 v5, s29, v5\n"
        "global_load_dwordx4 v[104:107], v5, s[6:7] nt\n"
        "v_add_u32 v5, s29, v5\n"
        "global_load_dwordx4 v[108:111], v5, s[6:7] nt\n"
        "v_add_u32 v5, s29, v5\n"
        "global_load_dwordx4 v[112:115], v5, s[6:7] nt\n"
        "v_add_u32 v5, s29, v5\n"
        "global_load_dwordx4 v[116:119], v5, s[6:7] nt\n"
        "v_add_u32 v5, s29, v5\n"
        "global_load_dwordx4 v[120:123], v5, s[6:7] nt\n"
        "v_add_u32 v5, s29, v5\n"
        "global_load_dwordx4 v[124:127], v5, s[6:7] nt\n"
        "v_add_u32 v5, 3569664, v5\n"
        "global_load_dwordx4 v[128:131], v4, s[4:5] nt\n"
        "v_add_u32 v4, s28, v4\n"
        "global_load_dwordx4 v[132:135], v4, s[4:5] nt\n"
        "v_add_u32 v4, s28, v4\n"
        "global_load_dwordx4 v[136:139], v4, s[4:5] nt\n"
        "v_add_u32 v4, s28, v4\n"
        "global_load_dwordx4 v[140:143], v4, s[4:5] nt\n"
        "v_add_u32 v4, s28, v4\n"
        "global_load_dwordx4 v[144:147], v4, s[4:5] nt\n"
        "v_add_u32 v4, s28, v4\n"
        "global_load_dwordx4 v[148:151], v4, s[4:5] nt\n"
        "v_add_u32 v4, s28, v4\n"
        "s_barrier\n"
        "s_lshl_b32 s26, s26, 2\n"
        "s_lshl_b32 s27, s22, 8\n"
        "s_add_u32 s27, s27, 8192\n"
        "s_add_u32 s27, s27, s26\n"
        "v_lshrrev_b32 v11, 4, v0\n"
        "v_mul_u32_u24 v12, 229376, v11\n"
        "v_and_b32 v13, 15, v0\n"
        "v_lshl_add_u32 v7, v13, 4, v12\n"
        "v_add_u32 v7, s27, v7\n"
        "s_lshl_b32 s27, s22, 7\n"
        "s_add_u32 s27, s27, 12288\n"
        "s_add_u32 s27, s27, s26\n"
        "v_bfe_u32 v14, v0, 3, 2\n"
        "v_mul_u32_u24 v12, 229376, v14\n"
        "v_and_b32 v15, 7, v0\n"
        "v_lshl_add_u32 v6, v15, 4, v12\n"
        "v_add3_u32 v6, v6, v9, s27\n"
        "v_lshlrev_b32 v16, 2, v0\n"
        "v_add_u32 v25, 135684, v16\n"
        "v_add_u32 v16, 98304, v16\n"
        "v_lshlrev_b32 v19, 4, v0\n"
        "v_add_u32 v17, 98304, v19\n"
        "v_lshlrev_b32 v21, 3, v0\n"
        "v_add_u32 v21, 98304, v21\n"
        "s_lshl_b32 s26, s22, 4\n"
        "s_add_u32 s26, s26, s21\n"
        "s_mul_i32 s24, s26, 6144\n"
        "s_add_u32 s24, s24, 4194304\n"
        "s_add_u32 s44, s8, s24\n"
        "s_addc_u32 s45, s9, 0\n"
        "s_lshl_b32 s24, s26, 10\n"
        "s_add_u32 s24, s24, 5767168\n"
        "s_add_u32 s46, s8, s24\n"
        "s_addc_u32 s47, s9, 0\n"
        "v_lshlrev_b32 v22, 4, v13\n"
        "v_lshl_add_u32 v22, v11, 2, v22\n"
        "v_lshlrev_b32 v23, 4, v15\n"
        "v_lshl_add_u32 v23, v11, 2, v23\n"
        "s_mul_i32 s24, s21, 512\n"
        "s_mul_i32 s25, s22, 8\n"
        "s_add_u32 s24, s24, s25\n"
        "s_add_u32 s24, s24, 6029312\n"
        "s_mul_i32 s25, s22, 1024\n"
        "s_mul_i32 s27, s21, 8\n"
        "s_add_u32 s25, s25, s27\n"
        "s_add_u32 s25, s25, 6291456\n"
        "s_sub_u32 s25, s25, s24\n"
        "v_mul_lo_u32 v24, v0, s25\n"
        "s_mul_i32 s25, 4, 128\n"
        "s_add_u32 s24, s24, s25\n"
        "v_add_u32 v24, s24, v24\n"
        "v_lshlrev_b32 v35, 8, v14\n"
        "v_lshl_add_u32 v35, v8, 7, v35\n"
        "v_lshl_add_u32 v35, v15, 4, v35\n"
        "v_add_u32 v35, 4096, v35\n"
        "v_mov_b32 v20, 1\n"
        "s_mov_b32 s30, 0x0c0c0400\n"
        "s_mov_b32 s31, 0x01010101\n"
        "v_mov_b32 v28, 0\n"
        "v_mov_b32 v29, 0\n"
        "v_mov_b32 v30, 0\n"
        "v_mov_b32 v31, 0\n"
        "v_mov_b32 v32, 0\n"
        "v_mov_b32 v33, 0\n"
        "v_lshlrev_b32 v12, 3, v0\n"
        "v_add_u32 v12, 136968, v12\n"
        "s_waitcnt vmcnt(20)\n"
        "v_add_co_u32 v62, vcc, 1, v62\n"
        "s_nop 1\n"
        "v_addc_co_u32 v63, vcc, 0, v63, vcc\n"
        "s_mov_b32 exec_hi, 0\n"
        "ds_write_b64 v12, v[62:63]\n"
        "s_mov_b32 exec_hi, -1\n"
        "ds_write_b32 v25, v20 offset:1024\n"
        "s_waitcnt vmcnt(19)\n"
        "global_load_dwordx4 v[152:155], v4, s[4:5] nt\n"
        "v_add_u32 v4, s28, v4\n"
        "global_load_dwordx4 v[156:159], v4, s[4:5] nt\n"
        "v_add_u32 v4, 4294853632, v4\n"
        "v_perm_b32 v64, v65, v64, s30\n"
        "v_perm_b32 v66, v67, v66, s30\n"
        "v_lshl_or_b32 v64, v66, 16, v64\n"
        "ds_write_b32 v16, v64 offset:0\n"
        "v_perm_b32 v68, v69, v68, s30\n"
        "v_perm_b32 v70, v71, v70, s30\n"
        "v_lshl_or_b32 v68, v70, 16, v68\n"
        "ds_write_b32 v16, v68 offset:256\n"
        "s_waitcnt vmcnt(19)\n"
        "global_load_dwordx4 v[160:163], v5, s[6:7] nt\n"
        "v_add_u32 v5, s29, v5\n"
        "global_load_dwordx4 v[164:167], v5, s[6:7] nt\n"
        "v_add_u32 v5, s29, v5\n"
        "v_perm_b32 v72, v73, v72, s30\n"
        "v_perm_b32 v74, v75, v74, s30\n"
        "v_lshl_or_b32 v72, v74, 16, v72\n"
        "ds_write_b32 v16, v72 offset:512\n"
        "v_perm_b32 v76, v77, v76, s30\n"
        "v_perm_b32 v78, v79, v78, s30\n"
        "v_lshl_or_b32 v76, v78, 16, v76\n"
        "ds_write_b32 v16, v76 offset:768\n"
        "s_waitcnt vmcnt(19)\n"
        "global_load_dwordx4 v[168:171], v5, s[6:7] nt\n"
        "v_add_u32 v5, s29, v5\n"
        "global_load_dwordx4 v[172:175], v5, s[6:7] nt\n"
        "v_add_u32 v5, s29, v5\n"
        "v_perm_b32 v80, v81, v80, s30\n"
        "v_perm_b32 v82, v83, v82, s30\n"
        "v_lshl_or_b32 v80, v82, 16, v80\n"
        "ds_write_b32 v16, v80 offset:1024\n"
        "v_perm_b32 v84, v85, v84, s30\n"
        "v_perm_b32 v86, v87, v86, s30\n"
        "v_lshl_or_b32 v84, v86, 16, v84\n"
        "ds_write_b32 v16, v84 offset:1280\n"
        "s_waitcnt vmcnt(19)\n"
        "global_load_dwordx4 v[176:179], v5, s[6:7] nt\n"
        "v_add_u32 v5, s29, v5\n"
        "global_load_dwordx4 v[180:183], v5, s[6:7] nt\n"
        "v_add_u32 v5, s29, v5\n"
        "v_perm_b32 v88, v89, v88, s30\n"
        "v_perm_b32 v90, v91, v90, s30\n"
        "v_lshl_or_b32 v88, v90, 16, v88\n"
        "ds_write_b32 v16, v88 offset:1536\n"
        "v_perm_b32 v92, v93, v92, s30\n"
        "v_perm_b32 v94, v95, v94, s30\n"
        "v_lshl_or_b32 v92, v94, 16, v92\n"
        "ds_write_b32 v16, v92 offset:1792\n"
        "s_waitcnt vmcnt(20)\n"
        "global_load_dwordx4 v[184:187], v5, s[6:7] nt\n"
        "v_add_u32 v5, s29, v5\n"
        "global_load_dwordx4 v[188:191], v5, s[6:7] nt\n"
        "v_add_u32 v5, 3569664, v5\n"
        "s_waitcnt vmcnt(20)\n"
        "global_load_dwordx4 v[192:195], v4, s[4:5] nt\n"
        "v_add_u32 v4, s28, v4\n"
        "global_load_dwordx4 v[196:199], v4, s[4:5] nt\n"
        "v_add_u32 v4, s28, v4\n"
        "v_perm_b32 v96, v100, v96, s30\n"
        "v_perm_b32 v104, v108, v104, s30\n"
        "v_perm_b32 v97, v101, v97, s30\n"
        "v_perm_b32 v105, v109, v105, s30\n"
        "v_perm_b32 v98, v102, v98, s30\n"
        "v_perm_b32 v106, v110, v106, s30\n"
        "v_perm_b32 v99, v103, v99, s30\n"
        "v_perm_b32 v107, v111, v107, s30\n"
        "v_lshl_or_b32 v36, v104, 16, v96\n"
        "v_lshl_or_b32 v40, v105, 16, v97\n"
        "v_lshl_or_b32 v38, v106, 16, v98\n"
        "v_lshl_or_b32 v42, v107, 16, v99\n"
        "s_waitcnt vmcnt(20)\n"
        "global_load_dwordx4 v[200:203], v4, s[4:5] nt\n"
        "v_add_u32 v4, s28, v4\n"
        "global_load_dwordx4 v[204:207], v4, s[4:5] nt\n"
        "v_add_u32 v4, s28, v4\n"
        "s_waitcnt vmcnt(20)\n"
        "global_load_dwordx4 v[208:211], v4, s[4:5] nt\n"
        "v_add_u32 v4, s28, v4\n"
        "global_load_dwordx4 v[212:215], v4, s[4:5] nt\n"
        "v_add_u32 v4, s28, v4\n"
        "v_perm_b32 v112, v116, v112, s30\n"
        "v_perm_b32 v120, v124, v120, s30\n"
        "v_perm_b32 v113, v117, v113, s30\n"
        "v_perm_b32 v121, v125, v121, s30\n"
        "v_perm_b32 v114, v118, v114, s30\n"
        "v_perm_b32 v122, v126, v122, s30\n"
        "v_perm_b32 v115, v119, v115, s30\n"
        "v_perm_b32 v123, v127, v123, s30\n"
        "v_lshl_or_b32 v37, v120, 16, v112\n"
        "v_lshl_or_b32 v41, v121, 16, v113\n"
        "v_lshl_or_b32 v39, v122, 16, v114\n"
        "v_lshl_or_b32 v43, v123, 16, v115\n"
        "s_nop 0\n"
        "v_permlane32_swap_b32 v36, v38\n"
        "v_permlane32_swap_b32 v37, v39\n"
        "v_permlane32_swap_b32 v40, v42\n"
        "v_permlane32_swap_b32 v41, v43\n"
        "s_nop 0\n"
        "ds_write_b128 v17, v[36:39] offset:2304\n"
        "ds_write_b128 v17, v[40:43] offset:3328\n"
        "ds_write_b32 v25, v20 offset:1544\n"
        "v_add_u32 v20, 1, v20\n"
        "v_dot4_i32_i8 v28, v36, s31, v28\n"
        "v_dot4_i32_i8 v29, v40, s31, v29\n"
        "v_dot4_i32_i8 v28, v37, s31, v28\n"
        "v_dot4_i32_i8 v29, v41, s31, v29\n"
        "v_dot4_i32_i8 v28, v38, s31, v28\n"
        "v_dot4_i32_i8 v29, v42, s31, v29\n"
        "v_dot4_i32_i8 v28, v39, s31, v28\n"
        "v_dot4_i32_i8 v29, v43, s31, v29\n"
        "s_waitcnt vmcnt(20)\n"
        "global_load_dwordx4 v[216:219], v4, s[4:5] nt\n"
        "v_add_u32 v4, s28, v4\n"
        "global_load_dwordx4 v[220:223], v4, s[4:5] nt\n"
        "v_add_u32 v4, 4294853632, v4\n"
        "v_perm_b32 v128, v129, v128, s30\n"
        "v_perm_b32 v130, v131, v130, s30\n"
        "v_lshl_or_b32 v128, v130, 16, v128\n"
        "ds_write_b32 v16, v128 offset:5120\n"
        "v_perm_b32 v132, v133, v132, s30\n"
        "v_perm_b32 v134, v135, v134, s30\n"
        "v_lshl_or_b32 v132, v134, 16, v132\n"
        "ds_write_b32 v16, v132 offset:5376\n"
        "s_waitcnt vmcnt(20)\n"
        "global_load_dwordx4 v[224:227], v5, s[6:7] nt\n"
        "v_add_u32 v5, s29, v5\n"
        "global_load_dwordx4 v[228:231], v5, s[6:7] nt\n"
        "v_add_u32 v5, s29, v5\n"
        "v_perm_b32 v136, v137, v136, s30\n"
        "v_perm_b32 v138, v139, v138, s30\n"
        "v_lshl_or_b32 v136, v138, 16, v136\n"
        "ds_write_b32 v16, v136 offset:5632\n"
        "v_perm_b32 v140, v141, v140, s30\n"
        "v_perm_b32 v142, v143, v142, s30\n"
        "v_lshl_or_b32 v140, v142, 16, v140\n"
        "ds_write_b32 v16, v140 offset:5888\n"
        "s_waitcnt vmcnt(20)\n"
        "global_load_dwordx4 v[232:235], v5, s[6:7] nt\n"
        "v_add_u32 v5, s29, v5\n"
        "global_load_dwordx4 v[236:239], v5, s[6:7] nt\n"
        "v_add_u32 v5, s29, v5\n"
        "v_perm_b32 v144, v145, v144, s30\n"
        "v_perm_b32 v146, v147, v146, s30\n"
        "v_lshl_or_b32 v144, v146, 16, v144\n"
        "ds_write_b32 v16, v144 offset:6144\n"
        "v_perm_b32 v148, v149, v148, s30\n"
        "v_perm_b32 v150, v151, v150, s30\n"
        "v_lshl_or_b32 v148, v150, 16, v148\n"
        "ds_write_b32 v16, v148 offset:6400\n"
        "s_waitcnt vmcnt(20)\n"
        "global_load_dwordx4 v[240:243], v5, s[6:7] nt\n"
        "v_add_u32 v5, s29, v5\n"
        "global_load_dwordx4 v[244:247], v5, s[6:7] nt\n"
        "v_add_u32 v5, s29, v5\n"
        "v_perm_b32 v152, v153, v152, s30\n"
        "v_perm_b32 v154, v155, v154, s30\n"
        "v_lshl_or_b32 v152, v154, 16, v152\n"
        "ds_write_b32 v16, v152 offset:6656\n"
        "v_perm_b32 v156, v157, v156, s30\n"
        "v_perm_b32 v158, v159, v158, s30\n"
        "v_lshl_or_b32 v156, v158, 16, v156\n"
        "ds_write_b32 v16, v156 offset:6912\n"
        "s_waitcnt vmcnt(20)\n"
        "global_load_dwordx4 v[64:67], v5, s[6:7] nt\n"
        "v_add_u32 v5, s29, v5\n"
        "global_load_dwordx4 v[68:71], v5, s[6:7] nt\n"
        "v_add_u32 v5, 3569664, v5\n"
        "s_waitcnt vmcnt(20)\n"
        "global_load_dwordx4 v[72:75], v4, s[4:5] nt\n"
        "v_add_u32 v4, s28, v4\n"
        "global_load_dwordx4 v[76:79], v4, s[4:5] nt\n"
        "v_add_u32 v4, s28, v4\n"
        "v_perm_b32 v160, v164, v160, s30\n"
        "v_perm_b32 v168, v172, v168, s30\n"
        "v_perm_b32 v161, v165, v161, s30\n"
        "v_perm_b32 v169, v173, v169, s30\n"
        "v_perm_b32 v162, v166, v162, s30\n"
        "v_perm_b32 v170, v174, v170, s30\n"
        "v_perm_b32 v163, v167, v163, s30\n"
        "v_perm_b32 v171, v175, v171, s30\n"
        "v_lshl_or_b32 v36, v168, 16, v160\n"
        "v_lshl_or_b32 v40, v169, 16, v161\n"
        "v_lshl_or_b32 v38, v170, 16, v162\n"
        "v_lshl_or_b32 v42, v171, 16, v163\n"
        "s_waitcnt vmcnt(20)\n"
        "global_load_dwordx4 v[80:83], v4, s[4:5] nt\n"
        "v_add_u32 v4, s28, v4\n"
        "global_load_dwordx4 v[84:87], v4, s[4:5] nt\n"
        "v_add_u32 v4, s28, v4\n"
        "s_waitcnt vmcnt(20)\n"
        "global_load_dwordx4 v[88:91], v4, s[4:5] nt\n"
        "v_add_u32 v4, s28, v4\n"
        "global_load_dwordx4 v[92:95], v4, s[4:5] nt\n"
        "v_add_u32 v4, s28, v4\n"
        "v_perm_b32 v176, v180, v176, s30\n"
        "v_perm_b32 v184, v188, v184, s30\n"
        "v_perm_b32 v177, v181, v177, s30\n"
        "v_perm_b32 v185, v189, v185, s30\n"
        "v_perm_b32 v178, v182, v178, s30\n"
        "v_perm_b32 v186, v190, v186, s30\n"
        "v_perm_b32 v179, v183, v179, s30\n"
        "v_perm_b32 v187, v191, v187, s30\n"
        "v_lshl_or_b32 v37, v184, 16, v176\n"
        "v_lshl_or_b32 v41, v185, 16, v177\n"
        "v_lshl_or_b32 v39, v186, 16, v178\n"
        "v_lshl_or_b32 v43, v187, 16, v179\n"
        "s_nop 0\n"
        "v_permlane32_swap_b32 v36, v38\n"
        "v_permlane32_swap_b32 v37, v39\n"
        "v_permlane32_swap_b32 v40, v42\n"
        "v_permlane32_swap_b32 v41, v43\n"
        "s_nop 0\n"
        "ds_write_b128 v17, v[36:39] offset:7424\n"
        "ds_write_b128 v17, v[40:43] offset:8448\n"
        "ds_write_b32 v25, v20 offset:1544\n"
        "v_add_u32 v20, 1, v20\n"
        "v_dot4_i32_i8 v28, v36, s31, v28\n"
        "v_dot4_i32_i8 v29, v40, s31, v29\n"
        "v_dot4_i32_i8 v28, v37, s31, v28\n"
        "v_dot4_i32_i8 v29, v41, s31, v29\n"
        "v_dot4_i32_i8 v28, v38, s31, v28\n"
        "v_dot4_i32_i8 v29, v42, s31, v29\n"
        "v_dot4_i32_i8 v28, v39, s31, v28\n"
        "v_dot4_i32_i8 v29, v43, s31, v29\n"
        "s_waitcnt vmcnt(20)\n"
        "global_load_dwordx4 v[96:99], v4, s[4:5] nt\n"
        "v_add_u32 v4, s28, v4\n"
        "global_load_dwordx4 v[100:103], v4, s[4:5] nt\n"
        "v_perm_b32 v192, v193, v192, s30\n"
        "v_perm_b32 v194, v195, v194, s30\n"
        "v_lshl_or_b32 v192, v194, 16, v192\n"
        "ds_write_b32 v16, v192 offset:10240\n"
        "v_perm_b32 v196, v197, v196, s30\n"
        "v_perm_b32 v198, v199, v198, s30\n"
        "v_lshl_or_b32 v196, v198, 16, v196\n"
        "ds_write_b32 v16, v196 offset:10496\n"
        "s_waitcnt vmcnt(20)\n"
        "global_load_dwordx4 v[104:107], v5, s[6:7] nt\n"
        "v_add_u32 v5, s29, v5\n"
        "global_load_dwordx4 v[108:111], v5, s[6:7] nt\n"
        "v_add_u32 v5, s29, v5\n"
        "v_perm_b32 v200, v201, v200, s30\n"
        "v_perm_b32 v202, v203, v202, s30\n"
        "v_lshl_or_b32 v200, v202, 16, v200\n"
        "ds_write_b32 v16, v200 offset:10752\n"
        "v_perm_b32 v204, v205, v204, s30\n"
        "v_perm_b32 v206, v207, v206, s30\n"
        "v_lshl_or_b32 v204, v206, 16, v204\n"
        "ds_write_b32 v16, v204 offset:11008\n"
        "s_waitcnt vmcnt(20)\n"
        "global_load_dwordx4 v[112:115], v5, s[6:7] nt\n"
        "v_add_u32 v5, s29, v5\n"
        "global_load_dwordx4 v[116:119], v5, s[6:7] nt\n"
        "v_add_u32 v5, s29, v5\n"
        "v_perm_b32 v208, v209, v208, s30\n"
        "v_perm_b32 v210, v211, v210, s30\n"
        "v_lshl_or_b32 v208, v210, 16, v208\n"
        "ds_write_b32 v16, v208 offset:11264\n"
        "v_perm_b32 v212, v213, v212, s30\n"
        "v_perm_b32 v214, v215, v214, s30\n"
        "v_lshl_or_b32 v212, v214, 16, v212\n"
        "ds_write_b32 v16, v212 offset:11520\n"
        "s_waitcnt vmcnt(20)\n"
        "global_load_dwordx4 v[120:123], v5, s[6:7] nt\n"
        "v_add_u32 v5, s29, v5\n"
        "global_load_dwordx4 v[124:127], v5, s[6:7] nt\n"
        "v_add_u32 v5, s29, v5\n"
        "v_perm_b32 v216, v217, v216, s30\n"
        "v_perm_b32 v218, v219, v218, s30\n"
        "v_lshl_or_b32 v216, v218, 16, v216\n"
        "ds_write_b32 v16, v216 offset:11776\n"
        "v_perm_b32 v220, v221, v220, s30\n"
        "v_perm_b32 v222, v223, v222, s30\n"
        "v_lshl_or_b32 v220, v222, 16, v220\n"
        "ds_write_b32 v16, v220 offset:12032\n"
        "s_waitcnt vmcnt(20)\n"
        "global_load_dwordx4 v[128:131], v5, s[6:7] nt\n"
        "v_add_u32 v5, s29, v5\n"
        "global_load_dwordx4 v[132:135], v5, s[6:7] nt\n"
        "s_waitcnt vmcnt(20)\n"
        "global_load_dwordx4 v[136:139], v6, s[6:7] nt\n"
        "v_add_u32 v6, s29, v6\n"
        "global_load_dwordx4 v[140:143], v6, s[6:7] nt\n"
        "v_add_u32 v6, s29, v6\n"
        "v_perm_b32 v224, v228, v224, s30\n"
        "v_perm_b32 v232, v236, v232, s30\n"
        "v_perm_b32 v225, v229, v225, s30\n"
        "v_perm_b32 v233, v237, v233, s30\n"
        "v_perm_b32 v226, v230, v226, s30\n"
        "v_perm_b32 v234, v238, v234, s30\n"
        "v_perm_b32 v227, v231, v227, s30\n"
        "v_perm_b32 v235, v239, v235, s30\n"
        "v_lshl_or_b32 v36, v232, 16, v224\n"
        "v_lshl_or_b32 v40, v233, 16, v225\n"
        "v_lshl_or_b32 v38, v234, 16, v226\n"
        "v_lshl_or_b32 v42, v235, 16, v227\n"
        "s_waitcnt vmcnt(20)\n"
        "global_load_dwordx4 v[144:147], v6, s[6:7] nt\n"
        "v_add_u32 v6, s29, v6\n"
        "global_load_dwordx4 v[148:151], v6, s[6:7] nt\n"
        "v_add_u32 v6, s29, v6\n"
        "s_waitcnt vmcnt(20)\n"
        "global_load_dwordx4 v[152:155], v6, s[6:7] nt\n"
        "v_add_u32 v6, s29, v6\n"
        "global_load_dwordx4 v[156:159], v6, s[6:7] nt\n"
        "v_add_u32 v6, s29, v6\n"
        "v_mov_b32 v253, 0\n"
        "s_lshl_b32 s56, s22, 7\n"
        "s_lshl_b32 s57, s22, 6\n"
        "s_add_u32 s57, s57, 1920\n"
        "v_mov_b32 v254, s56\n"
        "v_mov_b32 v255, s57\n"
        "v_cmp_gt_u32 vcc, 32, v0\n"
        "s_lshl_b32 s56, s22, 5\n"
        "global_load_ushort v252, v253, s[16:17]\n"
        "v_cndmask_b32 v254, v255, v254, vcc\n"
        "s_add_u32 s56, s56, 2880\n"
        "v_mov_b32 v255, s56\n"
        "v_cmp_gt_u32 vcc, 48, v0\n"
        "s_nop 1\n"
        "v_cndmask_b32 v254, v255, v254, vcc\n"
        "v_lshl_add_u32 v254, v0, 2, v254\n"
        "v_lshlrev_b32 v254, 1, v254\n"
        "s_mov_b32 exec_hi, 0x00ffffff\n"
        "global_load_dwordx2 v[248:249], v254, s[12:13]\n"
        "global_load_dwordx2 v[250:251], v254, s[14:15]\n"
        "s_mov_b32 exec_hi, -1\n"
        "v_perm_b32 v240, v244, v240, s30\n"
        "v_perm_b32 v64, v68, v64, s30\n"
        "v_perm_b32 v241, v245, v241, s30\n"
        "v_perm_b32 v65, v69, v65, s30\n"
        "v_perm_b32 v242, v246, v242, s30\n"
        "v_perm_b32 v66, v70, v66, s30\n"
        "v_perm_b32 v243, v247, v243, s30\n"
        "v_perm_b32 v67, v71, v67, s30\n"
        "v_lshl_or_b32 v37, v64, 16, v240\n"
        "v_lshl_or_b32 v41, v65, 16, v241\n"
        "v_lshl_or_b32 v39, v66, 16, v242\n"
        "v_lshl_or_b32 v43, v67, 16, v243\n"
        "s_nop 0\n"
        "v_permlane32_swap_b32 v36, v38\n"
        "v_permlane32_swap_b32 v37, v39\n"
        "v_permlane32_swap_b32 v40, v42\n"
        "v_permlane32_swap_b32 v41, v43\n"
        "s_nop 0\n"
        "ds_write_b128 v17, v[36:39] offset:12544\n"
        "ds_write_b128 v17, v[40:43] offset:13568\n"
        "ds_write_b32 v25, v20 offset:1544\n"
        "v_add_u32 v20, 1, v20\n"
        "v_dot4_i32_i8 v28, v36, s31, v28\n"
        "v_dot4_i32_i8 v29, v40, s31, v29\n"
        "v_dot4_i32_i8 v28, v37, s31, v28\n"
        "v_dot4_i32_i8 v29, v41, s31, v29\n"
        "v_dot4_i32_i8 v28, v38, s31, v28\n"
        "v_dot4_i32_i8 v29, v42, s31, v29\n"
        "v_dot4_i32_i8 v28, v39, s31, v28\n"
        "v_dot4_i32_i8 v29, v43, s31, v29\n"
        "s_waitcnt vmcnt(23)\n"
        "global_load_dwordx4 v[160:163], v6, s[6:7] nt\n"
        "v_add_u32 v6, s29, v6\n"
        "global_load_dwordx4 v[164:167], v6, s[6:7] nt\n"
        "v_perm_b32 v72, v73, v72, s30\n"
        "v_perm_b32 v74, v75, v74, s30\n"
        "v_lshl_or_b32 v72, v74, 16, v72\n"
        "ds_write_b32 v16, v72 offset:15360\n"
        "v_perm_b32 v76, v77, v76, s30\n"
        "v_perm_b32 v78, v79, v78, s30\n"
        "v_lshl_or_b32 v76, v78, 16, v76\n"
        "ds_write_b32 v16, v76 offset:15616\n"
        "s_waitcnt vmcnt(23)\n"
        "global_load_dwordx4 v[168:171], v7, s[6:7] nt\n"
        "v_add_u32 v7, s29, v7\n"
        "global_load_dwordx4 v[172:175], v7, s[6:7] nt\n"
        "v_add_u32 v7, s29, v7\n"
        "v_perm_b32 v80, v81, v80, s30\n"
        "v_perm_b32 v82, v83, v82, s30\n"
        "v_lshl_or_b32 v80, v82, 16, v80\n"
        "ds_write_b32 v16, v80 offset:15872\n"
        "v_perm_b32 v84, v85, v84, s30\n"
        "v_perm_b32 v86, v87, v86, s30\n"
        "v_lshl_or_b32 v84, v86, 16, v84\n"
        "ds_write_b32 v16, v84 offset:16128\n"
        "s_waitcnt vmcnt(23)\n"
        "global_load_dwordx4 v[176:179], v7, s[6:7] nt\n"
        "v_add_u32 v7, s29, v7\n"
        "global_load_dwordx4 v[180:183], v7, s[6:7] nt\n"
        "v_add_u32 v7, s29, v7\n"
        "v_perm_b32 v88, v89, v88, s30\n"
        "v_perm_b32 v90, v91, v90, s30\n"
        "v_lshl_or_b32 v88, v90, 16, v88\n"
        "ds_write_b32 v16, v88 offset:16384\n"
        "v_perm_b32 v92, v93, v92, s30\n"
        "v_perm_b32 v94, v95, v94, s30\n"
        "v_lshl_or_b32 v92, v94, 16, v92\n"
        "ds_write_b32 v16, v92 offset:16640\n"
        "s_waitcnt vmcnt(23)\n"
        "global_load_dwordx4 v[184:187], v7, s[6:7] nt\n"
        "v_add_u32 v7, s29, v7\n"
        "global_load_dwordx4 v[188:191], v7, s[6:7] nt\n"
        "v_add_u32 v7, s29, v7\n"
        "v_perm_b32 v96, v97, v96, s30\n"
        "v_perm_b32 v98, v99, v98, s30\n"
        "v_lshl_or_b32 v96, v98, 16, v96\n"
        "ds_write_b32 v16, v96 offset:16896\n"
        "v_perm_b32 v100, v101, v100, s30\n"
        "v_perm_b32 v102, v103, v102, s30\n"
        "v_lshl_or_b32 v100, v102, 16, v100\n"
        "ds_write_b32 v16, v100 offset:17152\n"
        "s_waitcnt vmcnt(23)\n"
        "global_load_dwordx4 v[192:195], v7, s[6:7] nt\n"
        "v_add_u32 v7, s29, v7\n"
        "global_load_dwordx4 v[196:199], v7, s[6:7] nt\n"
        "v_add_u32 v7, s29, v7\n"
        "s_waitcnt vmcnt(23)\n"
        "global_load_dwordx4 v[200:203], v7, s[6:7] nt\n"
        "v_add_u32 v7, s29, v7\n"
        "global_load_dwordx4 v[204:207], v7, s[6:7] nt\n"
        "v_add_u32 v7, s29, v7\n"
        "v_perm_b32 v104, v108, v104, s30\n"
        "v_perm_b32 v112, v116, v112, s30\n"
        "v_perm_b32 v105, v109, v105, s30\n"
        "v_perm_b32 v113, v117, v113, s30\n"
        "v_perm_b32 v106, v110, v106, s30\n"
        "v_perm_b32 v114, v118, v114, s30\n"
        "v_perm_b32 v107, v111, v107, s30\n"
        "v_perm_b32 v115, v119, v115, s30\n"
        "v_lshl_or_b32 v36, v112, 16, v104\n"
        "v_lshl_or_b32 v40, v113, 16, v105\n"
        "v_lshl_or_b32 v38, v114, 16, v106\n"
        "v_lshl_or_b32 v42, v115, 16, v107\n"
        "s_waitcnt vmcnt(23)\n"
        "global_load_dwordx4 v[208:211], v7, s[6:7] nt\n"
        "v_add_u32 v7, s29, v7\n"
        "global_load_dwordx4 v[212:215], v7, s[6:7] nt\n"
        "v_add_u32 v7, s29, v7\n"
        "s_waitcnt vmcnt(23)\n"
        "global_load_dwordx4 v[216:219], v7, s[6:7] nt\n"
        "v_add_u32 v7, s29, v7\n"
        "global_load_dwordx4 v[220:223], v7, s[6:7] nt\n"
        "v_add_u32 v7, s29, v7\n"
        "v_perm_b32 v120, v124, v120, s30\n"
        "v_perm_b32 v128, v132, v128, s30\n"
        "v_perm_b32 v121, v125, v121, s30\n"
        "v_perm_b32 v129, v133, v129, s30\n"
        "v_perm_b32 v122, v126, v122, s30\n"
        "v_perm_b32 v130, v134, v130, s30\n"
        "v_perm_b32 v123, v127, v123, s30\n"
        "v_perm_b32 v131, v135, v131, s30\n"
        "v_lshl_or_b32 v37, v128, 16, v120\n"
        "v_lshl_or_b32 v41, v129, 16, v121\n"
        "v_lshl_or_b32 v39, v130, 16, v122\n"
        "v_lshl_or_b32 v43, v131, 16, v123\n"
        "s_nop 0\n"
        "v_permlane32_swap_b32 v36, v38\n"
        "v_permlane32_swap_b32 v37, v39\n"
        "v_permlane32_swap_b32 v40, v42\n"
        "v_permlane32_swap_b32 v41, v43\n"
        "s_nop 0\n"
        "ds_write_b128 v17, v[36:39] offset:17664\n"
        "ds_write_b128 v17, v[40:43] offset:18688\n"
        "v_dot4_i32_i8 v28, v36, s31, v28\n"
        "v_dot4_i32_i8 v29, v40, s31, v29\n"
        "v_dot4_i32_i8 v28, v37, s31, v28\n"
        "v_dot4_i32_i8 v29, v41, s31, v29\n"
        "v_dot4_i32_i8 v28, v38, s31, v28\n"
        "v_dot4_i32_i8 v29, v42, s31, v29\n"
        "v_dot4_i32_i8 v28, v39, s31, v28\n"
        "v_dot4_i32_i8 v29, v43, s31, v29\n"
        "s_nop 2\n"
        "ds_write_b64 v21, v[28:29] offset:19712\n"
        "ds_write_b32 v25, v20 offset:1544\n"
        "v_add_u32 v20, 1, v20\n"
        "s_waitcnt vmcnt(23)\n"
        "global_load_dwordx4 v[224:227], v7, s[6:7] nt\n"
        "v_add_u32 v7, s29, v7\n"
        "global_load_dwordx4 v[228:231], v7, s[6:7] nt\n"
        "v_mov_b32 v12, 1\n"
        "ds_write_b32 v25, v12 offset:512\n"
        "s_waitcnt vmcnt(23)\n"
        "v_perm_b32 v136, v140, v136, s30\n"
        "v_perm_b32 v144, v148, v144, s30\n"
        "v_perm_b32 v137, v141, v137, s30\n"
        "v_perm_b32 v145, v149, v145, s30\n"
        "v_perm_b32 v138, v142, v138, s30\n"
        "v_perm_b32 v146, v150, v146, s30\n"
        "v_perm_b32 v139, v143, v139, s30\n"
        "v_perm_b32 v147, v151, v147, s30\n"
        "v_lshl_or_b32 v36, v144, 16, v136\n"
        "v_lshl_or_b32 v40, v145, 16, v137\n"
        "v_lshl_or_b32 v38, v146, 16, v138\n"
        "v_lshl_or_b32 v42, v147, 16, v139\n"
        "s_waitcnt vmcnt(21)\n"
        "s_waitcnt vmcnt(16)\n"
        "v_perm_b32 v152, v156, v152, s30\n"
        "v_perm_b32 v160, v164, v160, s30\n"
        "v_perm_b32 v153, v157, v153, s30\n"
        "v_perm_b32 v161, v165, v161, s30\n"
        "v_perm_b32 v154, v158, v154, s30\n"
        "v_perm_b32 v162, v166, v162, s30\n"
        "v_perm_b32 v155, v159, v155, s30\n"
        "v_perm_b32 v163, v167, v163, s30\n"
        "v_lshl_or_b32 v37, v160, 16, v152\n"
        "v_lshl_or_b32 v41, v161, 16, v153\n"
        "v_lshl_or_b32 v39, v162, 16, v154\n"
        "v_lshl_or_b32 v43, v163, 16, v155\n"
        "s_nop 0\n"
        "v_permlane32_swap_b32 v36, v38\n"
        "v_permlane32_swap_b32 v37, v39\n"
        "v_permlane32_swap_b32 v40, v42\n"
        "v_permlane32_swap_b32 v41, v43\n"
        "s_nop 0\n"
        "global_store_dwordx4 v35, v[36:39], s[44:45] sc1\n"
        "global_store_dwordx4 v35, v[40:43], s[44:45] offset:1024 sc1\n"
        "v_dot4_i32_i8 v30, v36, s31, v30\n"
        "v_dot4_i32_i8 v31, v40, s31, v31\n"
        "v_dot4_i32_i8 v30, v37, s31, v30\n"
        "v_dot4_i32_i8 v31, v41, s31, v31\n"
        "v_dot4_i32_i8 v30, v38, s31, v30\n"
        "v_dot4_i32_i8 v31, v42, s31, v31\n"
        "v_dot4_i32_i8 v30, v39, s31, v30\n"
        "v_dot4_i32_i8 v31, v43, s31, v31\n"
        "s_nop 2\n"
        "v_permlane16_swap_b32 v30, v31\n"
        "s_nop 0\n"
        "v_add_u32 v30, v30, v31\n"
        "v_mov_b32 v31, 0\n"
        "s_nop 0\n"
        "v_add_u32_dpp v34, v30, v30 row_ror:8 row_mask:0xf bank_mask:0xf\n"
        "v_mov_b32 v30, 0\n"
        "s_mov_b32 s38, 8388608\n"
        "W8_tg0:\n"
        "ds_read_b32 v12, v25 offset:1024\n"
        "s_sub_u32 s38, s38, 1\n"
        "s_waitcnt lgkmcnt(0)\n"
        "s_cbranch_scc1 W8_tg0_x\n"
        "v_readfirstlane_b32 s39, v12\n"
        "s_cmp_ge_u32 s39, 1\n"
        "s_cbranch_scc1 W8_tg0_x\n"
        "s_sleep 1\n"
        "s_branch W8_tg0\n"
        "W8_tg0_x:\n"
        "v_lshrrev_b32 v12, 0, v0\n"
        "v_lshlrev_b32 v12, 7, v12\n"
        "v_add_u32 v12, 136968, v12\n"
        "s_mov_b64 exec, 3\n"
        "ds_read_b64 v[26:27], v12\n"
        "s_mov_b64 exec, -1\n"
        "s_waitcnt vmcnt(16)\n"
        "s_waitcnt vmcnt(14)\n"
        "v_perm_b32 v168, v172, v168, s30\n"
        "v_perm_b32 v176, v180, v176, s30\n"
        "v_perm_b32 v169, v173, v169, s30\n"
        "v_perm_b32 v177, v181, v177, s30\n"
        "v_perm_b32 v170, v174, v170, s30\n"
        "v_perm_b32 v178, v182, v178, s30\n"
        "v_perm_b32 v171, v175, v171, s30\n"
        "v_perm_b32 v179, v183, v179, s30\n"
        "v_lshl_or_b32 v36, v176, 16, v168\n"
        "v_lshl_or_b32 v40, v177, 16, v169\n"
        "v_lshl_or_b32 v44, v178, 16, v170\n"
        "v_lshl_or_b32 v48, v179, 16, v171\n"
        "v_dot4_i32_i8 v30, v36, s31, v30\n"
        "v_dot4_i32_i8 v31, v40, s31, v31\n"
        "v_dot4_i32_i8 v32, v44, s31, v32\n"
        "v_dot4_i32_i8 v33, v48, s31, v33\n"
        "s_waitcnt vmcnt(12)\n"
        "s_waitcnt vmcnt(10)\n"
        "v_perm_b32 v184, v188, v184, s30\n"
        "v_perm_b32 v192, v196, v192, s30\n"
        "v_perm_b32 v185, v189, v185, s30\n"
        "v_perm_b32 v193, v197, v193, s30\n"
        "v_perm_b32 v186, v190, v186, s30\n"
        "v_perm_b32 v194, v198, v194, s30\n"
        "v_perm_b32 v187, v191, v187, s30\n"
        "v_perm_b32 v195, v199, v195, s30\n"
        "v_lshl_or_b32 v37, v192, 16, v184\n"
        "v_lshl_or_b32 v41, v193, 16, v185\n"
        "v_lshl_or_b32 v45, v194, 16, v186\n"
        "v_lshl_or_b32 v49, v195, 16, v187\n"
        "v_dot4_i32_i8 v30, v37, s31, v30\n"
        "v_dot4_i32_i8 v31, v41, s31, v31\n"
        "v_dot4_i32_i8 v32, v45, s31, v32\n"
        "v_dot4_i32_i8 v33, v49, s31, v33\n"
        "s_waitcnt vmcnt(8)\n"
        "s_waitcnt vmcnt(6)\n"
        "v_perm_b32 v200, v204, v200, s30\n"
        "v_perm_b32 v208, v212, v208, s30\n"
        "v_perm_b32 v201, v205, v201, s30\n"
        "v_perm_b32 v209, v213, v209, s30\n"
        "v_perm_b32 v202, v206, v202, s30\n"
        "v_perm_b32 v210, v214, v210, s30\n"
        "v_perm_b32 v203, v207, v203, s30\n"
        "v_perm_b32 v211, v215, v211, s30\n"
        "v_lshl_or_b32 v38, v208, 16, v200\n"
        "v_lshl_or_b32 v42, v209, 16, v201\n"
        "v_lshl_or_b32 v46, v210, 16, v202\n"
        "v_lshl_or_b32 v50, v211, 16, v203\n"
        "v_dot4_i32_i8 v30, v38, s31, v30\n"
        "v_dot4_i32_i8 v31, v42, s31, v31\n"
        "v_dot4_i32_i8 v32, v46, s31, v32\n"
        "v_dot4_i32_i8 v33, v50, s31, v33\n"
        "s_waitcnt vmcnt(4)\n"
        "s_waitcnt vmcnt(2)\n"
        "v_perm_b32 v216, v220, v216, s30\n"
        "v_perm_b32 v224, v228, v224, s30\n"
        "v_perm_b32 v217, v221, v217, s30\n"
        "v_perm_b32 v225, v229, v225, s30\n"
        "v_perm_b32 v218, v222, v218, s30\n"
        "v_perm_b32 v226, v230, v226, s30\n"
        "v_perm_b32 v219, v223, v219, s30\n"
        "v_perm_b32 v227, v231, v227, s30\n"
        "v_lshl_or_b32 v39, v224, 16, v216\n"
        "v_lshl_or_b32 v43, v225, 16, v217\n"
        "v_lshl_or_b32 v47, v226, 16, v218\n"
        "v_lshl_or_b32 v51, v227, 16, v219\n"
        "ds_write_b32 v25, v20 offset:1544\n"
        "global_store_dwordx4 v19, v[36:39], s[44:45] offset:0 sc1\n"
        "global_store_dwordx4 v19, v[40:43], s[44:45] offset:1024 sc1\n"
        "global_store_dwordx4 v19, v[44:47], s[44:45] offset:2048 sc1\n"
        "global_store_dwordx4 v19, v[48:51], s[44:45] offset:3072 sc1\n"
        "v_dot4_i32_i8 v30, v39, s31, v30\n"
        "v_dot4_i32_i8 v31, v43, s31, v31\n"
        "v_dot4_i32_i8 v32, v47, s31, v32\n"
        "v_dot4_i32_i8 v33, v51, s31, v33\n"
        "s_nop 2\n"
        "v_permlane32_swap_b32 v30, v32\n"
        "v_permlane32_swap_b32 v31, v33\n"
        "s_nop 0\n"
        "v_add_u32 v30, v30, v32\n"
        "v_add_u32 v31, v31, v33\n"
        "s_nop 1\n"
        "v_permlane16_swap_b32 v30, v31\n"
        "s_nop 0\n"
        "v_add_u32 v30, v30, v31\n"
        "global_store_dword v22, v30, s[46:47] offset:512 sc1\n"
        "s_mov_b32 exec_lo, 0x00ff00ff\n"
        "s_mov_b32 exec_hi, 0x00ff00ff\n"
        "global_store_dword v23, v34, s[46:47] offset:768 sc1\n"
        "s_mov_b64 exec, 2\n"
        "s_waitcnt vmcnt(0) lgkmcnt(0)\n"
        "global_atomic_swap_x2 v24, v[26:27], s[8:9] sc1\n"
        "s_mov_b64 exec, -1\n"
        "s_load_dword s48, s[18:19], 0x0\n"
        "s_mov_b32 exec_hi, 0x00ffffff\n"
        "v_mov_b32 v32, v252\n"
        "v_mov_b32 v34, v248\n"
        "v_mov_b32 v35, v249\n"
        "v_mov_b32 v38, v250\n"
        "v_mov_b32 v39, v251\n"
        "s_lshl_b32 s24, s22, 14\n"
        "s_add_u32 s24, s24, 5771264\n"
        "v_add_u32 v21, s24, v19\n"
        "v_add_u32 v22, 8192, v21\n"
        "v_lshlrev_b32 v23, 5, v0\n"
        "v_add_u32 v23, 133632, v23\n"
        "s_waitcnt vmcnt(0)\n"
        "v_lshlrev_b32 v32, 16, v32\n"
        "v_and_b32 v37, 0xffff0000, v35\n"
        "v_lshlrev_b32 v36, 16, v35\n"
        "v_and_b32 v35, 0xffff0000, v34\n"
        "v_lshlrev_b32 v34, 16, v34\n"
        "v_and_b32 v41, 0xffff0000, v39\n"
        "v_lshlrev_b32 v40, 16, v39\n"
        "v_and_b32 v39, 0xffff0000, v38\n"
        "v_lshlrev_b32 v38, 16, v38\n"
        "v_mul_f32 v44, v32, v34\n"
        "v_mul_f32 v46, v32, v35\n"
        "v_mul_f32 v48, v32, v36\n"
        "v_mul_f32 v50, v32, v37\n"
        "s_mov_b32 s49, 6\n"
        "s_mov_b32 s50, -1\n"
        "s_mov_b32 s51, 16777215\n"
        "v_mov_b32 v33, 3\n"
        "W8_tab:\n"
        "s_mov_b64 exec, -1\n"
        "s_mov_b32 s38, 8388608\n"
        "W8_tr:\n"
        "ds_read_b32 v12, v25 offset:1024\n"
        "s_sub_u32 s38, s38, 1\n"
        "s_waitcnt lgkmcnt(0)\n"
        "s_cbranch_scc1 W8_tr_x\n"
        "v_readfirstlane_b32 s39, v12\n"
        "s_cmp_ge_u32 s39, 21\n"
        "s_cbranch_scc1 W8_tr_x\n"
        "s_sleep 2\n"
        "s_branch W8_tr\n"
        "W8_tr_x:\n"
        "s_mov_b32 s38, 8388608\n"
        "W8_tf:\n"
        "ds_read_b32 v12, v25 offset:0\n"
        "s_sub_u32 s38, s38, 1\n"
        "s_waitcnt lgkmcnt(0)\n"
        "s_cbranch_scc1 W8_tf_x\n"
        "v_readfirstlane_b32 s39, v12\n"
        "s_cmp_ge_u32 s39, 12\n"
        "s_cbranch_scc1 W8_tf_x\n"
        "s_sleep 2\n"
        "s_branch W8_tf\n"
        "W8_tf_x:\n"
        "s_mov_b64 exec, s[50:51]\n"
        "global_load_dwordx4 v[64:67], v21, s[8:9] offset:-4096 sc1\n"
        "global_load_dwordx4 v[68:71], v21, s[8:9] offset:-3072 sc1\n"
        "global_load_dwordx4 v[72:75], v21, s[8:9] offset:-2048 sc1\n"
        "global_load_dwordx4 v[76:79], v21, s[8:9] offset:-1024 sc1\n"
        "global_load_dwordx4 v[80:83], v21, s[8:9] offset:0 sc1\n"
        "global_load_dwordx4 v[84:87], v21, s[8:9] offset:1024 sc1\n"
        "global_load_dwordx4 v[88:91], v21, s[8:9] offset:2048 sc1\n"
        "global_load_dwordx4 v[92:95], v21, s[8:9] offset:3072 sc1\n"
        "global_load_dwordx4 v[96:99], v22, s[8:9] offset:-4096 sc1\n"
        "global_load_dwordx4 v[100:103], v22, s[8:9] offset:-3072 sc1\n"
        "global_load_dwordx4 v[104:107], v22, s[8:9] offset:-2048 sc1\n"
        "global_load_dwordx4 v[108:111], v22, s[8:9] offset:-1024 sc1\n"
        "global_load_dwordx4 v[112:115], v22, s[8:9] offset:0 sc1\n"
        "global_load_dwordx4 v[116:119], v22, s[8:9] offset:1024 sc1\n"
        "global_load_dwordx4 v[120:123], v22, s[8:9] offset:2048 sc1\n"
        "global_load_dwordx4 v[124:127], v22, s[8:9] offset:3072 sc1\n"
        "s_waitcnt vmcnt(0)\n"
        "v_add3_u32 v52, v64, v68, v72\n"
        "v_add3_u32 v53, v65, v69, v73\n"
        "v_add3_u32 v54, v66, v70, v74\n"
        "v_add3_u32 v55, v67, v71, v75\n"
        "v_add3_u32 v52, v52, v76, v80\n"
        "v_add3_u32 v53, v53, v77, v81\n"
        "v_add3_u32 v54, v54, v78, v82\n"
        "v_add3_u32 v55, v55, v79, v83\n"
        "v_add3_u32 v52, v52, v84, v88\n"
        "v_add3_u32 v53, v53, v85, v89\n"
        "v_add3_u32 v54, v54, v86, v90\n"
        "v_add3_u32 v55, v55, v87, v91\n"
        "v_add3_u32 v52, v52, v92, v96\n"
        "v_add3_u32 v53, v53, v93, v97\n"
        "v_add3_u32 v54, v54, v94, v98\n"
        "v_add3_u32 v55, v55, v95, v99\n"
        "v_add3_u32 v52, v52, v100, v104\n"
        "v_add3_u32 v53, v53, v101, v105\n"
        "v_add3_u32 v54, v54, v102, v106\n"
        "v_add3_u32 v55, v55, v103, v107\n"
        "v_add3_u32 v52, v52, v108, v112\n"
        "v_add3_u32 v53, v53, v109, v113\n"
        "v_add3_u32 v54, v54, v110, v114\n"
        "v_add3_u32 v55, v55, v111, v115\n"
        "v_add3_u32 v52, v52, v116, v120\n"
        "v_add3_u32 v53, v53, v117, v121\n"
        "v_add3_u32 v54, v54, v118, v122\n"
        "v_add3_u32 v55, v55, v119, v123\n"
        "v_add_u32 v52, v52, v124\n"
        "v_add_u32 v53, v53, v125\n"
        "v_add_u32 v54, v54, v126\n"
        "v_add_u32 v55, v55, v127\n"
        "v_mul_lo_u32 v52, v52, s48\n"
        "v_mul_lo_u32 v53, v53, s48\n"
        "v_mul_lo_u32 v54, v54, s48\n"
        "v_mul_lo_u32 v55, v55, s48\n"
        "v_cvt_f32_i32 v52, v52\n"
        "v_cvt_f32_i32 v53, v53\n"
        "v_cvt_f32_i32 v54, v54\n"
        "v_cvt_f32_i32 v55, v55\n"
        "v_fma_f32 v45, -v52, v44, v38\n"
        "v_fma_f32 v47, -v53, v46, v39\n"
        "v_fma_f32 v49, -v54, v48, v40\n"
        "v_fma_f32 v51, -v55, v50, v41\n"
        "ds_write_b128 v23, v[44:47]\n"
        "ds_write_b128 v23, v[48:51] offset:16\n"
        "s_mov_b64 exec, -1\n"
        "ds_write_b32 v25, v33 offset:512\n"
        "v_add_u32 v33, 1, v33\n"
        "s_mov_b32 s50, 0\n"
        "s_mov_b32 s51, 0x00ffffff\n"
        "s_add_u32 s49, s49, 2\n"
        "s_cmp_eq_u32 s49, 6\n"
        "s_cbranch_scc1 W8_tab\n"
        "s_branch W8_fin\n"
        "W8_other:\n"
        "v_and_b32 v1, 63, v0\n"
        "s_add_u32 s33, s21, s22\n"
        "s_cmp_ge_u32 s20, 4\n"
        "s_cbranch_scc1 W8_comp\n"
        "v_lshlrev_b32 v25, 2, v1\n"
        "v_add_u32 v25, 135684, v25\n"
        "s_cmp_eq_u32 s20, 1\n"
        "s_cbranch_scc1 W8_load\n"
        "s_and_b32 s30, s20, 1\n"
        "s_lshl_b32 s24, s21, 9\n"
        "s_add_u32 s24, s24, 6029312\n"
        "s_lshl_b32 s25, s22, 10\n"
        "s_add_u32 s25, s25, 6291456\n"
        "s_add_u32 s26, s22, 16\n"
        "s_cmp_eq_u32 s30, 0\n"
        "s_cselect_b32 s24, s24, s25\n"
        "s_cselect_b32 s26, s21, s26\n"
        "s_cselect_b32 s31, s22, s21\n"
        "s_mul_i32 s27, s30, 768\n"
        "s_add_u32 s27, s27, 256\n"
        "s_lshl_b32 s26, s26, 7\n"
        "s_add_u32 s26, s26, 6815744\n"
        "v_mov_b32 v5, s26\n"
        "v_add_u32 v8, s27, v25\n"
        "s_lshl_b32 s26, s30, 7\n"
        "s_add_u32 s26, s26, 136968\n"
        "v_mov_b32 v12, s26\n"
        "s_add_u32 s24, s8, s24\n"
        "s_addc_u32 s25, s9, 0\n"
        "s_add_u32 s28, s30, 1\n"
        "v_mov_b32 v13, s28\n"
        "s_lshl_b32 s28, s30, 0\n"
        "s_add_u32 s28, s28, 4\n"
        "s_mov_b32 s38, 8388608\n"
        "W8_tg2:\n"
        "ds_read_b32 v14, v25 offset:1024\n"
        "s_sub_u32 s38, s38, 1\n"
        "s_waitcnt lgkmcnt(0)\n"
        "s_cbranch_scc1 W8_tg2_x\n"
        "v_readfirstlane_b32 s39, v14\n"
        "s_cmp_ge_u32 s39, 1\n"
        "s_cbranch_scc1 W8_tg2_x\n"
        "s_sleep 2\n"
        "s_branch W8_tg2\n"
        "W8_tg2_x:\n"
        "ds_read_b64 v[6:7], v12\n"
        "s_mov_b32 s38, 8388608\n"
        "W8_pst:\n"
        "ds_read_b32 v14, v25 offset:1544\n"
        "s_sub_u32 s38, s38, 1\n"
        "s_waitcnt lgkmcnt(0)\n"
        "s_cbranch_scc1 W8_pst_x\n"
        "v_readfirstlane_b32 s39, v14\n"
        "s_cmp_ge_u32 s39, 3\n"
        "s_cbranch_scc1 W8_pst_x\n"
        "s_sleep 4\n"
        "s_branch W8_pst\n"
        "W8_pst_x:\n"
        "s_add_u32 s14, s21, s22\n"
        "s_and_b32 s12, s14, 2\n"
        "s_lshl_b32 s12, s12, 5\n"
        "s_xor_b32 s13, s12, 64\n"
        "s_and_b32 s14, s14, 1\n"
        "v_add_u32 v16, 1, v13\n"
        "v_add_u32 v17, 2, v13\n"
        "v_add_u32 v18, 3, v13\n"
        "v_mov_b32 v15, v13\n"
        "s_mov_b32 s29, 2097152\n"
        "W8_pl:\n"
        "s_load_dwordx16 s[32:47], s[24:25], s12 glc\n"
        "s_load_dwordx16 s[48:63], s[24:25], s13 glc\n"
        "s_sub_u32 s29, s29, 1\n"
        "s_cselect_b32 s27, 1, 0\n"
        "s_cmp_eq_u32 s14, 1\n"
        "s_waitcnt lgkmcnt(0)\n"
        "s_cbranch_scc1 W8_plb\n"
        "v_cmpx_eq_u64 vcc, s[32:33], v[6:7]\n"
        "v_cmpx_eq_u64 vcc, s[34:35], v[6:7]\n"
        "v_cmpx_eq_u64 vcc, s[36:37], v[6:7]\n"
        "v_cmpx_eq_u64 vcc, s[38:39], v[6:7]\n"
        "ds_write_b32 v8, v15\n"
        "v_cmpx_eq_u64 vcc, s[40:41], v[6:7]\n"
        "v_cmpx_eq_u64 vcc, s[42:43], v[6:7]\n"
        "v_cmpx_eq_u64 vcc, s[44:45], v[6:7]\n"
        "v_cmpx_eq_u64 vcc, s[46:47], v[6:7]\n"
        "ds_write_b32 v8, v16\n"
        "v_cmpx_eq_u64 vcc, s[48:49], v[6:7]\n"
        "v_cmpx_eq_u64 vcc, s[50:51], v[6:7]\n"
        "v_cmpx_eq_u64 vcc, s[52:53], v[6:7]\n"
        "v_cmpx_eq_u64 vcc, s[54:55], v[6:7]\n"
        "ds_write_b32 v8, v17\n"
        "v_cmpx_eq_u64 vcc, s[56:57], v[6:7]\n"
        "v_cmpx_eq_u64 vcc, s[58:59], v[6:7]\n"
        "v_cmpx_eq_u64 vcc, s[60:61], v[6:7]\n"
        "v_cmpx_eq_u64 vcc, s[62:63], v[6:7]\n"
        "s_branch W8_plc\n"
        "W8_plb:\n"
        "v_cmpx_eq_u64 vcc, s[40:41], v[6:7]\n"
        "v_cmpx_eq_u64 vcc, s[42:43], v[6:7]\n"
        "v_cmpx_eq_u64 vcc, s[44:45], v[6:7]\n"
        "v_cmpx_eq_u64 vcc, s[46:47], v[6:7]\n"
        "ds_write_b32 v8, v15\n"
        "v_cmpx_eq_u64 vcc, s[32:33], v[6:7]\n"
        "v_cmpx_eq_u64 vcc, s[34:35], v[6:7]\n"
        "v_cmpx_eq_u64 vcc, s[36:37], v[6:7]\n"
        "v_cmpx_eq_u64 vcc, s[38:39], v[6:7]\n"
        "ds_write_b32 v8, v16\n"
        "v_cmpx_eq_u64 vcc, s[56:57], v[6:7]\n"
        "v_cmpx_eq_u64 vcc, s[58:59], v[6:7]\n"
        "v_cmpx_eq_u64 vcc, s[60:61], v[6:7]\n"
        "v_cmpx_eq_u64 vcc, s[62:63], v[6:7]\n"
        "ds_write_b32 v8, v17\n"
        "v_cmpx_eq_u64 vcc, s[48:49], v[6:7]\n"
        "v_cmpx_eq_u64 vcc, s[50:51], v[6:7]\n"
        "v_cmpx_eq_u64 vcc, s[52:53], v[6:7]\n"
        "v_cmpx_eq_u64 vcc, s[54:55], v[6:7]\n"
        "W8_plc:\n"
        "s_nop 4\n"
        "s_cbranch_execnz W8_pg\n"
        "s_mov_b64 exec, -1\n"
        "s_cmp_eq_u32 s27, 1\n"
        "s_cbranch_scc1 W8_pg\n"
        "s_sleep 1\n"
        "s_branch W8_pl\n"
        "W8_pg:\n"
        "ds_write_b32 v8, v18\n"
        "s_max_i32 s29, s29, 0\n"
        "v_add_u32 v15, 4, v15\n"
        "v_add_u32 v16, 4, v16\n"
        "v_add_u32 v17, 4, v17\n"
        "v_add_u32 v18, 4, v18\n"
        "s_add_u32 s24, s24, 128\n"
        "s_addc_u32 s25, s25, 0\n"
        "s_sub_u32 s28, s28, 1\n"
        "s_cmp_lg_u32 s28, 0\n"
        "s_cbranch_scc1 W8_pl\n"
        "s_cmp_lg_u32 s31, 0\n"
        "s_cbranch_scc1 W8_fin\n"
        "s_mov_b64 exec, 1\n"
        "global_store_dwordx2 v5, v[6:7], s[8:9] sc1\n"
        "s_branch W8_fin\n"
        "W8_load:\n"
        "v_lshlrev_b32 v2, 4, v1\n"
        "s_xor_b32 s24, s33, 0\n"
        "s_and_b32 s24, s24, 3\n"
        "s_lshl_b32 s25, s24, 13\n"
        "v_add_u32 v4, s25, v2\n"
        "v_add_u32 v8, 4096, v4\n"
        "s_mul_i32 s25, s24, 6144\n"
        "v_add_u32 v12, s25, v2\n"
        "s_xor_b32 s24, s33, 1\n"
        "s_and_b32 s24, s24, 3\n"
        "s_lshl_b32 s25, s24, 13\n"
        "v_add_u32 v5, s25, v2\n"
        "v_add_u32 v9, 4096, v5\n"
        "s_mul_i32 s25, s24, 6144\n"
        "v_add_u32 v13, s25, v2\n"
        "s_xor_b32 s24, s33, 2\n"
        "s_and_b32 s24, s24, 3\n"
        "s_lshl_b32 s25, s24, 13\n"
        "v_add_u32 v6, s25, v2\n"
        "v_add_u32 v10, 4096, v6\n"
        "s_mul_i32 s25, s24, 6144\n"
        "v_add_u32 v14, s25, v2\n"
        "s_xor_b32 s24, s33, 3\n"
        "s_and_b32 s24, s24, 3\n"
        "s_lshl_b32 s25, s24, 13\n"
        "v_add_u32 v7, s25, v2\n"
        "v_add_u32 v11, 4096, v7\n"
        "s_mul_i32 s25, s24, 6144\n"
        "v_add_u32 v15, s25, v2\n"
        "s_waitcnt lgkmcnt(0)\n"
        "s_lshl_b32 s24, s22, 17\n"
        "s_add_u32 s24, s24, 2097152\n"
        "s_add_u32 s40, s8, s24\n"
        "s_addc_u32 s41, s9, 0\n"
        "s_mul_i32 s24, s22, 98304\n"
        "s_add_u32 s24, s24, 4194304\n"
        "s_add_u32 s42, s8, s24\n"
        "s_addc_u32 s43, s9, 0\n"
        "s_mov_b32 s34, 0\n"
        "s_mov_b32 s35, 2\n"
        "v_mov_b32 v3, -2\n"
        "s_mov_b32 s38, 8388608\n"
        "W8_lgo:\n"
        "ds_read_b32 v20, v25 offset:512\n"
        "s_sub_u32 s38, s38, 1\n"
        "s_waitcnt lgkmcnt(0)\n"
        "s_cbranch_scc1 W8_lgo_x\n"
        "v_readfirstlane_b32 s39, v20\n"
        "s_cmp_ge_u32 s39, 1\n"
        "s_cbranch_scc1 W8_lgo_x\n"
        "s_sleep 2\n"
        "s_branch W8_lgo\n"
        "W8_lgo_x:\n"
        "s_mov_b32 s38, 8388608\n"
        "W8_lr00:\n"
        "ds_read_b32 v20, v25 offset:1024\n"
        "s_sub_u32 s38, s38, 1\n"
        "s_waitcnt lgkmcnt(0)\n"
        "s_cbranch_scc1 W8_lr00_x\n"
        "v_readfirstlane_b32 s39, v20\n"
        "s_cmp_ge_u32 s39, 2\n"
        "s_cbranch_scc1 W8_lr00_x\n"
        "s_sleep 1\n"
        "s_branch W8_lr00\n"
        "W8_lr00_x:\n"
        "s_mov_b32 m0, s34\n"
        "s_add_u32 s34, s34, 4096\n"
        "global_load_lds_dwordx4 v4, s[40:41] offset:0 sc1\n"
        "global_load_lds_dwordx4 v4, s[40:41] offset:1024 sc1\n"
        "global_load_lds_dwordx4 v4, s[40:41] offset:2048 sc1\n"
        "global_load_lds_dwordx4 v4, s[40:41] offset:3072 sc1\n"
        "s_mov_b32 m0, s34\n"
        "s_add_u32 s34, s34, 4096\n"
        "global_load_lds_dwordx4 v8, s[40:41] offset:0 sc1\n"
        "global_load_lds_dwordx4 v8, s[40:41] offset:1024 sc1\n"
        "global_load_lds_dwordx4 v8, s[40:41] offset:2048 sc1\n"
        "global_load_lds_dwordx4 v8, s[40:41] offset:3072 sc1\n"
        "s_cmp_ge_u32 s39, 3\n"
        "s_cbranch_scc1 W8_lg1_x\n"
        "ds_read_b32 v20, v25 offset:1024\n"
        "s_waitcnt lgkmcnt(0)\n"
        "v_readfirstlane_b32 s39, v20\n"
        "s_cmp_ge_u32 s39, 3\n"
        "s_cbranch_scc1 W8_lg1_x\n"
        "v_mov_b32 v21, 1\n"
        "s_waitcnt vmcnt(0)\n"
        "ds_max_i32 v25, v21 offset:0\n"
        "s_mov_b32 s38, 8388608\n"
        "W8_lg1:\n"
        "ds_read_b32 v20, v25 offset:1024\n"
        "s_sub_u32 s38, s38, 1\n"
        "s_waitcnt lgkmcnt(0)\n"
        "s_cbranch_scc1 W8_lg1_x\n"
        "v_readfirstlane_b32 s39, v20\n"
        "s_cmp_ge_u32 s39, 3\n"
        "s_cbranch_scc1 W8_lg1_x\n"
        "s_sleep 1\n"
        "s_branch W8_lg1\n"
        "W8_lg1_x:\n"
        "s_mov_b32 m0, s34\n"
        "s_add_u32 s34, s34, 4096\n"
        "global_load_lds_dwordx4 v5, s[40:41] offset:0 sc1\n"
        "global_load_lds_dwordx4 v5, s[40:41] offset:1024 sc1\n"
        "global_load_lds_dwordx4 v5, s[40:41] offset:2048 sc1\n"
        "global_load_lds_dwordx4 v5, s[40:41] offset:3072 sc1\n"
        "s_mov_b32 m0, s34\n"
        "s_add_u32 s34, s34, 4096\n"
        "global_load_lds_dwordx4 v9, s[40:41] offset:0 sc1\n"
        "global_load_lds_dwordx4 v9, s[40:41] offset:1024 sc1\n"
        "global_load_lds_dwordx4 v9, s[40:41] offset:2048 sc1\n"
        "global_load_lds_dwordx4 v9, s[40:41] offset:3072 sc1\n"
        "v_mov_b32 v21, 1\n"
        "s_waitcnt vmcnt(8)\n"
        "ds_max_i32 v25, v21 offset:0\n"
        "s_cmp_ge_u32 s39, 4\n"
        "s_cbranch_scc1 W8_lg2_x\n"
        "ds_read_b32 v20, v25 offset:1024\n"
        "s_waitcnt lgkmcnt(0)\n"
        "v_readfirstlane_b32 s39, v20\n"
        "s_cmp_ge_u32 s39, 4\n"
        "s_cbranch_scc1 W8_lg2_x\n"
        "v_mov_b32 v21, 2\n"
        "s_waitcnt vmcnt(0)\n"
        "ds_max_i32 v25, v21 offset:0\n"
        "s_mov_b32 s38, 8388608\n"
        "W8_lg2:\n"
        "ds_read_b32 v20, v25 offset:1024\n"
        "s_sub_u32 s38, s38, 1\n"
        "s_waitcnt lgkmcnt(0)\n"
        "s_cbranch_scc1 W8_lg2_x\n"
        "v_readfirstlane_b32 s39, v20\n"
        "s_cmp_ge_u32 s39, 4\n"
        "s_cbranch_scc1 W8_lg2_x\n"
        "s_sleep 1\n"
        "s_branch W8_lg2\n"
        "W8_lg2_x:\n"
        "s_mov_b32 m0, s34\n"
        "s_add_u32 s34, s34, 4096\n"
        "global_load_lds_dwordx4 v6, s[40:41] offset:0 sc1\n"
        "global_load_lds_dwordx4 v6, s[40:41] offset:1024 sc1\n"
        "global_load_lds_dwordx4 v6, s[40:41] offset:2048 sc1\n"
        "global_load_lds_dwordx4 v6, s[40:41] offset:3072 sc1\n"
        "s_mov_b32 m0, s34\n"
        "s_add_u32 s34, s34, 4096\n"
        "global_load_lds_dwordx4 v10, s[40:41] offset:0 sc1\n"
        "global_load_lds_dwordx4 v10, s[40:41] offset:1024 sc1\n"
        "global_load_lds_dwordx4 v10, s[40:41] offset:2048 sc1\n"
        "global_load_lds_dwordx4 v10, s[40:41] offset:3072 sc1\n"
        "s_cmp_ge_u32 s39, 5\n"
        "s_cbranch_scc1 W8_lg3_x\n"
        "ds_read_b32 v20, v25 offset:1024\n"
        "s_waitcnt lgkmcnt(0)\n"
        "v_readfirstlane_b32 s39, v20\n"
        "s_cmp_ge_u32 s39, 5\n"
        "s_cbranch_scc1 W8_lg3_x\n"
        "v_mov_b32 v21, 2\n"
        "s_waitcnt vmcnt(8)\n"
        "ds_max_i32 v25, v21 offset:0\n"
        "v_mov_b32 v21, 3\n"
        "s_waitcnt vmcnt(0)\n"
        "ds_max_i32 v25, v21 offset:0\n"
        "s_mov_b32 s38, 8388608\n"
        "W8_lg3:\n"
        "ds_read_b32 v20, v25 offset:1024\n"
        "s_sub_u32 s38, s38, 1\n"
        "s_waitcnt lgkmcnt(0)\n"
        "s_cbranch_scc1 W8_lg3_x\n"
        "v_readfirstlane_b32 s39, v20\n"
        "s_cmp_ge_u32 s39, 5\n"
        "s_cbranch_scc1 W8_lg3_x\n"
        "s_sleep 1\n"
        "s_branch W8_lg3\n"
        "W8_lg3_x:\n"
        "s_mov_b32 m0, s34\n"
        "s_add_u32 s34, s34, 4096\n"
        "global_load_lds_dwordx4 v7, s[40:41] offset:0 sc1\n"
        "global_load_lds_dwordx4 v7, s[40:41] offset:1024 sc1\n"
        "global_load_lds_dwordx4 v7, s[40:41] offset:2048 sc1\n"
        "global_load_lds_dwordx4 v7, s[40:41] offset:3072 sc1\n"
        "s_mov_b32 m0, s34\n"
        "s_add_u32 s34, s34, 4096\n"
        "global_load_lds_dwordx4 v11, s[40:41] offset:0 sc1\n"
        "global_load_lds_dwordx4 v11, s[40:41] offset:1024 sc1\n"
        "global_load_lds_dwordx4 v11, s[40:41] offset:2048 sc1\n"
        "global_load_lds_dwordx4 v11, s[40:41] offset:3072 sc1\n"
        "v_mov_b32 v21, 2\n"
        "s_waitcnt vmcnt(16)\n"
        "ds_max_i32 v25, v21 offset:0\n"
        "v_mov_b32 v3, 2\n"
        "s_add_u32 s40, s40, 32768\n"
        "s_addc_u32 s41, s41, 0\n"
        "s_mov_b32 s35, 3\n"
        "s_mov_b32 s36, 6\n"
        "W8_lq:\n"
        "ds_read_b32 v20, v25 offset:1024\n"
        "s_waitcnt lgkmcnt(0)\n"
        "v_readfirstlane_b32 s39, v20\n"
        "s_cmp_ge_u32 s39, s36\n"
        "s_cbranch_scc1 W8_lr_x\n"
        "v_add_u32 v21, 1, v3\n"
        "s_waitcnt vmcnt(8)\n"
        "ds_max_i32 v25, v21 offset:0\n"
        "v_add_u32 v21, 2, v3\n"
        "s_waitcnt vmcnt(0)\n"
        "ds_max_i32 v25, v21 offset:0\n"
        "s_mov_b32 s38, 8388608\n"
        "W8_lr:\n"
        "ds_read_b32 v20, v25 offset:1024\n"
        "s_sub_u32 s38, s38, 1\n"
        "s_waitcnt lgkmcnt(0)\n"
        "s_cbranch_scc1 W8_lr_x\n"
        "v_readfirstlane_b32 s39, v20\n"
        "s_cmp_ge_u32 s39, s36\n"
        "s_cbranch_scc1 W8_lr_x\n"
        "s_sleep 1\n"
        "s_branch W8_lr\n"
        "W8_lr_x:\n"
        "s_mov_b32 m0, s34\n"
        "s_add_u32 s34, s34, 4096\n"
        "global_load_lds_dwordx4 v4, s[40:41] offset:0 sc1\n"
        "global_load_lds_dwordx4 v4, s[40:41] offset:1024 sc1\n"
        "global_load_lds_dwordx4 v4, s[40:41] offset:2048 sc1\n"
        "global_load_lds_dwordx4 v4, s[40:41] offset:3072 sc1\n"
        "s_mov_b32 m0, s34\n"
        "s_add_u32 s34, s34, 4096\n"
        "global_load_lds_dwordx4 v8, s[40:41] offset:0 sc1\n"
        "global_load_lds_dwordx4 v8, s[40:41] offset:1024 sc1\n"
        "global_load_lds_dwordx4 v8, s[40:41] offset:2048 sc1\n"
        "global_load_lds_dwordx4 v8, s[40:41] offset:3072 sc1\n"
        "v_add_u32 v3, 1, v3\n"
        "s_waitcnt vmcnt(16)\n"
        "ds_max_i32 v25, v3 offset:0\n"
        "s_add_u32 s36, s36, 1\n"
        "s_cmp_ge_u32 s39, s36\n"
        "s_cbranch_scc1 W8_lu1_x\n"
        "ds_read_b32 v20, v25 offset:1024\n"
        "s_waitcnt lgkmcnt(0)\n"
        "v_readfirstlane_b32 s39, v20\n"
        "s_cmp_ge_u32 s39, s36\n"
        "s_cbranch_scc1 W8_lu1_x\n"
        "v_add_u32 v21, 1, v3\n"
        "s_waitcnt vmcnt(8)\n"
        "ds_max_i32 v25, v21 offset:0\n"
        "v_add_u32 v21, 2, v3\n"
        "s_waitcnt vmcnt(0)\n"
        "ds_max_i32 v25, v21 offset:0\n"
        "s_mov_b32 s38, 8388608\n"
        "W8_lu1:\n"
        "ds_read_b32 v20, v25 offset:1024\n"
        "s_sub_u32 s38, s38, 1\n"
        "s_waitcnt lgkmcnt(0)\n"
        "s_cbranch_scc1 W8_lu1_x\n"
        "v_readfirstlane_b32 s39, v20\n"
        "s_cmp_ge_u32 s39, s36\n"
        "s_cbranch_scc1 W8_lu1_x\n"
        "s_sleep 1\n"
        "s_branch W8_lu1\n"
        "W8_lu1_x:\n"
        "s_mov_b32 m0, s34\n"
        "s_add_u32 s34, s34, 4096\n"
        "global_load_lds_dwordx4 v5, s[40:41] offset:0 sc1\n"
        "global_load_lds_dwordx4 v5, s[40:41] offset:1024 sc1\n"
        "global_load_lds_dwordx4 v5, s[40:41] offset:2048 sc1\n"
        "global_load_lds_dwordx4 v5, s[40:41] offset:3072 sc1\n"
        "s_mov_b32 m0, s34\n"
        "s_add_u32 s34, s34, 4096\n"
        "global_load_lds_dwordx4 v9, s[40:41] offset:0 sc1\n"
        "global_load_lds_dwordx4 v9, s[40:41] offset:1024 sc1\n"
        "global_load_lds_dwordx4 v9, s[40:41] offset:2048 sc1\n"
        "global_load_lds_dwordx4 v9, s[40:41] offset:3072 sc1\n"
        "v_add_u32 v3, 1, v3\n"
        "s_waitcnt vmcnt(16)\n"
        "ds_max_i32 v25, v3 offset:0\n"
        "s_add_u32 s36, s36, 1\n"
        "s_cmp_ge_u32 s39, s36\n"
        "s_cbranch_scc1 W8_lu2_x\n"
        "ds_read_b32 v20, v25 offset:1024\n"
        "s_waitcnt lgkmcnt(0)\n"
        "v_readfirstlane_b32 s39, v20\n"
        "s_cmp_ge_u32 s39, s36\n"
        "s_cbranch_scc1 W8_lu2_x\n"
        "v_add_u32 v21, 1, v3\n"
        "s_waitcnt vmcnt(8)\n"
        "ds_max_i32 v25, v21 offset:0\n"
        "v_add_u32 v21, 2, v3\n"
        "s_waitcnt vmcnt(0)\n"
        "ds_max_i32 v25, v21 offset:0\n"
        "s_mov_b32 s38, 8388608\n"
        "W8_lu2:\n"
        "ds_read_b32 v20, v25 offset:1024\n"
        "s_sub_u32 s38, s38, 1\n"
        "s_waitcnt lgkmcnt(0)\n"
        "s_cbranch_scc1 W8_lu2_x\n"
        "v_readfirstlane_b32 s39, v20\n"
        "s_cmp_ge_u32 s39, s36\n"
        "s_cbranch_scc1 W8_lu2_x\n"
        "s_sleep 1\n"
        "s_branch W8_lu2\n"
        "W8_lu2_x:\n"
        "s_mov_b32 m0, s34\n"
        "s_add_u32 s34, s34, 4096\n"
        "global_load_lds_dwordx4 v6, s[40:41] offset:0 sc1\n"
        "global_load_lds_dwordx4 v6, s[40:41] offset:1024 sc1\n"
        "global_load_lds_dwordx4 v6, s[40:41] offset:2048 sc1\n"
        "global_load_lds_dwordx4 v6, s[40:41] offset:3072 sc1\n"
        "s_mov_b32 m0, s34\n"
        "s_add_u32 s34, s34, 4096\n"
        "global_load_lds_dwordx4 v10, s[40:41] offset:0 sc1\n"
        "global_load_lds_dwordx4 v10, s[40:41] offset:1024 sc1\n"
        "global_load_lds_dwordx4 v10, s[40:41] offset:2048 sc1\n"
        "global_load_lds_dwordx4 v10, s[40:41] offset:3072 sc1\n"
        "v_add_u32 v3, 1, v3\n"
        "s_waitcnt vmcnt(16)\n"
        "ds_max_i32 v25, v3 offset:0\n"
        "s_add_u32 s36, s36, 1\n"
        "s_cmp_ge_u32 s39, s36\n"
        "s_cbranch_scc1 W8_lu3_x\n"
        "ds_read_b32 v20, v25 offset:1024\n"
        "s_waitcnt lgkmcnt(0)\n"
        "v_readfirstlane_b32 s39, v20\n"
        "s_cmp_ge_u32 s39, s36\n"
        "s_cbranch_scc1 W8_lu3_x\n"
        "v_add_u32 v21, 1, v3\n"
        "s_waitcnt vmcnt(8)\n"
        "ds_max_i32 v25, v21 offset:0\n"
        "v_add_u32 v21, 2, v3\n"
        "s_waitcnt vmcnt(0)\n"
        "ds_max_i32 v25, v21 offset:0\n"
        "s_mov_b32 s38, 8388608\n"
        "W8_lu3:\n"
        "ds_read_b32 v20, v25 offset:1024\n"
        "s_sub_u32 s38, s38, 1\n"
        "s_waitcnt lgkmcnt(0)\n"
        "s_cbranch_scc1 W8_lu3_x\n"
        "v_readfirstlane_b32 s39, v20\n"
        "s_cmp_ge_u32 s39, s36\n"
        "s_cbranch_scc1 W8_lu3_x\n"
        "s_sleep 1\n"
        "s_branch W8_lu3\n"
        "W8_lu3_x:\n"
        "s_mov_b32 m0, s34\n"
        "s_add_u32 s34, s34, 4096\n"
        "global_load_lds_dwordx4 v7, s[40:41] offset:0 sc1\n"
        "global_load_lds_dwordx4 v7, s[40:41] offset:1024 sc1\n"
        "global_load_lds_dwordx4 v7, s[40:41] offset:2048 sc1\n"
        "global_load_lds_dwordx4 v7, s[40:41] offset:3072 sc1\n"
        "s_mov_b32 m0, s34\n"
        "s_add_u32 s34, s34, 4096\n"
        "global_load_lds_dwordx4 v11, s[40:41] offset:0 sc1\n"
        "global_load_lds_dwordx4 v11, s[40:41] offset:1024 sc1\n"
        "global_load_lds_dwordx4 v11, s[40:41] offset:2048 sc1\n"
        "global_load_lds_dwordx4 v11, s[40:41] offset:3072 sc1\n"
        "v_add_u32 v3, 1, v3\n"
        "s_waitcnt vmcnt(16)\n"
        "ds_max_i32 v25, v3 offset:0\n"
        "s_add_u32 s40, s40, 32768\n"
        "s_addc_u32 s41, s41, 0\n"
        "s_add_u32 s36, s36, 1\n"
        "s_add_u32 s35, s35, 1\n"
        "s_cmp_lt_u32 s35, 6\n"
        "s_cbranch_scc1 W8_lq\n"
        "s_mov_b32 s34, 0\n"
        "ds_read_b32 v20, v25 offset:1024\n"
        "s_waitcnt lgkmcnt(0)\n"
        "v_readfirstlane_b32 s39, v20\n"
        "s_cmp_ge_u32 s39, 21\n"
        "s_cbranch_scc1 W8_lr0_x\n"
        "v_add_u32 v21, 1, v3\n"
        "s_waitcnt vmcnt(8)\n"
        "ds_max_i32 v25, v21 offset:0\n"
        "v_add_u32 v21, 2, v3\n"
        "s_waitcnt vmcnt(0)\n"
        "ds_max_i32 v25, v21 offset:0\n"
        "s_mov_b32 s38, 8388608\n"
        "W8_lr0:\n"
        "ds_read_b32 v20, v25 offset:1024\n"
        "s_sub_u32 s38, s38, 1\n"
        "s_waitcnt lgkmcnt(0)\n"
        "s_cbranch_scc1 W8_lr0_x\n"
        "v_readfirstlane_b32 s39, v20\n"
        "s_cmp_ge_u32 s39, 21\n"
        "s_cbranch_scc1 W8_lr0_x\n"
        "s_sleep 1\n"
        "s_branch W8_lr0\n"
        "W8_lr0_x:\n"
        "ds_read_b32 v20, v25 offset:768\n"
        "s_waitcnt lgkmcnt(0)\n"
        "v_readfirstlane_b32 s39, v20\n"
        "s_cmp_ge_u32 s39, 543744\n"
        "s_cbranch_scc1 W8_lf0_x\n"
        "v_add_u32 v21, 1, v3\n"
        "s_waitcnt vmcnt(8)\n"
        "ds_max_i32 v25, v21 offset:0\n"
        "v_add_u32 v21, 2, v3\n"
        "s_waitcnt vmcnt(0)\n"
        "ds_max_i32 v25, v21 offset:0\n"
        "s_mov_b32 s38, 8388608\n"
        "W8_lf0:\n"
        "ds_read_b32 v20, v25 offset:768\n"
        "s_sub_u32 s38, s38, 1\n"
        "s_waitcnt lgkmcnt(0)\n"
        "s_cbranch_scc1 W8_lf0_x\n"
        "v_readfirstlane_b32 s39, v20\n"
        "s_cmp_ge_u32 s39, 543744\n"
        "s_cbranch_scc1 W8_lf0_x\n"
        "s_sleep 1\n"
        "s_branch W8_lf0\n"
        "W8_lf0_x:\n"
        "s_add_u32 s40, s42, 4096\n"
        "s_addc_u32 s41, s43, 0\n"
        "W8_ll0:\n"
        "s_mov_b32 m0, s34\n"
        "s_add_u32 s34, s34, 2048\n"
        "global_load_lds_dwordx4 v12, s[40:41] sc1\n"
        "global_load_lds_dwordx4 v12, s[40:41] offset:1024 sc1\n"
        "s_mov_b32 m0, s34\n"
        "s_add_u32 s34, s34, 2048\n"
        "global_load_lds_dwordx4 v13, s[40:41] sc1\n"
        "global_load_lds_dwordx4 v13, s[40:41] offset:1024 sc1\n"
        "s_mov_b32 m0, s34\n"
        "s_add_u32 s34, s34, 2048\n"
        "global_load_lds_dwordx4 v14, s[40:41] sc1\n"
        "global_load_lds_dwordx4 v14, s[40:41] offset:1024 sc1\n"
        "s_mov_b32 m0, s34\n"
        "s_add_u32 s34, s34, 2048\n"
        "global_load_lds_dwordx4 v15, s[40:41] sc1\n"
        "global_load_lds_dwordx4 v15, s[40:41] offset:1024 sc1\n"
        "s_add_u32 s40, s40, 24576\n"
        "s_addc_u32 s41, s41, 0\n"
        "v_add_u32 v3, 1, v3\n"
        "s_waitcnt vmcnt(16)\n"
        "ds_max_i32 v25, v3 offset:0\n"
        "s_cmp_lt_u32 s34, 32768\n"
        "s_cbranch_scc1 W8_ll0\n"
        "v_add_u32 v3, -1, v3\n"
        "ds_read_b32 v20, v25 offset:772\n"
        "s_waitcnt lgkmcnt(0)\n"
        "v_readfirstlane_b32 s39, v20\n"
        "s_cmp_ge_u32 s39, 543744\n"
        "s_cbranch_scc1 W8_lf1_x\n"
        "v_add_u32 v21, 1, v3\n"
        "s_waitcnt vmcnt(16)\n"
        "ds_max_i32 v25, v21 offset:0\n"
        "v_add_u32 v21, 2, v3\n"
        "s_waitcnt vmcnt(8)\n"
        "ds_max_i32 v25, v21 offset:0\n"
        "v_add_u32 v21, 3, v3\n"
        "s_waitcnt vmcnt(0)\n"
        "ds_max_i32 v25, v21 offset:0\n"
        "s_mov_b32 s38, 8388608\n"
        "W8_lf1:\n"
        "ds_read_b32 v20, v25 offset:772\n"
        "s_sub_u32 s38, s38, 1\n"
        "s_waitcnt lgkmcnt(0)\n"
        "s_cbranch_scc1 W8_lf1_x\n"
        "v_readfirstlane_b32 s39, v20\n"
        "s_cmp_ge_u32 s39, 543744\n"
        "s_cbranch_scc1 W8_lf1_x\n"
        "s_sleep 1\n"
        "s_branch W8_lf1\n"
        "W8_lf1_x:\n"
        "s_add_u32 s40, s42, 0\n"
        "s_addc_u32 s41, s43, 0\n"
        "W8_ll1:\n"
        "s_mov_b32 m0, s34\n"
        "s_add_u32 s34, s34, 2048\n"
        "global_load_lds_dwordx4 v12, s[40:41] sc1\n"
        "global_load_lds_dwordx4 v12, s[40:41] offset:1024 sc1\n"
        "s_mov_b32 m0, s34\n"
        "s_add_u32 s34, s34, 2048\n"
        "global_load_lds_dwordx4 v13, s[40:41] sc1\n"
        "global_load_lds_dwordx4 v13, s[40:41] offset:1024 sc1\n"
        "s_mov_b32 m0, s34\n"
        "s_add_u32 s34, s34, 2048\n"
        "global_load_lds_dwordx4 v14, s[40:41] sc1\n"
        "global_load_lds_dwordx4 v14, s[40:41] offset:1024 sc1\n"
        "s_mov_b32 m0, s34\n"
        "s_add_u32 s34, s34, 2048\n"
        "global_load_lds_dwordx4 v15, s[40:41] sc1\n"
        "global_load_lds_dwordx4 v15, s[40:41] offset:1024 sc1\n"
        "s_add_u32 s40, s40, 24576\n"
        "s_addc_u32 s41, s41, 0\n"
        "v_add_u32 v3, 1, v3\n"
        "s_waitcnt vmcnt(24)\n"
        "ds_max_i32 v25, v3 offset:0\n"
        "s_cmp_lt_u32 s34, 65536\n"
        "s_cbranch_scc1 W8_ll1\n"
        "v_add_u32 v3, -1, v3\n"
        "ds_read_b32 v20, v25 offset:776\n"
        "s_waitcnt lgkmcnt(0)\n"
        "v_readfirstlane_b32 s39, v20\n"
        "s_cmp_ge_u32 s39, 543744\n"
        "s_cbranch_scc1 W8_lf2_x\n"
        "v_add_u32 v21, 1, v3\n"
        "s_waitcnt vmcnt(24)\n"
        "ds_max_i32 v25, v21 offset:0\n"
        "v_add_u32 v21, 2, v3\n"
        "s_waitcnt vmcnt(16)\n"
        "ds_max_i32 v25, v21 offset:0\n"
        "v_add_u32 v21, 3, v3\n"
        "s_waitcnt vmcnt(8)\n"
        "ds_max_i32 v25, v21 offset:0\n"
        "v_add_u32 v21, 4, v3\n"
        "s_waitcnt vmcnt(0)\n"
        "ds_max_i32 v25, v21 offset:0\n"
        "s_mov_b32 s38, 8388608\n"
        "W8_lf2:\n"
        "ds_read_b32 v20, v25 offset:776\n"
        "s_sub_u32 s38, s38, 1\n"
        "s_waitcnt lgkmcnt(0)\n"
        "s_cbranch_scc1 W8_lf2_x\n"
        "v_readfirstlane_b32 s39, v20\n"
        "s_cmp_ge_u32 s39, 543744\n"
        "s_cbranch_scc1 W8_lf2_x\n"
        "s_sleep 1\n"
        "s_branch W8_lf2\n"
        "W8_lf2_x:\n"
        "s_add_u32 s40, s42, 2048\n"
        "s_addc_u32 s41, s43, 0\n"
        "W8_ll2:\n"
        "s_mov_b32 m0, s34\n"
        "s_add_u32 s34, s34, 2048\n"
        "global_load_lds_dwordx4 v12, s[40:41] sc1\n"
        "global_load_lds_dwordx4 v12, s[40:41] offset:1024 sc1\n"
        "s_mov_b32 m0, s34\n"
        "s_add_u32 s34, s34, 2048\n"
        "global_load_lds_dwordx4 v13, s[40:41] sc1\n"
        "global_load_lds_dwordx4 v13, s[40:41] offset:1024 sc1\n"
        "s_mov_b32 m0, s34\n"
        "s_add_u32 s34, s34, 2048\n"
        "global_load_lds_dwordx4 v14, s[40:41] sc1\n"
        "global_load_lds_dwordx4 v14, s[40:41] offset:1024 sc1\n"
        "s_mov_b32 m0, s34\n"
        "s_add_u32 s34, s34, 2048\n"
        "global_load_lds_dwordx4 v15, s[40:41] sc1\n"
        "global_load_lds_dwordx4 v15, s[40:41] offset:1024 sc1\n"
        "s_add_u32 s40, s40, 24576\n"
        "s_addc_u32 s41, s41, 0\n"
        "v_add_u32 v3, 1, v3\n"
        "s_waitcnt vmcnt(32)\n"
        "ds_max_i32 v25, v3 offset:0\n"
        "s_cmp_lt_u32 s34, 98304\n"
        "s_cbranch_scc1 W8_ll2\n"
        "v_add_u32 v3, 1, v3\n"
        "s_waitcnt vmcnt(24)\n"
        "ds_max_i32 v25, v3 offset:0\n"
        "v_add_u32 v3, 1, v3\n"
        "s_waitcnt vmcnt(16)\n"
        "ds_max_i32 v25, v3 offset:0\n"
        "v_add_u32 v3, 1, v3\n"
        "s_waitcnt vmcnt(8)\n"
        "ds_max_i32 v25, v3 offset:0\n"
        "v_add_u32 v3, 1, v3\n"
        "s_waitcnt vmcnt(0)\n"
        "ds_max_i32 v25, v3 offset:0\n"
        "s_branch W8_fin\n"
        "W8_comp:\n"
        "s_setprio 3\n"
        "s_lshl_b32 s26, s21, 2\n"
        "s_add_u32 s26, s26, s20\n"
        "s_sub_u32 s26, s26, 4\n"
        "v_lshrrev_b32 v2, 4, v1\n"
        "v_and_b32 v3, 15, v1\n"
        "v_lshlrev_b32 v248, 11, v2\n"
        "v_lshl_add_u32 v248, v3, 4, v248\n"
        "v_add_u32 v249, 65536, v248\n"
        "v_mov_b32 v250, 135936\n"
        "v_lshlrev_b32 v240, 4, v1\n"
        "v_mul_u32_u24 v2, 28672, v2\n"
        "v_lshl_add_u32 v251, v3, 3, v2\n"
        "v_and_b32 v4, 7, v1\n"
        "v_lshl_add_u32 v252, v4, 3, v2\n"
        "v_bfe_u32 v4, v1, 3, 1\n"
        "v_lshl_add_u32 v252, v4, 2, v252\n"
        "s_mul_i32 s24, s22, 192\n"
        "s_sub_u32 s24, 6144, s24\n"
        "s_lshl_b32 s25, s22, 7\n"
        "s_sub_u32 s37, 3840, s25\n"
        "v_add_u32 v252, s24, v252\n"
        "v_lshlrev_b32 v253, 5, v3\n"
        "v_add_u32 v253, 133632, v253\n"
        "s_waitcnt lgkmcnt(0)\n"
        "s_lshl_b32 s27, s26, 15\n"
        "s_xor_b32 s24, s33, 0\n"
        "s_and_b32 s24, s24, 3\n"
        "s_lshl_b32 s24, s24, 11\n"
        "s_add_u32 s24, s24, s27\n"
        "s_add_u32 s56, s8, s24\n"
        "s_addc_u32 s57, s9, 0\n"
        "s_xor_b32 s24, s33, 1\n"
        "s_and_b32 s24, s24, 3\n"
        "s_lshl_b32 s24, s24, 11\n"
        "s_add_u32 s24, s24, s27\n"
        "s_add_u32 s58, s8, s24\n"
        "s_addc_u32 s59, s9, 0\n"
        "s_xor_b32 s24, s33, 2\n"
        "s_and_b32 s24, s24, 3\n"
        "s_lshl_b32 s24, s24, 11\n"
        "s_add_u32 s24, s24, s27\n"
        "s_add_u32 s60, s8, s24\n"
        "s_addc_u32 s61, s9, 0\n"
        "s_xor_b32 s24, s33, 3\n"
        "s_and_b32 s24, s24, 3\n"
        "s_lshl_b32 s24, s24, 11\n"
        "s_add_u32 s24, s24, s27\n"
        "s_add_u32 s62, s8, s24\n"
        "s_addc_u32 s63, s9, 0\n"
        "s_mul_i32 s24, s26, 229376\n"
        "s_mul_i32 s25, s22, 256\n"
        "s_add_u32 s24, s24, s25\n"
        "s_add_u32 s40, s10, s24\n"
        "s_addc_u32 s41, s11, 0\n"
        "s_add_u32 s42, s40, 7168\n"
        "s_addc_u32 s43, s41, 0\n"
        "s_add_u32 s44, s40, 14336\n"
        "s_addc_u32 s45, s41, 0\n"
        "s_add_u32 s46, s40, 21504\n"
        "s_addc_u32 s47, s41, 0\n"
        "s_add_u32 s48, s40, 114688\n"
        "s_addc_u32 s49, s41, 0\n"
        "s_add_u32 s50, s40, 121856\n"
        "s_addc_u32 s51, s41, 0\n"
        "s_add_u32 s52, s40, 129024\n"
        "s_addc_u32 s53, s41, 0\n"
        "s_add_u32 s54, s40, 136192\n"
        "s_addc_u32 s55, s41, 0\n"
        "s_sub_u32 s28, s20, 4\n"
        "s_mul_i32 s24, s21, 512\n"
        "s_mul_i32 s25, s22, 8\n"
        "s_add_u32 s24, s24, s25\n"
        "s_add_u32 s24, s24, 6029312\n"
        "s_mul_i32 s25, s22, 1024\n"
        "s_mul_i32 s27, s21, 8\n"
        "s_add_u32 s25, s25, s27\n"
        "s_add_u32 s25, s25, 6291456\n"
        "s_sub_u32 s25, s25, s24\n"
        "v_mul_lo_u32 v24, v1, s25\n"
        "s_mul_i32 s25, s28, 128\n"
        "s_add_u32 s24, s24, s25\n"
        "v_add_u32 v24, s24, v24\n"
        "s_mul_i32 s29, s28, 5120\n"
        "s_add_u32 s29, s29, 98304\n"
        "v_and_b32 v6, 7, v1\n"
        "v_lshrrev_b32 v7, 3, v1\n"
        "v_lshrrev_b32 v8, 4, v1\n"
        "v_lshl_add_u32 v8, v8, 4, s29\n"
        "v_lshl_add_u32 v8, v3, 6, v8\n"
        "v_add_u32 v9, s29, v240\n"
        "s_and_b32 s24, s22, 3\n"
        "s_lshl2_add_u32 s24, s21, s24\n"
        "s_lshl2_add_u32 s24, s24, s28\n"
        "s_lshr_b32 s25, s22, 2\n"
        "s_lshl2_add_u32 s24, s24, s25\n"
        "s_lshl_b32 s24, s24, 11\n"
        "v_add_u32 v11, s24, v240\n"
        "v_lshrrev_b32 v10, 5, v1\n"
        "s_lshl_b32 s24, s22, 2\n"
        "s_add_u32 s24, s24, s28\n"
        "s_lshl_b32 s24, s24, 4\n"
        "s_add_u32 s24, s24, s21\n"
        "s_lshl_b32 s24, s24, 11\n"
        "s_add_u32 s24, s24, 2097152\n"
        "v_add_u32 v13, s24, v240\n"
        "s_lshl_b32 s24, s22, 4\n"
        "s_add_u32 s24, s24, s21\n"
        "s_lshl_b32 s24, s24, 10\n"
        "s_add_u32 s24, s24, 5767168\n"
        "v_and_b32 v14, 31, v1\n"
        "v_lshl_add_u32 v14, v14, 4, s24\n"
        "v_lshl_add_u32 v14, v10, 3, v14\n"
        "v_lshlrev_b32 v15, 3, v1\n"
        "v_add_u32 v15, 118016, v15\n"
        "s_add_u32 s28, s28, 1\n"
        "s_mov_b32 s38, 8388608\n"
        "W8_tgc:\n"
        "ds_read_b32 v12, v250 offset:772\n"
        "s_sub_u32 s38, s38, 1\n"
        "s_waitcnt lgkmcnt(0)\n"
        "s_cbranch_scc1 W8_tgc_x\n"
        "v_readfirstlane_b32 s39, v12\n"
        "s_cmp_ge_u32 s39, 1\n"
        "s_cbranch_scc1 W8_tgc_x\n"
        "s_sleep 1\n"
        "s_branch W8_tgc\n"
        "W8_tgc_x:\n"
        "v_lshrrev_b32 v12, 0, v1\n"
        "v_lshlrev_b32 v12, 7, v12\n"
        "v_add_u32 v12, 136968, v12\n"
        "s_mov_b64 exec, 3\n"
        "ds_read_b64 v[26:27], v12\n"
        "s_mov_b64 exec, -1\n"
        "s_mov_b32 s38, 8388608\n"
        "W8_pkc:\n"
        "ds_read_b32 v12, v250 offset:1292\n"
        "s_sub_u32 s38, s38, 1\n"
        "s_waitcnt lgkmcnt(0)\n"
        "s_cbranch_scc1 W8_pkc_x\n"
        "v_readfirstlane_b32 s39, v12\n"
        "s_cmp_ge_u32 s39, s28\n"
        "s_cbranch_scc1 W8_pkc_x\n"
        "s_sleep 1\n"
        "s_branch W8_pkc\n"
        "W8_pkc_x:\n"
        "ds_read_b128 v[28:31], v8\n"
        "ds_read_b128 v[32:35], v8 offset:1024\n"
        "ds_read_b128 v[36:39], v9 offset:2304\n"
        "ds_read_b128 v[40:43], v9 offset:3328\n"
        "ds_read_b64 v[44:45], v15\n"
        "s_waitcnt lgkmcnt(3)\n"
        "global_store_dwordx4 v11, v[28:31], s[8:9] offset:0 sc1\n"
        "global_store_dwordx4 v11, v[32:35], s[8:9] offset:1024 sc1\n"
        "s_waitcnt lgkmcnt(0)\n"
        "global_store_dwordx4 v13, v[36:39], s[8:9] sc1\n"
        "global_store_dwordx4 v13, v[40:43], s[8:9] offset:1024 sc1\n"
        "s_cmp_lg_u32 s28, 4\n"
        "s_cbranch_scc1 W8_ncs\n"
        "global_store_dwordx2 v14, v[44:45], s[8:9] sc1\n"
        "W8_ncs:\n"
        "s_mov_b64 exec, 3\n"
        "s_waitcnt vmcnt(0)\n"
        "global_atomic_swap_x2 v24, v[26:27], s[8:9] sc1\n"
        "s_mov_b64 exec, -1\n"
        "s_mov_b32 s38, 8388608\n"
        "W8_cgo:\n"
        "ds_read_b32 v254, v250 offset:260\n"
        "s_sub_u32 s38, s38, 1\n"
        "s_waitcnt lgkmcnt(0)\n"
        "s_cbranch_scc1 W8_cgo_x\n"
        "v_readfirstlane_b32 s39, v254\n"
        "s_cmp_ge_u32 s39, 1\n"
        "s_cbranch_scc1 W8_cgo_x\n"
        "s_sleep 2\n"
        "s_branch W8_cgo\n"
        "W8_cgo_x:\n"
        "s_mov_b32 s38, 8388608\n"
        "W8_rd0:\n"
        "ds_read_b32 v254, v250 offset:4\n"
        "s_sub_u32 s38, s38, 1\n"
        "s_waitcnt lgkmcnt(0)\n"
        "s_cbranch_scc1 W8_rd0_x\n"
        "v_readfirstlane_b32 s39, v254\n"
        "s_cmp_ge_u32 s39, 1\n"
        "s_cbranch_scc1 W8_rd0_x\n"
        "s_sleep 1\n"
        "s_branch W8_rd0\n"
        "W8_rd0_x:\n"
        "global_load_dwordx4 v[0:3], v240, s[56:57] offset:0 sc1\n"
        "global_load_dwordx4 v[4:7], v240, s[56:57] offset:1024 sc1\n"
        "s_sleep 2\n"
        "s_cmp_ge_u32 s39, 2\n"
        "s_cbranch_scc1 W8_rd1_x\n"
        "s_mov_b32 s38, 8388608\n"
        "W8_rd1:\n"
        "ds_read_b32 v254, v250 offset:4\n"
        "s_sub_u32 s38, s38, 1\n"
        "s_waitcnt lgkmcnt(0)\n"
        "s_cbranch_scc1 W8_rd1_x\n"
        "v_readfirstlane_b32 s39, v254\n"
        "s_cmp_ge_u32 s39, 2\n"
        "s_cbranch_scc1 W8_rd1_x\n"
        "s_sleep 1\n"
        "s_branch W8_rd1\n"
        "W8_rd1_x:\n"
        "global_load_dwordx4 v[8:11], v240, s[58:59] offset:0 sc1\n"
        "global_load_dwordx4 v[12:15], v240, s[58:59] offset:1024 sc1\n"
        "W8_dwx:\n"
        "ds_read_b32 v254, v250 offset:0\n"
        "s_waitcnt lgkmcnt(0)\n"
        "v_cmp_gt_u32 vcc, 1, v254\n"
        "s_cbranch_vccnz W8_s0\n"
        "W8_c0:\n"
        "ds_read_b32 v241, v250 offset:4\n"
        "ds_read_b128 v[208:211], v248 offset:0\n"
        "ds_read_b128 v[212:215], v248 offset:1024\n"
        "ds_read_b128 v[216:219], v248 offset:512\n"
        "ds_read_b128 v[220:223], v248 offset:1536\n"
        "ds_read_b128 v[224:227], v248 offset:256\n"
        "ds_read_b128 v[228:231], v248 offset:1280\n"
        "s_waitcnt vmcnt(2)\n"
        "s_waitcnt lgkmcnt(5)\n"
        "v_mfma_i32_16x16x64_i8 v[128:131], v[0:3], v[208:211], 0\n"
        "v_cmp_gt_u32 vcc, 4, v241\n"
        "s_cbranch_vccnz W8_sZ\n"
        "W8_cZ:\n"
        "ds_read_b32 v254, v250 offset:0\n"
        "v_mfma_i32_16x16x64_i8 v[132:135], v[4:7], v[208:211], 0\n"
        "ds_read_b128 v[232:235], v248 offset:768\n"
        "s_waitcnt lgkmcnt(6)\n"
        "v_mfma_i32_16x16x64_i8 v[136:139], v[0:3], v[212:215], 0\n"
        "global_load_dwordx4 v[16:19], v240, s[60:61] offset:0 sc1\n"
        "v_mfma_i32_16x16x64_i8 v[140:143], v[4:7], v[212:215], 0\n"
        "ds_read_b128 v[236:239], v248 offset:1792\n"
        "s_waitcnt lgkmcnt(2)\n"
        "v_cmp_gt_u32 vcc, 2, v254\n"
        "v_mfma_i32_16x16x64_i8 v[144:147], v[0:3], v[216:219], 0\n"
        "s_cbranch_vccnz W8_s1\n"
        "W8_c1:\n"
        "v_mfma_i32_16x16x64_i8 v[148:151], v[4:7], v[216:219], 0\n"
        "v_cmp_gt_u32 vcc, 5, v241\n"
        "ds_read_b128 v[208:211], v248 offset:8192\n"
        "v_mfma_i32_16x16x64_i8 v[152:155], v[0:3], v[220:223], 0\n"
        "s_cbranch_vccnz W8_sR0\n"
        "W8_cR0:\n"
        "ds_read_b32 v254, v250 offset:0\n"
        "v_mfma_i32_16x16x64_i8 v[156:159], v[4:7], v[220:223], 0\n"
        "ds_read_b128 v[212:215], v248 offset:9216\n"
        "v_mfma_i32_16x16x64_i8 v[160:163], v[0:3], v[224:227], 0\n"
        "ds_read_b32 v241, v250 offset:4\n"
        "global_load_dwordx4 v[20:23], v240, s[60:61] offset:1024 sc1\n"
        "v_mfma_i32_16x16x64_i8 v[164:167], v[4:7], v[224:227], 0\n"
        "ds_read_b128 v[216:219], v248 offset:8704\n"
        "v_mfma_i32_16x16x64_i8 v[168:171], v[0:3], v[228:231], 0\n"
        "global_load_dwordx4 v[24:27], v240, s[62:63] offset:0 sc1\n"
        "v_mfma_i32_16x16x64_i8 v[172:175], v[4:7], v[228:231], 0\n"
        "ds_read_b128 v[220:223], v248 offset:9728\n"
        "s_waitcnt lgkmcnt(7)\n"
        "v_mfma_i32_16x16x64_i8 v[176:179], v[0:3], v[232:235], 0\n"
        "global_load_dwordx4 v[28:31], v240, s[62:63] offset:1024 sc1\n"
        "v_add_u32 v240, 8192, v240\n"
        "v_mfma_i32_16x16x64_i8 v[180:183], v[4:7], v[232:235], 0\n"
        "ds_read_b128 v[224:227], v248 offset:8448\n"
        "s_waitcnt lgkmcnt(7)\n"
        "v_mfma_i32_16x16x64_i8 v[184:187], v[0:3], v[236:239], 0\n"
        "v_mfma_i32_16x16x64_i8 v[188:191], v[4:7], v[236:239], 0\n"
        "ds_read_b128 v[228:231], v248 offset:9472\n"
        "s_waitcnt vmcnt(4)\n"
        "s_waitcnt lgkmcnt(7)\n"
        "v_mfma_i32_16x16x64_i8 v[128:131], v[8:11], v[208:211], v[128:131]\n"
        "v_mfma_i32_16x16x64_i8 v[132:135], v[12:15], v[208:211], v[132:135]\n"
        "ds_read_b128 v[232:235], v248 offset:8960\n"
        "s_waitcnt lgkmcnt(6)\n"
        "v_mfma_i32_16x16x64_i8 v[136:139], v[8:11], v[212:215], v[136:139]\n"
        "global_load_dwordx4 v[32:35], v240, s[56:57] offset:0 sc1\n"
        "v_mfma_i32_16x16x64_i8 v[140:143], v[12:15], v[212:215], v[140:143]\n"
        "ds_read_b128 v[236:239], v248 offset:9984\n"
        "v_cmp_gt_u32 vcc, 3, v254\n"
        "s_waitcnt lgkmcnt(5)\n"
        "v_mfma_i32_16x16x64_i8 v[144:147], v[8:11], v[216:219], v[144:147]\n"
        "s_cbranch_vccnz W8_s2\n"
        "W8_c2:\n"
        "v_mfma_i32_16x16x64_i8 v[148:151], v[12:15], v[216:219], v[148:151]\n"
        "v_cmp_gt_u32 vcc, 6, v241\n"
        "ds_read_b128 v[208:211], v248 offset:16384\n"
        "s_waitcnt lgkmcnt(5)\n"
        "v_mfma_i32_16x16x64_i8 v[152:155], v[8:11], v[220:223], v[152:155]\n"
        "s_cbranch_vccnz W8_sR1\n"
        "W8_cR1:\n"
        "ds_read_b32 v254, v250 offset:0\n"
        "v_mfma_i32_16x16x64_i8 v[156:159], v[12:15], v[220:223], v[156:159]\n"
        "ds_read_b128 v[212:215], v248 offset:17408\n"
        "s_waitcnt lgkmcnt(6)\n"
        "v_mfma_i32_16x16x64_i8 v[160:163], v[8:11], v[224:227], v[160:163]\n"
        "ds_read_b32 v241, v250 offset:4\n"
        "global_load_dwordx4 v[36:39], v240, s[56:57] offset:1024 sc1\n"
        "v_mfma_i32_16x16x64_i8 v[164:167], v[12:15], v[224:227], v[164:167]\n"
        "ds_read_b128 v[216:219], v248 offset:16896\n"
        "s_waitcnt lgkmcnt(7)\n"
        "v_mfma_i32_16x16x64_i8 v[168:171], v[8:11], v[228:231], v[168:171]\n"
        "v_mfma_i32_16x16x64_i8 v[172:175], v[12:15], v[228:231], v[172:175]\n"
        "ds_read_b128 v[220:223], v248 offset:17920\n"
        "s_waitcnt lgkmcnt(7)\n"
        "v_mfma_i32_16x16x64_i8 v[176:179], v[8:11], v[232:235], v[176:179]\n"
        "v_mfma_i32_16x16x64_i8 v[180:183], v[12:15], v[232:235], v[180:183]\n"
        "ds_read_b128 v[224:227], v248 offset:16640\n"
        "s_waitcnt lgkmcnt(7)\n"
        "v_mfma_i32_16x16x64_i8 v[184:187], v[8:11], v[236:239], v[184:187]\n"
        "global_load_dwordx4 v[40:43], v240, s[58:59] offset:0 sc1\n"
        "v_mfma_i32_16x16x64_i8 v[188:191], v[12:15], v[236:239], v[188:191]\n"
        "ds_read_b128 v[228:231], v248 offset:17664\n"
        "s_waitcnt vmcnt(5)\n"
        "s_waitcnt lgkmcnt(7)\n"
        "v_mfma_i32_16x16x64_i8 v[128:131], v[16:19], v[208:211], v[128:131]\n"
        "v_mfma_i32_16x16x64_i8 v[132:135], v[20:23], v[208:211], v[132:135]\n"
        "ds_read_b128 v[232:235], v248 offset:17152\n"
        "s_waitcnt lgkmcnt(6)\n"
        "v_mfma_i32_16x16x64_i8 v[136:139], v[16:19], v[212:215], v[136:139]\n"
        "v_mfma_i32_16x16x64_i8 v[140:143], v[20:23], v[212:215], v[140:143]\n"
        "ds_read_b128 v[236:239], v248 offset:18176\n"
        "v_cmp_gt_u32 vcc, 4, v254\n"
        "s_waitcnt lgkmcnt(5)\n"
        "v_mfma_i32_16x16x64_i8 v[144:147], v[16:19], v[216:219], v[144:147]\n"
        "s_cbranch_vccnz W8_s3\n"
        "W8_c3:\n"
        "v_mfma_i32_16x16x64_i8 v[148:151], v[20:23], v[216:219], v[148:151]\n"
        "v_cmp_gt_u32 vcc, 7, v241\n"
        "ds_read_b128 v[208:211], v248 offset:24576\n"
        "s_waitcnt lgkmcnt(5)\n"
        "v_mfma_i32_16x16x64_i8 v[152:155], v[16:19], v[220:223], v[152:155]\n"
        "s_cbranch_vccnz W8_sR2\n"
        "W8_cR2:\n"
        "ds_read_b32 v254, v250 offset:0\n"
        "v_mfma_i32_16x16x64_i8 v[156:159], v[20:23], v[220:223], v[156:159]\n"
        "ds_read_b128 v[212:215], v248 offset:25600\n"
        "s_waitcnt lgkmcnt(6)\n"
        "v_mfma_i32_16x16x64_i8 v[160:163], v[16:19], v[224:227], v[160:163]\n"
        "ds_read_b32 v241, v250 offset:4\n"
        "global_load_dwordx4 v[44:47], v240, s[58:59] offset:1024 sc1\n"
        "v_mfma_i32_16x16x64_i8 v[164:167], v[20:23], v[224:227], v[164:167]\n"
        "ds_read_b128 v[216:219], v248 offset:25088\n"
        "s_waitcnt lgkmcnt(7)\n"
        "v_mfma_i32_16x16x64_i8 v[168:171], v[16:19], v[228:231], v[168:171]\n"
        "v_mfma_i32_16x16x64_i8 v[172:175], v[20:23], v[228:231], v[172:175]\n"
        "ds_read_b128 v[220:223], v248 offset:26112\n"
        "s_waitcnt lgkmcnt(7)\n"
        "v_mfma_i32_16x16x64_i8 v[176:179], v[16:19], v[232:235], v[176:179]\n"
        "v_mfma_i32_16x16x64_i8 v[180:183], v[20:23], v[232:235], v[180:183]\n"
        "ds_read_b128 v[224:227], v248 offset:24832\n"
        "s_waitcnt lgkmcnt(7)\n"
        "v_mfma_i32_16x16x64_i8 v[184:187], v[16:19], v[236:239], v[184:187]\n"
        "global_load_dwordx4 v[48:51], v240, s[60:61] offset:0 sc1\n"
        "v_mfma_i32_16x16x64_i8 v[188:191], v[20:23], v[236:239], v[188:191]\n"
        "ds_read_b128 v[228:231], v248 offset:25856\n"
        "s_waitcnt vmcnt(5)\n"
        "s_waitcnt lgkmcnt(7)\n"
        "v_mfma_i32_16x16x64_i8 v[128:131], v[24:27], v[208:211], v[128:131]\n"
        "v_mfma_i32_16x16x64_i8 v[132:135], v[28:31], v[208:211], v[132:135]\n"
        "ds_read_b128 v[232:235], v248 offset:25344\n"
        "s_waitcnt lgkmcnt(6)\n"
        "v_mfma_i32_16x16x64_i8 v[136:139], v[24:27], v[212:215], v[136:139]\n"
        "v_mfma_i32_16x16x64_i8 v[140:143], v[28:31], v[212:215], v[140:143]\n"
        "ds_read_b128 v[236:239], v248 offset:26368\n"
        "v_cmp_gt_u32 vcc, 5, v254\n"
        "s_waitcnt lgkmcnt(5)\n"
        "v_mfma_i32_16x16x64_i8 v[144:147], v[24:27], v[216:219], v[144:147]\n"
        "s_cbranch_vccnz W8_s4\n"
        "W8_c4:\n"
        "v_mfma_i32_16x16x64_i8 v[148:151], v[28:31], v[216:219], v[148:151]\n"
        "v_cmp_gt_u32 vcc, 8, v241\n"
        "ds_read_b128 v[208:211], v248 offset:32768\n"
        "s_waitcnt lgkmcnt(5)\n"
        "v_mfma_i32_16x16x64_i8 v[152:155], v[24:27], v[220:223], v[152:155]\n"
        "s_cbranch_vccnz W8_sR3\n"
        "W8_cR3:\n"
        "ds_read_b32 v254, v250 offset:0\n"
        "v_mfma_i32_16x16x64_i8 v[156:159], v[28:31], v[220:223], v[156:159]\n"
        "ds_read_b128 v[212:215], v248 offset:33792\n"
        "s_waitcnt lgkmcnt(6)\n"
        "v_mfma_i32_16x16x64_i8 v[160:163], v[24:27], v[224:227], v[160:163]\n"
        "ds_read_b32 v241, v250 offset:4\n"
        "s_mov_b64 exec, 1\n"
        "ds_add_u32 v250, v250 offset:516\n"
        "s_mov_b64 exec, -1\n"
        "v_mfma_i32_16x16x64_i8 v[164:167], v[28:31], v[224:227], v[164:167]\n"
        "ds_read_b128 v[216:219], v248 offset:33280\n"
        "s_waitcnt lgkmcnt(8)\n"
        "v_mfma_i32_16x16x64_i8 v[168:171], v[24:27], v[228:231], v[168:171]\n"
        "global_load_dwordx4 v[52:55], v240, s[60:61] offset:1024 sc1\n"
        "v_mfma_i32_16x16x64_i8 v[172:175], v[28:31], v[228:231], v[172:175]\n"
        "ds_read_b128 v[220:223], v248 offset:34304\n"
        "s_waitcnt lgkmcnt(8)\n"
        "v_mfma_i32_16x16x64_i8 v[176:179], v[24:27], v[232:235], v[176:179]\n"
        "v_mfma_i32_16x16x64_i8 v[180:183], v[28:31], v[232:235], v[180:183]\n"
        "ds_read_b128 v[224:227], v248 offset:33024\n"
        "s_waitcnt lgkmcnt(8)\n"
        "v_mfma_i32_16x16x64_i8 v[184:187], v[24:27], v[236:239], v[184:187]\n"
        "v_mfma_i32_16x16x64_i8 v[188:191], v[28:31], v[236:239], v[188:191]\n"
        "ds_read_b128 v[228:231], v248 offset:34048\n"
        "s_waitcnt vmcnt(4)\n"
        "s_waitcnt lgkmcnt(8)\n"
        "v_mfma_i32_16x16x64_i8 v[128:131], v[32:35], v[208:211], v[128:131]\n"
        "global_load_dwordx4 v[56:59], v240, s[62:63] offset:0 sc1\n"
        "v_mfma_i32_16x16x64_i8 v[132:135], v[36:39], v[208:211], v[132:135]\n"
        "ds_read_b128 v[232:235], v248 offset:33536\n"
        "s_waitcnt lgkmcnt(7)\n"
        "v_mfma_i32_16x16x64_i8 v[136:139], v[32:35], v[212:215], v[136:139]\n"
        "v_mfma_i32_16x16x64_i8 v[140:143], v[36:39], v[212:215], v[140:143]\n"
        "ds_read_b128 v[236:239], v248 offset:34560\n"
        "v_cmp_gt_u32 vcc, 6, v254\n"
        "s_waitcnt lgkmcnt(5)\n"
        "v_mfma_i32_16x16x64_i8 v[144:147], v[32:35], v[216:219], v[144:147]\n"
        "s_cbranch_vccnz W8_s5\n"
        "W8_c5:\n"
        "v_mfma_i32_16x16x64_i8 v[148:151], v[36:39], v[216:219], v[148:151]\n"
        "v_cmp_gt_u32 vcc, 9, v241\n"
        "ds_read_b128 v[208:211], v248 offset:40960\n"
        "s_waitcnt lgkmcnt(5)\n"
        "v_mfma_i32_16x16x64_i8 v[152:155], v[32:35], v[220:223], v[152:155]\n"
        "s_cbranch_vccnz W8_sR4\n"
        "W8_cR4:\n"
        "ds_read_b32 v254, v250 offset:0\n"
        "v_mfma_i32_16x16x64_i8 v[156:159], v[36:39], v[220:223], v[156:159]\n"
        "ds_read_b128 v[212:215], v248 offset:41984\n"
        "s_waitcnt lgkmcnt(6)\n"
        "v_mfma_i32_16x16x64_i8 v[160:163], v[32:35], v[224:227], v[160:163]\n"
        "ds_read_b32 v241, v250 offset:4\n"
        "global_load_dwordx4 v[60:63], v240, s[62:63] offset:1024 sc1\n"
        "v_add_u32 v240, 8192, v240\n"
        "v_mfma_i32_16x16x64_i8 v[164:167], v[36:39], v[224:227], v[164:167]\n"
        "ds_read_b128 v[216:219], v248 offset:41472\n"
        "s_waitcnt lgkmcnt(7)\n"
        "v_mfma_i32_16x16x64_i8 v[168:171], v[32:35], v[228:231], v[168:171]\n"
        "v_mfma_i32_16x16x64_i8 v[172:175], v[36:39], v[228:231], v[172:175]\n"
        "ds_read_b128 v[220:223], v248 offset:42496\n"
        "s_waitcnt lgkmcnt(7)\n"
        "v_mfma_i32_16x16x64_i8 v[176:179], v[32:35], v[232:235], v[176:179]\n"
        "v_mfma_i32_16x16x64_i8 v[180:183], v[36:39], v[232:235], v[180:183]\n"
        "ds_read_b128 v[224:227], v248 offset:41216\n"
        "s_waitcnt lgkmcnt(7)\n"
        "v_mfma_i32_16x16x64_i8 v[184:187], v[32:35], v[236:239], v[184:187]\n"
        "global_load_dwordx4 v[64:67], v240, s[56:57] offset:0 sc1\n"
        "v_mfma_i32_16x16x64_i8 v[188:191], v[36:39], v[236:239], v[188:191]\n"
        "ds_read_b128 v[228:231], v248 offset:42240\n"
        "s_waitcnt vmcnt(5)\n"
        "s_waitcnt lgkmcnt(7)\n"
        "v_mfma_i32_16x16x64_i8 v[128:131], v[40:43], v[208:211], v[128:131]\n"
        "v_mfma_i32_16x16x64_i8 v[132:135], v[44:47], v[208:211], v[132:135]\n"
        "ds_read_b128 v[232:235], v248 offset:41728\n"
        "s_waitcnt lgkmcnt(6)\n"
        "v_mfma_i32_16x16x64_i8 v[136:139], v[40:43], v[212:215], v[136:139]\n"
        "v_mfma_i32_16x16x64_i8 v[140:143], v[44:47], v[212:215], v[140:143]\n"
        "ds_read_b128 v[236:239], v248 offset:42752\n"
        "v_cmp_gt_u32 vcc, 7, v254\n"
        "s_waitcnt lgkmcnt(5)\n"
        "v_mfma_i32_16x16x64_i8 v[144:147], v[40:43], v[216:219], v[144:147]\n"
        "s_cbranch_vccnz W8_s6\n"
        "W8_c6:\n"
        "v_mfma_i32_16x16x64_i8 v[148:151], v[44:47], v[216:219], v[148:151]\n"
        "v_cmp_gt_u32 vcc, 10, v241\n"
        "ds_read_b128 v[208:211], v248 offset:49152\n"
        "s_waitcnt lgkmcnt(5)\n"
        "v_mfma_i32_16x16x64_i8 v[152:155], v[40:43], v[220:223], v[152:155]\n"
        "s_cbranch_vccnz W8_sR5\n"
        "W8_cR5:\n"
        "ds_read_b32 v254, v250 offset:0\n"
        "v_mfma_i32_16x16x64_i8 v[156:159], v[44:47], v[220:223], v[156:159]\n"
        "ds_read_b128 v[212:215], v248 offset:50176\n"
        "s_waitcnt lgkmcnt(6)\n"
        "v_mfma_i32_16x16x64_i8 v[160:163], v[40:43], v[224:227], v[160:163]\n"
        "ds_read_b32 v241, v250 offset:4\n"
        "global_load_dwordx4 v[68:71], v240, s[56:57] offset:1024 sc1\n"
        "v_mfma_i32_16x16x64_i8 v[164:167], v[44:47], v[224:227], v[164:167]\n"
        "ds_read_b128 v[216:219], v248 offset:49664\n"
        "s_waitcnt lgkmcnt(7)\n"
        "v_mfma_i32_16x16x64_i8 v[168:171], v[40:43], v[228:231], v[168:171]\n"
        "v_mfma_i32_16x16x64_i8 v[172:175], v[44:47], v[228:231], v[172:175]\n"
        "ds_read_b128 v[220:223], v248 offset:50688\n"
        "s_waitcnt lgkmcnt(7)\n"
        "v_mfma_i32_16x16x64_i8 v[176:179], v[40:43], v[232:235], v[176:179]\n"
        "v_mfma_i32_16x16x64_i8 v[180:183], v[44:47], v[232:235], v[180:183]\n"
        "ds_read_b128 v[224:227], v248 offset:49408\n"
        "s_waitcnt lgkmcnt(7)\n"
        "v_mfma_i32_16x16x64_i8 v[184:187], v[40:43], v[236:239], v[184:187]\n"
        "global_load_dwordx4 v[72:75], v240, s[58:59] offset:0 sc1\n"
        "v_mfma_i32_16x16x64_i8 v[188:191], v[44:47], v[236:239], v[188:191]\n"
        "ds_read_b128 v[228:231], v248 offset:50432\n"
        "s_waitcnt vmcnt(5)\n"
        "s_waitcnt lgkmcnt(7)\n"
        "v_mfma_i32_16x16x64_i8 v[128:131], v[48:51], v[208:211], v[128:131]\n"
        "v_mfma_i32_16x16x64_i8 v[132:135], v[52:55], v[208:211], v[132:135]\n"
        "ds_read_b128 v[232:235], v248 offset:49920\n"
        "s_waitcnt lgkmcnt(6)\n"
        "v_mfma_i32_16x16x64_i8 v[136:139], v[48:51], v[212:215], v[136:139]\n"
        "v_mfma_i32_16x16x64_i8 v[140:143], v[52:55], v[212:215], v[140:143]\n"
        "ds_read_b128 v[236:239], v248 offset:50944\n"
        "v_cmp_gt_u32 vcc, 8, v254\n"
        "s_waitcnt lgkmcnt(5)\n"
        "v_mfma_i32_16x16x64_i8 v[144:147], v[48:51], v[216:219], v[144:147]\n"
        "s_cbranch_vccnz W8_s7\n"
        "W8_c7:\n"
        "v_mfma_i32_16x16x64_i8 v[148:151], v[52:55], v[216:219], v[148:151]\n"
        "v_cmp_gt_u32 vcc, 11, v241\n"
        "ds_read_b128 v[208:211], v248 offset:57344\n"
        "s_waitcnt lgkmcnt(5)\n"
        "v_mfma_i32_16x16x64_i8 v[152:155], v[48:51], v[220:223], v[152:155]\n"
        "s_cbranch_vccnz W8_sR6\n"
        "W8_cR6:\n"
        "ds_read_b32 v254, v250 offset:0\n"
        "v_mfma_i32_16x16x64_i8 v[156:159], v[52:55], v[220:223], v[156:159]\n"
        "ds_read_b128 v[212:215], v248 offset:58368\n"
        "s_waitcnt lgkmcnt(6)\n"
        "v_mfma_i32_16x16x64_i8 v[160:163], v[48:51], v[224:227], v[160:163]\n"
        "ds_read_b32 v241, v250 offset:4\n"
        "global_load_dwordx4 v[76:79], v240, s[58:59] offset:1024 sc1\n"
        "v_mfma_i32_16x16x64_i8 v[164:167], v[52:55], v[224:227], v[164:167]\n"
        "ds_read_b128 v[216:219], v248 offset:57856\n"
        "s_waitcnt lgkmcnt(7)\n"
        "v_mfma_i32_16x16x64_i8 v[168:171], v[48:51], v[228:231], v[168:171]\n"
        "v_mfma_i32_16x16x64_i8 v[172:175], v[52:55], v[228:231], v[172:175]\n"
        "ds_read_b128 v[220:223], v248 offset:58880\n"
        "s_waitcnt lgkmcnt(7)\n"
        "v_mfma_i32_16x16x64_i8 v[176:179], v[48:51], v[232:235], v[176:179]\n"
        "v_mfma_i32_16x16x64_i8 v[180:183], v[52:55], v[232:235], v[180:183]\n"
        "ds_read_b128 v[224:227], v248 offset:57600\n"
        "s_waitcnt lgkmcnt(7)\n"
        "v_mfma_i32_16x16x64_i8 v[184:187], v[48:51], v[236:239], v[184:187]\n"
        "global_load_dwordx4 v[80:83], v240, s[60:61] offset:0 sc1\n"
        "v_mfma_i32_16x16x64_i8 v[188:191], v[52:55], v[236:239], v[188:191]\n"
        "ds_read_b128 v[228:231], v248 offset:58624\n"
        "s_waitcnt vmcnt(5)\n"
        "s_waitcnt lgkmcnt(7)\n"
        "v_mfma_i32_16x16x64_i8 v[128:131], v[56:59], v[208:211], v[128:131]\n"
        "v_mfma_i32_16x16x64_i8 v[132:135], v[60:63], v[208:211], v[132:135]\n"
        "ds_read_b128 v[232:235], v248 offset:58112\n"
        "s_waitcnt lgkmcnt(6)\n"
        "v_mfma_i32_16x16x64_i8 v[136:139], v[56:59], v[212:215], v[136:139]\n"
        "v_mfma_i32_16x16x64_i8 v[140:143], v[60:63], v[212:215], v[140:143]\n"
        "ds_read_b128 v[236:239], v248 offset:59136\n"
        "v_cmp_gt_u32 vcc, 9, v254\n"
        "s_waitcnt lgkmcnt(5)\n"
        "v_mfma_i32_16x16x64_i8 v[144:147], v[56:59], v[216:219], v[144:147]\n"
        "s_cbranch_vccnz W8_s8\n"
        "W8_c8:\n"
        "v_mfma_i32_16x16x64_i8 v[148:151], v[60:63], v[216:219], v[148:151]\n"
        "v_cmp_gt_u32 vcc, 12, v241\n"
        "ds_read_b128 v[208:211], v249 offset:0\n"
        "s_waitcnt lgkmcnt(5)\n"
        "v_mfma_i32_16x16x64_i8 v[152:155], v[56:59], v[220:223], v[152:155]\n"
        "s_cbranch_vccnz W8_sR7\n"
        "W8_cR7:\n"
        "ds_read_b32 v254, v250 offset:0\n"
        "v_mfma_i32_16x16x64_i8 v[156:159], v[60:63], v[220:223], v[156:159]\n"
        "ds_read_b128 v[212:215], v249 offset:1024\n"
        "s_waitcnt lgkmcnt(6)\n"
        "v_mfma_i32_16x16x64_i8 v[160:163], v[56:59], v[224:227], v[160:163]\n"
        "ds_read_b32 v241, v250 offset:4\n"
        "s_mov_b64 exec, 1\n"
        "ds_add_u32 v250, v250 offset:520\n"
        "s_mov_b64 exec, -1\n"
        "v_mfma_i32_16x16x64_i8 v[164:167], v[60:63], v[224:227], v[164:167]\n"
        "ds_read_b128 v[216:219], v249 offset:512\n"
        "s_waitcnt lgkmcnt(8)\n"
        "v_mfma_i32_16x16x64_i8 v[168:171], v[56:59], v[228:231], v[168:171]\n"
        "global_load_dwordx4 v[84:87], v240, s[60:61] offset:1024 sc1\n"
        "v_mfma_i32_16x16x64_i8 v[172:175], v[60:63], v[228:231], v[172:175]\n"
        "ds_read_b128 v[220:223], v249 offset:1536\n"
        "s_waitcnt lgkmcnt(8)\n"
        "v_mfma_i32_16x16x64_i8 v[176:179], v[56:59], v[232:235], v[176:179]\n"
        "v_mfma_i32_16x16x64_i8 v[180:183], v[60:63], v[232:235], v[180:183]\n"
        "ds_read_b128 v[224:227], v249 offset:256\n"
        "s_waitcnt lgkmcnt(8)\n"
        "v_mfma_i32_16x16x64_i8 v[184:187], v[56:59], v[236:239], v[184:187]\n"
        "v_mfma_i32_16x16x64_i8 v[188:191], v[60:63], v[236:239], v[188:191]\n"
        "ds_read_b128 v[228:231], v249 offset:1280\n"
        "s_waitcnt vmcnt(4)\n"
        "s_waitcnt lgkmcnt(8)\n"
        "v_mfma_i32_16x16x64_i8 v[128:131], v[64:67], v[208:211], v[128:131]\n"
        "global_load_dwordx4 v[88:91], v240, s[62:63] offset:0 sc1\n"
        "v_mfma_i32_16x16x64_i8 v[132:135], v[68:71], v[208:211], v[132:135]\n"
        "ds_read_b128 v[232:235], v249 offset:768\n"
        "s_waitcnt lgkmcnt(7)\n"
        "v_mfma_i32_16x16x64_i8 v[136:139], v[64:67], v[212:215], v[136:139]\n"
        "v_mfma_i32_16x16x64_i8 v[140:143], v[68:71], v[212:215], v[140:143]\n"
        "ds_read_b128 v[236:239], v249 offset:1792\n"
        "v_cmp_gt_u32 vcc, 10, v254\n"
        "s_waitcnt lgkmcnt(5)\n"
        "v_mfma_i32_16x16x64_i8 v[144:147], v[64:67], v[216:219], v[144:147]\n"
        "s_cbranch_vccnz W8_s9\n"
        "W8_c9:\n"
        "v_mfma_i32_16x16x64_i8 v[148:151], v[68:71], v[216:219], v[148:151]\n"
        "v_cmp_gt_u32 vcc, 13, v241\n"
        "ds_read_b128 v[208:211], v249 offset:8192\n"
        "s_waitcnt lgkmcnt(5)\n"
        "v_mfma_i32_16x16x64_i8 v[152:155], v[64:67], v[220:223], v[152:155]\n"
        "s_cbranch_vccnz W8_sR8\n"
        "W8_cR8:\n"
        "ds_read_b32 v254, v250 offset:0\n"
        "v_mfma_i32_16x16x64_i8 v[156:159], v[68:71], v[220:223], v[156:159]\n"
        "ds_read_b128 v[212:215], v249 offset:9216\n"
        "s_waitcnt lgkmcnt(6)\n"
        "v_mfma_i32_16x16x64_i8 v[160:163], v[64:67], v[224:227], v[160:163]\n"
        "ds_read_b32 v241, v250 offset:4\n"
        "global_load_dwordx4 v[92:95], v240, s[62:63] offset:1024 sc1\n"
        "v_add_u32 v240, 8192, v240\n"
        "v_mfma_i32_16x16x64_i8 v[164:167], v[68:71], v[224:227], v[164:167]\n"
        "ds_read_b128 v[216:219], v249 offset:8704\n"
        "s_waitcnt lgkmcnt(7)\n"
        "v_mfma_i32_16x16x64_i8 v[168:171], v[64:67], v[228:231], v[168:171]\n"
        "v_mfma_i32_16x16x64_i8 v[172:175], v[68:71], v[228:231], v[172:175]\n"
        "ds_read_b128 v[220:223], v249 offset:9728\n"
        "s_waitcnt lgkmcnt(7)\n"
        "v_mfma_i32_16x16x64_i8 v[176:179], v[64:67], v[232:235], v[176:179]\n"
        "v_mfma_i32_16x16x64_i8 v[180:183], v[68:71], v[232:235], v[180:183]\n"
        "ds_read_b128 v[224:227], v249 offset:8448\n"
        "s_waitcnt lgkmcnt(7)\n"
        "v_mfma_i32_16x16x64_i8 v[184:187], v[64:67], v[236:239], v[184:187]\n"
        "global_load_dwordx4 v[96:99], v240, s[56:57] offset:0 sc1\n"
        "v_mfma_i32_16x16x64_i8 v[188:191], v[68:71], v[236:239], v[188:191]\n"
        "ds_read_b128 v[228:231], v249 offset:9472\n"
        "s_waitcnt vmcnt(5)\n"
        "s_waitcnt lgkmcnt(7)\n"
        "v_mfma_i32_16x16x64_i8 v[128:131], v[72:75], v[208:211], v[128:131]\n"
        "v_mfma_i32_16x16x64_i8 v[132:135], v[76:79], v[208:211], v[132:135]\n"
        "ds_read_b128 v[232:235], v249 offset:8960\n"
        "s_waitcnt lgkmcnt(6)\n"
        "v_mfma_i32_16x16x64_i8 v[136:139], v[72:75], v[212:215], v[136:139]\n"
        "v_mfma_i32_16x16x64_i8 v[140:143], v[76:79], v[212:215], v[140:143]\n"
        "ds_read_b128 v[236:239], v249 offset:9984\n"
        "v_cmp_gt_u32 vcc, 11, v254\n"
        "s_waitcnt lgkmcnt(5)\n"
        "v_mfma_i32_16x16x64_i8 v[144:147], v[72:75], v[216:219], v[144:147]\n"
        "s_cbranch_vccnz W8_s10\n"
        "W8_c10:\n"
        "v_mfma_i32_16x16x64_i8 v[148:151], v[76:79], v[216:219], v[148:151]\n"
        "v_cmp_gt_u32 vcc, 14, v241\n"
        "ds_read_b128 v[208:211], v249 offset:16384\n"
        "s_waitcnt lgkmcnt(5)\n"
        "v_mfma_i32_16x16x64_i8 v[152:155], v[72:75], v[220:223], v[152:155]\n"
        "s_cbranch_vccnz W8_sR9\n"
        "W8_cR9:\n"
        "ds_read_b32 v254, v250 offset:0\n"
        "v_mfma_i32_16x16x64_i8 v[156:159], v[76:79], v[220:223], v[156:159]\n"
        "ds_read_b128 v[212:215], v249 offset:17408\n"
        "s_waitcnt lgkmcnt(6)\n"
        "v_mfma_i32_16x16x64_i8 v[160:163], v[72:75], v[224:227], v[160:163]\n"
        "ds_read_b32 v241, v250 offset:4\n"
        "global_load_dwordx4 v[100:103], v240, s[56:57] offset:1024 sc1\n"
        "v_mfma_i32_16x16x64_i8 v[164:167], v[76:79], v[224:227], v[164:167]\n"
        "ds_read_b128 v[216:219], v249 offset:16896\n"
        "s_waitcnt lgkmcnt(7)\n"
        "v_mfma_i32_16x16x64_i8 v[168:171], v[72:75], v[228:231], v[168:171]\n"
        "v_mfma_i32_16x16x64_i8 v[172:175], v[76:79], v[228:231], v[172:175]\n"
        "ds_read_b128 v[220:223], v249 offset:17920\n"
        "s_waitcnt lgkmcnt(7)\n"
        "v_mfma_i32_16x16x64_i8 v[176:179], v[72:75], v[232:235], v[176:179]\n"
        "v_mfma_i32_16x16x64_i8 v[180:183], v[76:79], v[232:235], v[180:183]\n"
        "ds_read_b128 v[224:227], v249 offset:16640\n"
        "s_waitcnt lgkmcnt(7)\n"
        "v_mfma_i32_16x16x64_i8 v[184:187], v[72:75], v[236:239], v[184:187]\n"
        "global_load_dwordx4 v[104:107], v240, s[58:59] offset:0 sc1\n"
        "v_mfma_i32_16x16x64_i8 v[188:191], v[76:79], v[236:239], v[188:191]\n"
        "ds_read_b128 v[228:231], v249 offset:17664\n"
        "s_waitcnt vmcnt(5)\n"
        "s_waitcnt lgkmcnt(7)\n"
        "v_mfma_i32_16x16x64_i8 v[128:131], v[80:83], v[208:211], v[128:131]\n"
        "v_mfma_i32_16x16x64_i8 v[132:135], v[84:87], v[208:211], v[132:135]\n"
        "ds_read_b128 v[232:235], v249 offset:17152\n"
        "s_waitcnt lgkmcnt(6)\n"
        "v_mfma_i32_16x16x64_i8 v[136:139], v[80:83], v[212:215], v[136:139]\n"
        "v_mfma_i32_16x16x64_i8 v[140:143], v[84:87], v[212:215], v[140:143]\n"
        "ds_read_b128 v[236:239], v249 offset:18176\n"
        "v_cmp_gt_u32 vcc, 12, v254\n"
        "s_waitcnt lgkmcnt(5)\n"
        "v_mfma_i32_16x16x64_i8 v[144:147], v[80:83], v[216:219], v[144:147]\n"
        "s_cbranch_vccnz W8_s11\n"
        "W8_c11:\n"
        "v_mfma_i32_16x16x64_i8 v[148:151], v[84:87], v[216:219], v[148:151]\n"
        "v_cmp_gt_u32 vcc, 15, v241\n"
        "ds_read_b128 v[208:211], v249 offset:24576\n"
        "s_waitcnt lgkmcnt(5)\n"
        "v_mfma_i32_16x16x64_i8 v[152:155], v[80:83], v[220:223], v[152:155]\n"
        "s_cbranch_vccnz W8_sR10\n"
        "W8_cR10:\n"
        "ds_read_b32 v254, v250 offset:0\n"
        "v_mfma_i32_16x16x64_i8 v[156:159], v[84:87], v[220:223], v[156:159]\n"
        "ds_read_b128 v[212:215], v249 offset:25600\n"
        "s_waitcnt lgkmcnt(6)\n"
        "v_mfma_i32_16x16x64_i8 v[160:163], v[80:83], v[224:227], v[160:163]\n"
        "ds_read_b32 v241, v250 offset:4\n"
        "global_load_dwordx4 v[108:111], v240, s[58:59] offset:1024 sc1\n"
        "v_mfma_i32_16x16x64_i8 v[164:167], v[84:87], v[224:227], v[164:167]\n"
        "ds_read_b128 v[216:219], v249 offset:25088\n"
        "s_waitcnt lgkmcnt(7)\n"
        "v_mfma_i32_16x16x64_i8 v[168:171], v[80:83], v[228:231], v[168:171]\n"
        "v_mfma_i32_16x16x64_i8 v[172:175], v[84:87], v[228:231], v[172:175]\n"
        "ds_read_b128 v[220:223], v249 offset:26112\n"
        "s_waitcnt lgkmcnt(7)\n"
        "v_mfma_i32_16x16x64_i8 v[176:179], v[80:83], v[232:235], v[176:179]\n"
        "v_mfma_i32_16x16x64_i8 v[180:183], v[84:87], v[232:235], v[180:183]\n"
        "ds_read_b128 v[224:227], v249 offset:24832\n"
        "s_waitcnt lgkmcnt(7)\n"
        "v_mfma_i32_16x16x64_i8 v[184:187], v[80:83], v[236:239], v[184:187]\n"
        "global_load_dwordx4 v[112:115], v240, s[60:61] offset:0 sc1\n"
        "v_mfma_i32_16x16x64_i8 v[188:191], v[84:87], v[236:239], v[188:191]\n"
        "ds_read_b128 v[228:231], v249 offset:25856\n"
        "s_waitcnt vmcnt(5)\n"
        "s_waitcnt lgkmcnt(7)\n"
        "v_mfma_i32_16x16x64_i8 v[128:131], v[88:91], v[208:211], v[128:131]\n"
        "v_mfma_i32_16x16x64_i8 v[132:135], v[92:95], v[208:211], v[132:135]\n"
        "ds_read_b128 v[232:235], v249 offset:25344\n"
        "s_waitcnt lgkmcnt(6)\n"
        "v_mfma_i32_16x16x64_i8 v[136:139], v[88:91], v[212:215], v[136:139]\n"
        "v_mfma_i32_16x16x64_i8 v[140:143], v[92:95], v[212:215], v[140:143]\n"
        "ds_read_b128 v[236:239], v249 offset:26368\n"
        "v_cmp_gt_u32 vcc, 13, v254\n"
        "s_waitcnt lgkmcnt(5)\n"
        "v_mfma_i32_16x16x64_i8 v[144:147], v[88:91], v[216:219], v[144:147]\n"
        "s_cbranch_vccnz W8_s12\n"
        "W8_c12:\n"
        "v_mfma_i32_16x16x64_i8 v[148:151], v[92:95], v[216:219], v[148:151]\n"
        "v_cmp_gt_u32 vcc, 16, v241\n"
        "ds_read_b128 v[208:211], v249 offset:32768\n"
        "s_waitcnt lgkmcnt(5)\n"
        "v_mfma_i32_16x16x64_i8 v[152:155], v[88:91], v[220:223], v[152:155]\n"
        "s_cbranch_vccnz W8_sR11\n"
        "W8_cR11:\n"
        "ds_read_b32 v254, v250 offset:0\n"
        "v_mfma_i32_16x16x64_i8 v[156:159], v[92:95], v[220:223], v[156:159]\n"
        "ds_read_b128 v[212:215], v249 offset:33792\n"
        "s_waitcnt lgkmcnt(6)\n"
        "v_mfma_i32_16x16x64_i8 v[160:163], v[88:91], v[224:227], v[160:163]\n"
        "s_mov_b64 exec, 1\n"
        "ds_add_u32 v250, v250 offset:524\n"
        "s_mov_b64 exec, -1\n"
        "v_mfma_i32_16x16x64_i8 v[164:167], v[92:95], v[224:227], v[164:167]\n"
        "ds_read_b128 v[216:219], v249 offset:33280\n"
        "s_waitcnt lgkmcnt(7)\n"
        "v_mfma_i32_16x16x64_i8 v[168:171], v[88:91], v[228:231], v[168:171]\n"
        "global_load_dwordx4 v[116:119], v240, s[60:61] offset:1024 sc1\n"
        "v_mfma_i32_16x16x64_i8 v[172:175], v[92:95], v[228:231], v[172:175]\n"
        "ds_read_b128 v[220:223], v249 offset:34304\n"
        "s_waitcnt lgkmcnt(7)\n"
        "v_mfma_i32_16x16x64_i8 v[176:179], v[88:91], v[232:235], v[176:179]\n"
        "v_mfma_i32_16x16x64_i8 v[180:183], v[92:95], v[232:235], v[180:183]\n"
        "ds_read_b128 v[224:227], v249 offset:33024\n"
        "s_waitcnt lgkmcnt(7)\n"
        "v_mfma_i32_16x16x64_i8 v[184:187], v[88:91], v[236:239], v[184:187]\n"
        "v_mfma_i32_16x16x64_i8 v[188:191], v[92:95], v[236:239], v[188:191]\n"
        "ds_read_b128 v[228:231], v249 offset:34048\n"
        "s_waitcnt vmcnt(4)\n"
        "s_waitcnt lgkmcnt(7)\n"
        "v_mfma_i32_16x16x64_i8 v[128:131], v[96:99], v[208:211], v[128:131]\n"
        "global_load_dwordx4 v[120:123], v240, s[62:63] offset:0 sc1\n"
        "v_mfma_i32_16x16x64_i8 v[132:135], v[100:103], v[208:211], v[132:135]\n"
        "ds_read_b128 v[232:235], v249 offset:33536\n"
        "s_waitcnt lgkmcnt(6)\n"
        "v_mfma_i32_16x16x64_i8 v[136:139], v[96:99], v[212:215], v[136:139]\n"
        "v_and_b32 v248, 0x3ff, v240\n"
        "v_mfma_i32_16x16x64_i8 v[140:143], v[100:103], v[212:215], v[140:143]\n"
        "ds_read_b128 v[236:239], v249 offset:34560\n"
        "v_cmp_gt_u32 vcc, 14, v254\n"
        "s_waitcnt lgkmcnt(5)\n"
        "v_mfma_i32_16x16x64_i8 v[144:147], v[96:99], v[216:219], v[144:147]\n"
        "s_cbranch_vccnz W8_s13\n"
        "W8_c13:\n"
        "v_mfma_i32_16x16x64_i8 v[148:151], v[100:103], v[216:219], v[148:151]\n"
        "ds_read_b128 v[208:211], v249 offset:40960\n"
        "s_waitcnt lgkmcnt(5)\n"
        "v_mfma_i32_16x16x64_i8 v[152:155], v[96:99], v[220:223], v[152:155]\n"
        "ds_read_b32 v254, v250 offset:0\n"
        "v_mfma_i32_16x16x64_i8 v[156:159], v[100:103], v[220:223], v[156:159]\n"
        "ds_read_b128 v[212:215], v249 offset:41984\n"
        "s_waitcnt lgkmcnt(6)\n"
        "v_mfma_i32_16x16x64_i8 v[160:163], v[96:99], v[224:227], v[160:163]\n"
        "global_load_dwordx4 v[124:127], v240, s[62:63] offset:1024 sc1\n"
        "v_add_u32 v240, 8192, v240\n"
        "v_mfma_i32_16x16x64_i8 v[164:167], v[100:103], v[224:227], v[164:167]\n"
        "ds_read_b128 v[216:219], v249 offset:41472\n"
        "s_waitcnt lgkmcnt(6)\n"
        "v_mfma_i32_16x16x64_i8 v[168:171], v[96:99], v[228:231], v[168:171]\n"
        "v_mfma_i32_16x16x64_i8 v[172:175], v[100:103], v[228:231], v[172:175]\n"
        "ds_read_b128 v[220:223], v249 offset:42496\n"
        "s_waitcnt lgkmcnt(6)\n"
        "v_mfma_i32_16x16x64_i8 v[176:179], v[96:99], v[232:235], v[176:179]\n"
        "v_mfma_i32_16x16x64_i8 v[180:183], v[100:103], v[232:235], v[180:183]\n"
        "ds_read_b128 v[224:227], v249 offset:41216\n"
        "s_waitcnt lgkmcnt(6)\n"
        "v_mfma_i32_16x16x64_i8 v[184:187], v[96:99], v[236:239], v[184:187]\n"
        "v_mfma_i32_16x16x64_i8 v[188:191], v[100:103], v[236:239], v[188:191]\n"
        "ds_read_b128 v[228:231], v249 offset:42240\n"
        "s_waitcnt vmcnt(4)\n"
        "s_waitcnt lgkmcnt(6)\n"
        "v_mfma_i32_16x16x64_i8 v[128:131], v[104:107], v[208:211], v[128:131]\n"
        "v_mfma_i32_16x16x64_i8 v[132:135], v[108:111], v[208:211], v[132:135]\n"
        "ds_read_b128 v[232:235], v249 offset:41728\n"
        "s_waitcnt lgkmcnt(5)\n"
        "v_mfma_i32_16x16x64_i8 v[136:139], v[104:107], v[212:215], v[136:139]\n"
        "v_mfma_i32_16x16x64_i8 v[140:143], v[108:111], v[212:215], v[140:143]\n"
        "ds_read_b128 v[236:239], v249 offset:42752\n"
        "v_cmp_gt_u32 vcc, 15, v254\n"
        "s_waitcnt lgkmcnt(5)\n"
        "v_mfma_i32_16x16x64_i8 v[144:147], v[104:107], v[216:219], v[144:147]\n"
        "s_cbranch_vccnz W8_s14\n"
        "W8_c14:\n"
        "v_mfma_i32_16x16x64_i8 v[148:151], v[108:111], v[216:219], v[148:151]\n"
        "ds_read_b128 v[208:211], v249 offset:49152\n"
        "s_waitcnt lgkmcnt(5)\n"
        "v_mfma_i32_16x16x64_i8 v[152:155], v[104:107], v[220:223], v[152:155]\n"
        "ds_read_b32 v254, v250 offset:0\n"
        "v_mfma_i32_16x16x64_i8 v[156:159], v[108:111], v[220:223], v[156:159]\n"
        "ds_read_b128 v[212:215], v249 offset:50176\n"
        "s_waitcnt lgkmcnt(6)\n"
        "v_mfma_i32_16x16x64_i8 v[160:163], v[104:107], v[224:227], v[160:163]\n"
        "v_mfma_i32_16x16x64_i8 v[164:167], v[108:111], v[224:227], v[164:167]\n"
        "ds_read_b128 v[216:219], v249 offset:49664\n"
        "s_waitcnt lgkmcnt(6)\n"
        "v_mfma_i32_16x16x64_i8 v[168:171], v[104:107], v[228:231], v[168:171]\n"
        "v_mfma_i32_16x16x64_i8 v[172:175], v[108:111], v[228:231], v[172:175]\n"
        "ds_read_b128 v[220:223], v249 offset:50688\n"
        "s_waitcnt lgkmcnt(6)\n"
        "v_mfma_i32_16x16x64_i8 v[176:179], v[104:107], v[232:235], v[176:179]\n"
        "v_mfma_i32_16x16x64_i8 v[180:183], v[108:111], v[232:235], v[180:183]\n"
        "ds_read_b128 v[224:227], v249 offset:49408\n"
        "s_waitcnt lgkmcnt(6)\n"
        "v_mfma_i32_16x16x64_i8 v[184:187], v[104:107], v[236:239], v[184:187]\n"
        "v_mfma_i32_16x16x64_i8 v[188:191], v[108:111], v[236:239], v[188:191]\n"
        "ds_read_b128 v[228:231], v249 offset:50432\n"
        "s_waitcnt vmcnt(2)\n"
        "s_waitcnt lgkmcnt(6)\n"
        "v_mfma_i32_16x16x64_i8 v[128:131], v[112:115], v[208:211], v[128:131]\n"
        "v_mfma_i32_16x16x64_i8 v[132:135], v[116:119], v[208:211], v[132:135]\n"
        "ds_read_b128 v[232:235], v249 offset:49920\n"
        "s_waitcnt lgkmcnt(5)\n"
        "v_mfma_i32_16x16x64_i8 v[136:139], v[112:115], v[212:215], v[136:139]\n"
        "v_mfma_i32_16x16x64_i8 v[140:143], v[116:119], v[212:215], v[140:143]\n"
        "ds_read_b128 v[236:239], v249 offset:50944\n"
        "v_cmp_gt_u32 vcc, 16, v254\n"
        "s_waitcnt lgkmcnt(5)\n"
        "v_mfma_i32_16x16x64_i8 v[144:147], v[112:115], v[216:219], v[144:147]\n"
        "s_cbranch_vccnz W8_s15\n"
        "W8_c15:\n"
        "v_mfma_i32_16x16x64_i8 v[148:151], v[116:119], v[216:219], v[148:151]\n"
        "ds_read_b128 v[208:211], v249 offset:57344\n"
        "s_waitcnt lgkmcnt(5)\n"
        "v_mfma_i32_16x16x64_i8 v[152:155], v[112:115], v[220:223], v[152:155]\n"
        "ds_read_b32 v254, v250 offset:0\n"
        "v_mfma_i32_16x16x64_i8 v[156:159], v[116:119], v[220:223], v[156:159]\n"
        "ds_read_b128 v[212:215], v249 offset:58368\n"
        "s_waitcnt lgkmcnt(6)\n"
        "v_mfma_i32_16x16x64_i8 v[160:163], v[112:115], v[224:227], v[160:163]\n"
        "v_mfma_i32_16x16x64_i8 v[164:167], v[116:119], v[224:227], v[164:167]\n"
        "ds_read_b128 v[216:219], v249 offset:57856\n"
        "s_waitcnt lgkmcnt(6)\n"
        "v_mfma_i32_16x16x64_i8 v[168:171], v[112:115], v[228:231], v[168:171]\n"
        "ds_read_b32 v255, v250 offset:260\n"
        "v_mfma_i32_16x16x64_i8 v[172:175], v[116:119], v[228:231], v[172:175]\n"
        "ds_read_b128 v[220:223], v249 offset:58880\n"
        "s_waitcnt lgkmcnt(7)\n"
        "v_mfma_i32_16x16x64_i8 v[176:179], v[112:115], v[232:235], v[176:179]\n"
        "v_mfma_i32_16x16x64_i8 v[180:183], v[116:119], v[232:235], v[180:183]\n"
        "ds_read_b128 v[224:227], v249 offset:57600\n"
        "s_waitcnt lgkmcnt(7)\n"
        "v_mfma_i32_16x16x64_i8 v[184:187], v[112:115], v[236:239], v[184:187]\n"
        "v_mfma_i32_16x16x64_i8 v[188:191], v[116:119], v[236:239], v[188:191]\n"
        "ds_read_b128 v[228:231], v249 offset:58624\n"
        "s_waitcnt vmcnt(0)\n"
        "s_waitcnt lgkmcnt(5)\n"
        "v_mfma_i32_16x16x64_i8 v[128:131], v[120:123], v[208:211], v[128:131]\n"
        "v_mfma_i32_16x16x64_i8 v[132:135], v[124:127], v[208:211], v[132:135]\n"
        "ds_read_b128 v[232:235], v249 offset:58112\n"
        "v_mfma_i32_16x16x64_i8 v[136:139], v[120:123], v[212:215], v[136:139]\n"
        "v_mfma_i32_16x16x64_i8 v[140:143], v[124:127], v[212:215], v[140:143]\n"
        "ds_read_b128 v[236:239], v249 offset:59136\n"
        "v_cmp_gt_u32 vcc, 18, v254\n"
        "s_waitcnt lgkmcnt(4)\n"
        "v_mfma_i32_16x16x64_i8 v[144:147], v[120:123], v[216:219], v[144:147]\n"
        "s_cbranch_vccnz W8_s16\n"
        "W8_c16:\n"
        "v_mfma_i32_16x16x64_i8 v[148:151], v[124:127], v[216:219], v[148:151]\n"
        "ds_read_b128 v[208:211], v248 offset:0\n"
        "v_mfma_i32_16x16x64_i8 v[152:155], v[120:123], v[220:223], v[152:155]\n"
        "v_mfma_i32_16x16x64_i8 v[156:159], v[124:127], v[220:223], v[156:159]\n"
        "ds_read_b128 v[212:215], v248 offset:1024\n"
        "s_waitcnt lgkmcnt(4)\n"
        "v_mfma_i32_16x16x64_i8 v[160:163], v[120:123], v[224:227], v[160:163]\n"
        "v_mfma_i32_16x16x64_i8 v[164:167], v[124:127], v[224:227], v[164:167]\n"
        "ds_read_b128 v[216:219], v248 offset:2048\n"
        "v_cmp_gt_u32 vcc, 2, v255\n"
        "v_mfma_i32_16x16x64_i8 v[168:171], v[120:123], v[228:231], v[168:171]\n"
        "v_add_u32 v249, 65536, v248\n"
        "s_cbranch_vccnz W8_sT\n"
        "W8_cT:\n"
        "v_mfma_i32_16x16x64_i8 v[172:175], v[124:127], v[228:231], v[172:175]\n"
        "ds_read_b128 v[220:223], v248 offset:3072\n"
        "s_waitcnt lgkmcnt(4)\n"
        "v_mfma_i32_16x16x64_i8 v[176:179], v[120:123], v[232:235], v[176:179]\n"
        "ds_read_b128 v[240:243], v253\n"
        "ds_read_b128 v[244:247], v253 offset:16\n"
        "v_mfma_i32_16x16x64_i8 v[180:183], v[124:127], v[232:235], v[180:183]\n"
        "ds_read_b128 v[224:227], v248 offset:4096\n"
        "v_mfma_i32_16x16x64_i8 v[184:187], v[120:123], v[236:239], v[184:187]\n"
        "v_cvt_f32_i32 v128, v128\n"
        "v_cvt_f32_i32 v129, v129\n"
        "v_cvt_f32_i32 v130, v130\n"
        "v_mfma_i32_16x16x64_i8 v[188:191], v[124:127], v[236:239], v[188:191]\n"
        "ds_read_b128 v[228:231], v248 offset:5120\n"
        "s_waitcnt lgkmcnt(6)\n"
        "v_mfma_i32_16x16x64_i8 v[192:195], v[0:3], v[208:211], 0\n"
        "v_cvt_f32_i32 v131, v131\n"
        "v_cvt_f32_i32 v136, v136\n"
        "v_mfma_i32_16x16x64_i8 v[196:199], v[4:7], v[208:211], 0\n"
        "ds_read_b128 v[232:235], v248 offset:6144\n"
        "v_cvt_f32_i32 v137, v137\n"
        "v_mfma_i32_16x16x64_i8 v[200:203], v[0:3], v[212:215], 0\n"
        "v_cvt_f32_i32 v138, v138\n"
        "v_cvt_f32_i32 v139, v139\n"
        "v_mfma_i32_16x16x64_i8 v[204:207], v[4:7], v[212:215], 0\n"
        "ds_read_b128 v[236:239], v248 offset:7168\n"
        "v_cvt_f32_i32 v144, v144\n"
        "s_waitcnt lgkmcnt(6)\n"
        "v_mfma_i32_16x16x64_i8 v[192:195], v[8:11], v[216:219], v[192:195]\n"
        "v_cvt_f32_i32 v145, v145\n"
        "v_cvt_f32_i32 v146, v146\n"
        "v_mfma_i32_16x16x64_i8 v[196:199], v[12:15], v[216:219], v[196:199]\n"
        "ds_read_b128 v[208:211], v248 offset:8192\n"
        "v_cvt_f32_i32 v147, v147\n"
        "v_mfma_i32_16x16x64_i8 v[200:203], v[8:11], v[220:223], v[200:203]\n"
        "ds_read_b32 v254, v250 offset:0\n"
        "v_cvt_f32_i32 v152, v152\n"
        "v_mfma_i32_16x16x64_i8 v[204:207], v[12:15], v[220:223], v[204:207]\n"
        "ds_read_b128 v[212:215], v248 offset:9216\n"
        "s_waitcnt lgkmcnt(5)\n"
        "v_mfma_i32_16x16x64_i8 v[192:195], v[16:19], v[224:227], v[192:195]\n"
        "v_cvt_f32_i32 v153, v153\n"
        "v_cvt_f32_i32 v154, v154\n"
        "v_cvt_f32_i32 v155, v155\n"
        "v_mfma_i32_16x16x64_i8 v[196:199], v[20:23], v[224:227], v[196:199]\n"
        "ds_read_b128 v[216:219], v248 offset:10240\n"
        "v_mfma_i32_16x16x64_i8 v[200:203], v[16:19], v[228:231], v[200:203]\n"
        "v_fma_f32 v128, v128, v240, v241\n"
        "v_fma_f32 v129, v129, v240, v241\n"
        "v_mfma_i32_16x16x64_i8 v[204:207], v[20:23], v[228:231], v[204:207]\n"
        "ds_read_b128 v[220:223], v248 offset:11264\n"
        "s_waitcnt lgkmcnt(5)\n"
        "v_mfma_i32_16x16x64_i8 v[192:195], v[24:27], v[232:235], v[192:195]\n"
        "v_fma_f32 v130, v130, v240, v241\n"
        "v_fma_f32 v131, v131, v240, v241\n"
        "v_mfma_i32_16x16x64_i8 v[196:199], v[28:31], v[232:235], v[196:199]\n"
        "ds_read_b128 v[224:227], v248 offset:12288\n"
        "v_fma_f32 v136, v136, v242, v243\n"
        "v_fma_f32 v137, v137, v242, v243\n"
        "v_mfma_i32_16x16x64_i8 v[200:203], v[24:27], v[236:239], v[200:203]\n"
        "v_fma_f32 v138, v138, v242, v243\n"
        "v_fma_f32 v139, v139, v242, v243\n"
        "v_mfma_i32_16x16x64_i8 v[204:207], v[28:31], v[236:239], v[204:207]\n"
        "ds_read_b128 v[228:231], v248 offset:13312\n"
        "s_waitcnt lgkmcnt(4)\n"
        "v_mfma_i32_16x16x64_i8 v[192:195], v[32:35], v[208:211], v[192:195]\n"
        "v_fma_f32 v144, v144, v244, v245\n"
        "v_fma_f32 v145, v145, v244, v245\n"
        "v_mfma_i32_16x16x64_i8 v[196:199], v[36:39], v[208:211], v[196:199]\n"
        "ds_read_b128 v[232:235], v248 offset:14336\n"
        "v_fma_f32 v146, v146, v244, v245\n"
        "v_mfma_i32_16x16x64_i8 v[200:203], v[32:35], v[212:215], v[200:203]\n"
        "v_fma_f32 v147, v147, v244, v245\n"
        "v_fma_f32 v152, v152, v246, v247\n"
        "v_fma_f32 v153, v153, v246, v247\n"
        "v_mfma_i32_16x16x64_i8 v[204:207], v[36:39], v[212:215], v[204:207]\n"
        "ds_read_b128 v[236:239], v248 offset:15360\n"
        "v_cmp_gt_u32 vcc, 20, v254\n"
        "s_waitcnt lgkmcnt(4)\n"
        "v_mfma_i32_16x16x64_i8 v[192:195], v[40:43], v[216:219], v[192:195]\n"
        "s_cbranch_vccnz W8_s18\n"
        "W8_c18:\n"
        "v_fma_f32 v154, v154, v246, v247\n"
        "v_mfma_i32_16x16x64_i8 v[196:199], v[44:47], v[216:219], v[196:199]\n"
        "ds_read_b128 v[208:211], v248 offset:16384\n"
        "v_fma_f32 v155, v155, v246, v247\n"
        "v_mfma_i32_16x16x64_i8 v[200:203], v[40:43], v[220:223], v[200:203]\n"
        "v_cvt_pk_bf16_f32 v128, v128, v136\n"
        "v_cvt_pk_bf16_f32 v136, v129, v137\n"
        "v_mfma_i32_16x16x64_i8 v[204:207], v[44:47], v[220:223], v[204:207]\n"
        "ds_read_b128 v[212:215], v248 offset:17408\n"
        "v_cvt_pk_bf16_f32 v130, v130, v138\n"
        "s_waitcnt lgkmcnt(4)\n"
        "v_mfma_i32_16x16x64_i8 v[192:195], v[48:51], v[224:227], v[192:195]\n"
        "v_cvt_pk_bf16_f32 v138, v131, v139\n"
        "v_cvt_pk_bf16_f32 v129, v144, v152\n"
        "v_mfma_i32_16x16x64_i8 v[196:199], v[52:55], v[224:227], v[196:199]\n"
        "ds_read_b128 v[216:219], v248 offset:18432\n"
        "global_store_dwordx2 v251, v[128:129], s[40:41] offset:0 sc1\n"
        "v_mfma_i32_16x16x64_i8 v[200:203], v[48:51], v[228:231], v[200:203]\n"
        "v_cvt_pk_bf16_f32 v137, v145, v153\n"
        "global_store_dwordx2 v251, v[136:137], s[42:43] offset:0 sc1\n"
        "v_mfma_i32_16x16x64_i8 v[204:207], v[52:55], v[228:231], v[204:207]\n"
        "ds_read_b128 v[220:223], v248 offset:19456\n"
        "s_waitcnt lgkmcnt(4)\n"
        "v_mfma_i32_16x16x64_i8 v[192:195], v[56:59], v[232:235], v[192:195]\n"
        "v_cvt_pk_bf16_f32 v131, v146, v154\n"
        "global_store_dwordx2 v251, v[130:131], s[44:45] offset:0 sc1\n"
        "v_cvt_pk_bf16_f32 v139, v147, v155\n"
        "v_mfma_i32_16x16x64_i8 v[196:199], v[60:63], v[232:235], v[196:199]\n"
        "ds_read_b128 v[224:227], v248 offset:20480\n"
        "global_store_dwordx2 v251, v[138:139], s[46:47] offset:0 sc1\n"
        "v_mfma_i32_16x16x64_i8 v[200:203], v[56:59], v[236:239], v[200:203]\n"
        "v_cvt_f32_i32 v132, v132\n"
        "v_cvt_f32_i32 v133, v133\n"
        "v_mfma_i32_16x16x64_i8 v[204:207], v[60:63], v[236:239], v[204:207]\n"
        "ds_read_b128 v[228:231], v248 offset:21504\n"
        "s_waitcnt lgkmcnt(4)\n"
        "v_mfma_i32_16x16x64_i8 v[192:195], v[64:67], v[208:211], v[192:195]\n"
        "v_cvt_f32_i32 v134, v134\n"
        "v_cvt_f32_i32 v135, v135\n"
        "v_mfma_i32_16x16x64_i8 v[196:199], v[68:71], v[208:211], v[196:199]\n"
        "ds_read_b128 v[232:235], v248 offset:22528\n"
        "v_cvt_f32_i32 v140, v140\n"
        "v_cvt_f32_i32 v141, v141\n"
        "v_mfma_i32_16x16x64_i8 v[200:203], v[64:67], v[212:215], v[200:203]\n"
        "v_cvt_f32_i32 v142, v142\n"
        "v_cvt_f32_i32 v143, v143\n"
        "v_mfma_i32_16x16x64_i8 v[204:207], v[68:71], v[212:215], v[204:207]\n"
        "ds_read_b128 v[236:239], v248 offset:23552\n"
        "s_waitcnt lgkmcnt(4)\n"
        "v_mfma_i32_16x16x64_i8 v[192:195], v[72:75], v[216:219], v[192:195]\n"
        "v_cvt_f32_i32 v148, v148\n"
        "v_cvt_f32_i32 v149, v149\n"
        "v_mfma_i32_16x16x64_i8 v[196:199], v[76:79], v[216:219], v[196:199]\n"
        "ds_read_b128 v[208:211], v248 offset:24576\n"
        "v_cvt_f32_i32 v150, v150\n"
        "v_mfma_i32_16x16x64_i8 v[200:203], v[72:75], v[220:223], v[200:203]\n"
        "ds_read_b32 v254, v250 offset:0\n"
        "v_cvt_f32_i32 v151, v151\n"
        "v_cvt_f32_i32 v156, v156\n"
        "v_mfma_i32_16x16x64_i8 v[204:207], v[76:79], v[220:223], v[204:207]\n"
        "ds_read_b128 v[212:215], v248 offset:25600\n"
        "s_waitcnt lgkmcnt(5)\n"
        "v_mfma_i32_16x16x64_i8 v[192:195], v[80:83], v[224:227], v[192:195]\n"
        "v_cvt_f32_i32 v157, v157\n"
        "v_cvt_f32_i32 v158, v158\n"
        "v_mfma_i32_16x16x64_i8 v[196:199], v[84:87], v[224:227], v[196:199]\n"
        "ds_read_b128 v[216:219], v248 offset:26624\n"
        "v_cvt_f32_i32 v159, v159\n"
        "v_mfma_i32_16x16x64_i8 v[200:203], v[80:83], v[228:231], v[200:203]\n"
        "v_fma_f32 v132, v132, v240, v241\n"
        "v_fma_f32 v133, v133, v240, v241\n"
        "v_mfma_i32_16x16x64_i8 v[204:207], v[84:87], v[228:231], v[204:207]\n"
        "ds_read_b128 v[220:223], v248 offset:27648\n"
        "v_fma_f32 v134, v134, v240, v241\n"
        "s_waitcnt lgkmcnt(5)\n"
        "v_mfma_i32_16x16x64_i8 v[192:195], v[88:91], v[232:235], v[192:195]\n"
        "v_fma_f32 v135, v135, v240, v241\n"
        "v_fma_f32 v140, v140, v242, v243\n"
        "v_mfma_i32_16x16x64_i8 v[196:199], v[92:95], v[232:235], v[196:199]\n"
        "ds_read_b128 v[224:227], v248 offset:28672\n"
        "v_fma_f32 v141, v141, v242, v243\n"
        "v_mfma_i32_16x16x64_i8 v[200:203], v[88:91], v[236:239], v[200:203]\n"
        "v_fma_f32 v142, v142, v242, v243\n"
        "v_fma_f32 v143, v143, v242, v243\n"
        "v_mfma_i32_16x16x64_i8 v[204:207], v[92:95], v[236:239], v[204:207]\n"
        "ds_read_b128 v[228:231], v248 offset:29696\n"
        "s_waitcnt lgkmcnt(4)\n"
        "v_mfma_i32_16x16x64_i8 v[192:195], v[96:99], v[208:211], v[192:195]\n"
        "v_fma_f32 v148, v148, v244, v245\n"
        "v_fma_f32 v149, v149, v244, v245\n"
        "v_fma_f32 v150, v150, v244, v245\n"
        "v_mfma_i32_16x16x64_i8 v[196:199], v[100:103], v[208:211], v[196:199]\n"
        "ds_read_b128 v[232:235], v248 offset:30720\n"
        "v_fma_f32 v151, v151, v244, v245\n"
        "v_mfma_i32_16x16x64_i8 v[200:203], v[96:99], v[212:215], v[200:203]\n"
        "v_fma_f32 v156, v156, v246, v247\n"
        "v_fma_f32 v157, v157, v246, v247\n"
        "v_mfma_i32_16x16x64_i8 v[204:207], v[100:103], v[212:215], v[204:207]\n"
        "ds_read_b128 v[236:239], v248 offset:31744\n"
        "v_cmp_gt_u32 vcc, 22, v254\n"
        "s_waitcnt lgkmcnt(4)\n"
        "v_mfma_i32_16x16x64_i8 v[192:195], v[104:107], v[216:219], v[192:195]\n"
        "s_cbranch_vccnz W8_s20\n"
        "W8_c20:\n"
        "v_fma_f32 v158, v158, v246, v247\n"
        "v_mfma_i32_16x16x64_i8 v[196:199], v[108:111], v[216:219], v[196:199]\n"
        "ds_read_b128 v[208:211], v248 offset:32768\n"
        "v_fma_f32 v159, v159, v246, v247\n"
        "v_cvt_pk_bf16_f32 v132, v132, v140\n"
        "v_mfma_i32_16x16x64_i8 v[200:203], v[104:107], v[220:223], v[200:203]\n"
        "v_cvt_pk_bf16_f32 v140, v133, v141\n"
        "v_cvt_pk_bf16_f32 v134, v134, v142\n"
        "v_mfma_i32_16x16x64_i8 v[204:207], v[108:111], v[220:223], v[204:207]\n"
        "ds_read_b128 v[212:215], v248 offset:33792\n"
        "s_waitcnt lgkmcnt(4)\n"
        "v_mfma_i32_16x16x64_i8 v[192:195], v[112:115], v[224:227], v[192:195]\n"
        "v_cvt_pk_bf16_f32 v142, v135, v143\n"
        "v_cvt_pk_bf16_f32 v133, v148, v156\n"
        "v_mfma_i32_16x16x64_i8 v[196:199], v[116:119], v[224:227], v[196:199]\n"
        "ds_read_b128 v[216:219], v248 offset:34816\n"
        "global_store_dwordx2 v251, v[132:133], s[48:49] offset:0 sc1\n"
        "v_mfma_i32_16x16x64_i8 v[200:203], v[112:115], v[228:231], v[200:203]\n"
        "v_cvt_pk_bf16_f32 v141, v149, v157\n"
        "global_store_dwordx2 v251, v[140:141], s[50:51] offset:0 sc1\n"
        "v_cvt_pk_bf16_f32 v135, v150, v158\n"
        "v_mfma_i32_16x16x64_i8 v[204:207], v[116:119], v[228:231], v[204:207]\n"
        "ds_read_b128 v[220:223], v248 offset:35840\n"
        "s_waitcnt lgkmcnt(4)\n"
        "v_mfma_i32_16x16x64_i8 v[192:195], v[120:123], v[232:235], v[192:195]\n"
        "global_store_dwordx2 v251, v[134:135], s[52:53] offset:0 sc1\n"
        "v_cvt_pk_bf16_f32 v143, v151, v159\n"
        "v_mfma_i32_16x16x64_i8 v[196:199], v[124:127], v[232:235], v[196:199]\n"
        "ds_read_b128 v[224:227], v248 offset:36864\n"
        "global_store_dwordx2 v251, v[142:143], s[54:55] offset:0 sc1\n"
        "v_mfma_i32_16x16x64_i8 v[200:203], v[120:123], v[236:239], v[200:203]\n"
        "ds_read_b128 v[240:243], v253 offset:512\n"
        "ds_read_b128 v[244:247], v253 offset:528\n"
        "v_mfma_i32_16x16x64_i8 v[204:207], v[124:127], v[236:239], v[204:207]\n"
        "ds_read_b128 v[228:231], v248 offset:37888\n"
        "v_cvt_f32_i32 v160, v160\n"
        "s_waitcnt lgkmcnt(6)\n"
        "v_mfma_i32_16x16x64_i8 v[128:131], v[0:3], v[208:211], 0\n"
        "v_cvt_f32_i32 v161, v161\n"
        "v_cvt_f32_i32 v162, v162\n"
        "v_mfma_i32_16x16x64_i8 v[132:135], v[4:7], v[208:211], 0\n"
        "ds_read_b128 v[232:235], v248 offset:38912\n"
        "v_cvt_f32_i32 v163, v163\n"
        "v_mfma_i32_16x16x64_i8 v[136:139], v[0:3], v[212:215], 0\n"
        "v_cvt_f32_i32 v168, v168\n"
        "v_cvt_f32_i32 v169, v169\n"
        "v_cvt_f32_i32 v170, v170\n"
        "v_mfma_i32_16x16x64_i8 v[140:143], v[4:7], v[212:215], 0\n"
        "ds_read_b128 v[236:239], v248 offset:39936\n"
        "s_waitcnt lgkmcnt(6)\n"
        "v_mfma_i32_16x16x64_i8 v[128:131], v[8:11], v[216:219], v[128:131]\n"
        "v_cvt_f32_i32 v171, v171\n"
        "v_cvt_f32_i32 v176, v176\n"
        "v_mfma_i32_16x16x64_i8 v[132:135], v[12:15], v[216:219], v[132:135]\n"
        "ds_read_b128 v[208:211], v248 offset:40960\n"
        "v_cvt_f32_i32 v177, v177\n"
        "v_mfma_i32_16x16x64_i8 v[136:139], v[8:11], v[220:223], v[136:139]\n"
        "ds_read_b32 v254, v250 offset:0\n"
        "v_cvt_f32_i32 v178, v178\n"
        "v_cvt_f32_i32 v179, v179\n"
        "v_mfma_i32_16x16x64_i8 v[140:143], v[12:15], v[220:223], v[140:143]\n"
        "ds_read_b128 v[212:215], v248 offset:41984\n"
        "s_waitcnt lgkmcnt(5)\n"
        "v_mfma_i32_16x16x64_i8 v[128:131], v[16:19], v[224:227], v[128:131]\n"
        "v_cvt_f32_i32 v184, v184\n"
        "v_cvt_f32_i32 v185, v185\n"
        "v_mfma_i32_16x16x64_i8 v[132:135], v[20:23], v[224:227], v[132:135]\n"
        "ds_read_b128 v[216:219], v248 offset:43008\n"
        "v_cvt_f32_i32 v186, v186\n"
        "v_mfma_i32_16x16x64_i8 v[136:139], v[16:19], v[228:231], v[136:139]\n"
        "v_cvt_f32_i32 v187, v187\n"
        "v_fma_f32 v160, v160, v240, v241\n"
        "v_mfma_i32_16x16x64_i8 v[140:143], v[20:23], v[228:231], v[140:143]\n"
        "ds_read_b128 v[220:223], v248 offset:44032\n"
        "s_waitcnt lgkmcnt(5)\n"
        "v_mfma_i32_16x16x64_i8 v[128:131], v[24:27], v[232:235], v[128:131]\n"
        "v_fma_f32 v161, v161, v240, v241\n"
        "v_fma_f32 v162, v162, v240, v241\n"
        "v_mfma_i32_16x16x64_i8 v[132:135], v[28:31], v[232:235], v[132:135]\n"
        "ds_read_b128 v[224:227], v248 offset:45056\n"
        "v_fma_f32 v163, v163, v240, v241\n"
        "v_mfma_i32_16x16x64_i8 v[136:139], v[24:27], v[236:239], v[136:139]\n"
        "v_fma_f32 v168, v168, v242, v243\n"
        "v_fma_f32 v169, v169, v242, v243\n"
        "v_fma_f32 v170, v170, v242, v243\n"
        "v_mfma_i32_16x16x64_i8 v[140:143], v[28:31], v[236:239], v[140:143]\n"
        "ds_read_b128 v[228:231], v248 offset:46080\n"
        "s_waitcnt lgkmcnt(4)\n"
        "v_mfma_i32_16x16x64_i8 v[128:131], v[32:35], v[208:211], v[128:131]\n"
        "v_fma_f32 v171, v171, v242, v243\n"
        "v_fma_f32 v176, v176, v244, v245\n"
        "v_mfma_i32_16x16x64_i8 v[132:135], v[36:39], v[208:211], v[132:135]\n"
        "ds_read_b128 v[232:235], v248 offset:47104\n"
        "v_fma_f32 v177, v177, v244, v245\n"
        "v_mfma_i32_16x16x64_i8 v[136:139], v[32:35], v[212:215], v[136:139]\n"
        "v_fma_f32 v178, v178, v244, v245\n"
        "v_fma_f32 v179, v179, v244, v245\n"
        "v_fma_f32 v184, v184, v246, v247\n"
        "v_mfma_i32_16x16x64_i8 v[140:143], v[36:39], v[212:215], v[140:143]\n"
        "ds_read_b128 v[236:239], v248 offset:48128\n"
        "v_cmp_gt_u32 vcc, 24, v254\n"
        "s_waitcnt lgkmcnt(4)\n"
        "v_mfma_i32_16x16x64_i8 v[128:131], v[40:43], v[216:219], v[128:131]\n"
        "s_cbranch_vccnz W8_s22\n"
        "W8_c22:\n"
        "v_fma_f32 v185, v185, v246, v247\n"
        "v_mfma_i32_16x16x64_i8 v[132:135], v[44:47], v[216:219], v[132:135]\n"
        "ds_read_b128 v[208:211], v248 offset:49152\n"
        "v_fma_f32 v186, v186, v246, v247\n"
        "v_mfma_i32_16x16x64_i8 v[136:139], v[40:43], v[220:223], v[136:139]\n"
        "v_fma_f32 v187, v187, v246, v247\n"
        "v_cvt_pk_bf16_f32 v160, v160, v168\n"
        "v_cvt_pk_bf16_f32 v168, v161, v169\n"
        "v_mfma_i32_16x16x64_i8 v[140:143], v[44:47], v[220:223], v[140:143]\n"
        "ds_read_b128 v[212:215], v248 offset:50176\n"
        "s_waitcnt lgkmcnt(4)\n"
        "v_mfma_i32_16x16x64_i8 v[128:131], v[48:51], v[224:227], v[128:131]\n"
        "v_cvt_pk_bf16_f32 v162, v162, v170\n"
        "v_cvt_pk_bf16_f32 v170, v163, v171\n"
        "v_mfma_i32_16x16x64_i8 v[132:135], v[52:55], v[224:227], v[132:135]\n"
        "ds_read_b128 v[216:219], v248 offset:51200\n"
        "v_cvt_pk_bf16_f32 v161, v176, v184\n"
        "v_mfma_i32_16x16x64_i8 v[136:139], v[48:51], v[228:231], v[136:139]\n"
        "global_store_dwordx2 v251, v[160:161], s[40:41] offset:128 sc1\n"
        "v_cvt_pk_bf16_f32 v169, v177, v185\n"
        "global_store_dwordx2 v251, v[168:169], s[42:43] offset:128 sc1\n"
        "v_mfma_i32_16x16x64_i8 v[140:143], v[52:55], v[228:231], v[140:143]\n"
        "ds_read_b128 v[220:223], v248 offset:52224\n"
        "s_waitcnt lgkmcnt(4)\n"
        "v_mfma_i32_16x16x64_i8 v[128:131], v[56:59], v[232:235], v[128:131]\n"
        "v_cvt_pk_bf16_f32 v163, v178, v186\n"
        "global_store_dwordx2 v251, v[162:163], s[44:45] offset:128 sc1\n"
        "v_mfma_i32_16x16x64_i8 v[132:135], v[60:63], v[232:235], v[132:135]\n"
        "ds_read_b128 v[224:227], v248 offset:53248\n"
        "v_cvt_pk_bf16_f32 v171, v179, v187\n"
        "v_mfma_i32_16x16x64_i8 v[136:139], v[56:59], v[236:239], v[136:139]\n"
        "global_store_dwordx2 v251, v[170:171], s[46:47] offset:128 sc1\n"
        "v_cvt_f32_i32 v164, v164\n"
        "v_cvt_f32_i32 v165, v165\n"
        "v_mfma_i32_16x16x64_i8 v[140:143], v[60:63], v[236:239], v[140:143]\n"
        "ds_read_b128 v[228:231], v248 offset:54272\n"
        "s_waitcnt lgkmcnt(4)\n"
        "v_mfma_i32_16x16x64_i8 v[128:131], v[64:67], v[208:211], v[128:131]\n"
        "v_cvt_f32_i32 v166, v166\n"
        "v_cvt_f32_i32 v167, v167\n"
        "v_mfma_i32_16x16x64_i8 v[132:135], v[68:71], v[208:211], v[132:135]\n"
        "ds_read_b128 v[232:235], v248 offset:55296\n"
        "v_cvt_f32_i32 v172, v172\n"
        "v_mfma_i32_16x16x64_i8 v[136:139], v[64:67], v[212:215], v[136:139]\n"
        "v_cvt_f32_i32 v173, v173\n"
        "v_cvt_f32_i32 v174, v174\n"
        "v_cvt_f32_i32 v175, v175\n"
        "v_mfma_i32_16x16x64_i8 v[140:143], v[68:71], v[212:215], v[140:143]\n"
        "ds_read_b128 v[236:239], v248 offset:56320\n"
        "s_waitcnt lgkmcnt(4)\n"
        "v_mfma_i32_16x16x64_i8 v[128:131], v[72:75], v[216:219], v[128:131]\n"
        "v_cvt_f32_i32 v180, v180\n"
        "v_cvt_f32_i32 v181, v181\n"
        "v_mfma_i32_16x16x64_i8 v[132:135], v[76:79], v[216:219], v[132:135]\n"
        "ds_read_b128 v[208:211], v248 offset:57344\n"
        "v_cvt_f32_i32 v182, v182\n"
        "v_mfma_i32_16x16x64_i8 v[136:139], v[72:75], v[220:223], v[136:139]\n"
        "ds_read_b32 v254, v250 offset:0\n"
        "v_cvt_f32_i32 v183, v183\n"
        "v_cvt_f32_i32 v188, v188\n"
        "v_mfma_i32_16x16x64_i8 v[140:143], v[76:79], v[220:223], v[140:143]\n"
        "ds_read_b128 v[212:215], v248 offset:58368\n"
        "s_waitcnt lgkmcnt(5)\n"
        "v_mfma_i32_16x16x64_i8 v[128:131], v[80:83], v[224:227], v[128:131]\n"
        "v_cvt_f32_i32 v189, v189\n"
        "v_cvt_f32_i32 v190, v190\n"
        "v_mfma_i32_16x16x64_i8 v[132:135], v[84:87], v[224:227], v[132:135]\n"
        "ds_read_b128 v[216:219], v248 offset:59392\n"
        "v_cvt_f32_i32 v191, v191\n"
        "v_mfma_i32_16x16x64_i8 v[136:139], v[80:83], v[228:231], v[136:139]\n"
        "v_fma_f32 v164, v164, v240, v241\n"
        "v_fma_f32 v165, v165, v240, v241\n"
        "v_fma_f32 v166, v166, v240, v241\n"
        "v_mfma_i32_16x16x64_i8 v[140:143], v[84:87], v[228:231], v[140:143]\n"
        "ds_read_b128 v[220:223], v248 offset:60416\n"
        "s_waitcnt lgkmcnt(5)\n"
        "v_mfma_i32_16x16x64_i8 v[128:131], v[88:91], v[232:235], v[128:131]\n"
        "v_fma_f32 v167, v167, v240, v241\n"
        "v_fma_f32 v172, v172, v242, v243\n"
        "v_mfma_i32_16x16x64_i8 v[132:135], v[92:95], v[232:235], v[132:135]\n"
        "ds_read_b128 v[224:227], v248 offset:61440\n"
        "v_fma_f32 v173, v173, v242, v243\n"
        "v_mfma_i32_16x16x64_i8 v[136:139], v[88:91], v[236:239], v[136:139]\n"
        "v_fma_f32 v174, v174, v242, v243\n"
        "v_fma_f32 v175, v175, v242, v243\n"
        "v_fma_f32 v180, v180, v244, v245\n"
        "v_mfma_i32_16x16x64_i8 v[140:143], v[92:95], v[236:239], v[140:143]\n"
        "ds_read_b128 v[228:231], v248 offset:62464\n"
        "s_waitcnt lgkmcnt(4)\n"
        "v_mfma_i32_16x16x64_i8 v[128:131], v[96:99], v[208:211], v[128:131]\n"
        "v_fma_f32 v181, v181, v244, v245\n"
        "v_fma_f32 v182, v182, v244, v245\n"
        "v_mfma_i32_16x16x64_i8 v[132:135], v[100:103], v[208:211], v[132:135]\n"
        "ds_read_b128 v[232:235], v248 offset:63488\n"
        "v_fma_f32 v183, v183, v244, v245\n"
        "v_mfma_i32_16x16x64_i8 v[136:139], v[96:99], v[212:215], v[136:139]\n"
        "v_fma_f32 v188, v188, v246, v247\n"
        "v_fma_f32 v189, v189, v246, v247\n"
        "v_fma_f32 v190, v190, v246, v247\n"
        "v_mfma_i32_16x16x64_i8 v[140:143], v[100:103], v[212:215], v[140:143]\n"
        "ds_read_b128 v[236:239], v248 offset:64512\n"
        "v_cmp_gt_u32 vcc, 26, v254\n"
        "s_waitcnt lgkmcnt(4)\n"
        "v_mfma_i32_16x16x64_i8 v[128:131], v[104:107], v[216:219], v[128:131]\n"
        "s_cbranch_vccnz W8_s24\n"
        "W8_c24:\n"
        "v_fma_f32 v191, v191, v246, v247\n"
        "v_mfma_i32_16x16x64_i8 v[132:135], v[108:111], v[216:219], v[132:135]\n"
        "ds_read_b128 v[208:211], v249 offset:0\n"
        "v_cvt_pk_bf16_f32 v164, v164, v172\n"
        "v_mfma_i32_16x16x64_i8 v[136:139], v[104:107], v[220:223], v[136:139]\n"
        "v_cvt_pk_bf16_f32 v172, v165, v173\n"
        "v_cvt_pk_bf16_f32 v166, v166, v174\n"
        "v_cvt_pk_bf16_f32 v174, v167, v175\n"
        "v_mfma_i32_16x16x64_i8 v[140:143], v[108:111], v[220:223], v[140:143]\n"
        "ds_read_b128 v[212:215], v249 offset:1024\n"
        "s_waitcnt lgkmcnt(4)\n"
        "v_mfma_i32_16x16x64_i8 v[128:131], v[112:115], v[224:227], v[128:131]\n"
        "v_cvt_pk_bf16_f32 v165, v180, v188\n"
        "global_store_dwordx2 v251, v[164:165], s[48:49] offset:128 sc1\n"
        "v_mfma_i32_16x16x64_i8 v[132:135], v[116:119], v[224:227], v[132:135]\n"
        "ds_read_b128 v[216:219], v249 offset:2048\n"
        "v_cvt_pk_bf16_f32 v173, v181, v189\n"
        "v_mfma_i32_16x16x64_i8 v[136:139], v[112:115], v[228:231], v[136:139]\n"
        "global_store_dwordx2 v251, v[172:173], s[50:51] offset:128 sc1\n"
        "v_cvt_pk_bf16_f32 v167, v182, v190\n"
        "global_store_dwordx2 v251, v[166:167], s[52:53] offset:128 sc1\n"
        "v_mfma_i32_16x16x64_i8 v[140:143], v[116:119], v[228:231], v[140:143]\n"
        "ds_read_b128 v[220:223], v249 offset:3072\n"
        "s_waitcnt lgkmcnt(4)\n"
        "v_mfma_i32_16x16x64_i8 v[128:131], v[120:123], v[232:235], v[128:131]\n"
        "v_cvt_pk_bf16_f32 v175, v183, v191\n"
        "global_store_dwordx2 v251, v[174:175], s[54:55] offset:128 sc1\n"
        "v_mfma_i32_16x16x64_i8 v[132:135], v[124:127], v[232:235], v[132:135]\n"
        "ds_read_b128 v[224:227], v249 offset:4096\n"
        "v_add_u32 v251, s37, v251\n"
        "v_mfma_i32_16x16x64_i8 v[136:139], v[120:123], v[236:239], v[136:139]\n"
        "v_mfma_i32_16x16x64_i8 v[140:143], v[124:127], v[236:239], v[140:143]\n"
        "ds_read_b128 v[228:231], v249 offset:5120\n"
        "v_and_b32 v255, 0xfffffeff, v253\n"
        "s_waitcnt lgkmcnt(4)\n"
        "v_mfma_i32_16x16x64_i8 v[144:147], v[0:3], v[208:211], 0\n"
        "v_bfe_u32 v240, v253, 8, 1\n"
        "v_cvt_f32_i32 v192, v192\n"
        "v_mfma_i32_16x16x64_i8 v[148:151], v[4:7], v[208:211], 0\n"
        "ds_read_b128 v[232:235], v249 offset:6144\n"
        "v_lshl_add_u32 v255, v240, 4, v255\n"
        "v_mfma_i32_16x16x64_i8 v[152:155], v[0:3], v[212:215], 0\n"
        "ds_read_b128 v[244:247], v255 offset:1536\n"
        "ds_read_b128 v[240:243], v253 offset:1024\n"
        "v_mfma_i32_16x16x64_i8 v[156:159], v[4:7], v[212:215], 0\n"
        "ds_read_b128 v[236:239], v249 offset:7168\n"
        "s_waitcnt lgkmcnt(6)\n"
        "v_mfma_i32_16x16x64_i8 v[144:147], v[8:11], v[216:219], v[144:147]\n"
        "v_cvt_f32_i32 v193, v193\n"
        "v_cvt_f32_i32 v194, v194\n"
        "v_mfma_i32_16x16x64_i8 v[148:151], v[12:15], v[216:219], v[148:151]\n"
        "ds_read_b128 v[208:211], v249 offset:8192\n"
        "v_cvt_f32_i32 v195, v195\n"
        "v_cvt_f32_i32 v200, v200\n"
        "v_mfma_i32_16x16x64_i8 v[152:155], v[8:11], v[220:223], v[152:155]\n"
        "ds_read_b32 v254, v250 offset:0\n"
        "v_cvt_f32_i32 v201, v201\n"
        "v_mfma_i32_16x16x64_i8 v[156:159], v[12:15], v[220:223], v[156:159]\n"
        "ds_read_b128 v[212:215], v249 offset:9216\n"
        "s_waitcnt lgkmcnt(7)\n"
        "v_mfma_i32_16x16x64_i8 v[144:147], v[16:19], v[224:227], v[144:147]\n"
        "v_cvt_f32_i32 v202, v202\n"
        "v_cvt_f32_i32 v203, v203\n"
        "v_mfma_i32_16x16x64_i8 v[148:151], v[20:23], v[224:227], v[148:151]\n"
        "ds_read_b128 v[216:219], v249 offset:10240\n"
        "v_cvt_f32_i32 v196, v196\n"
        "v_mfma_i32_16x16x64_i8 v[152:155], v[16:19], v[228:231], v[152:155]\n"
        "v_cvt_f32_i32 v197, v197\n"
        "v_cvt_f32_i32 v198, v198\n"
        "v_mfma_i32_16x16x64_i8 v[156:159], v[20:23], v[228:231], v[156:159]\n"
        "ds_read_b128 v[220:223], v249 offset:11264\n"
        "s_waitcnt lgkmcnt(5)\n"
        "v_mfma_i32_16x16x64_i8 v[144:147], v[24:27], v[232:235], v[144:147]\n"
        "v_cvt_f32_i32 v199, v199\n"
        "v_cvt_f32_i32 v204, v204\n"
        "v_cvt_f32_i32 v205, v205\n"
        "v_mfma_i32_16x16x64_i8 v[148:151], v[28:31], v[232:235], v[148:151]\n"
        "ds_read_b128 v[224:227], v249 offset:12288\n"
        "v_cvt_f32_i32 v206, v206\n"
        "v_mfma_i32_16x16x64_i8 v[152:155], v[24:27], v[236:239], v[152:155]\n"
        "v_cvt_f32_i32 v207, v207\n"
        "v_mfma_i32_16x16x64_i8 v[156:159], v[28:31], v[236:239], v[156:159]\n"
        "ds_read_b128 v[228:231], v249 offset:13312\n"
        "s_waitcnt lgkmcnt(4)\n"
        "v_mfma_i32_16x16x64_i8 v[144:147], v[32:35], v[208:211], v[144:147]\n"
        "v_fma_f32 v192, v192, v244, v245\n"
        "v_fma_f32 v193, v193, v244, v245\n"
        "v_mfma_i32_16x16x64_i8 v[148:151], v[36:39], v[208:211], v[148:151]\n"
        "ds_read_b128 v[232:235], v249 offset:14336\n"
        "v_fma_f32 v194, v194, v244, v245\n"
        "v_mfma_i32_16x16x64_i8 v[152:155], v[32:35], v[212:215], v[152:155]\n"
        "v_fma_f32 v195, v195, v244, v245\n"
        "v_fma_f32 v200, v200, v246, v247\n"
        "v_fma_f32 v201, v201, v246, v247\n"
        "v_mfma_i32_16x16x64_i8 v[156:159], v[36:39], v[212:215], v[156:159]\n"
        "ds_read_b128 v[236:239], v249 offset:15360\n"
        "v_cmp_gt_u32 vcc, 28, v254\n"
        "s_waitcnt lgkmcnt(4)\n"
        "v_mfma_i32_16x16x64_i8 v[144:147], v[40:43], v[216:219], v[144:147]\n"
        "s_cbranch_vccnz W8_s26\n"
        "W8_c26:\n"
        "v_fma_f32 v202, v202, v246, v247\n"
        "v_mfma_i32_16x16x64_i8 v[148:151], v[44:47], v[216:219], v[148:151]\n"
        "ds_read_b128 v[208:211], v249 offset:16384\n"
        "v_fma_f32 v203, v203, v246, v247\n"
        "v_mfma_i32_16x16x64_i8 v[152:155], v[40:43], v[220:223], v[152:155]\n"
        "v_cvt_pk_bf16_f32 v192, v192, v200\n"
        "v_cvt_pk_bf16_f32 v200, v193, v201\n"
        "v_mfma_i32_16x16x64_i8 v[156:159], v[44:47], v[220:223], v[156:159]\n"
        "ds_read_b128 v[212:215], v249 offset:17408\n"
        "s_waitcnt lgkmcnt(4)\n"
        "v_mfma_i32_16x16x64_i8 v[144:147], v[48:51], v[224:227], v[144:147]\n"
        "v_cvt_pk_bf16_f32 v194, v194, v202\n"
        "v_cvt_pk_bf16_f32 v202, v195, v203\n"
        "v_mfma_i32_16x16x64_i8 v[148:151], v[52:55], v[224:227], v[148:151]\n"
        "ds_read_b128 v[216:219], v249 offset:18432\n"
        "global_store_dword v252, v192, s[40:41] sc1\n"
        "global_store_dword v252, v200, s[42:43] sc1\n"
        "v_mfma_i32_16x16x64_i8 v[152:155], v[48:51], v[228:231], v[152:155]\n"
        "global_store_dword v252, v194, s[44:45] sc1\n"
        "global_store_dword v252, v202, s[46:47] sc1\n"
        "v_mfma_i32_16x16x64_i8 v[156:159], v[52:55], v[228:231], v[156:159]\n"
        "ds_read_b128 v[220:223], v249 offset:19456\n"
        "s_waitcnt lgkmcnt(4)\n"
        "v_mfma_i32_16x16x64_i8 v[144:147], v[56:59], v[232:235], v[144:147]\n"
        "v_fma_f32 v196, v196, v244, v245\n"
        "v_fma_f32 v197, v197, v244, v245\n"
        "v_mfma_i32_16x16x64_i8 v[148:151], v[60:63], v[232:235], v[148:151]\n"
        "ds_read_b128 v[224:227], v249 offset:20480\n"
        "v_fma_f32 v198, v198, v244, v245\n"
        "v_mfma_i32_16x16x64_i8 v[152:155], v[56:59], v[236:239], v[152:155]\n"
        "v_fma_f32 v199, v199, v244, v245\n"
        "v_fma_f32 v204, v204, v246, v247\n"
        "v_mfma_i32_16x16x64_i8 v[156:159], v[60:63], v[236:239], v[156:159]\n"
        "ds_read_b128 v[228:231], v249 offset:21504\n"
        "v_fma_f32 v205, v205, v246, v247\n"
        "s_waitcnt lgkmcnt(4)\n"
        "v_mfma_i32_16x16x64_i8 v[144:147], v[64:67], v[208:211], v[144:147]\n"
        "v_fma_f32 v206, v206, v246, v247\n"
        "v_fma_f32 v207, v207, v246, v247\n"
        "v_mfma_i32_16x16x64_i8 v[148:151], v[68:71], v[208:211], v[148:151]\n"
        "ds_read_b128 v[232:235], v249 offset:22528\n"
        "v_cvt_pk_bf16_f32 v196, v196, v204\n"
        "v_mfma_i32_16x16x64_i8 v[152:155], v[64:67], v[212:215], v[152:155]\n"
        "v_cvt_pk_bf16_f32 v204, v197, v205\n"
        "v_cvt_pk_bf16_f32 v198, v198, v206\n"
        "v_mfma_i32_16x16x64_i8 v[156:159], v[68:71], v[212:215], v[156:159]\n"
        "ds_read_b128 v[236:239], v249 offset:23552\n"
        "s_waitcnt lgkmcnt(4)\n"
        "v_mfma_i32_16x16x64_i8 v[144:147], v[72:75], v[216:219], v[144:147]\n"
        "v_cvt_pk_bf16_f32 v206, v199, v207\n"
        "global_store_dword v252, v196, s[48:49] sc1\n"
        "v_mfma_i32_16x16x64_i8 v[148:151], v[76:79], v[216:219], v[148:151]\n"
        "ds_read_b128 v[208:211], v249 offset:24576\n"
        "global_store_dword v252, v204, s[50:51] sc1\n"
        "v_mfma_i32_16x16x64_i8 v[152:155], v[72:75], v[220:223], v[152:155]\n"
        "global_store_dword v252, v198, s[52:53] sc1\n"
        "global_store_dword v252, v206, s[54:55] sc1\n"
        "ds_read_b128 v[244:247], v253 offset:1040\n"
        "v_mfma_i32_16x16x64_i8 v[156:159], v[76:79], v[220:223], v[156:159]\n"
        "ds_read_b128 v[212:215], v249 offset:25600\n"
        "s_waitcnt lgkmcnt(5)\n"
        "v_mfma_i32_16x16x64_i8 v[144:147], v[80:83], v[224:227], v[144:147]\n"
        "v_cvt_f32_i32 v128, v128\n"
        "v_cvt_f32_i32 v129, v129\n"
        "v_mfma_i32_16x16x64_i8 v[148:151], v[84:87], v[224:227], v[148:151]\n"
        "ds_read_b128 v[216:219], v249 offset:26624\n"
        "v_cvt_f32_i32 v130, v130\n"
        "v_mfma_i32_16x16x64_i8 v[152:155], v[80:83], v[228:231], v[152:155]\n"
        "v_cvt_f32_i32 v131, v131\n"
        "v_cvt_f32_i32 v136, v136\n"
        "v_mfma_i32_16x16x64_i8 v[156:159], v[84:87], v[228:231], v[156:159]\n"
        "ds_read_b128 v[220:223], v249 offset:27648\n"
        "s_waitcnt lgkmcnt(5)\n"
        "v_mfma_i32_16x16x64_i8 v[144:147], v[88:91], v[232:235], v[144:147]\n"
        "v_cvt_f32_i32 v137, v137\n"
        "v_cvt_f32_i32 v138, v138\n"
        "v_cvt_f32_i32 v139, v139\n"
        "v_mfma_i32_16x16x64_i8 v[148:151], v[92:95], v[232:235], v[148:151]\n"
        "ds_read_b128 v[224:227], v249 offset:28672\n"
        "v_fma_f32 v128, v128, v240, v241\n"
        "v_mfma_i32_16x16x64_i8 v[152:155], v[88:91], v[236:239], v[152:155]\n"
        "v_fma_f32 v129, v129, v240, v241\n"
        "v_fma_f32 v130, v130, v240, v241\n"
        "v_mfma_i32_16x16x64_i8 v[156:159], v[92:95], v[236:239], v[156:159]\n"
        "ds_read_b128 v[228:231], v249 offset:29696\n"
        "s_waitcnt lgkmcnt(4)\n"
        "v_mfma_i32_16x16x64_i8 v[144:147], v[96:99], v[208:211], v[144:147]\n"
        "v_fma_f32 v131, v131, v240, v241\n"
        "v_fma_f32 v136, v136, v242, v243\n"
        "v_mfma_i32_16x16x64_i8 v[148:151], v[100:103], v[208:211], v[148:151]\n"
        "ds_read_b128 v[232:235], v249 offset:30720\n"
        "v_fma_f32 v137, v137, v242, v243\n"
        "v_mfma_i32_16x16x64_i8 v[152:155], v[96:99], v[212:215], v[152:155]\n"
        "v_fma_f32 v138, v138, v242, v243\n"
        "v_fma_f32 v139, v139, v242, v243\n"
        "v_mfma_i32_16x16x64_i8 v[156:159], v[100:103], v[212:215], v[156:159]\n"
        "ds_read_b128 v[236:239], v249 offset:31744\n"
        "v_cvt_pk_bf16_f32 v128, v128, v136\n"
        "s_waitcnt lgkmcnt(4)\n"
        "v_mfma_i32_16x16x64_i8 v[144:147], v[104:107], v[216:219], v[144:147]\n"
        "v_cvt_pk_bf16_f32 v136, v129, v137\n"
        "v_cvt_pk_bf16_f32 v130, v130, v138\n"
        "v_mfma_i32_16x16x64_i8 v[148:151], v[108:111], v[216:219], v[148:151]\n"
        "v_cvt_pk_bf16_f32 v138, v131, v139\n"
        "v_cvt_f32_i32 v132, v132\n"
        "v_mfma_i32_16x16x64_i8 v[152:155], v[104:107], v[220:223], v[152:155]\n"
        "v_cvt_f32_i32 v133, v133\n"
        "v_cvt_f32_i32 v134, v134\n"
        "v_mfma_i32_16x16x64_i8 v[156:159], v[108:111], v[220:223], v[156:159]\n"
        "v_cvt_f32_i32 v135, v135\n"
        "s_waitcnt lgkmcnt(2)\n"
        "v_mfma_i32_16x16x64_i8 v[144:147], v[112:115], v[224:227], v[144:147]\n"
        "v_cvt_f32_i32 v140, v140\n"
        "v_cvt_f32_i32 v141, v141\n"
        "v_mfma_i32_16x16x64_i8 v[148:151], v[116:119], v[224:227], v[148:151]\n"
        "v_cvt_f32_i32 v142, v142\n"
        "v_cvt_f32_i32 v143, v143\n"
        "v_fma_f32 v132, v132, v240, v241\n"
        "v_mfma_i32_16x16x64_i8 v[152:155], v[112:115], v[228:231], v[152:155]\n"
        "v_fma_f32 v133, v133, v240, v241\n"
        "v_fma_f32 v134, v134, v240, v241\n"
        "v_mfma_i32_16x16x64_i8 v[156:159], v[116:119], v[228:231], v[156:159]\n"
        "v_fma_f32 v135, v135, v240, v241\n"
        "s_waitcnt lgkmcnt(0)\n"
        "v_mfma_i32_16x16x64_i8 v[144:147], v[120:123], v[232:235], v[144:147]\n"
        "v_fma_f32 v140, v140, v242, v243\n"
        "v_fma_f32 v141, v141, v242, v243\n"
        "v_mfma_i32_16x16x64_i8 v[148:151], v[124:127], v[232:235], v[148:151]\n"
        "v_fma_f32 v142, v142, v242, v243\n"
        "v_fma_f32 v143, v143, v242, v243\n"
        "v_mfma_i32_16x16x64_i8 v[152:155], v[120:123], v[236:239], v[152:155]\n"
        "v_cvt_pk_bf16_f32 v132, v132, v140\n"
        "v_cvt_pk_bf16_f32 v140, v133, v141\n"
        "v_mfma_i32_16x16x64_i8 v[156:159], v[124:127], v[236:239], v[156:159]\n"
        "v_cvt_pk_bf16_f32 v134, v134, v142\n"
        "v_cvt_pk_bf16_f32 v142, v135, v143\n"
        "s_nop 5\n"
        "v_cvt_f32_i32 v144, v144\n"
        "v_cvt_f32_i32 v145, v145\n"
        "v_cvt_f32_i32 v146, v146\n"
        "v_cvt_f32_i32 v147, v147\n"
        "v_cvt_f32_i32 v152, v152\n"
        "v_cvt_f32_i32 v153, v153\n"
        "v_cvt_f32_i32 v154, v154\n"
        "v_cvt_f32_i32 v155, v155\n"
        "v_fma_f32 v144, v144, v244, v245\n"
        "v_fma_f32 v145, v145, v244, v245\n"
        "v_fma_f32 v146, v146, v244, v245\n"
        "v_fma_f32 v147, v147, v244, v245\n"
        "v_fma_f32 v152, v152, v246, v247\n"
        "v_fma_f32 v153, v153, v246, v247\n"
        "v_fma_f32 v154, v154, v246, v247\n"
        "v_fma_f32 v155, v155, v246, v247\n"
        "v_cvt_pk_bf16_f32 v129, v144, v152\n"
        "global_store_dwordx2 v251, v[128:129], s[40:41] offset:256 sc1\n"
        "v_cvt_pk_bf16_f32 v137, v145, v153\n"
        "global_store_dwordx2 v251, v[136:137], s[42:43] offset:256 sc1\n"
        "v_cvt_pk_bf16_f32 v131, v146, v154\n"
        "global_store_dwordx2 v251, v[130:131], s[44:45] offset:256 sc1\n"
        "v_cvt_pk_bf16_f32 v139, v147, v155\n"
        "global_store_dwordx2 v251, v[138:139], s[46:47] offset:256 sc1\n"
        "v_cvt_f32_i32 v148, v148\n"
        "v_cvt_f32_i32 v149, v149\n"
        "v_cvt_f32_i32 v150, v150\n"
        "v_cvt_f32_i32 v151, v151\n"
        "v_cvt_f32_i32 v156, v156\n"
        "v_cvt_f32_i32 v157, v157\n"
        "v_cvt_f32_i32 v158, v158\n"
        "v_cvt_f32_i32 v159, v159\n"
        "v_fma_f32 v148, v148, v244, v245\n"
        "v_fma_f32 v149, v149, v244, v245\n"
        "v_fma_f32 v150, v150, v244, v245\n"
        "v_fma_f32 v151, v151, v244, v245\n"
        "v_fma_f32 v156, v156, v246, v247\n"
        "v_fma_f32 v157, v157, v246, v247\n"
        "v_fma_f32 v158, v158, v246, v247\n"
        "v_fma_f32 v159, v159, v246, v247\n"
        "v_cvt_pk_bf16_f32 v133, v148, v156\n"
        "global_store_dwordx2 v251, v[132:133], s[48:49] offset:256 sc1\n"
        "v_cvt_pk_bf16_f32 v141, v149, v157\n"
        "global_store_dwordx2 v251, v[140:141], s[50:51] offset:256 sc1\n"
        "v_cvt_pk_bf16_f32 v135, v150, v158\n"
        "global_store_dwordx2 v251, v[134:135], s[52:53] offset:256 sc1\n"
        "v_cvt_pk_bf16_f32 v143, v151, v159\n"
        "global_store_dwordx2 v251, v[142:143], s[54:55] offset:256 sc1\n"
        "W8_fin:\n"
        "s_endpgm\n"
        "W8_s0:\n"
        "s_mov_b32 s38, 8388608\n"
        "W8_r0:\n"
        "ds_read_b32 v254, v250 offset:0\n"
        "s_sub_u32 s38, s38, 1\n"
        "s_waitcnt lgkmcnt(0)\n"
        "s_cbranch_scc1 W8_c0\n"
        "v_readfirstlane_b32 s39, v254\n"
        "s_cmp_lt_u32 s39, 1\n"
        "s_cbranch_scc1 W8_r0\n"
        "s_branch W8_c0\n"
        "W8_sZ:\n"
        "s_mov_b32 s38, 8388608\n"
        "W8_rZ:\n"
        "ds_read_b32 v241, v250 offset:4\n"
        "s_sub_u32 s38, s38, 1\n"
        "s_waitcnt lgkmcnt(0)\n"
        "s_cbranch_scc1 W8_cZ\n"
        "v_readfirstlane_b32 s39, v241\n"
        "s_cmp_lt_u32 s39, 4\n"
        "s_cbranch_scc1 W8_rZ\n"
        "s_branch W8_cZ\n"
        "W8_s1:\n"
        "s_mov_b32 s38, 8388608\n"
        "W8_r1:\n"
        "ds_read_b32 v254, v250 offset:0\n"
        "s_sub_u32 s38, s38, 1\n"
        "s_waitcnt lgkmcnt(0)\n"
        "s_cbranch_scc1 W8_c1\n"
        "v_readfirstlane_b32 s39, v254\n"
        "s_cmp_lt_u32 s39, 2\n"
        "s_cbranch_scc1 W8_r1\n"
        "s_branch W8_c1\n"
        "W8_sR0:\n"
        "s_mov_b32 s38, 8388608\n"
        "W8_rR0:\n"
        "ds_read_b32 v241, v250 offset:4\n"
        "s_sub_u32 s38, s38, 1\n"
        "s_waitcnt lgkmcnt(0)\n"
        "s_cbranch_scc1 W8_cR0\n"
        "v_readfirstlane_b32 s39, v241\n"
        "s_cmp_lt_u32 s39, 5\n"
        "s_cbranch_scc1 W8_rR0\n"
        "s_branch W8_cR0\n"
        "W8_s2:\n"
        "s_mov_b32 s38, 8388608\n"
        "W8_r2:\n"
        "ds_read_b32 v254, v250 offset:0\n"
        "s_sub_u32 s38, s38, 1\n"
        "s_waitcnt lgkmcnt(0)\n"
        "s_cbranch_scc1 W8_c2\n"
        "v_readfirstlane_b32 s39, v254\n"
        "s_cmp_lt_u32 s39, 3\n"
        "s_cbranch_scc1 W8_r2\n"
        "s_branch W8_c2\n"
        "W8_sR1:\n"
        "s_mov_b32 s38, 8388608\n"
        "W8_rR1:\n"
        "ds_read_b32 v241, v250 offset:4\n"
        "s_sub_u32 s38, s38, 1\n"
        "s_waitcnt lgkmcnt(0)\n"
        "s_cbranch_scc1 W8_cR1\n"
        "v_readfirstlane_b32 s39, v241\n"
        "s_cmp_lt_u32 s39, 6\n"
        "s_cbranch_scc1 W8_rR1\n"
        "s_branch W8_cR1\n"
        "W8_s3:\n"
        "s_mov_b32 s38, 8388608\n"
        "W8_r3:\n"
        "ds_read_b32 v254, v250 offset:0\n"
        "s_sub_u32 s38, s38, 1\n"
        "s_waitcnt lgkmcnt(0)\n"
        "s_cbranch_scc1 W8_c3\n"
        "v_readfirstlane_b32 s39, v254\n"
        "s_cmp_lt_u32 s39, 4\n"
        "s_cbranch_scc1 W8_r3\n"
        "s_branch W8_c3\n"
        "W8_sR2:\n"
        "s_mov_b32 s38, 8388608\n"
        "W8_rR2:\n"
        "ds_read_b32 v241, v250 offset:4\n"
        "s_sub_u32 s38, s38, 1\n"
        "s_waitcnt lgkmcnt(0)\n"
        "s_cbranch_scc1 W8_cR2\n"
        "v_readfirstlane_b32 s39, v241\n"
        "s_cmp_lt_u32 s39, 7\n"
        "s_cbranch_scc1 W8_rR2\n"
        "s_branch W8_cR2\n"
        "W8_s4:\n"
        "s_mov_b32 s38, 8388608\n"
        "W8_r4:\n"
        "ds_read_b32 v254, v250 offset:0\n"
        "s_sub_u32 s38, s38, 1\n"
        "s_waitcnt lgkmcnt(0)\n"
        "s_cbranch_scc1 W8_c4\n"
        "v_readfirstlane_b32 s39, v254\n"
        "s_cmp_lt_u32 s39, 5\n"
        "s_cbranch_scc1 W8_r4\n"
        "s_branch W8_c4\n"
        "W8_sR3:\n"
        "s_mov_b32 s38, 8388608\n"
        "W8_rR3:\n"
        "ds_read_b32 v241, v250 offset:4\n"
        "s_sub_u32 s38, s38, 1\n"
        "s_waitcnt lgkmcnt(0)\n"
        "s_cbranch_scc1 W8_cR3\n"
        "v_readfirstlane_b32 s39, v241\n"
        "s_cmp_lt_u32 s39, 8\n"
        "s_cbranch_scc1 W8_rR3\n"
        "s_branch W8_cR3\n"
        "W8_s5:\n"
        "s_mov_b32 s38, 8388608\n"
        "W8_r5:\n"
        "ds_read_b32 v254, v250 offset:0\n"
        "s_sub_u32 s38, s38, 1\n"
        "s_waitcnt lgkmcnt(0)\n"
        "s_cbranch_scc1 W8_c5\n"
        "v_readfirstlane_b32 s39, v254\n"
        "s_cmp_lt_u32 s39, 6\n"
        "s_cbranch_scc1 W8_r5\n"
        "s_branch W8_c5\n"
        "W8_sR4:\n"
        "s_mov_b32 s38, 8388608\n"
        "W8_rR4:\n"
        "ds_read_b32 v241, v250 offset:4\n"
        "s_sub_u32 s38, s38, 1\n"
        "s_waitcnt lgkmcnt(0)\n"
        "s_cbranch_scc1 W8_cR4\n"
        "v_readfirstlane_b32 s39, v241\n"
        "s_cmp_lt_u32 s39, 9\n"
        "s_cbranch_scc1 W8_rR4\n"
        "s_branch W8_cR4\n"
        "W8_s6:\n"
        "s_mov_b32 s38, 8388608\n"
        "W8_r6:\n"
        "ds_read_b32 v254, v250 offset:0\n"
        "s_sub_u32 s38, s38, 1\n"
        "s_waitcnt lgkmcnt(0)\n"
        "s_cbranch_scc1 W8_c6\n"
        "v_readfirstlane_b32 s39, v254\n"
        "s_cmp_lt_u32 s39, 7\n"
        "s_cbranch_scc1 W8_r6\n"
        "s_branch W8_c6\n"
        "W8_sR5:\n"
        "s_mov_b32 s38, 8388608\n"
        "W8_rR5:\n"
        "ds_read_b32 v241, v250 offset:4\n"
        "s_sub_u32 s38, s38, 1\n"
        "s_waitcnt lgkmcnt(0)\n"
        "s_cbranch_scc1 W8_cR5\n"
        "v_readfirstlane_b32 s39, v241\n"
        "s_cmp_lt_u32 s39, 10\n"
        "s_cbranch_scc1 W8_rR5\n"
        "s_branch W8_cR5\n"
        "W8_s7:\n"
        "s_mov_b32 s38, 8388608\n"
        "W8_r7:\n"
        "ds_read_b32 v254, v250 offset:0\n"
        "s_sub_u32 s38, s38, 1\n"
        "s_waitcnt lgkmcnt(0)\n"
        "s_cbranch_scc1 W8_c7\n"
        "v_readfirstlane_b32 s39, v254\n"
        "s_cmp_lt_u32 s39, 8\n"
        "s_cbranch_scc1 W8_r7\n"
        "s_branch W8_c7\n"
        "W8_sR6:\n"
        "s_mov_b32 s38, 8388608\n"
        "W8_rR6:\n"
        "ds_read_b32 v241, v250 offset:4\n"
        "s_sub_u32 s38, s38, 1\n"
        "s_waitcnt lgkmcnt(0)\n"
        "s_cbranch_scc1 W8_cR6\n"
        "v_readfirstlane_b32 s39, v241\n"
        "s_cmp_lt_u32 s39, 11\n"
        "s_cbranch_scc1 W8_rR6\n"
        "s_branch W8_cR6\n"
        "W8_s8:\n"
        "s_mov_b32 s38, 8388608\n"
        "W8_r8:\n"
        "ds_read_b32 v254, v250 offset:0\n"
        "s_sub_u32 s38, s38, 1\n"
        "s_waitcnt lgkmcnt(0)\n"
        "s_cbranch_scc1 W8_c8\n"
        "v_readfirstlane_b32 s39, v254\n"
        "s_cmp_lt_u32 s39, 9\n"
        "s_cbranch_scc1 W8_r8\n"
        "s_branch W8_c8\n"
        "W8_sR7:\n"
        "s_mov_b32 s38, 8388608\n"
        "W8_rR7:\n"
        "ds_read_b32 v241, v250 offset:4\n"
        "s_sub_u32 s38, s38, 1\n"
        "s_waitcnt lgkmcnt(0)\n"
        "s_cbranch_scc1 W8_cR7\n"
        "v_readfirstlane_b32 s39, v241\n"
        "s_cmp_lt_u32 s39, 12\n"
        "s_cbranch_scc1 W8_rR7\n"
        "s_branch W8_cR7\n"
        "W8_s9:\n"
        "s_mov_b32 s38, 8388608\n"
        "W8_r9:\n"
        "ds_read_b32 v254, v250 offset:0\n"
        "s_sub_u32 s38, s38, 1\n"
        "s_waitcnt lgkmcnt(0)\n"
        "s_cbranch_scc1 W8_c9\n"
        "v_readfirstlane_b32 s39, v254\n"
        "s_cmp_lt_u32 s39, 10\n"
        "s_cbranch_scc1 W8_r9\n"
        "s_branch W8_c9\n"
        "W8_sR8:\n"
        "s_mov_b32 s38, 8388608\n"
        "W8_rR8:\n"
        "ds_read_b32 v241, v250 offset:4\n"
        "s_sub_u32 s38, s38, 1\n"
        "s_waitcnt lgkmcnt(0)\n"
        "s_cbranch_scc1 W8_cR8\n"
        "v_readfirstlane_b32 s39, v241\n"
        "s_cmp_lt_u32 s39, 13\n"
        "s_cbranch_scc1 W8_rR8\n"
        "s_branch W8_cR8\n"
        "W8_s10:\n"
        "s_mov_b32 s38, 8388608\n"
        "W8_r10:\n"
        "ds_read_b32 v254, v250 offset:0\n"
        "s_sub_u32 s38, s38, 1\n"
        "s_waitcnt lgkmcnt(0)\n"
        "s_cbranch_scc1 W8_c10\n"
        "v_readfirstlane_b32 s39, v254\n"
        "s_cmp_lt_u32 s39, 11\n"
        "s_cbranch_scc1 W8_r10\n"
        "s_branch W8_c10\n"
        "W8_sR9:\n"
        "s_mov_b32 s38, 8388608\n"
        "W8_rR9:\n"
        "ds_read_b32 v241, v250 offset:4\n"
        "s_sub_u32 s38, s38, 1\n"
        "s_waitcnt lgkmcnt(0)\n"
        "s_cbranch_scc1 W8_cR9\n"
        "v_readfirstlane_b32 s39, v241\n"
        "s_cmp_lt_u32 s39, 14\n"
        "s_cbranch_scc1 W8_rR9\n"
        "s_branch W8_cR9\n"
        "W8_s11:\n"
        "s_mov_b32 s38, 8388608\n"
        "W8_r11:\n"
        "ds_read_b32 v254, v250 offset:0\n"
        "s_sub_u32 s38, s38, 1\n"
        "s_waitcnt lgkmcnt(0)\n"
        "s_cbranch_scc1 W8_c11\n"
        "v_readfirstlane_b32 s39, v254\n"
        "s_cmp_lt_u32 s39, 12\n"
        "s_cbranch_scc1 W8_r11\n"
        "s_branch W8_c11\n"
        "W8_sR10:\n"
        "s_mov_b32 s38, 8388608\n"
        "W8_rR10:\n"
        "ds_read_b32 v241, v250 offset:4\n"
        "s_sub_u32 s38, s38, 1\n"
        "s_waitcnt lgkmcnt(0)\n"
        "s_cbranch_scc1 W8_cR10\n"
        "v_readfirstlane_b32 s39, v241\n"
        "s_cmp_lt_u32 s39, 15\n"
        "s_cbranch_scc1 W8_rR10\n"
        "s_branch W8_cR10\n"
        "W8_s12:\n"
        "s_mov_b32 s38, 8388608\n"
        "W8_r12:\n"
        "ds_read_b32 v254, v250 offset:0\n"
        "s_sub_u32 s38, s38, 1\n"
        "s_waitcnt lgkmcnt(0)\n"
        "s_cbranch_scc1 W8_c12\n"
        "v_readfirstlane_b32 s39, v254\n"
        "s_cmp_lt_u32 s39, 13\n"
        "s_cbranch_scc1 W8_r12\n"
        "s_branch W8_c12\n"
        "W8_sR11:\n"
        "s_mov_b32 s38, 8388608\n"
        "W8_rR11:\n"
        "ds_read_b32 v241, v250 offset:4\n"
        "s_sub_u32 s38, s38, 1\n"
        "s_waitcnt lgkmcnt(0)\n"
        "s_cbranch_scc1 W8_cR11\n"
        "v_readfirstlane_b32 s39, v241\n"
        "s_cmp_lt_u32 s39, 16\n"
        "s_cbranch_scc1 W8_rR11\n"
        "s_branch W8_cR11\n"
        "W8_s13:\n"
        "s_mov_b32 s38, 8388608\n"
        "W8_r13:\n"
        "ds_read_b32 v254, v250 offset:0\n"
        "s_sub_u32 s38, s38, 1\n"
        "s_waitcnt lgkmcnt(0)\n"
        "s_cbranch_scc1 W8_c13\n"
        "v_readfirstlane_b32 s39, v254\n"
        "s_cmp_lt_u32 s39, 14\n"
        "s_cbranch_scc1 W8_r13\n"
        "s_branch W8_c13\n"
        "W8_s14:\n"
        "s_mov_b32 s38, 8388608\n"
        "W8_r14:\n"
        "ds_read_b32 v254, v250 offset:0\n"
        "s_sub_u32 s38, s38, 1\n"
        "s_waitcnt lgkmcnt(0)\n"
        "s_cbranch_scc1 W8_c14\n"
        "v_readfirstlane_b32 s39, v254\n"
        "s_cmp_lt_u32 s39, 15\n"
        "s_cbranch_scc1 W8_r14\n"
        "s_branch W8_c14\n"
        "W8_s15:\n"
        "s_mov_b32 s38, 8388608\n"
        "W8_r15:\n"
        "ds_read_b32 v254, v250 offset:0\n"
        "s_sub_u32 s38, s38, 1\n"
        "s_waitcnt lgkmcnt(0)\n"
        "s_cbranch_scc1 W8_c15\n"
        "v_readfirstlane_b32 s39, v254\n"
        "s_cmp_lt_u32 s39, 16\n"
        "s_cbranch_scc1 W8_r15\n"
        "s_branch W8_c15\n"
        "W8_s16:\n"
        "s_mov_b32 s38, 8388608\n"
        "W8_r16:\n"
        "ds_read_b32 v254, v250 offset:0\n"
        "s_sub_u32 s38, s38, 1\n"
        "s_waitcnt lgkmcnt(0)\n"
        "s_cbranch_scc1 W8_c16\n"
        "v_readfirstlane_b32 s39, v254\n"
        "s_cmp_lt_u32 s39, 18\n"
        "s_cbranch_scc1 W8_r16\n"
        "s_branch W8_c16\n"
        "W8_sT:\n"
        "s_mov_b32 s38, 8388608\n"
        "W8_rT:\n"
        "ds_read_b32 v255, v250 offset:260\n"
        "s_sub_u32 s38, s38, 1\n"
        "s_waitcnt lgkmcnt(0)\n"
        "s_cbranch_scc1 W8_cT\n"
        "v_readfirstlane_b32 s39, v255\n"
        "s_cmp_lt_u32 s39, 2\n"
        "s_cbranch_scc1 W8_rT\n"
        "s_branch W8_cT\n"
        "W8_s18:\n"
        "s_mov_b32 s38, 8388608\n"
        "W8_r18:\n"
        "ds_read_b32 v254, v250 offset:0\n"
        "s_sub_u32 s38, s38, 1\n"
        "s_waitcnt lgkmcnt(0)\n"
        "s_cbranch_scc1 W8_c18\n"
        "v_readfirstlane_b32 s39, v254\n"
        "s_cmp_lt_u32 s39, 20\n"
        "s_cbranch_scc1 W8_r18\n"
        "s_branch W8_c18\n"
        "W8_s20:\n"
        "s_mov_b32 s38, 8388608\n"
        "W8_r20:\n"
        "ds_read_b32 v254, v250 offset:0\n"
        "s_sub_u32 s38, s38, 1\n"
        "s_waitcnt lgkmcnt(0)\n"
        "s_cbranch_scc1 W8_c20\n"
        "v_readfirstlane_b32 s39, v254\n"
        "s_cmp_lt_u32 s39, 22\n"
        "s_cbranch_scc1 W8_r20\n"
        "s_branch W8_c20\n"
        "W8_s22:\n"
        "s_mov_b32 s38, 8388608\n"
        "W8_r22:\n"
        "ds_read_b32 v254, v250 offset:0\n"
        "s_sub_u32 s38, s38, 1\n"
        "s_waitcnt lgkmcnt(0)\n"
        "s_cbranch_scc1 W8_c22\n"
        "v_readfirstlane_b32 s39, v254\n"
        "s_cmp_lt_u32 s39, 24\n"
        "s_cbranch_scc1 W8_r22\n"
        "s_branch W8_c22\n"
        "W8_s24:\n"
        "s_mov_b32 s38, 8388608\n"
        "W8_r24:\n"
        "ds_read_b32 v254, v250 offset:0\n"
        "s_sub_u32 s38, s38, 1\n"
        "s_waitcnt lgkmcnt(0)\n"
        "s_cbranch_scc1 W8_c24\n"
        "v_readfirstlane_b32 s39, v254\n"
        "s_cmp_lt_u32 s39, 26\n"
        "s_cbranch_scc1 W8_r24\n"
        "s_branch W8_c24\n"
        "W8_s26:\n"
        "s_mov_b32 s38, 8388608\n"
        "W8_r26:\n"
        "ds_read_b32 v254, v250 offset:0\n"
        "s_sub_u32 s38, s38, 1\n"
        "s_waitcnt lgkmcnt(0)\n"
        "s_cbranch_scc1 W8_c26\n"
        "v_readfirstlane_b32 s39, v254\n"
        "s_cmp_lt_u32 s39, 28\n"
        "s_cbranch_scc1 W8_r26\n"
        "s_branch W8_c26\n"
        "W8_end:\n"
        : "+v"(tid)
        : "s"(__builtin_amdgcn_kernarg_segment_ptr()), "s"(blockIdx.x)
        : "v1", "v2", "v3", "v4", "v5", "v6", "v7", "v8", "v9", "v10", "v11", "v12", "v13", "v14", "v15", "v16", "v17", "v18", "v19", "v20", "v21", "v22", "v23", "v24", "v25", "v26", "v27", "v28", "v29", "v30", "v31", "v32", "v33", "v34", "v35", "v36", "v37", "v38", "v39", "v40", "v41", "v42", "v43", "v44", "v45", "v46", "v47", "v48", "v49", "v50", "v51", "v52", "v53", "v54", "v55", "v56", "v57", "v58", "v59", "v60", "v61", "v62", "v63", "v64", "v65", "v66", "v67", "v68", "v69", "v70", "v71", "v72", "v73", "v74", "v75", "v76", "v77", "v78", "v79", "v80", "v81", "v82", "v83", "v84", "v85", "v86", "v87", "v88", "v89", "v90", "v91", "v92", "v93", "v94", "v95", "v96", "v97", "v98", "v99", "v100", "v101", "v102", "v103", "v104", "v105", "v106", "v107", "v108", "v109", "v110", "v111", "v112", "v113", "v114", "v115", "v116", "v117", "v118", "v119", "v120", "v121", "v122", "v123", "v124", "v125", "v126", "v127", "v128", "v129", "v130", "v131", "v132", "v133", "v134", "v135", "v136", "v137", "v138", "v139", "v140", "v141", "v142", "v143", "v144", "v145", "v146", "v147", "v148", "v149", "v150", "v151", "v152", "v153", "v154", "v155", "v156", "v157", "v158", "v159", "v160", "v161", "v162", "v163", "v164", "v165", "v166", "v167", "v168", "v169", "v170", "v171", "v172", "v173", "v174", "v175", "v176", "v177", "v178", "v179", "v180", "v181", "v182", "v183", "v184", "v185", "v186", "v187", "v188", "v189", "v190", "v191", "v192", "v193", "v194", "v195", "v196", "v197", "v198", "v199", "v200", "v201", "v202", "v203", "v204", "v205", "v206", "v207", "v208", "v209", "v210", "v211", "v212", "v213", "v214", "v215", "v216", "v217", "v218", "v219", "v220", "v221", "v222", "v223", "v224", "v225", "v226", "v227", "v228", "v229", "v230", "v231", "v232", "v233", "v234", "v235", "v236", "v237", "v238", "v239", "v240", "v241", "v242", "v243", "v244", "v245", "v246", "v247", "v248", "v249", "v250", "v251", "v252", "v253", "v254", "v255", "s3", "s4", "s5", "s6", "s7", "s8", "s9", "s10", "s11", "s12", "s13", "s14", "s15", "s16", "s17", "s18", "s19", "s20", "s21", "s22", "s23", "s24", "s25", "s26", "s27", "s28", "s29", "s30", "s31", "s33", "s34", "s35", "s36", "s37", "s38", "s39", "s40", "s41", "s42", "s43", "s44", "s45", "s46", "s47", "s48", "s49", "s50", "s51", "s52", "s53", "s54", "s55", "s56", "s57", "s58", "s59", "s60", "s61", "s62", "s63", "s64", "s65", "s66", "s67", "s68", "s69", "s70", "s71", "s72", "s73", "s74", "s75", "s76", "s77", "s78", "s79", "s80", "s81", "s82", "s83", "s84", "s85", "s86", "s87", "s88", "s89", "s90", "s91", "s92", "s93", "s94", "s95", "s96", "s97", "s98", "s99", "vcc", "scc", "memory");
}

extern "C" void kernel_launch(void* const* d_in, const int* in_sizes, int n_in,
                              void* d_out, int out_size, void* d_ws, size_t ws_size,
                              hipStream_t stream) {
    (void)in_sizes; (void)n_in; (void)out_size; (void)ws_size;
    if (ws_size >= (size_t)(128u<<20) + (8u << 20)) d_ws = (char*)d_ws + (128u<<20);
    constexpr int lds_bytes = 137792;
    hipFuncSetAttribute((const void*)w8a8_fused, hipFuncAttributeMaxDynamicSharedMemorySize, lds_bytes);
    w8a8_fused<<<256, 512, lds_bytes, stream>>>(d_in[0], d_in[1], d_ws, d_out, d_in[4], d_in[5], d_in[2], d_in[3]);
}
```

```cpp
#include <hip/hip_runtime.h>
#include <stdint.h>

__global__ __launch_bounds__(512, 2) void w8a8_fused(const void* qx, const void* qw, void* ws, void* out,
                                                     const void* sw, const void* bias, const void* sx, const void* zx) {
    int tid = threadIdx.x;
    asm volatile(
        "W8_start:\n"
        "s_load_dwordx16 s[4:19], %1, 0x0\n"
        "s_mov_b32 s3, %2\n"
        "v_readfirstlane_b32 s20, v0\n"
        "s_and_b32 s24, s3, 1\n"
        "s_lshl_b32 s24, s24, 3\n"
        "s_bfe_u32 s21, s3, 0x30003\n"
        "s_or_b32 s21, s21, s24\n"
        "s_bfe_u32 s24, s3, 0x20001\n"
        "s_lshl_b32 s24, s24, 2\n"
        "s_lshr_b32 s22, s3, 6\n"
        "s_or_b32 s22, s22, s24\n"
        "v_lshlrev_b32 v2, 2, v0\n"
        "v_add_u32 v2, 133124, v2\n"
        "v_mov_b32 v3, 0\n"
        "ds_write_b32 v2, v3\n"
        "s_and_b32 s23, s22, 1\n"
        "s_lshr_b32 s20, s20, 6\n"
        "s_cbranch_scc0 W8_prod\n"
        "s_bfe_u32 s24, s3, 0x20003\n"
        "s_add_u32 s24, s24, 4\n"
        "s_cmp_lg_u32 s20, s24\n"
        "s_cbranch_scc1 W8_np\n"
        "s_getpc_b64 s[24:25]\n"
        "W8_pc:\n"
        "v_and_b32 v2, 255, v0\n"
        "v_lshlrev_b32 v2, 7, v2\n"
        "v_min_u32 v2, W8_end-W8_pc-4, v2\n"
        "global_load_dword v255, v2, s[24:25]\n"
        "W8_np:\n"
        "s_waitcnt lgkmcnt(0)\n"
        "s_barrier\n"
        "s_branch W8_other\n"
        "W8_prod:\n"
        "s_and_b32 s24, s22, 3\n"
        "s_lshl2_add_u32 s24, s21, s24\n"
        "s_lshl_b32 s24, s24, 17\n"
        "s_lshr_b32 s25, s22, 2\n"
        "s_lshl_b32 s25, s25, 8\n"
        "s_add_u32 s24, s24, s25\n"
        "v_lshrrev_b32 v11, 4, v0\n"
        "v_and_b32 v13, 15, v0\n"
        "v_lshl_add_u32 v4, v11, 12, s24\n"
        "v_lshl_add_u32 v4, v13, 4, v4\n"
        "s_lshl_b32 s25, s22, 9\n"
        "s_mul_i32 s26, s21, 229376\n"
        "s_add_u32 s27, s26, s25\n"
        "v_lshrrev_b32 v8, 5, v0\n"
        "v_mul_u32_u24 v9, 114688, v8\n"
        "v_and_b32 v10, 31, v0\n"
        "v_lshl_add_u32 v5, v10, 4, v9\n"
        "v_add_u32 v5, s27, v5\n"
        "s_movk_i32 s28, 16384\n"
        "s_movk_i32 s29, 14336\n"
        "v_bfe_u32 v60, v0, 4, 1\n"
        "s_add_u32 s27, s22, 16\n"
        "s_sub_u32 s27, s27, s21\n"
        "v_mul_lo_u32 v60, v60, s27\n"
        "v_add_lshl_u32 v60, v60, s21, 7\n"
        "v_add_u32 v60, 6815744, v60\n"
        "s_waitcnt lgkmcnt(0)\n"
        "global_load_dwordx4 v[64:67], v4, s[4:5] nt\n"
        "v_add_u32 v4, s28, v4\n"
        "global_load_dwordx4 v[96:99], v5, s[6:7] nt\n"
        "v_add_u32 v5, s29, v5\n"
        "global_load_dwordx2 v[62:63], v60, s[8:9] sc1\n"
        "global_load_dwordx4 v[68:71], v4, s[4:5] nt\n"
        "v_add_u32 v4, s28, v4\n"
        "global_load_dwordx4 v[72:75], v4, s[4:5] nt\n"
        "v_add_u32 v4, s28, v4\n"
        "global_load_dwordx4 v[76:79], v4, s[4:5] nt\n"
        "v_add_u32 v4, s28, v4\n"
        "global_load_dwordx4 v[80:83], v4, s[4:5] nt\n"
        "v_add_u32 v4, s28, v4\n"
        "global_load_dwordx4 v[84:87], v4, s[4:5] nt\n"
        "v_add_u32 v4, s28, v4\n"
        "global_load_dwordx4 v[88:91], v4, s[4:5] nt\n"
        "v_add_u32 v4, s28, v4\n"
        "global_load_dwordx4 v[92:95], v4, s[4:5] nt\n"
        "v_add_u32 v4, 4294853632, v4\n"
        "global_load_dwordx4 v[100:103], v5, s[6:7] nt\n"
        "v_add_u32 v5, s29, v5\n"
        "global_load_dwordx4 v[104:107], v5, s[6:7] nt\n"
        "v_add_u32 v5, s29, v5\n"
        "global_load_dwordx4 v[108:111], v5, s[6:7] nt\n"
        "v_add_u32 v5, s29, v5\n"
        "global_load_dwordx4 v[112:115], v5, s[6:7] nt\n"
        "v_add_u32 v5, s29, v5\n"
        "global_load_dwordx4 v[116:119], v5, s[6:7] nt\n"
        "v_add_u32 v5, s29, v5\n"
        "global_load_dwordx4 v[120:123], v5, s[6:7] nt\n"
        "v_add_u32 v5, s29, v5\n"
        "global_load_dwordx4 v[124:127], v5, s[6:7] nt\n"
        "v_add_u32 v5, 3569664, v5\n"
        "global_load_dwordx4 v[128:131], v4, s[4:5] nt\n"
        "v_add_u32 v4, s28, v4\n"
        "global_load_dwordx4 v[132:135], v4, s[4:5] nt\n"
        "v_add_u32 v4, s28, v4\n"
        "global_load_dwordx4 v[136:139], v4, s[4:5] nt\n"
        "v_add_u32 v4, s28, v4\n"
        "global_load_dwordx4 v[140:143], v4, s[4:5] nt\n"
        "v_add_u32 v4, s28, v4\n"
        "global_load_dwordx4 v[144:147], v4, s[4:5] nt\n"
        "v_add_u32 v4, s28, v4\n"
        "global_load_dwordx4 v[148:151], v4, s[4:5] nt\n"
        "v_add_u32 v4, s28, v4\n"
        "s_barrier\n"
        "s_lshl_b32 s26, s26, 2\n"
        "s_lshl_b32 s27, s22, 8\n"
        "s_add_u32 s27, s27, 8192\n"
        "s_add_u32 s27, s27, s26\n"
        "v_lshrrev_b32 v11, 4, v0\n"
        "v_mul_u32_u24 v12, 229376, v11\n"
        "v_and_b32 v13, 15, v0\n"
        "v_lshl_add_u32 v7, v13, 4, v12\n"
        "v_add_u32 v7, s27, v7\n"
        "s_lshl_b32 s27, s22, 7\n"
        "s_add_u32 s27, s27, 12288\n"
        "s_add_u32 s27, s27, s26\n"
        "v_bfe_u32 v14, v0, 3, 2\n"
        "v_mul_u32_u24 v12, 229376, v14\n"
        "v_and_b32 v15, 7, v0\n"
        "v_lshl_add_u32 v6, v15, 4, v12\n"
        "v_add3_u32 v6, v6, v9, s27\n"
        "v_lshlrev_b32 v16, 2, v0\n"
        "v_add_u32 v25, 133124, v16\n"
        "v_add_u32 v16, 135232, v16\n"
        "v_lshlrev_b32 v19, 4, v0\n"
        "v_add_u32 v17, 135232, v19\n"
        "v_lshlrev_b32 v21, 3, v0\n"
        "v_add_u32 v21, 135232, v21\n"
        "s_lshl_b32 s26, s22, 4\n"
        "s_add_u32 s26, s26, s21\n"
        "s_mul_i32 s24, s26, 6144\n"
        "s_add_u32 s24, s24, 4194304\n"
        "s_add_u32 s44, s8, s24\n"
        "s_addc_u32 s45, s9, 0\n"
        "s_lshl_b32 s24, s26, 10\n"
        "s_add_u32 s24, s24, 5767168\n"
        "s_add_u32 s46, s8, s24\n"
        "s_addc_u32 s47, s9, 0\n"
        "v_lshlrev_b32 v22, 4, v13\n"
        "v_lshl_add_u32 v22, v11, 2, v22\n"
        "v_lshlrev_b32 v23, 4, v15\n"
        "v_lshl_add_u32 v23, v11, 2, v23\n"
        "s_mul_i32 s24, s21, 512\n"
        "s_mul_i32 s25, s22, 8\n"
        "s_add_u32 s24, s24, s25\n"
        "s_add_u32 s24, s24, 6029312\n"
        "s_mul_i32 s25, s22, 1024\n"
        "s_mul_i32 s27, s21, 8\n"
        "s_add_u32 s25, s25, s27\n"
        "s_add_u32 s25, s25, 6291456\n"
        "s_sub_u32 s25, s25, s24\n"
        "v_mul_lo_u32 v24, v0, s25\n"
        "s_mul_i32 s25, 4, 128\n"
        "s_add_u32 s24, s24, s25\n"
        "v_add_u32 v24, s24, v24\n"
        "v_lshlrev_b32 v35, 8, v14\n"
        "v_lshl_add_u32 v35, v8, 7, v35\n"
        "v_lshl_add_u32 v35, v15, 4, v35\n"
        "v_add_u32 v35, 4096, v35\n"
        "v_mov_b32 v20, 1\n"
        "s_mov_b32 s30, 0x0c0c0400\n"
        "s_mov_b32 s31, 0x01010101\n"
        "v_mov_b32 v28, 0\n"
        "v_mov_b32 v29, 0\n"
        "v_mov_b32 v30, 0\n"
        "v_mov_b32 v31, 0\n"
        "v_mov_b32 v32, 0\n"
        "v_mov_b32 v33, 0\n"
        "v_lshlrev_b32 v12, 3, v0\n"
        "v_add_u32 v12, 134408, v12\n"
        "s_waitcnt vmcnt(20)\n"
        "v_add_co_u32 v62, vcc, 1, v62\n"
        "s_nop 1\n"
        "v_addc_co_u32 v63, vcc, 0, v63, vcc\n"
        "s_mov_b32 exec_hi, 0\n"
        "ds_write_b64 v12, v[62:63]\n"
        "s_mov_b32 exec_hi, -1\n"
        "ds_write_b32 v25, v20 offset:1024\n"
        "s_waitcnt vmcnt(19)\n"
        "global_load_dwordx4 v[152:155], v4, s[4:5] nt\n"
        "v_add_u32 v4, s28, v4\n"
        "global_load_dwordx4 v[156:159], v4, s[4:5] nt\n"
        "v_add_u32 v4, 4294853632, v4\n"
        "v_perm_b32 v64, v65, v64, s30\n"
        "v_perm_b32 v66, v67, v66, s30\n"
        "v_lshl_or_b32 v64, v66, 16, v64\n"
        "ds_write_b32 v16, v64 offset:0\n"
        "v_perm_b32 v68, v69, v68, s30\n"
        "v_perm_b32 v70, v71, v70, s30\n"
        "v_lshl_or_b32 v68, v70, 16, v68\n"
        "ds_write_b32 v16, v68 offset:256\n"
        "s_waitcnt vmcnt(19)\n"
        "global_load_dwordx4 v[160:163], v5, s[6:7] nt\n"
        "v_add_u32 v5, s29, v5\n"
        "global_load_dwordx4 v[164:167], v5, s[6:7] nt\n"
        "v_add_u32 v5, s29, v5\n"
        "v_perm_b32 v72, v73, v72, s30\n"
        "v_perm_b32 v74, v75, v74, s30\n"
        "v_lshl_or_b32 v72, v74, 16, v72\n"
        "ds_write_b32 v16, v72 offset:512\n"
        "v_perm_b32 v76, v77, v76, s30\n"
        "v_perm_b32 v78, v79, v78, s30\n"
        "v_lshl_or_b32 v76, v78, 16, v76\n"
        "ds_write_b32 v16, v76 offset:768\n"
        "s_waitcnt vmcnt(19)\n"
        "global_load_dwordx4 v[168:171], v5, s[6:7] nt\n"
        "v_add_u32 v5, s29, v5\n"
        "global_load_dwordx4 v[172:175], v5, s[6:7] nt\n"
        "v_add_u32 v5, s29, v5\n"
        "v_perm_b32 v80, v81, v80, s30\n"
        "v_perm_b32 v82, v83, v82, s30\n"
        "v_lshl_or_b32 v80, v82, 16, v80\n"
        "ds_write_b32 v16, v80 offset:1024\n"
        "v_perm_b32 v84, v85, v84, s30\n"
        "v_perm_b32 v86, v87, v86, s30\n"
        "v_lshl_or_b32 v84, v86, 16, v84\n"
        "ds_write_b32 v16, v84 offset:1280\n"
        "s_waitcnt vmcnt(19)\n"
        "global_load_dwordx4 v[176:179], v5, s[6:7] nt\n"
        "v_add_u32 v5, s29, v5\n"
        "global_load_dwordx4 v[180:183], v5, s[6:7] nt\n"
        "v_add_u32 v5, s29, v5\n"
        "v_perm_b32 v88, v89, v88, s30\n"
        "v_perm_b32 v90, v91, v90, s30\n"
        "v_lshl_or_b32 v88, v90, 16, v88\n"
        "ds_write_b32 v16, v88 offset:1536\n"
        "v_perm_b32 v92, v93, v92, s30\n"
        "v_perm_b32 v94, v95, v94, s30\n"
        "v_lshl_or_b32 v92, v94, 16, v92\n"
        "ds_write_b32 v16, v92 offset:1792\n"
        "s_waitcnt vmcnt(20)\n"
        "global_load_dwordx4 v[184:187], v5, s[6:7] nt\n"
        "v_add_u32 v5, s29, v5\n"
        "global_load_dwordx4 v[188:191], v5, s[6:7] nt\n"
        "v_add_u32 v5, 3569664, v5\n"
        "s_waitcnt vmcnt(20)\n"
        "global_load_dwordx4 v[192:195], v4, s[4:5] nt\n"
        "v_add_u32 v4, s28, v4\n"
        "global_load_dwordx4 v[196:199], v4, s[4:5] nt\n"
        "v_add_u32 v4, s28, v4\n"
        "v_perm_b32 v96, v100, v96, s30\n"
        "v_perm_b32 v104, v108, v104, s30\n"
        "v_perm_b32 v97, v101, v97, s30\n"
        "v_perm_b32 v105, v109, v105, s30\n"
        "v_perm_b32 v98, v102, v98, s30\n"
        "v_perm_b32 v106, v110, v106, s30\n"
        "v_perm_b32 v99, v103, v99, s30\n"
        "v_perm_b32 v107, v111, v107, s30\n"
        "v_lshl_or_b32 v36, v104, 16, v96\n"
        "v_lshl_or_b32 v40, v105, 16, v97\n"
        "v_lshl_or_b32 v38, v106, 16, v98\n"
        "v_lshl_or_b32 v42, v107, 16, v99\n"
        "s_waitcnt vmcnt(20)\n"
        "global_load_dwordx4 v[200:203], v4, s[4:5] nt\n"
        "v_add_u32 v4, s28, v4\n"
        "global_load_dwordx4 v[204:207], v4, s[4:5] nt\n"
        "v_add_u32 v4, s28, v4\n"
        "s_waitcnt vmcnt(20)\n"
        "global_load_dwordx4 v[208:211], v4, s[4:5] nt\n"
        "v_add_u32 v4, s28, v4\n"
        "global_load_dwordx4 v[212:215], v4, s[4:5] nt\n"
        "v_add_u32 v4, s28, v4\n"
        "v_perm_b32 v112, v116, v112, s30\n"
        "v_perm_b32 v120, v124, v120, s30\n"
        "v_perm_b32 v113, v117, v113, s30\n"
        "v_perm_b32 v121, v125, v121, s30\n"
        "v_perm_b32 v114, v118, v114, s30\n"
        "v_perm_b32 v122, v126, v122, s30\n"
        "v_perm_b32 v115, v119, v115, s30\n"
        "v_perm_b32 v123, v127, v123, s30\n"
        "v_lshl_or_b32 v37, v120, 16, v112\n"
        "v_lshl_or_b32 v41, v121, 16, v113\n"
        "v_lshl_or_b32 v39, v122, 16, v114\n"
        "v_lshl_or_b32 v43, v123, 16, v115\n"
        "s_nop 0\n"
        "v_permlane32_swap_b32 v36, v38\n"
        "v_permlane32_swap_b32 v37, v39\n"
        "v_permlane32_swap_b32 v40, v42\n"
        "v_permlane32_swap_b32 v41, v43\n"
        "s_nop 0\n"
        "ds_write_b128 v17, v[36:39] offset:2304\n"
        "ds_write_b128 v17, v[40:43] offset:3328\n"
        "ds_write_b32 v25, v20 offset:1544\n"
        "v_add_u32 v20, 1, v20\n"
        "v_dot4_i32_i8 v28, v36, s31, v28\n"
        "v_dot4_i32_i8 v29, v40, s31, v29\n"
        "v_dot4_i32_i8 v28, v37, s31, v28\n"
        "v_dot4_i32_i8 v29, v41, s31, v29\n"
        "v_dot4_i32_i8 v28, v38, s31, v28\n"
        "v_dot4_i32_i8 v29, v42, s31, v29\n"
        "v_dot4_i32_i8 v28, v39, s31, v28\n"
        "v_dot4_i32_i8 v29, v43, s31, v29\n"
        "s_waitcnt vmcnt(20)\n"
        "global_load_dwordx4 v[216:219], v4, s[4:5] nt\n"
        "v_add_u32 v4, s28, v4\n"
        "global_load_dwordx4 v[220:223], v4, s[4:5] nt\n"
        "v_add_u32 v4, 4294853632, v4\n"
        "v_perm_b32 v128, v129, v128, s30\n"
        "v_perm_b32 v130, v131, v130, s30\n"
        "v_lshl_or_b32 v128, v130, 16, v128\n"
        "ds_write_b32 v16, v128 offset:5120\n"
        "v_perm_b32 v132, v133, v132, s30\n"
        "v_perm_b32 v134, v135, v134, s30\n"
        "v_lshl_or_b32 v132, v134, 16, v132\n"
        "ds_write_b32 v16, v132 offset:5376\n"
        "s_waitcnt vmcnt(20)\n"
        "global_load_dwordx4 v[224:227], v5, s[6:7] nt\n"
        "v_add_u32 v5, s29, v5\n"
        "global_load_dwordx4 v[228:231], v5, s[6:7] nt\n"
        "v_add_u32 v5, s29, v5\n"
        "v_perm_b32 v136, v137, v136, s30\n"
        "v_perm_b32 v138, v139, v138, s30\n"
        "v_lshl_or_b32 v136, v138, 16, v136\n"
        "ds_write_b32 v16, v136 offset:5632\n"
        "v_perm_b32 v140, v141, v140, s30\n"
        "v_perm_b32 v142, v143, v142, s30\n"
        "v_lshl_or_b32 v140, v142, 16, v140\n"
        "ds_write_b32 v16, v140 offset:5888\n"
        "s_waitcnt vmcnt(20)\n"
        "global_load_dwordx4 v[232:235], v5, s[6:7] nt\n"
        "v_add_u32 v5, s29, v5\n"
        "global_load_dwordx4 v[236:239], v5, s[6:7] nt\n"
        "v_add_u32 v5, s29, v5\n"
        "v_perm_b32 v144, v145, v144, s30\n"
        "v_perm_b32 v146, v147, v146, s30\n"
        "v_lshl_or_b32 v144, v146, 16, v144\n"
        "ds_write_b32 v16, v144 offset:6144\n"
        "v_perm_b32 v148, v149, v148, s30\n"
        "v_perm_b32 v150, v151, v150, s30\n"
        "v_lshl_or_b32 v148, v150, 16, v148\n"
        "ds_write_b32 v16, v148 offset:6400\n"
        "s_waitcnt vmcnt(20)\n"
        "global_load_dwordx4 v[240:243], v5, s[6:7] nt\n"
        "v_add_u32 v5, s29, v5\n"
        "global_load_dwordx4 v[244:247], v5, s[6:7] nt\n"
        "v_add_u32 v5, s29, v5\n"
        "v_perm_b32 v152, v153, v152, s30\n"
        "v_perm_b32 v154, v155, v154, s30\n"
        "v_lshl_or_b32 v152, v154, 16, v152\n"
        "ds_write_b32 v16, v152 offset:6656\n"
        "v_perm_b32 v156, v157, v156, s30\n"
        "v_perm_b32 v158, v159, v158, s30\n"
        "v_lshl_or_b32 v156, v158, 16, v156\n"
        "ds_write_b32 v16, v156 offset:6912\n"
        "s_waitcnt vmcnt(20)\n"
        "global_load_dwordx4 v[64:67], v5, s[6:7] nt\n"
        "v_add_u32 v5, s29, v5\n"
        "global_load_dwordx4 v[68:71], v5, s[6:7] nt\n"
        "v_add_u32 v5, 3569664, v5\n"
        "s_waitcnt vmcnt(20)\n"
        "global_load_dwordx4 v[72:75], v4, s[4:5] nt\n"
        "v_add_u32 v4, s28, v4\n"
        "global_load_dwordx4 v[76:79], v4, s[4:5] nt\n"
        "v_add_u32 v4, s28, v4\n"
        "v_perm_b32 v160, v164, v160, s30\n"
        "v_perm_b32 v168, v172, v168, s30\n"
        "v_perm_b32 v161, v165, v161, s30\n"
        "v_perm_b32 v169, v173, v169, s30\n"
        "v_perm_b32 v162, v166, v162, s30\n"
        "v_perm_b32 v170, v174, v170, s30\n"
        "v_perm_b32 v163, v167, v163, s30\n"
        "v_perm_b32 v171, v175, v171, s30\n"
        "v_lshl_or_b32 v36, v168, 16, v160\n"
        "v_lshl_or_b32 v40, v169, 16, v161\n"
        "v_lshl_or_b32 v38, v170, 16, v162\n"
        "v_lshl_or_b32 v42, v171, 16, v163\n"
        "s_waitcnt vmcnt(20)\n"
        "global_load_dwordx4 v[80:83], v4, s[4:5] nt\n"
        "v_add_u32 v4, s28, v4\n"
        "global_load_dwordx4 v[84:87], v4, s[4:5] nt\n"
        "v_add_u32 v4, s28, v4\n"
        "s_waitcnt vmcnt(20)\n"
        "global_load_dwordx4 v[88:91], v4, s[4:5] nt\n"
        "v_add_u32 v4, s28, v4\n"
        "global_load_dwordx4 v[92:95], v4, s[4:5] nt\n"
        "v_add_u32 v4, s28, v4\n"
        "v_perm_b32 v176, v180, v176, s30\n"
        "v_perm_b32 v184, v188, v184, s30\n"
        "v_perm_b32 v177, v181, v177, s30\n"
        "v_perm_b32 v185, v189, v185, s30\n"
        "v_perm_b32 v178, v182, v178, s30\n"
        "v_perm_b32 v186, v190, v186, s30\n"
        "v_perm_b32 v179, v183, v179, s30\n"
        "v_perm_b32 v187, v191, v187, s30\n"
        "v_lshl_or_b32 v37, v184, 16, v176\n"
        "v_lshl_or_b32 v41, v185, 16, v177\n"
        "v_lshl_or_b32 v39, v186, 16, v178\n"
        "v_lshl_or_b32 v43, v187, 16, v179\n"
        "s_nop 0\n"
        "v_permlane32_swap_b32 v36, v38\n"
        "v_permlane32_swap_b32 v37, v39\n"
        "v_permlane32_swap_b32 v40, v42\n"
        "v_permlane32_swap_b32 v41, v43\n"
        "s_nop 0\n"
        "ds_write_b128 v17, v[36:39] offset:7424\n"
        "ds_write_b128 v17, v[40:43] offset:8448\n"
        "ds_write_b32 v25, v20 offset:1544\n"
        "v_add_u32 v20, 1, v20\n"
        "v_dot4_i32_i8 v28, v36, s31, v28\n"
        "v_dot4_i32_i8 v29, v40, s31, v29\n"
        "v_dot4_i32_i8 v28, v37, s31, v28\n"
        "v_dot4_i32_i8 v29, v41, s31, v29\n"
        "v_dot4_i32_i8 v28, v38, s31, v28\n"
        "v_dot4_i32_i8 v29, v42, s31, v29\n"
        "v_dot4_i32_i8 v28, v39, s31, v28\n"
        "v_dot4_i32_i8 v29, v43, s31, v29\n"
        "s_waitcnt vmcnt(20)\n"
        "global_load_dwordx4 v[96:99], v4, s[4:5] nt\n"
        "v_add_u32 v4, s28, v4\n"
        "global_load_dwordx4 v[100:103], v4, s[4:5] nt\n"
        "v_perm_b32 v192, v193, v192, s30\n"
        "v_perm_b32 v194, v195, v194, s30\n"
        "v_lshl_or_b32 v192, v194, 16, v192\n"
        "ds_write_b32 v16, v192 offset:10240\n"
        "v_perm_b32 v196, v197, v196, s30\n"
        "v_perm_b32 v198, v199, v198, s30\n"
        "v_lshl_or_b32 v196, v198, 16, v196\n"
        "ds_write_b32 v16, v196 offset:10496\n"
        "s_waitcnt vmcnt(20)\n"
        "global_load_dwordx4 v[104:107], v5, s[6:7] nt\n"
        "v_add_u32 v5, s29, v5\n"
        "global_load_dwordx4 v[108:111], v5, s[6:7] nt\n"
        "v_add_u32 v5, s29, v5\n"
        "v_perm_b32 v200, v201, v200, s30\n"
        "v_perm_b32 v202, v203, v202, s30\n"
        "v_lshl_or_b32 v200, v202, 16, v200\n"
        "ds_write_b32 v16, v200 offset:10752\n"
        "v_perm_b32 v204, v205, v204, s30\n"
        "v_perm_b32 v206, v207, v206, s30\n"
        "v_lshl_or_b32 v204, v206, 16, v204\n"
        "ds_write_b32 v16, v204 offset:11008\n"
        "s_waitcnt vmcnt(20)\n"
        "global_load_dwordx4 v[112:115], v5, s[6:7] nt\n"
        "v_add_u32 v5, s29, v5\n"
        "global_load_dwordx4 v[116:119], v5, s[6:7] nt\n"
        "v_add_u32 v5, s29, v5\n"
        "v_perm_b32 v208, v209, v208, s30\n"
        "v_perm_b32 v210, v211, v210, s30\n"
        "v_lshl_or_b32 v208, v210, 16, v208\n"
        "ds_write_b32 v16, v208 offset:11264\n"
        "v_perm_b32 v212, v213, v212, s30\n"
        "v_perm_b32 v214, v215, v214, s30\n"
        "v_lshl_or_b32 v212, v214, 16, v212\n"
        "ds_write_b32 v16, v212 offset:11520\n"
        "s_waitcnt vmcnt(20)\n"
        "global_load_dwordx4 v[120:123], v5, s[6:7] nt\n"
        "v_add_u32 v5, s29, v5\n"
        "global_load_dwordx4 v[124:127], v5, s[6:7] nt\n"
        "v_add_u32 v5, s29, v5\n"
        "v_perm_b32 v216, v217, v216, s30\n"
        "v_perm_b32 v218, v219, v218, s30\n"
        "v_lshl_or_b32 v216, v218, 16, v216\n"
        "ds_write_b32 v16, v216 offset:11776\n"
        "v_perm_b32 v220, v221, v220, s30\n"
        "v_perm_b32 v222, v223, v222, s30\n"
        "v_lshl_or_b32 v220, v222, 16, v220\n"
        "ds_write_b32 v16, v220 offset:12032\n"
        "s_waitcnt vmcnt(20)\n"
        "global_load_dwordx4 v[128:131], v5, s[6:7] nt\n"
        "v_add_u32 v5, s29, v5\n"
        "global_load_dwordx4 v[132:135], v5, s[6:7] nt\n"
        "s_waitcnt vmcnt(20)\n"
        "global_load_dwordx4 v[136:139], v6, s[6:7] nt\n"
        "v_add_u32 v6, s29, v6\n"
        "global_load_dwordx4 v[140:143], v6, s[6:7] nt\n"
        "v_add_u32 v6, s29, v6\n"
        "v_perm_b32 v224, v228, v224, s30\n"
        "v_perm_b32 v232, v236, v232, s30\n"
        "v_perm_b32 v225, v229, v225, s30\n"
        "v_perm_b32 v233, v237, v233, s30\n"
        "v_perm_b32 v226, v230, v226, s30\n"
        "v_perm_b32 v234, v238, v234, s30\n"
        "v_perm_b32 v227, v231, v227, s30\n"
        "v_perm_b32 v235, v239, v235, s30\n"
        "v_lshl_or_b32 v36, v232, 16, v224\n"
        "v_lshl_or_b32 v40, v233, 16, v225\n"
        "v_lshl_or_b32 v38, v234, 16, v226\n"
        "v_lshl_or_b32 v42, v235, 16, v227\n"
        "s_waitcnt vmcnt(20)\n"
        "global_load_dwordx4 v[144:147], v6, s[6:7] nt\n"
        "v_add_u32 v6, s29, v6\n"
        "global_load_dwordx4 v[148:151], v6, s[6:7] nt\n"
        "v_add_u32 v6, s29, v6\n"
        "s_waitcnt vmcnt(20)\n"
        "global_load_dwordx4 v[152:155], v6, s[6:7] nt\n"
        "v_add_u32 v6, s29, v6\n"
        "global_load_dwordx4 v[156:159], v6, s[6:7] nt\n"
        "v_add_u32 v6, s29, v6\n"
        "v_mov_b32 v253, 0\n"
        "s_lshl_b32 s56, s22, 7\n"
        "s_lshl_b32 s57, s22, 6\n"
        "s_add_u32 s57, s57, 1920\n"
        "v_mov_b32 v254, s56\n"
        "v_mov_b32 v255, s57\n"
        "v_cmp_gt_u32 vcc, 32, v0\n"
        "s_lshl_b32 s56, s22, 5\n"
        "global_load_ushort v252, v253, s[16:17]\n"
        "v_cndmask_b32 v254, v255, v254, vcc\n"
        "s_add_u32 s56, s56, 2880\n"
        "v_mov_b32 v255, s56\n"
        "v_cmp_gt_u32 vcc, 48, v0\n"
        "s_nop 1\n"
        "v_cndmask_b32 v254, v255, v254, vcc\n"
        "v_lshl_add_u32 v254, v0, 2, v254\n"
        "v_lshlrev_b32 v254, 1, v254\n"
        "s_mov_b32 exec_hi, 0x00ffffff\n"
        "global_load_dwordx2 v[248:249], v254, s[12:13]\n"
        "global_load_dwordx2 v[250:251], v254, s[14:15]\n"
        "s_mov_b32 exec_hi, -1\n"
        "v_perm_b32 v240, v244, v240, s30\n"
        "v_perm_b32 v64, v68, v64, s30\n"
        "v_perm_b32 v241, v245, v241, s30\n"
        "v_perm_b32 v65, v69, v65, s30\n"
        "v_perm_b32 v242, v246, v242, s30\n"
        "v_perm_b32 v66, v70, v66, s30\n"
        "v_perm_b32 v243, v247, v243, s30\n"
        "v_perm_b32 v67, v71, v67, s30\n"
        "v_lshl_or_b32 v37, v64, 16, v240\n"
        "v_lshl_or_b32 v41, v65, 16, v241\n"
        "v_lshl_or_b32 v39, v66, 16, v242\n"
        "v_lshl_or_b32 v43, v67, 16, v243\n"
        "s_nop 0\n"
        "v_permlane32_swap_b32 v36, v38\n"
        "v_permlane32_swap_b32 v37, v39\n"
        "v_permlane32_swap_b32 v40, v42\n"
        "v_permlane32_swap_b32 v41, v43\n"
        "s_nop 0\n"
        "ds_write_b128 v17, v[36:39] offset:12544\n"
        "ds_write_b128 v17, v[40:43] offset:13568\n"
        "ds_write_b32 v25, v20 offset:1544\n"
        "v_add_u32 v20, 1, v20\n"
        "v_dot4_i32_i8 v28, v36, s31, v28\n"
        "v_dot4_i32_i8 v29, v40, s31, v29\n"
        "v_dot4_i32_i8 v28, v37, s31, v28\n"
        "v_dot4_i32_i8 v29, v41, s31, v29\n"
        "v_dot4_i32_i8 v28, v38, s31, v28\n"
        "v_dot4_i32_i8 v29, v42, s31, v29\n"
        "v_dot4_i32_i8 v28, v39, s31, v28\n"
        "v_dot4_i32_i8 v29, v43, s31, v29\n"
        "s_waitcnt vmcnt(23)\n"
        "global_load_dwordx4 v[160:163], v6, s[6:7] nt\n"
        "v_add_u32 v6, s29, v6\n"
        "global_load_dwordx4 v[164:167], v6, s[6:7] nt\n"
        "v_perm_b32 v72, v73, v72, s30\n"
        "v_perm_b32 v74, v75, v74, s30\n"
        "v_lshl_or_b32 v72, v74, 16, v72\n"
        "ds_write_b32 v16, v72 offset:15360\n"
        "v_perm_b32 v76, v77, v76, s30\n"
        "v_perm_b32 v78, v79, v78, s30\n"
        "v_lshl_or_b32 v76, v78, 16, v76\n"
        "ds_write_b32 v16, v76 offset:15616\n"
        "s_waitcnt vmcnt(23)\n"
        "global_load_dwordx4 v[168:171], v7, s[6:7] nt\n"
        "v_add_u32 v7, s29, v7\n"
        "global_load_dwordx4 v[172:175], v7, s[6:7] nt\n"
        "v_add_u32 v7, s29, v7\n"
        "v_perm_b32 v80, v81, v80, s30\n"
        "v_perm_b32 v82, v83, v82, s30\n"
        "v_lshl_or_b32 v80, v82, 16, v80\n"
        "ds_write_b32 v16, v80 offset:15872\n"
        "v_perm_b32 v84, v85, v84, s30\n"
        "v_perm_b32 v86, v87, v86, s30\n"
        "v_lshl_or_b32 v84, v86, 16, v84\n"
        "ds_write_b32 v16, v84 offset:16128\n"
        "s_waitcnt vmcnt(23)\n"
        "global_load_dwordx4 v[176:179], v7, s[6:7] nt\n"
        "v_add_u32 v7, s29, v7\n"
        "global_load_dwordx4 v[180:183], v7, s[6:7] nt\n"
        "v_add_u32 v7, s29, v7\n"
        "v_perm_b32 v88, v89, v88, s30\n"
        "v_perm_b32 v90, v91, v90, s30\n"
        "v_lshl_or_b32 v88, v90, 16, v88\n"
        "ds_write_b32 v16, v88 offset:16384\n"
        "v_perm_b32 v92, v93, v92, s30\n"
        "v_perm_b32 v94, v95, v94, s30\n"
        "v_lshl_or_b32 v92, v94, 16, v92\n"
        "ds_write_b32 v16, v92 offset:16640\n"
        "s_waitcnt vmcnt(23)\n"
        "global_load_dwordx4 v[184:187], v7, s[6:7] nt\n"
        "v_add_u32 v7, s29, v7\n"
        "global_load_dwordx4 v[188:191], v7, s[6:7] nt\n"
        "v_add_u32 v7, s29, v7\n"
        "v_perm_b32 v96, v97, v96, s30\n"
        "v_perm_b32 v98, v99, v98, s30\n"
        "v_lshl_or_b32 v96, v98, 16, v96\n"
        "ds_write_b32 v16, v96 offset:16896\n"
        "v_perm_b32 v100, v101, v100, s30\n"
        "v_perm_b32 v102, v103, v102, s30\n"
        "v_lshl_or_b32 v100, v102, 16, v100\n"
        "ds_write_b32 v16, v100 offset:17152\n"
        "s_waitcnt vmcnt(23)\n"
        "global_load_dwordx4 v[192:195], v7, s[6:7] nt\n"
        "v_add_u32 v7, s29, v7\n"
        "global_load_dwordx4 v[196:199], v7, s[6:7] nt\n"
        "v_add_u32 v7, s29, v7\n"
        "s_waitcnt vmcnt(23)\n"
        "global_load_dwordx4 v[200:203], v7, s[6:7] nt\n"
        "v_add_u32 v7, s29, v7\n"
        "global_load_dwordx4 v[204:207], v7, s[6:7] nt\n"
        "v_add_u32 v7, s29, v7\n"
        "v_perm_b32 v104, v108, v104, s30\n"
        "v_perm_b32 v112, v116, v112, s30\n"
        "v_perm_b32 v105, v109, v105, s30\n"
        "v_perm_b32 v113, v117, v113, s30\n"
        "v_perm_b32 v106, v110, v106, s30\n"
        "v_perm_b32 v114, v118, v114, s30\n"
        "v_perm_b32 v107, v111, v107, s30\n"
        "v_perm_b32 v115, v119, v115, s30\n"
        "v_lshl_or_b32 v36, v112, 16, v104\n"
        "v_lshl_or_b32 v40, v113, 16, v105\n"
        "v_lshl_or_b32 v38, v114, 16, v106\n"
        "v_lshl_or_b32 v42, v115, 16, v107\n"
        "s_waitcnt vmcnt(23)\n"
        "global_load_dwordx4 v[208:211], v7, s[6:7] nt\n"
        "v_add_u32 v7, s29, v7\n"
        "global_load_dwordx4 v[212:215], v7, s[6:7] nt\n"
        "v_add_u32 v7, s29, v7\n"
        "s_waitcnt vmcnt(23)\n"
        "global_load_dwordx4 v[216:219], v7, s[6:7] nt\n"
        "v_add_u32 v7, s29, v7\n"
        "global_load_dwordx4 v[220:223], v7, s[6:7] nt\n"
        "v_add_u32 v7, s29, v7\n"
        "v_perm_b32 v120, v124, v120, s30\n"
        "v_perm_b32 v128, v132, v128, s30\n"
        "v_perm_b32 v121, v125, v121, s30\n"
        "v_perm_b32 v129, v133, v129, s30\n"
        "v_perm_b32 v122, v126, v122, s30\n"
        "v_perm_b32 v130, v134, v130, s30\n"
        "v_perm_b32 v123, v127, v123, s30\n"
        "v_perm_b32 v131, v135, v131, s30\n"
        "v_lshl_or_b32 v37, v128, 16, v120\n"
        "v_lshl_or_b32 v41, v129, 16, v121\n"
        "v_lshl_or_b32 v39, v130, 16, v122\n"
        "v_lshl_or_b32 v43, v131, 16, v123\n"
        "s_nop 0\n"
        "v_permlane32_swap_b32 v36, v38\n"
        "v_permlane32_swap_b32 v37, v39\n"
        "v_permlane32_swap_b32 v40, v42\n"
        "v_permlane32_swap_b32 v41, v43\n"
        "s_nop 0\n"
        "ds_write_b128 v17, v[36:39] offset:17664\n"
        "ds_write_b128 v17, v[40:43] offset:18688\n"
        "v_dot4_i32_i8 v28, v36, s31, v28\n"
        "v_dot4_i32_i8 v29, v40, s31, v29\n"
        "v_dot4_i32_i8 v28, v37, s31, v28\n"
        "v_dot4_i32_i8 v29, v41, s31, v29\n"
        "v_dot4_i32_i8 v28, v38, s31, v28\n"
        "v_dot4_i32_i8 v29, v42, s31, v29\n"
        "v_dot4_i32_i8 v28, v39, s31, v28\n"
        "v_dot4_i32_i8 v29, v43, s31, v29\n"
        "s_nop 2\n"
        "ds_write_b64 v21, v[28:29] offset:19712\n"
        "ds_write_b32 v25, v20 offset:1544\n"
        "v_add_u32 v20, 1, v20\n"
        "s_waitcnt vmcnt(23)\n"
        "global_load_dwordx4 v[224:227], v7, s[6:7] nt\n"
        "v_add_u32 v7, s29, v7\n"
        "global_load_dwordx4 v[228:231], v7, s[6:7] nt\n"
        "v_mov_b32 v12, 1\n"
        "ds_write_b32 v25, v12 offset:512\n"
        "s_waitcnt vmcnt(23)\n"
        "v_perm_b32 v136, v140, v136, s30\n"
        "v_perm_b32 v144, v148, v144, s30\n"
        "v_perm_b32 v137, v141, v137, s30\n"
        "v_perm_b32 v145, v149, v145, s30\n"
        "v_perm_b32 v138, v142, v138, s30\n"
        "v_perm_b32 v146, v150, v146, s30\n"
        "v_perm_b32 v139, v143, v139, s30\n"
        "v_perm_b32 v147, v151, v147, s30\n"
        "v_lshl_or_b32 v36, v144, 16, v136\n"
        "v_lshl_or_b32 v40, v145, 16, v137\n"
        "v_lshl_or_b32 v38, v146, 16, v138\n"
        "v_lshl_or_b32 v42, v147, 16, v139\n"
        "s_waitcnt vmcnt(21)\n"
        "s_waitcnt vmcnt(16)\n"
        "v_perm_b32 v152, v156, v152, s30\n"
        "v_perm_b32 v160, v164, v160, s30\n"
        "v_perm_b32 v153, v157, v153, s30\n"
        "v_perm_b32 v161, v165, v161, s30\n"
        "v_perm_b32 v154, v158, v154, s30\n"
        "v_perm_b32 v162, v166, v162, s30\n"
        "v_perm_b32 v155, v159, v155, s30\n"
        "v_perm_b32 v163, v167, v163, s30\n"
        "v_lshl_or_b32 v37, v160, 16, v152\n"
        "v_lshl_or_b32 v41, v161, 16, v153\n"
        "v_lshl_or_b32 v39, v162, 16, v154\n"
        "v_lshl_or_b32 v43, v163, 16, v155\n"
        "s_nop 0\n"
        "v_permlane32_swap_b32 v36, v38\n"
        "v_permlane32_swap_b32 v37, v39\n"
        "v_permlane32_swap_b32 v40, v42\n"
        "v_permlane32_swap_b32 v41, v43\n"
        "s_nop 0\n"
        "global_store_dwordx4 v35, v[36:39], s[44:45] sc1\n"
        "global_store_dwordx4 v35, v[40:43], s[44:45] offset:1024 sc1\n"
        "v_dot4_i32_i8 v30, v36, s31, v30\n"
        "v_dot4_i32_i8 v31, v40, s31, v31\n"
        "v_dot4_i32_i8 v30, v37, s31, v30\n"
        "v_dot4_i32_i8 v31, v41, s31, v31\n"
        "v_dot4_i32_i8 v30, v38, s31, v30\n"
        "v_dot4_i32_i8 v31, v42, s31, v31\n"
        "v_dot4_i32_i8 v30, v39, s31, v30\n"
        "v_dot4_i32_i8 v31, v43, s31, v31\n"
        "s_nop 2\n"
        "v_permlane16_swap_b32 v30, v31\n"
        "s_nop 0\n"
        "v_add_u32 v30, v30, v31\n"
        "v_mov_b32 v31, 0\n"
        "s_nop 0\n"
        "v_add_u32_dpp v34, v30, v30 row_ror:8 row_mask:0xf bank_mask:0xf\n"
        "v_mov_b32 v30, 0\n"
        "s_mov_b32 s38, 8388608\n"
        "W8_tg0:\n"
        "ds_read_b32 v12, v25 offset:1024\n"
        "s_sub_u32 s38, s38, 1\n"
        "s_waitcnt lgkmcnt(0)\n"
        "s_cbranch_scc1 W8_tg0_x\n"
        "v_readfirstlane_b32 s39, v12\n"
        "s_cmp_ge_u32 s39, 1\n"
        "s_cbranch_scc1 W8_tg0_x\n"
        "s_sleep 1\n"
        "s_branch W8_tg0\n"
        "W8_tg0_x:\n"
        "v_lshrrev_b32 v12, 0, v0\n"
        "v_lshlrev_b32 v12, 7, v12\n"
        "v_add_u32 v12, 134408, v12\n"
        "s_mov_b64 exec, 3\n"
        "ds_read_b64 v[26:27], v12\n"
        "s_mov_b64 exec, -1\n"
        "s_waitcnt vmcnt(16)\n"
        "s_waitcnt vmcnt(14)\n"
        "v_perm_b32 v168, v172, v168, s30\n"
        "v_perm_b32 v176, v180, v176, s30\n"
        "v_perm_b32 v169, v173, v169, s30\n"
        "v_perm_b32 v177, v181, v177, s30\n"
        "v_perm_b32 v170, v174, v170, s30\n"
        "v_perm_b32 v178, v182, v178, s30\n"
        "v_perm_b32 v171, v175, v171, s30\n"
        "v_perm_b32 v179, v183, v179, s30\n"
        "v_lshl_or_b32 v36, v176, 16, v168\n"
        "v_lshl_or_b32 v40, v177, 16, v169\n"
        "v_lshl_or_b32 v44, v178, 16, v170\n"
        "v_lshl_or_b32 v48, v179, 16, v171\n"
        "v_dot4_i32_i8 v30, v36, s31, v30\n"
        "v_dot4_i32_i8 v31, v40, s31, v31\n"
        "v_dot4_i32_i8 v32, v44, s31, v32\n"
        "v_dot4_i32_i8 v33, v48, s31, v33\n"
        "s_waitcnt vmcnt(12)\n"
        "s_waitcnt vmcnt(10)\n"
        "v_perm_b32 v184, v188, v184, s30\n"
        "v_perm_b32 v192, v196, v192, s30\n"
        "v_perm_b32 v185, v189, v185, s30\n"
        "v_perm_b32 v193, v197, v193, s30\n"
        "v_perm_b32 v186, v190, v186, s30\n"
        "v_perm_b32 v194, v198, v194, s30\n"
        "v_perm_b32 v187, v191, v187, s30\n"
        "v_perm_b32 v195, v199, v195, s30\n"
        "v_lshl_or_b32 v37, v192, 16, v184\n"
        "v_lshl_or_b32 v41, v193, 16, v185\n"
        "v_lshl_or_b32 v45, v194, 16, v186\n"
        "v_lshl_or_b32 v49, v195, 16, v187\n"
        "v_dot4_i32_i8 v30, v37, s31, v30\n"
        "v_dot4_i32_i8 v31, v41, s31, v31\n"
        "v_dot4_i32_i8 v32, v45, s31, v32\n"
        "v_dot4_i32_i8 v33, v49, s31, v33\n"
        "s_waitcnt vmcnt(8)\n"
        "s_waitcnt vmcnt(6)\n"
        "v_perm_b32 v200, v204, v200, s30\n"
        "v_perm_b32 v208, v212, v208, s30\n"
        "v_perm_b32 v201, v205, v201, s30\n"
        "v_perm_b32 v209, v213, v209, s30\n"
        "v_perm_b32 v202, v206, v202, s30\n"
        "v_perm_b32 v210, v214, v210, s30\n"
        "v_perm_b32 v203, v207, v203, s30\n"
        "v_perm_b32 v211, v215, v211, s30\n"
        "v_lshl_or_b32 v38, v208, 16, v200\n"
        "v_lshl_or_b32 v42, v209, 16, v201\n"
        "v_lshl_or_b32 v46, v210, 16, v202\n"
        "v_lshl_or_b32 v50, v211, 16, v203\n"
        "v_dot4_i32_i8 v30, v38, s31, v30\n"
        "v_dot4_i32_i8 v31, v42, s31, v31\n"
        "v_dot4_i32_i8 v32, v46, s31, v32\n"
        "v_dot4_i32_i8 v33, v50, s31, v33\n"
        "s_waitcnt vmcnt(4)\n"
        "s_waitcnt vmcnt(2)\n"
        "v_perm_b32 v216, v220, v216, s30\n"
        "v_perm_b32 v224, v228, v224, s30\n"
        "v_perm_b32 v217, v221, v217, s30\n"
        "v_perm_b32 v225, v229, v225, s30\n"
        "v_perm_b32 v218, v222, v218, s30\n"
        "v_perm_b32 v226, v230, v226, s30\n"
        "v_perm_b32 v219, v223, v219, s30\n"
        "v_perm_b32 v227, v231, v227, s30\n"
        "v_lshl_or_b32 v39, v224, 16, v216\n"
        "v_lshl_or_b32 v43, v225, 16, v217\n"
        "v_lshl_or_b32 v47, v226, 16, v218\n"
        "v_lshl_or_b32 v51, v227, 16, v219\n"
        "ds_write_b32 v25, v20 offset:1544\n"
        "global_store_dwordx4 v19, v[36:39], s[44:45] offset:0 sc1\n"
        "global_store_dwordx4 v19, v[40:43], s[44:45] offset:1024 sc1\n"
        "global_store_dwordx4 v19, v[44:47], s[44:45] offset:2048 sc1\n"
        "global_store_dwordx4 v19, v[48:51], s[44:45] offset:3072 sc1\n"
        "v_dot4_i32_i8 v30, v39, s31, v30\n"
        "v_dot4_i32_i8 v31, v43, s31, v31\n"
        "v_dot4_i32_i8 v32, v47, s31, v32\n"
        "v_dot4_i32_i8 v33, v51, s31, v33\n"
        "s_nop 2\n"
        "v_permlane32_swap_b32 v30, v32\n"
        "v_permlane32_swap_b32 v31, v33\n"
        "s_nop 0\n"
        "v_add_u32 v30, v30, v32\n"
        "v_add_u32 v31, v31, v33\n"
        "s_nop 1\n"
        "v_permlane16_swap_b32 v30, v31\n"
        "s_nop 0\n"
        "v_add_u32 v30, v30, v31\n"
        "global_store_dword v22, v30, s[46:47] offset:512 sc1\n"
        "s_mov_b32 exec_lo, 0x00ff00ff\n"
        "s_mov_b32 exec_hi, 0x00ff00ff\n"
        "global_store_dword v23, v34, s[46:47] offset:768 sc1\n"
        "s_mov_b64 exec, 2\n"
        "s_waitcnt vmcnt(0) lgkmcnt(0)\n"
        "global_atomic_swap_x2 v24, v[26:27], s[8:9] sc1\n"
        "s_mov_b64 exec, -1\n"
        "s_load_dword s48, s[18:19], 0x0\n"
        "s_mov_b32 exec_hi, 0x00ffffff\n"
        "v_mov_b32 v32, v252\n"
        "v_mov_b32 v34, v248\n"
        "v_mov_b32 v35, v249\n"
        "v_mov_b32 v38, v250\n"
        "v_mov_b32 v39, v251\n"
        "s_lshl_b32 s24, s22, 14\n"
        "s_add_u32 s24, s24, 5771264\n"
        "v_add_u32 v21, s24, v19\n"
        "v_add_u32 v22, 8192, v21\n"
        "v_lshlrev_b32 v23, 5, v0\n"
        "v_add_u32 v23, 131072, v23\n"
        "s_waitcnt vmcnt(0)\n"
        "v_lshlrev_b32 v32, 16, v32\n"
        "v_and_b32 v37, 0xffff0000, v35\n"
        "v_lshlrev_b32 v36, 16, v35\n"
        "v_and_b32 v35, 0xffff0000, v34\n"
        "v_lshlrev_b32 v34, 16, v34\n"
        "v_and_b32 v41, 0xffff0000, v39\n"
        "v_lshlrev_b32 v40, 16, v39\n"
        "v_and_b32 v39, 0xffff0000, v38\n"
        "v_lshlrev_b32 v38, 16, v38\n"
        "v_mul_f32 v44, v32, v34\n"
        "v_mul_f32 v46, v32, v35\n"
        "v_mul_f32 v48, v32, v36\n"
        "v_mul_f32 v50, v32, v37\n"
        "s_mov_b32 s49, 6\n"
        "s_mov_b32 s50, -1\n"
        "s_mov_b32 s51, 16777215\n"
        "v_mov_b32 v33, 3\n"
        "W8_tab:\n"
        "s_mov_b64 exec, -1\n"
        "s_mov_b32 s38, 8388608\n"
        "W8_tr:\n"
        "ds_read_b32 v12, v25 offset:1024\n"
        "s_sub_u32 s38, s38, 1\n"
        "s_waitcnt lgkmcnt(0)\n"
        "s_cbranch_scc1 W8_tr_x\n"
        "v_readfirstlane_b32 s39, v12\n"
        "s_cmp_ge_u32 s39, 21\n"
        "s_cbranch_scc1 W8_tr_x\n"
        "s_sleep 2\n"
        "s_branch W8_tr\n"
        "W8_tr_x:\n"
        "s_mov_b32 s38, 8388608\n"
        "W8_tf:\n"
        "ds_read_b32 v12, v25 offset:0\n"
        "s_sub_u32 s38, s38, 1\n"
        "s_waitcnt lgkmcnt(0)\n"
        "s_cbranch_scc1 W8_tf_x\n"
        "v_readfirstlane_b32 s39, v12\n"
        "s_cmp_ge_u32 s39, 12\n"
        "s_cbranch_scc1 W8_tf_x\n"
        "s_sleep 2\n"
        "s_branch W8_tf\n"
        "W8_tf_x:\n"
        "s_mov_b64 exec, s[50:51]\n"
        "global_load_dwordx4 v[64:67], v21, s[8:9] offset:-4096 sc1\n"
        "global_load_dwordx4 v[68:71], v21, s[8:9] offset:-3072 sc1\n"
        "global_load_dwordx4 v[72:75], v21, s[8:9] offset:-2048 sc1\n"
        "global_load_dwordx4 v[76:79], v21, s[8:9] offset:-1024 sc1\n"
        "global_load_dwordx4 v[80:83], v21, s[8:9] offset:0 sc1\n"
        "global_load_dwordx4 v[84:87], v21, s[8:9] offset:1024 sc1\n"
        "global_load_dwordx4 v[88:91], v21, s[8:9] offset:2048 sc1\n"
        "global_load_dwordx4 v[92:95], v21, s[8:9] offset:3072 sc1\n"
        "global_load_dwordx4 v[96:99], v22, s[8:9] offset:-4096 sc1\n"
        "global_load_dwordx4 v[100:103], v22, s[8:9] offset:-3072 sc1\n"
        "global_load_dwordx4 v[104:107], v22, s[8:9] offset:-2048 sc1\n"
        "global_load_dwordx4 v[108:111], v22, s[8:9] offset:-1024 sc1\n"
        "global_load_dwordx4 v[112:115], v22, s[8:9] offset:0 sc1\n"
        "global_load_dwordx4 v[116:119], v22, s[8:9] offset:1024 sc1\n"
        "global_load_dwordx4 v[120:123], v22, s[8:9] offset:2048 sc1\n"
        "global_load_dwordx4 v[124:127], v22, s[8:9] offset:3072 sc1\n"
        "s_waitcnt vmcnt(0)\n"
        "v_add3_u32 v52, v64, v68, v72\n"
        "v_add3_u32 v53, v65, v69, v73\n"
        "v_add3_u32 v54, v66, v70, v74\n"
        "v_add3_u32 v55, v67, v71, v75\n"
        "v_add3_u32 v52, v52, v76, v80\n"
        "v_add3_u32 v53, v53, v77, v81\n"
        "v_add3_u32 v54, v54, v78, v82\n"
        "v_add3_u32 v55, v55, v79, v83\n"
        "v_add3_u32 v52, v52, v84, v88\n"
        "v_add3_u32 v53, v53, v85, v89\n"
        "v_add3_u32 v54, v54, v86, v90\n"
        "v_add3_u32 v55, v55, v87, v91\n"
        "v_add3_u32 v52, v52, v92, v96\n"
        "v_add3_u32 v53, v53, v93, v97\n"
        "v_add3_u32 v54, v54, v94, v98\n"
        "v_add3_u32 v55, v55, v95, v99\n"
        "v_add3_u32 v52, v52, v100, v104\n"
        "v_add3_u32 v53, v53, v101, v105\n"
        "v_add3_u32 v54, v54, v102, v106\n"
        "v_add3_u32 v55, v55, v103, v107\n"
        "v_add3_u32 v52, v52, v108, v112\n"
        "v_add3_u32 v53, v53, v109, v113\n"
        "v_add3_u32 v54, v54, v110, v114\n"
        "v_add3_u32 v55, v55, v111, v115\n"
        "v_add3_u32 v52, v52, v116, v120\n"
        "v_add3_u32 v53, v53, v117, v121\n"
        "v_add3_u32 v54, v54, v118, v122\n"
        "v_add3_u32 v55, v55, v119, v123\n"
        "v_add_u32 v52, v52, v124\n"
        "v_add_u32 v53, v53, v125\n"
        "v_add_u32 v54, v54, v126\n"
        "v_add_u32 v55, v55, v127\n"
        "v_mul_lo_u32 v52, v52, s48\n"
        "v_mul_lo_u32 v53, v53, s48\n"
        "v_mul_lo_u32 v54, v54, s48\n"
        "v_mul_lo_u32 v55, v55, s48\n"
        "v_cvt_f32_i32 v52, v52\n"
        "v_cvt_f32_i32 v53, v53\n"
        "v_cvt_f32_i32 v54, v54\n"
        "v_cvt_f32_i32 v55, v55\n"
        "v_fma_f32 v45, -v52, v44, v38\n"
        "v_fma_f32 v47, -v53, v46, v39\n"
        "v_fma_f32 v49, -v54, v48, v40\n"
        "v_fma_f32 v51, -v55, v50, v41\n"
        "ds_write_b128 v23, v[44:47]\n"
        "ds_write_b128 v23, v[48:51] offset:16\n"
        "s_mov_b64 exec, -1\n"
        "ds_write_b32 v25, v33 offset:512\n"
        "v_add_u32 v33, 1, v33\n"
        "s_mov_b32 s50, 0\n"
        "s_mov_b32 s51, 0x00ffffff\n"
        "s_add_u32 s49, s49, 2\n"
        "s_cmp_eq_u32 s49, 6\n"
        "s_cbranch_scc1 W8_tab\n"
        "s_branch W8_fin\n"
        "W8_other:\n"
        "v_and_b32 v1, 63, v0\n"
        "s_add_u32 s33, s21, s22\n"
        "s_cmp_ge_u32 s20, 4\n"
        "s_cbranch_scc1 W8_comp\n"
        "v_lshlrev_b32 v25, 2, v1\n"
        "v_add_u32 v25, 133124, v25\n"
        "s_cmp_eq_u32 s20, 1\n"
        "s_cbranch_scc1 W8_load\n"
        "s_and_b32 s30, s20, 1\n"
        "s_lshl_b32 s24, s21, 9\n"
        "s_add_u32 s24, s24, 6029312\n"
        "s_lshl_b32 s25, s22, 10\n"
        "s_add_u32 s25, s25, 6291456\n"
        "s_add_u32 s26, s22, 16\n"
        "s_cmp_eq_u32 s30, 0\n"
        "s_cselect_b32 s24, s24, s25\n"
        "s_cselect_b32 s26, s21, s26\n"
        "s_cselect_b32 s31, s22, s21\n"
        "s_mul_i32 s27, s30, 768\n"
        "s_add_u32 s27, s27, 256\n"
        "s_lshl_b32 s26, s26, 7\n"
        "s_add_u32 s26, s26, 6815744\n"
        "v_mov_b32 v5, s26\n"
        "v_add_u32 v8, s27, v25\n"
        "s_lshl_b32 s26, s30, 7\n"
        "s_add_u32 s26, s26, 134408\n"
        "v_mov_b32 v12, s26\n"
        "s_add_u32 s24, s8, s24\n"
        "s_addc_u32 s25, s9, 0\n"
        "s_add_u32 s28, s30, 1\n"
        "v_mov_b32 v13, s28\n"
        "s_lshl_b32 s28, s30, 0\n"
        "s_add_u32 s28, s28, 4\n"
        "s_mov_b32 s38, 8388608\n"
        "W8_tg2:\n"
        "ds_read_b32 v14, v25 offset:1024\n"
        "s_sub_u32 s38, s38, 1\n"
        "s_waitcnt lgkmcnt(0)\n"
        "s_cbranch_scc1 W8_tg2_x\n"
        "v_readfirstlane_b32 s39, v14\n"
        "s_cmp_ge_u32 s39, 1\n"
        "s_cbranch_scc1 W8_tg2_x\n"
        "s_sleep 2\n"
        "s_branch W8_tg2\n"
        "W8_tg2_x:\n"
        "ds_read_b64 v[6:7], v12\n"
        "s_mov_b32 s38, 8388608\n"
        "W8_pst:\n"
        "ds_read_b32 v14, v25 offset:1544\n"
        "s_sub_u32 s38, s38, 1\n"
        "s_waitcnt lgkmcnt(0)\n"
        "s_cbranch_scc1 W8_pst_x\n"
        "v_readfirstlane_b32 s39, v14\n"
        "s_cmp_ge_u32 s39, 3\n"
        "s_cbranch_scc1 W8_pst_x\n"
        "s_sleep 4\n"
        "s_branch W8_pst\n"
        "W8_pst_x:\n"
        "s_add_u32 s14, s21, s22\n"
        "s_and_b32 s12, s14, 2\n"
        "s_lshl_b32 s12, s12, 5\n"
        "s_xor_b32 s13, s12, 64\n"
        "s_and_b32 s14, s14, 1\n"
        "v_add_u32 v16, 1, v13\n"
        "v_add_u32 v17, 2, v13\n"
        "v_add_u32 v18, 3, v13\n"
        "v_mov_b32 v15, v13\n"
        "s_mov_b32 s29, 2097152\n"
        "W8_pl:\n"
        "s_load_dwordx16 s[32:47], s[24:25], s12 glc\n"
        "s_load_dwordx16 s[48:63], s[24:25], s13 glc\n"
        "s_sub_u32 s29, s29, 1\n"
        "s_cselect_b32 s27, 1, 0\n"
        "s_cmp_eq_u32 s14, 1\n"
        "s_waitcnt lgkmcnt(0)\n"
        "s_cbranch_scc1 W8_plb\n"
        "v_cmpx_eq_u64 vcc, s[32:33], v[6:7]\n"
        "v_cmpx_eq_u64 vcc, s[34:35], v[6:7]\n"
        "v_cmpx_eq_u64 vcc, s[36:37], v[6:7]\n"
        "v_cmpx_eq_u64 vcc, s[38:39], v[6:7]\n"
        "ds_write_b32 v8, v15\n"
        "v_cmpx_eq_u64 vcc, s[40:41], v[6:7]\n"
        "v_cmpx_eq_u64 vcc, s[42:43], v[6:7]\n"
        "v_cmpx_eq_u64 vcc, s[44:45], v[6:7]\n"
        "v_cmpx_eq_u64 vcc, s[46:47], v[6:7]\n"
        "ds_write_b32 v8, v16\n"
        "v_cmpx_eq_u64 vcc, s[48:49], v[6:7]\n"
        "v_cmpx_eq_u64 vcc, s[50:51], v[6:7]\n"
        "v_cmpx_eq_u64 vcc, s[52:53], v[6:7]\n"
        "v_cmpx_eq_u64 vcc, s[54:55], v[6:7]\n"
        "ds_write_b32 v8, v17\n"
        "v_cmpx_eq_u64 vcc, s[56:57], v[6:7]\n"
        "v_cmpx_eq_u64 vcc, s[58:59], v[6:7]\n"
        "v_cmpx_eq_u64 vcc, s[60:61], v[6:7]\n"
        "v_cmpx_eq_u64 vcc, s[62:63], v[6:7]\n"
        "s_branch W8_plc\n"
        "W8_plb:\n"
        "v_cmpx_eq_u64 vcc, s[40:41], v[6:7]\n"
        "v_cmpx_eq_u64 vcc, s[42:43], v[6:7]\n"
        "v_cmpx_eq_u64 vcc, s[44:45], v[6:7]\n"
        "v_cmpx_eq_u64 vcc, s[46:47], v[6:7]\n"
        "ds_write_b32 v8, v15\n"
        "v_cmpx_eq_u64 vcc, s[32:33], v[6:7]\n"
        "v_cmpx_eq_u64 vcc, s[34:35], v[6:7]\n"
        "v_cmpx_eq_u64 vcc, s[36:37], v[6:7]\n"
        "v_cmpx_eq_u64 vcc, s[38:39], v[6:7]\n"
        "ds_write_b32 v8, v16\n"
        "v_cmpx_eq_u64 vcc, s[56:57], v[6:7]\n"
        "v_cmpx_eq_u64 vcc, s[58:59], v[6:7]\n"
        "v_cmpx_eq_u64 vcc, s[60:61], v[6:7]\n"
        "v_cmpx_eq_u64 vcc, s[62:63], v[6:7]\n"
        "ds_write_b32 v8, v17\n"
        "v_cmpx_eq_u64 vcc, s[48:49], v[6:7]\n"
        "v_cmpx_eq_u64 vcc, s[50:51], v[6:7]\n"
        "v_cmpx_eq_u64 vcc, s[52:53], v[6:7]\n"
        "v_cmpx_eq_u64 vcc, s[54:55], v[6:7]\n"
        "W8_plc:\n"
        "s_nop 4\n"
        "s_cbranch_execnz W8_pg\n"
        "s_mov_b64 exec, -1\n"
        "s_cmp_eq_u32 s27, 1\n"
        "s_cbranch_scc1 W8_pg\n"
        "s_sleep 1\n"
        "s_branch W8_pl\n"
        "W8_pg:\n"
        "ds_write_b32 v8, v18\n"
        "s_max_i32 s29, s29, 0\n"
        "v_add_u32 v15, 4, v15\n"
        "v_add_u32 v16, 4, v16\n"
        "v_add_u32 v17, 4, v17\n"
        "v_add_u32 v18, 4, v18\n"
        "s_add_u32 s24, s24, 128\n"
        "s_addc_u32 s25, s25, 0\n"
        "s_sub_u32 s28, s28, 1\n"
        "s_cmp_lg_u32 s28, 0\n"
        "s_cbranch_scc1 W8_pl\n"
        "s_cmp_lg_u32 s31, 0\n"
        "s_cbranch_scc1 W8_fin\n"
        "s_mov_b64 exec, 1\n"
        "global_store_dwordx2 v5, v[6:7], s[8:9] sc1\n"
        "s_branch W8_fin\n"
        "W8_load:\n"
        "v_lshlrev_b32 v2, 4, v1\n"
        "s_xor_b32 s24, s33, 0\n"
        "s_and_b32 s24, s24, 3\n"
        "s_lshl_b32 s25, s24, 13\n"
        "v_add_u32 v4, s25, v2\n"
        "v_add_u32 v8, 4096, v4\n"
        "s_mul_i32 s25, s24, 6144\n"
        "v_add_u32 v12, s25, v2\n"
        "s_xor_b32 s24, s33, 1\n"
        "s_and_b32 s24, s24, 3\n"
        "s_lshl_b32 s25, s24, 13\n"
        "v_add_u32 v5, s25, v2\n"
        "v_add_u32 v9, 4096, v5\n"
        "s_mul_i32 s25, s24, 6144\n"
        "v_add_u32 v13, s25, v2\n"
        "s_xor_b32 s24, s33, 2\n"
        "s_and_b32 s24, s24, 3\n"
        "s_lshl_b32 s25, s24, 13\n"
        "v_add_u32 v6, s25, v2\n"
        "v_add_u32 v10, 4096, v6\n"
        "s_mul_i32 s25, s24, 6144\n"
        "v_add_u32 v14, s25, v2\n"
        "s_xor_b32 s24, s33, 3\n"
        "s_and_b32 s24, s24, 3\n"
        "s_lshl_b32 s25, s24, 13\n"
        "v_add_u32 v7, s25, v2\n"
        "v_add_u32 v11, 4096, v7\n"
        "s_mul_i32 s25, s24, 6144\n"
        "v_add_u32 v15, s25, v2\n"
        "s_waitcnt lgkmcnt(0)\n"
        "s_lshl_b32 s24, s22, 17\n"
        "s_add_u32 s24, s24, 2097152\n"
        "s_add_u32 s40, s8, s24\n"
        "s_addc_u32 s41, s9, 0\n"
        "s_mul_i32 s24, s22, 98304\n"
        "s_add_u32 s24, s24, 4194304\n"
        "s_add_u32 s42, s8, s24\n"
        "s_addc_u32 s43, s9, 0\n"
        "s_mov_b32 s34, 0\n"
        "s_mov_b32 s35, 2\n"
        "v_mov_b32 v3, -2\n"
        "s_mov_b32 s38, 8388608\n"
        "W8_lgo:\n"
        "ds_read_b32 v20, v25 offset:512\n"
        "s_sub_u32 s38, s38, 1\n"
        "s_waitcnt lgkmcnt(0)\n"
        "s_cbranch_scc1 W8_lgo_x\n"
        "v_readfirstlane_b32 s39, v20\n"
        "s_cmp_ge_u32 s39, 1\n"
        "s_cbranch_scc1 W8_lgo_x\n"
        "s_sleep 2\n"
        "s_branch W8_lgo\n"
        "W8_lgo_x:\n"
        "s_mov_b32 s38, 8388608\n"
        "W8_lr00:\n"
        "ds_read_b32 v20, v25 offset:1024\n"
        "s_sub_u32 s38, s38, 1\n"
        "s_waitcnt lgkmcnt(0)\n"
        "s_cbranch_scc1 W8_lr00_x\n"
        "v_readfirstlane_b32 s39, v20\n"
        "s_cmp_ge_u32 s39, 2\n"
        "s_cbranch_scc1 W8_lr00_x\n"
        "s_sleep 1\n"
        "s_branch W8_lr00\n"
        "W8_lr00_x:\n"
        "s_mov_b32 m0, s34\n"
        "s_add_u32 s34, s34, 4096\n"
        "global_load_lds_dwordx4 v4, s[40:41] offset:0 sc1\n"
        "global_load_lds_dwordx4 v4, s[40:41] offset:1024 sc1\n"
        "global_load_lds_dwordx4 v4, s[40:41] offset:2048 sc1\n"
        "global_load_lds_dwordx4 v4, s[40:41] offset:3072 sc1\n"
        "s_mov_b32 m0, s34\n"
        "s_add_u32 s34, s34, 4096\n"
        "global_load_lds_dwordx4 v8, s[40:41] offset:0 sc1\n"
        "global_load_lds_dwordx4 v8, s[40:41] offset:1024 sc1\n"
        "global_load_lds_dwordx4 v8, s[40:41] offset:2048 sc1\n"
        "global_load_lds_dwordx4 v8, s[40:41] offset:3072 sc1\n"
        "s_cmp_ge_u32 s39, 3\n"
        "s_cbranch_scc1 W8_lg1_x\n"
        "ds_read_b32 v20, v25 offset:1024\n"
        "s_waitcnt lgkmcnt(0)\n"
        "v_readfirstlane_b32 s39, v20\n"
        "s_cmp_ge_u32 s39, 3\n"
        "s_cbranch_scc1 W8_lg1_x\n"
        "v_mov_b32 v21, 1\n"
        "s_waitcnt vmcnt(0)\n"
        "ds_max_i32 v25, v21 offset:0\n"
        "s_mov_b32 s38, 8388608\n"
        "W8_lg1:\n"
        "ds_read_b32 v20, v25 offset:1024\n"
        "s_sub_u32 s38, s38, 1\n"
        "s_waitcnt lgkmcnt(0)\n"
        "s_cbranch_scc1 W8_lg1_x\n"
        "v_readfirstlane_b32 s39, v20\n"
        "s_cmp_ge_u32 s39, 3\n"
        "s_cbranch_scc1 W8_lg1_x\n"
        "s_sleep 1\n"
        "s_branch W8_lg1\n"
        "W8_lg1_x:\n"
        "s_mov_b32 m0, s34\n"
        "s_add_u32 s34, s34, 4096\n"
        "global_load_lds_dwordx4 v5, s[40:41] offset:0 sc1\n"
        "global_load_lds_dwordx4 v5, s[40:41] offset:1024 sc1\n"
        "global_load_lds_dwordx4 v5, s[40:41] offset:2048 sc1\n"
        "global_load_lds_dwordx4 v5, s[40:41] offset:3072 sc1\n"
        "s_mov_b32 m0, s34\n"
        "s_add_u32 s34, s34, 4096\n"
        "global_load_lds_dwordx4 v9, s[40:41] offset:0 sc1\n"
        "global_load_lds_dwordx4 v9, s[40:41] offset:1024 sc1\n"
        "global_load_lds_dwordx4 v9, s[40:41] offset:2048 sc1\n"
        "global_load_lds_dwordx4 v9, s[40:41] offset:3072 sc1\n"
        "v_mov_b32 v21, 1\n"
        "s_waitcnt vmcnt(8)\n"
        "ds_max_i32 v25, v21 offset:0\n"
        "s_cmp_ge_u32 s39, 4\n"
        "s_cbranch_scc1 W8_lg2_x\n"
        "ds_read_b32 v20, v25 offset:1024\n"
        "s_waitcnt lgkmcnt(0)\n"
        "v_readfirstlane_b32 s39, v20\n"
        "s_cmp_ge_u32 s39, 4\n"
        "s_cbranch_scc1 W8_lg2_x\n"
        "v_mov_b32 v21, 2\n"
        "s_waitcnt vmcnt(0)\n"
        "ds_max_i32 v25, v21 offset:0\n"
        "s_mov_b32 s38, 8388608\n"
        "W8_lg2:\n"
        "ds_read_b32 v20, v25 offset:1024\n"
        "s_sub_u32 s38, s38, 1\n"
        "s_waitcnt lgkmcnt(0)\n"
        "s_cbranch_scc1 W8_lg2_x\n"
        "v_readfirstlane_b32 s39, v20\n"
        "s_cmp_ge_u32 s39, 4\n"
        "s_cbranch_scc1 W8_lg2_x\n"
        "s_sleep 1\n"
        "s_branch W8_lg2\n"
        "W8_lg2_x:\n"
        "s_mov_b32 m0, s34\n"
        "s_add_u32 s34, s34, 4096\n"
        "global_load_lds_dwordx4 v6, s[40:41] offset:0 sc1\n"
        "global_load_lds_dwordx4 v6, s[40:41] offset:1024 sc1\n"
        "global_load_lds_dwordx4 v6, s[40:41] offset:2048 sc1\n"
        "global_load_lds_dwordx4 v6, s[40:41] offset:3072 sc1\n"
        "s_mov_b32 m0, s34\n"
        "s_add_u32 s34, s34, 4096\n"
        "global_load_lds_dwordx4 v10, s[40:41] offset:0 sc1\n"
        "global_load_lds_dwordx4 v10, s[40:41] offset:1024 sc1\n"
        "global_load_lds_dwordx4 v10, s[40:41] offset:2048 sc1\n"
        "global_load_lds_dwordx4 v10, s[40:41] offset:3072 sc1\n"
        "s_cmp_ge_u32 s39, 5\n"
        "s_cbranch_scc1 W8_lg3_x\n"
        "ds_read_b32 v20, v25 offset:1024\n"
        "s_waitcnt lgkmcnt(0)\n"
        "v_readfirstlane_b32 s39, v20\n"
        "s_cmp_ge_u32 s39, 5\n"
        "s_cbranch_scc1 W8_lg3_x\n"
        "v_mov_b32 v21, 2\n"
        "s_waitcnt vmcnt(8)\n"
        "ds_max_i32 v25, v21 offset:0\n"
        "v_mov_b32 v21, 3\n"
        "s_waitcnt vmcnt(0)\n"
        "ds_max_i32 v25, v21 offset:0\n"
        "s_mov_b32 s38, 8388608\n"
        "W8_lg3:\n"
        "ds_read_b32 v20, v25 offset:1024\n"
        "s_sub_u32 s38, s38, 1\n"
        "s_waitcnt lgkmcnt(0)\n"
        "s_cbranch_scc1 W8_lg3_x\n"
        "v_readfirstlane_b32 s39, v20\n"
        "s_cmp_ge_u32 s39, 5\n"
        "s_cbranch_scc1 W8_lg3_x\n"
        "s_sleep 1\n"
        "s_branch W8_lg3\n"
        "W8_lg3_x:\n"
        "s_mov_b32 m0, s34\n"
        "s_add_u32 s34, s34, 4096\n"
        "global_load_lds_dwordx4 v7, s[40:41] offset:0 sc1\n"
        "global_load_lds_dwordx4 v7, s[40:41] offset:1024 sc1\n"
        "global_load_lds_dwordx4 v7, s[40:41] offset:2048 sc1\n"
        "global_load_lds_dwordx4 v7, s[40:41] offset:3072 sc1\n"
        "s_mov_b32 m0, s34\n"
        "s_add_u32 s34, s34, 4096\n"
        "global_load_lds_dwordx4 v11, s[40:41] offset:0 sc1\n"
        "global_load_lds_dwordx4 v11, s[40:41] offset:1024 sc1\n"
        "global_load_lds_dwordx4 v11, s[40:41] offset:2048 sc1\n"
        "global_load_lds_dwordx4 v11, s[40:41] offset:3072 sc1\n"
        "v_mov_b32 v21, 2\n"
        "s_waitcnt vmcnt(16)\n"
        "ds_max_i32 v25, v21 offset:0\n"
        "v_mov_b32 v3, 2\n"
        "s_add_u32 s40, s40, 32768\n"
        "s_addc_u32 s41, s41, 0\n"
        "s_mov_b32 s35, 3\n"
        "s_mov_b32 s36, 6\n"
        "W8_lq:\n"
        "ds_read_b32 v20, v25 offset:1024\n"
        "s_waitcnt lgkmcnt(0)\n"
        "v_readfirstlane_b32 s39, v20\n"
        "s_cmp_ge_u32 s39, s36\n"
        "s_cbranch_scc1 W8_lr_x\n"
        "v_add_u32 v21, 1, v3\n"
        "s_waitcnt vmcnt(8)\n"
        "ds_max_i32 v25, v21 offset:0\n"
        "v_add_u32 v21, 2, v3\n"
        "s_waitcnt vmcnt(0)\n"
        "ds_max_i32 v25, v21 offset:0\n"
        "s_mov_b32 s38, 8388608\n"
        "W8_lr:\n"
        "ds_read_b32 v20, v25 offset:1024\n"
        "s_sub_u32 s38, s38, 1\n"
        "s_waitcnt lgkmcnt(0)\n"
        "s_cbranch_scc1 W8_lr_x\n"
        "v_readfirstlane_b32 s39, v20\n"
        "s_cmp_ge_u32 s39, s36\n"
        "s_cbranch_scc1 W8_lr_x\n"
        "s_sleep 1\n"
        "s_branch W8_lr\n"
        "W8_lr_x:\n"
        "s_mov_b32 m0, s34\n"
        "s_add_u32 s34, s34, 4096\n"
        "global_load_lds_dwordx4 v4, s[40:41] offset:0 sc1\n"
        "global_load_lds_dwordx4 v4, s[40:41] offset:1024 sc1\n"
        "global_load_lds_dwordx4 v4, s[40:41] offset:2048 sc1\n"
        "global_load_lds_dwordx4 v4, s[40:41] offset:3072 sc1\n"
        "s_mov_b32 m0, s34\n"
        "s_add_u32 s34, s34, 4096\n"
        "global_load_lds_dwordx4 v8, s[40:41] offset:0 sc1\n"
        "global_load_lds_dwordx4 v8, s[40:41] offset:1024 sc1\n"
        "global_load_lds_dwordx4 v8, s[40:41] offset:2048 sc1\n"
        "global_load_lds_dwordx4 v8, s[40:41] offset:3072 sc1\n"
        "v_add_u32 v3, 1, v3\n"
        "s_waitcnt vmcnt(16)\n"
        "ds_max_i32 v25, v3 offset:0\n"
        "s_add_u32 s36, s36, 1\n"
        "s_cmp_ge_u32 s39, s36\n"
        "s_cbranch_scc1 W8_lu1_x\n"
        "ds_read_b32 v20, v25 offset:1024\n"
        "s_waitcnt lgkmcnt(0)\n"
        "v_readfirstlane_b32 s39, v20\n"
        "s_cmp_ge_u32 s39, s36\n"
        "s_cbranch_scc1 W8_lu1_x\n"
        "v_add_u32 v21, 1, v3\n"
        "s_waitcnt vmcnt(8)\n"
        "ds_max_i32 v25, v21 offset:0\n"
        "v_add_u32 v21, 2, v3\n"
        "s_waitcnt vmcnt(0)\n"
        "ds_max_i32 v25, v21 offset:0\n"
        "s_mov_b32 s38, 8388608\n"
        "W8_lu1:\n"
        "ds_read_b32 v20, v25 offset:1024\n"
        "s_sub_u32 s38, s38, 1\n"
        "s_waitcnt lgkmcnt(0)\n"
        "s_cbranch_scc1 W8_lu1_x\n"
        "v_readfirstlane_b32 s39, v20\n"
        "s_cmp_ge_u32 s39, s36\n"
        "s_cbranch_scc1 W8_lu1_x\n"
        "s_sleep 1\n"
        "s_branch W8_lu1\n"
        "W8_lu1_x:\n"
        "s_mov_b32 m0, s34\n"
        "s_add_u32 s34, s34, 4096\n"
        "global_load_lds_dwordx4 v5, s[40:41] offset:0 sc1\n"
        "global_load_lds_dwordx4 v5, s[40:41] offset:1024 sc1\n"
        "global_load_lds_dwordx4 v5, s[40:41] offset:2048 sc1\n"
        "global_load_lds_dwordx4 v5, s[40:41] offset:3072 sc1\n"
        "s_mov_b32 m0, s34\n"
        "s_add_u32 s34, s34, 4096\n"
        "global_load_lds_dwordx4 v9, s[40:41] offset:0 sc1\n"
        "global_load_lds_dwordx4 v9, s[40:41] offset:1024 sc1\n"
        "global_load_lds_dwordx4 v9, s[40:41] offset:2048 sc1\n"
        "global_load_lds_dwordx4 v9, s[40:41] offset:3072 sc1\n"
        "v_add_u32 v3, 1, v3\n"
        "s_waitcnt vmcnt(16)\n"
        "ds_max_i32 v25, v3 offset:0\n"
        "s_add_u32 s36, s36, 1\n"
        "s_cmp_ge_u32 s39, s36\n"
        "s_cbranch_scc1 W8_lu2_x\n"
        "ds_read_b32 v20, v25 offset:1024\n"
        "s_waitcnt lgkmcnt(0)\n"
        "v_readfirstlane_b32 s39, v20\n"
        "s_cmp_ge_u32 s39, s36\n"
        "s_cbranch_scc1 W8_lu2_x\n"
        "v_add_u32 v21, 1, v3\n"
        "s_waitcnt vmcnt(8)\n"
        "ds_max_i32 v25, v21 offset:0\n"
        "v_add_u32 v21, 2, v3\n"
        "s_waitcnt vmcnt(0)\n"
        "ds_max_i32 v25, v21 offset:0\n"
        "s_mov_b32 s38, 8388608\n"
        "W8_lu2:\n"
        "ds_read_b32 v20, v25 offset:1024\n"
        "s_sub_u32 s38, s38, 1\n"
        "s_waitcnt lgkmcnt(0)\n"
        "s_cbranch_scc1 W8_lu2_x\n"
        "v_readfirstlane_b32 s39, v20\n"
        "s_cmp_ge_u32 s39, s36\n"
        "s_cbranch_scc1 W8_lu2_x\n"
        "s_sleep 1\n"
        "s_branch W8_lu2\n"
        "W8_lu2_x:\n"
        "s_mov_b32 m0, s34\n"
        "s_add_u32 s34, s34, 4096\n"
        "global_load_lds_dwordx4 v6, s[40:41] offset:0 sc1\n"
        "global_load_lds_dwordx4 v6, s[40:41] offset:1024 sc1\n"
        "global_load_lds_dwordx4 v6, s[40:41] offset:2048 sc1\n"
        "global_load_lds_dwordx4 v6, s[40:41] offset:3072 sc1\n"
        "s_mov_b32 m0, s34\n"
        "s_add_u32 s34, s34, 4096\n"
        "global_load_lds_dwordx4 v10, s[40:41] offset:0 sc1\n"
        "global_load_lds_dwordx4 v10, s[40:41] offset:1024 sc1\n"
        "global_load_lds_dwordx4 v10, s[40:41] offset:2048 sc1\n"
        "global_load_lds_dwordx4 v10, s[40:41] offset:3072 sc1\n"
        "v_add_u32 v3, 1, v3\n"
        "s_waitcnt vmcnt(16)\n"
        "ds_max_i32 v25, v3 offset:0\n"
        "s_add_u32 s36, s36, 1\n"
        "s_cmp_ge_u32 s39, s36\n"
        "s_cbranch_scc1 W8_lu3_x\n"
        "ds_read_b32 v20, v25 offset:1024\n"
        "s_waitcnt lgkmcnt(0)\n"
        "v_readfirstlane_b32 s39, v20\n"
        "s_cmp_ge_u32 s39, s36\n"
        "s_cbranch_scc1 W8_lu3_x\n"
        "v_add_u32 v21, 1, v3\n"
        "s_waitcnt vmcnt(8)\n"
        "ds_max_i32 v25, v21 offset:0\n"
        "v_add_u32 v21, 2, v3\n"
        "s_waitcnt vmcnt(0)\n"
        "ds_max_i32 v25, v21 offset:0\n"
        "s_mov_b32 s38, 8388608\n"
        "W8_lu3:\n"
        "ds_read_b32 v20, v25 offset:1024\n"
        "s_sub_u32 s38, s38, 1\n"
        "s_waitcnt lgkmcnt(0)\n"
        "s_cbranch_scc1 W8_lu3_x\n"
        "v_readfirstlane_b32 s39, v20\n"
        "s_cmp_ge_u32 s39, s36\n"
        "s_cbranch_scc1 W8_lu3_x\n"
        "s_sleep 1\n"
        "s_branch W8_lu3\n"
        "W8_lu3_x:\n"
        "s_mov_b32 m0, s34\n"
        "s_add_u32 s34, s34, 4096\n"
        "global_load_lds_dwordx4 v7, s[40:41] offset:0 sc1\n"
        "global_load_lds_dwordx4 v7, s[40:41] offset:1024 sc1\n"
        "global_load_lds_dwordx4 v7, s[40:41] offset:2048 sc1\n"
        "global_load_lds_dwordx4 v7, s[40:41] offset:3072 sc1\n"
        "s_mov_b32 m0, s34\n"
        "s_add_u32 s34, s34, 4096\n"
        "global_load_lds_dwordx4 v11, s[40:41] offset:0 sc1\n"
        "global_load_lds_dwordx4 v11, s[40:41] offset:1024 sc1\n"
        "global_load_lds_dwordx4 v11, s[40:41] offset:2048 sc1\n"
        "global_load_lds_dwordx4 v11, s[40:41] offset:3072 sc1\n"
        "v_add_u32 v3, 1, v3\n"
        "s_waitcnt vmcnt(16)\n"
        "ds_max_i32 v25, v3 offset:0\n"
        "s_add_u32 s40, s40, 32768\n"
        "s_addc_u32 s41, s41, 0\n"
        "s_add_u32 s36, s36, 1\n"
        "s_add_u32 s35, s35, 1\n"
        "s_cmp_lt_u32 s35, 6\n"
        "s_cbranch_scc1 W8_lq\n"
        "s_mov_b32 s34, 0\n"
        "ds_read_b32 v20, v25 offset:1024\n"
        "s_waitcnt lgkmcnt(0)\n"
        "v_readfirstlane_b32 s39, v20\n"
        "s_cmp_ge_u32 s39, 21\n"
        "s_cbranch_scc1 W8_lr0_x\n"
        "v_add_u32 v21, 1, v3\n"
        "s_waitcnt vmcnt(8)\n"
        "ds_max_i32 v25, v21 offset:0\n"
        "v_add_u32 v21, 2, v3\n"
        "s_waitcnt vmcnt(0)\n"
        "ds_max_i32 v25, v21 offset:0\n"
        "s_mov_b32 s38, 8388608\n"
        "W8_lr0:\n"
        "ds_read_b32 v20, v25 offset:1024\n"
        "s_sub_u32 s38, s38, 1\n"
        "s_waitcnt lgkmcnt(0)\n"
        "s_cbranch_scc1 W8_lr0_x\n"
        "v_readfirstlane_b32 s39, v20\n"
        "s_cmp_ge_u32 s39, 21\n"
        "s_cbranch_scc1 W8_lr0_x\n"
        "s_sleep 1\n"
        "s_branch W8_lr0\n"
        "W8_lr0_x:\n"
        "ds_read_b32 v20, v25 offset:768\n"
        "s_waitcnt lgkmcnt(0)\n"
        "v_readfirstlane_b32 s39, v20\n"
        "s_cmp_ge_u32 s39, 533504\n"
        "s_cbranch_scc1 W8_lf0_x\n"
        "v_add_u32 v21, 1, v3\n"
        "s_waitcnt vmcnt(8)\n"
        "ds_max_i32 v25, v21 offset:0\n"
        "v_add_u32 v21, 2, v3\n"
        "s_waitcnt vmcnt(0)\n"
        "ds_max_i32 v25, v21 offset:0\n"
        "s_mov_b32 s38, 8388608\n"
        "W8_lf0:\n"
        "ds_read_b32 v20, v25 offset:768\n"
        "s_sub_u32 s38, s38, 1\n"
        "s_waitcnt lgkmcnt(0)\n"
        "s_cbranch_scc1 W8_lf0_x\n"
        "v_readfirstlane_b32 s39, v20\n"
        "s_cmp_ge_u32 s39, 533504\n"
        "s_cbranch_scc1 W8_lf0_x\n"
        "s_sleep 1\n"
        "s_branch W8_lf0\n"
        "W8_lf0_x:\n"
        "s_add_u32 s40, s42, 4096\n"
        "s_addc_u32 s41, s43, 0\n"
        "W8_ll0:\n"
        "s_mov_b32 m0, s34\n"
        "s_add_u32 s34, s34, 2048\n"
        "global_load_lds_dwordx4 v12, s[40:41] sc1\n"
        "global_load_lds_dwordx4 v12, s[40:41] offset:1024 sc1\n"
        "s_mov_b32 m0, s34\n"
        "s_add_u32 s34, s34, 2048\n"
        "global_load_lds_dwordx4 v13, s[40:41] sc1\n"
        "global_load_lds_dwordx4 v13, s[40:41] offset:1024 sc1\n"
        "s_mov_b32 m0, s34\n"
        "s_add_u32 s34, s34, 2048\n"
        "global_load_lds_dwordx4 v14, s[40:41] sc1\n"
        "global_load_lds_dwordx4 v14, s[40:41] offset:1024 sc1\n"
        "s_mov_b32 m0, s34\n"
        "s_add_u32 s34, s34, 2048\n"
        "global_load_lds_dwordx4 v15, s[40:41] sc1\n"
        "global_load_lds_dwordx4 v15, s[40:41] offset:1024 sc1\n"
        "s_add_u32 s40, s40, 24576\n"
        "s_addc_u32 s41, s41, 0\n"
        "v_add_u32 v3, 1, v3\n"
        "s_waitcnt vmcnt(16)\n"
        "ds_max_i32 v25, v3 offset:0\n"
        "s_cmp_lt_u32 s34, 32768\n"
        "s_cbranch_scc1 W8_ll0\n"
        "v_add_u32 v3, -1, v3\n"
        "ds_read_b32 v20, v25 offset:772\n"
        "s_waitcnt lgkmcnt(0)\n"
        "v_readfirstlane_b32 s39, v20\n"
        "s_cmp_ge_u32 s39, 533504\n"
        "s_cbranch_scc1 W8_lf1_x\n"
        "v_add_u32 v21, 1, v3\n"
        "s_waitcnt vmcnt(16)\n"
        "ds_max_i32 v25, v21 offset:0\n"
        "v_add_u32 v21, 2, v3\n"
        "s_waitcnt vmcnt(8)\n"
        "ds_max_i32 v25, v21 offset:0\n"
        "v_add_u32 v21, 3, v3\n"
        "s_waitcnt vmcnt(0)\n"
        "ds_max_i32 v25, v21 offset:0\n"
        "s_mov_b32 s38, 8388608\n"
        "W8_lf1:\n"
        "ds_read_b32 v20, v25 offset:772\n"
        "s_sub_u32 s38, s38, 1\n"
        "s_waitcnt lgkmcnt(0)\n"
        "s_cbranch_scc1 W8_lf1_x\n"
        "v_readfirstlane_b32 s39, v20\n"
        "s_cmp_ge_u32 s39, 533504\n"
        "s_cbranch_scc1 W8_lf1_x\n"
        "s_sleep 1\n"
        "s_branch W8_lf1\n"
        "W8_lf1_x:\n"
        "s_add_u32 s40, s42, 0\n"
        "s_addc_u32 s41, s43, 0\n"
        "W8_ll1:\n"
        "s_mov_b32 m0, s34\n"
        "s_add_u32 s34, s34, 2048\n"
        "global_load_lds_dwordx4 v12, s[40:41] sc1\n"
        "global_load_lds_dwordx4 v12, s[40:41] offset:1024 sc1\n"
        "s_mov_b32 m0, s34\n"
        "s_add_u32 s34, s34, 2048\n"
        "global_load_lds_dwordx4 v13, s[40:41] sc1\n"
        "global_load_lds_dwordx4 v13, s[40:41] offset:1024 sc1\n"
        "s_mov_b32 m0, s34\n"
        "s_add_u32 s34, s34, 2048\n"
        "global_load_lds_dwordx4 v14, s[40:41] sc1\n"
        "global_load_lds_dwordx4 v14, s[40:41] offset:1024 sc1\n"
        "s_mov_b32 m0, s34\n"
        "s_add_u32 s34, s34, 2048\n"
        "global_load_lds_dwordx4 v15, s[40:41] sc1\n"
        "global_load_lds_dwordx4 v15, s[40:41] offset:1024 sc1\n"
        "s_add_u32 s40, s40, 24576\n"
        "s_addc_u32 s41, s41, 0\n"
        "v_add_u32 v3, 1, v3\n"
        "s_waitcnt vmcnt(24)\n"
        "ds_max_i32 v25, v3 offset:0\n"
        "s_cmp_lt_u32 s34, 65536\n"
        "s_cbranch_scc1 W8_ll1\n"
        "v_add_u32 v3, -1, v3\n"
        "ds_read_b32 v20, v25 offset:776\n"
        "s_waitcnt lgkmcnt(0)\n"
        "v_readfirstlane_b32 s39, v20\n"
        "s_cmp_ge_u32 s39, 533504\n"
        "s_cbranch_scc1 W8_lf2_x\n"
        "v_add_u32 v21, 1, v3\n"
        "s_waitcnt vmcnt(24)\n"
        "ds_max_i32 v25, v21 offset:0\n"
        "v_add_u32 v21, 2, v3\n"
        "s_waitcnt vmcnt(16)\n"
        "ds_max_i32 v25, v21 offset:0\n"
        "v_add_u32 v21, 3, v3\n"
        "s_waitcnt vmcnt(8)\n"
        "ds_max_i32 v25, v21 offset:0\n"
        "v_add_u32 v21, 4, v3\n"
        "s_waitcnt vmcnt(0)\n"
        "ds_max_i32 v25, v21 offset:0\n"
        "s_mov_b32 s38, 8388608\n"
        "W8_lf2:\n"
        "ds_read_b32 v20, v25 offset:776\n"
        "s_sub_u32 s38, s38, 1\n"
        "s_waitcnt lgkmcnt(0)\n"
        "s_cbranch_scc1 W8_lf2_x\n"
        "v_readfirstlane_b32 s39, v20\n"
        "s_cmp_ge_u32 s39, 533504\n"
        "s_cbranch_scc1 W8_lf2_x\n"
        "s_sleep 1\n"
        "s_branch W8_lf2\n"
        "W8_lf2_x:\n"
        "s_add_u32 s40, s42, 2048\n"
        "s_addc_u32 s41, s43, 0\n"
        "W8_ll2:\n"
        "s_mov_b32 m0, s34\n"
        "s_add_u32 s34, s34, 2048\n"
        "global_load_lds_dwordx4 v12, s[40:41] sc1\n"
        "global_load_lds_dwordx4 v12, s[40:41] offset:1024 sc1\n"
        "s_mov_b32 m0, s34\n"
        "s_add_u32 s34, s34, 2048\n"
        "global_load_lds_dwordx4 v13, s[40:41] sc1\n"
        "global_load_lds_dwordx4 v13, s[40:41] offset:1024 sc1\n"
        "s_mov_b32 m0, s34\n"
        "s_add_u32 s34, s34, 2048\n"
        "global_load_lds_dwordx4 v14, s[40:41] sc1\n"
        "global_load_lds_dwordx4 v14, s[40:41] offset:1024 sc1\n"
        "s_mov_b32 m0, s34\n"
        "s_add_u32 s34, s34, 2048\n"
        "global_load_lds_dwordx4 v15, s[40:41] sc1\n"
        "global_load_lds_dwordx4 v15, s[40:41] offset:1024 sc1\n"
        "s_add_u32 s40, s40, 24576\n"
        "s_addc_u32 s41, s41, 0\n"
        "v_add_u32 v3, 1, v3\n"
        "s_waitcnt vmcnt(32)\n"
        "ds_max_i32 v25, v3 offset:0\n"
        "s_cmp_lt_u32 s34, 98304\n"
        "s_cbranch_scc1 W8_ll2\n"
        "v_add_u32 v3, 1, v3\n"
        "s_waitcnt vmcnt(24)\n"
        "ds_max_i32 v25, v3 offset:0\n"
        "v_add_u32 v3, 1, v3\n"
        "s_waitcnt vmcnt(16)\n"
        "ds_max_i32 v25, v3 offset:0\n"
        "v_add_u32 v3, 1, v3\n"
        "s_waitcnt vmcnt(8)\n"
        "ds_max_i32 v25, v3 offset:0\n"
        "v_add_u32 v3, 1, v3\n"
        "s_waitcnt vmcnt(0)\n"
        "ds_max_i32 v25, v3 offset:0\n"
        "s_branch W8_fin\n"
        "W8_comp:\n"
        "s_setprio 3\n"
        "s_lshl_b32 s26, s21, 2\n"
        "s_add_u32 s26, s26, s20\n"
        "s_sub_u32 s26, s26, 4\n"
        "v_lshrrev_b32 v2, 4, v1\n"
        "v_and_b32 v3, 15, v1\n"
        "v_lshlrev_b32 v248, 11, v2\n"
        "v_lshl_add_u32 v248, v3, 4, v248\n"
        "v_add_u32 v249, 65536, v248\n"
        "v_mov_b32 v250, 133376\n"
        "v_lshlrev_b32 v240, 4, v1\n"
        "v_mul_u32_u24 v2, 28672, v2\n"
        "v_lshl_add_u32 v251, v3, 3, v2\n"
        "v_and_b32 v4, 7, v1\n"
        "v_lshl_add_u32 v252, v4, 3, v2\n"
        "v_bfe_u32 v4, v1, 3, 1\n"
        "v_lshl_add_u32 v252, v4, 2, v252\n"
        "s_mul_i32 s24, s22, 192\n"
        "s_sub_u32 s24, 6144, s24\n"
        "s_lshl_b32 s25, s22, 7\n"
        "s_sub_u32 s37, 3840, s25\n"
        "v_add_u32 v252, s24, v252\n"
        "v_lshlrev_b32 v253, 5, v3\n"
        "v_add_u32 v253, 131072, v253\n"
        "s_waitcnt lgkmcnt(0)\n"
        "s_lshl_b32 s27, s26, 15\n"
        "s_xor_b32 s24, s33, 0\n"
        "s_and_b32 s24, s24, 3\n"
        "s_lshl_b32 s24, s24, 11\n"
        "s_add_u32 s24, s24, s27\n"
        "s_add_u32 s56, s8, s24\n"
        "s_addc_u32 s57, s9, 0\n"
        "s_xor_b32 s24, s33, 1\n"
        "s_and_b32 s24, s24, 3\n"
        "s_lshl_b32 s24, s24, 11\n"
        "s_add_u32 s24, s24, s27\n"
        "s_add_u32 s58, s8, s24\n"
        "s_addc_u32 s59, s9, 0\n"
        "s_xor_b32 s24, s33, 2\n"
        "s_and_b32 s24, s24, 3\n"
        "s_lshl_b32 s24, s24, 11\n"
        "s_add_u32 s24, s24, s27\n"
        "s_add_u32 s60, s8, s24\n"
        "s_addc_u32 s61, s9, 0\n"
        "s_xor_b32 s24, s33, 3\n"
        "s_and_b32 s24, s24, 3\n"
        "s_lshl_b32 s24, s24, 11\n"
        "s_add_u32 s24, s24, s27\n"
        "s_add_u32 s62, s8, s24\n"
        "s_addc_u32 s63, s9, 0\n"
        "s_mul_i32 s24, s26, 229376\n"
        "s_mul_i32 s25, s22, 256\n"
        "s_add_u32 s24, s24, s25\n"
        "s_add_u32 s40, s10, s24\n"
        "s_addc_u32 s41, s11, 0\n"
        "s_add_u32 s42, s40, 7168\n"
        "s_addc_u32 s43, s41, 0\n"
        "s_add_u32 s44, s40, 14336\n"
        "s_addc_u32 s45, s41, 0\n"
        "s_add_u32 s46, s40, 21504\n"
        "s_addc_u32 s47, s41, 0\n"
        "s_add_u32 s48, s40, 114688\n"
        "s_addc_u32 s49, s41, 0\n"
        "s_add_u32 s50, s40, 121856\n"
        "s_addc_u32 s51, s41, 0\n"
        "s_add_u32 s52, s40, 129024\n"
        "s_addc_u32 s53, s41, 0\n"
        "s_add_u32 s54, s40, 136192\n"
        "s_addc_u32 s55, s41, 0\n"
        "s_sub_u32 s28, s20, 4\n"
        "s_mul_i32 s24, s21, 512\n"
        "s_mul_i32 s25, s22, 8\n"
        "s_add_u32 s24, s24, s25\n"
        "s_add_u32 s24, s24, 6029312\n"
        "s_mul_i32 s25, s22, 1024\n"
        "s_mul_i32 s27, s21, 8\n"
        "s_add_u32 s25, s25, s27\n"
        "s_add_u32 s25, s25, 6291456\n"
        "s_sub_u32 s25, s25, s24\n"
        "v_mul_lo_u32 v24, v1, s25\n"
        "s_mul_i32 s25, s28, 128\n"
        "s_add_u32 s24, s24, s25\n"
        "v_add_u32 v24, s24, v24\n"
        "s_mul_i32 s29, s28, 5120\n"
        "s_add_u32 s29, s29, 135232\n"
        "v_and_b32 v6, 7, v1\n"
        "v_lshrrev_b32 v7, 3, v1\n"
        "v_lshrrev_b32 v8, 4, v1\n"
        "v_lshl_add_u32 v8, v8, 4, s29\n"
        "v_lshl_add_u32 v8, v3, 6, v8\n"
        "v_add_u32 v9, s29, v240\n"
        "s_and_b32 s24, s22, 3\n"
        "s_lshl2_add_u32 s24, s21, s24\n"
        "s_lshl2_add_u32 s24, s24, s28\n"
        "s_lshr_b32 s25, s22, 2\n"
        "s_lshl2_add_u32 s24, s24, s25\n"
        "s_lshl_b32 s24, s24, 11\n"
        "v_add_u32 v11, s24, v240\n"
        "v_lshrrev_b32 v10, 5, v1\n"
        "s_lshl_b32 s24, s22, 2\n"
        "s_add_u32 s24, s24, s28\n"
        "s_lshl_b32 s24, s24, 4\n"
        "s_add_u32 s24, s24, s21\n"
        "s_lshl_b32 s24, s24, 11\n"
        "s_add_u32 s24, s24, 2097152\n"
        "v_add_u32 v13, s24, v240\n"
        "s_lshl_b32 s24, s22, 4\n"
        "s_add_u32 s24, s24, s21\n"
        "s_lshl_b32 s24, s24, 10\n"
        "s_add_u32 s24, s24, 5767168\n"
        "v_and_b32 v14, 31, v1\n"
        "v_lshl_add_u32 v14, v14, 4, s24\n"
        "v_lshl_add_u32 v14, v10, 3, v14\n"
        "v_lshlrev_b32 v15, 3, v1\n"
        "v_add_u32 v15, 154944, v15\n"
        "s_add_u32 s28, s28, 1\n"
        "s_mov_b32 s38, 8388608\n"
        "W8_tgc:\n"
        "ds_read_b32 v12, v250 offset:772\n"
        "s_sub_u32 s38, s38, 1\n"
        "s_waitcnt lgkmcnt(0)\n"
        "s_cbranch_scc1 W8_tgc_x\n"
        "v_readfirstlane_b32 s39, v12\n"
        "s_cmp_ge_u32 s39, 1\n"
        "s_cbranch_scc1 W8_tgc_x\n"
        "s_sleep 1\n"
        "s_branch W8_tgc\n"
        "W8_tgc_x:\n"
        "v_lshrrev_b32 v12, 0, v1\n"
        "v_lshlrev_b32 v12, 7, v12\n"
        "v_add_u32 v12, 134408, v12\n"
        "s_mov_b64 exec, 3\n"
        "ds_read_b64 v[26:27], v12\n"
        "s_mov_b64 exec, -1\n"
        "s_mov_b32 s38, 8388608\n"
        "W8_pkc:\n"
        "ds_read_b32 v12, v250 offset:1292\n"
        "s_sub_u32 s38, s38, 1\n"
        "s_waitcnt lgkmcnt(0)\n"
        "s_cbranch_scc1 W8_pkc_x\n"
        "v_readfirstlane_b32 s39, v12\n"
        "s_cmp_ge_u32 s39, s28\n"
        "s_cbranch_scc1 W8_pkc_x\n"
        "s_sleep 1\n"
        "s_branch W8_pkc\n"
        "W8_pkc_x:\n"
        "ds_read_b128 v[28:31], v8\n"
        "ds_read_b128 v[32:35], v8 offset:1024\n"
        "ds_read_b128 v[36:39], v9 offset:2304\n"
        "ds_read_b128 v[40:43], v9 offset:3328\n"
        "ds_read_b64 v[44:45], v15\n"
        "s_waitcnt lgkmcnt(3)\n"
        "global_store_dwordx4 v11, v[28:31], s[8:9] offset:0 sc1\n"
        "global_store_dwordx4 v11, v[32:35], s[8:9] offset:1024 sc1\n"
        "s_waitcnt lgkmcnt(0)\n"
        "global_store_dwordx4 v13, v[36:39], s[8:9] sc1\n"
        "global_store_dwordx4 v13, v[40:43], s[8:9] offset:1024 sc1\n"
        "s_cmp_lg_u32 s28, 4\n"
        "s_cbranch_scc1 W8_ncs\n"
        "global_store_dwordx2 v14, v[44:45], s[8:9] sc1\n"
        "W8_ncs:\n"
        "s_mov_b64 exec, 3\n"
        "s_waitcnt vmcnt(0)\n"
        "global_atomic_swap_x2 v24, v[26:27], s[8:9] sc1\n"
        "s_mov_b64 exec, -1\n"
        "s_mov_b32 s38, 8388608\n"
        "W8_cgo:\n"
        "ds_read_b32 v254, v250 offset:260\n"
        "s_sub_u32 s38, s38, 1\n"
        "s_waitcnt lgkmcnt(0)\n"
        "s_cbranch_scc1 W8_cgo_x\n"
        "v_readfirstlane_b32 s39, v254\n"
        "s_cmp_ge_u32 s39, 1\n"
        "s_cbranch_scc1 W8_cgo_x\n"
        "s_sleep 2\n"
        "s_branch W8_cgo\n"
        "W8_cgo_x:\n"
        "s_mov_b32 s38, 8388608\n"
        "W8_rd0:\n"
        "ds_read_b32 v254, v250 offset:4\n"
        "s_sub_u32 s38, s38, 1\n"
        "s_waitcnt lgkmcnt(0)\n"
        "s_cbranch_scc1 W8_rd0_x\n"
        "v_readfirstlane_b32 s39, v254\n"
        "s_cmp_ge_u32 s39, 1\n"
        "s_cbranch_scc1 W8_rd0_x\n"
        "s_sleep 1\n"
        "s_branch W8_rd0\n"
        "W8_rd0_x:\n"
        "global_load_dwordx4 v[0:3], v240, s[56:57] offset:0 sc1\n"
        "global_load_dwordx4 v[4:7], v240, s[56:57] offset:1024 sc1\n"
        "s_sleep 2\n"
        "s_cmp_ge_u32 s39, 2\n"
        "s_cbranch_scc1 W8_rd1_x\n"
        "s_mov_b32 s38, 8388608\n"
        "W8_rd1:\n"
        "ds_read_b32 v254, v250 offset:4\n"
        "s_sub_u32 s38, s38, 1\n"
        "s_waitcnt lgkmcnt(0)\n"
        "s_cbranch_scc1 W8_rd1_x\n"
        "v_readfirstlane_b32 s39, v254\n"
        "s_cmp_ge_u32 s39, 2\n"
        "s_cbranch_scc1 W8_rd1_x\n"
        "s_sleep 1\n"
        "s_branch W8_rd1\n"
        "W8_rd1_x:\n"
        "global_load_dwordx4 v[8:11], v240, s[58:59] offset:0 sc1\n"
        "global_load_dwordx4 v[12:15], v240, s[58:59] offset:1024 sc1\n"
        "W8_dwx:\n"
        "ds_read_b32 v254, v250 offset:0\n"
        "s_waitcnt lgkmcnt(0)\n"
        "v_cmp_gt_u32 vcc, 1, v254\n"
        "s_cbranch_vccnz W8_s0\n"
        "W8_c0:\n"
        "ds_read_b32 v241, v250 offset:4\n"
        "ds_read_b128 v[208:211], v248 offset:0\n"
        "ds_read_b128 v[212:215], v248 offset:1024\n"
        "ds_read_b128 v[216:219], v248 offset:512\n"
        "ds_read_b128 v[220:223], v248 offset:1536\n"
        "ds_read_b128 v[224:227], v248 offset:256\n"
        "ds_read_b128 v[228:231], v248 offset:1280\n"
        "s_waitcnt vmcnt(2)\n"
        "s_waitcnt lgkmcnt(5)\n"
        "v_mfma_i32_16x16x64_i8 v[128:131], v[0:3], v[208:211], 0\n"
        "v_cmp_gt_u32 vcc, 4, v241\n"
        "s_cbranch_vccnz W8_sZ\n"
        "W8_cZ:\n"
        "ds_read_b32 v254, v250 offset:0\n"
        "v_mfma_i32_16x16x64_i8 v[132:135], v[4:7], v[208:211], 0\n"
        "ds_read_b128 v[232:235], v248 offset:768\n"
        "s_waitcnt lgkmcnt(6)\n"
        "v_mfma_i32_16x16x64_i8 v[136:139], v[0:3], v[212:215], 0\n"
        "global_load_dwordx4 v[16:19], v240, s[60:61] offset:0 sc1\n"
        "v_mfma_i32_16x16x64_i8 v[140:143], v[4:7], v[212:215], 0\n"
        "ds_read_b128 v[236:239], v248 offset:1792\n"
        "s_waitcnt lgkmcnt(2)\n"
        "v_cmp_gt_u32 vcc, 2, v254\n"
        "v_mfma_i32_16x16x64_i8 v[144:147], v[0:3], v[216:219], 0\n"
        "s_cbranch_vccnz W8_s1\n"
        "W8_c1:\n"
        "v_mfma_i32_16x16x64_i8 v[148:151], v[4:7], v[216:219], 0\n"
        "v_cmp_gt_u32 vcc, 5, v241\n"
        "ds_read_b128 v[208:211], v248 offset:8192\n"
        "v_mfma_i32_16x16x64_i8 v[152:155], v[0:3], v[220:223], 0\n"
        "s_cbranch_vccnz W8_sR0\n"
        "W8_cR0:\n"
        "ds_read_b32 v254, v250 offset:0\n"
        "v_mfma_i32_16x16x64_i8 v[156:159], v[4:7], v[220:223], 0\n"
        "ds_read_b128 v[212:215], v248 offset:9216\n"
        "v_mfma_i32_16x16x64_i8 v[160:163], v[0:3], v[224:227], 0\n"
        "ds_read_b32 v241, v250 offset:4\n"
        "global_load_dwordx4 v[20:23], v240, s[60:61] offset:1024 sc1\n"
        "v_mfma_i32_16x16x64_i8 v[164:167], v[4:7], v[224:227], 0\n"
        "ds_read_b128 v[216:219], v248 offset:8704\n"
        "v_mfma_i32_16x16x64_i8 v[168:171], v[0:3], v[228:231], 0\n"
        "global_load_dwordx4 v[24:27], v240, s[62:63] offset:0 sc1\n"
        "v_mfma_i32_16x16x64_i8 v[172:175], v[4:7], v[228:231], 0\n"
        "ds_read_b128 v[220:223], v248 offset:9728\n"
        "s_waitcnt lgkmcnt(7)\n"
        "v_mfma_i32_16x16x64_i8 v[176:179], v[0:3], v[232:235], 0\n"
        "global_load_dwordx4 v[28:31], v240, s[62:63] offset:1024 sc1\n"
        "v_add_u32 v240, 8192, v240\n"
        "v_mfma_i32_16x16x64_i8 v[180:183], v[4:7], v[232:235], 0\n"
        "ds_read_b128 v[224:227], v248 offset:8448\n"
        "s_waitcnt lgkmcnt(7)\n"
        "v_mfma_i32_16x16x64_i8 v[184:187], v[0:3], v[236:239], 0\n"
        "v_mfma_i32_16x16x64_i8 v[188:191], v[4:7], v[236:239], 0\n"
        "ds_read_b128 v[228:231], v248 offset:9472\n"
        "s_waitcnt vmcnt(4)\n"
        "s_waitcnt lgkmcnt(7)\n"
        "v_mfma_i32_16x16x64_i8 v[128:131], v[8:11], v[208:211], v[128:131]\n"
        "v_mfma_i32_16x16x64_i8 v[132:135], v[12:15], v[208:211], v[132:135]\n"
        "ds_read_b128 v[232:235], v248 offset:8960\n"
        "s_waitcnt lgkmcnt(6)\n"
        "v_mfma_i32_16x16x64_i8 v[136:139], v[8:11], v[212:215], v[136:139]\n"
        "global_load_dwordx4 v[32:35], v240, s[56:57] offset:0 sc1\n"
        "v_mfma_i32_16x16x64_i8 v[140:143], v[12:15], v[212:215], v[140:143]\n"
        "ds_read_b128 v[236:239], v248 offset:9984\n"
        "v_cmp_gt_u32 vcc, 3, v254\n"
        "s_waitcnt lgkmcnt(5)\n"
        "v_mfma_i32_16x16x64_i8 v[144:147], v[8:11], v[216:219], v[144:147]\n"
        "s_cbranch_vccnz W8_s2\n"
        "W8_c2:\n"
        "v_mfma_i32_16x16x64_i8 v[148:151], v[12:15], v[216:219], v[148:151]\n"
        "v_cmp_gt_u32 vcc, 6, v241\n"
        "ds_read_b128 v[208:211], v248 offset:16384\n"
        "s_waitcnt lgkmcnt(5)\n"
        "v_mfma_i32_16x16x64_i8 v[152:155], v[8:11], v[220:223], v[152:155]\n"
        "s_cbranch_vccnz W8_sR1\n"
        "W8_cR1:\n"
        "ds_read_b32 v254, v250 offset:0\n"
        "v_mfma_i32_16x16x64_i8 v[156:159], v[12:15], v[220:223], v[156:159]\n"
        "ds_read_b128 v[212:215], v248 offset:17408\n"
        "s_waitcnt lgkmcnt(6)\n"
        "v_mfma_i32_16x16x64_i8 v[160:163], v[8:11], v[224:227], v[160:163]\n"
        "ds_read_b32 v241, v250 offset:4\n"
        "global_load_dwordx4 v[36:39], v240, s[56:57] offset:1024 sc1\n"
        "v_mfma_i32_16x16x64_i8 v[164:167], v[12:15], v[224:227], v[164:167]\n"
        "ds_read_b128 v[216:219], v248 offset:16896\n"
        "s_waitcnt lgkmcnt(7)\n"
        "v_mfma_i32_16x16x64_i8 v[168:171], v[8:11], v[228:231], v[168:171]\n"
        "v_mfma_i32_16x16x64_i8 v[172:175], v[12:15], v[228:231], v[172:175]\n"
        "ds_read_b128 v[220:223], v248 offset:17920\n"
        "s_waitcnt lgkmcnt(7)\n"
        "v_mfma_i32_16x16x64_i8 v[176:179], v[8:11], v[232:235], v[176:179]\n"
        "v_mfma_i32_16x16x64_i8 v[180:183], v[12:15], v[232:235], v[180:183]\n"
        "ds_read_b128 v[224:227], v248 offset:16640\n"
        "s_waitcnt lgkmcnt(7)\n"
        "v_mfma_i32_16x16x64_i8 v[184:187], v[8:11], v[236:239], v[184:187]\n"
        "global_load_dwordx4 v[40:43], v240, s[58:59] offset:0 sc1\n"
        "v_mfma_i32_16x16x64_i8 v[188:191], v[12:15], v[236:239], v[188:191]\n"
        "ds_read_b128 v[228:231], v248 offset:17664\n"
        "s_waitcnt vmcnt(5)\n"
        "s_waitcnt lgkmcnt(7)\n"
        "v_mfma_i32_16x16x64_i8 v[128:131], v[16:19], v[208:211], v[128:131]\n"
        "v_mfma_i32_16x16x64_i8 v[132:135], v[20:23], v[208:211], v[132:135]\n"
        "ds_read_b128 v[232:235], v248 offset:17152\n"
        "s_waitcnt lgkmcnt(6)\n"
        "v_mfma_i32_16x16x64_i8 v[136:139], v[16:19], v[212:215], v[136:139]\n"
        "v_mfma_i32_16x16x64_i8 v[140:143], v[20:23], v[212:215], v[140:143]\n"
        "ds_read_b128 v[236:239], v248 offset:18176\n"
        "v_cmp_gt_u32 vcc, 4, v254\n"
        "s_waitcnt lgkmcnt(5)\n"
        "v_mfma_i32_16x16x64_i8 v[144:147], v[16:19], v[216:219], v[144:147]\n"
        "s_cbranch_vccnz W8_s3\n"
        "W8_c3:\n"
        "v_mfma_i32_16x16x64_i8 v[148:151], v[20:23], v[216:219], v[148:151]\n"
        "v_cmp_gt_u32 vcc, 7, v241\n"
        "ds_read_b128 v[208:211], v248 offset:24576\n"
        "s_waitcnt lgkmcnt(5)\n"
        "v_mfma_i32_16x16x64_i8 v[152:155], v[16:19], v[220:223], v[152:155]\n"
        "s_cbranch_vccnz W8_sR2\n"
        "W8_cR2:\n"
        "ds_read_b32 v254, v250 offset:0\n"
        "v_mfma_i32_16x16x64_i8 v[156:159], v[20:23], v[220:223], v[156:159]\n"
        "ds_read_b128 v[212:215], v248 offset:25600\n"
        "s_waitcnt lgkmcnt(6)\n"
        "v_mfma_i32_16x16x64_i8 v[160:163], v[16:19], v[224:227], v[160:163]\n"
        "ds_read_b32 v241, v250 offset:4\n"
        "global_load_dwordx4 v[44:47], v240, s[58:59] offset:1024 sc1\n"
        "v_mfma_i32_16x16x64_i8 v[164:167], v[20:23], v[224:227], v[164:167]\n"
        "ds_read_b128 v[216:219], v248 offset:25088\n"
        "s_waitcnt lgkmcnt(7)\n"
        "v_mfma_i32_16x16x64_i8 v[168:171], v[16:19], v[228:231], v[168:171]\n"
        "v_mfma_i32_16x16x64_i8 v[172:175], v[20:23], v[228:231], v[172:175]\n"
        "ds_read_b128 v[220:223], v248 offset:26112\n"
        "s_waitcnt lgkmcnt(7)\n"
        "v_mfma_i32_16x16x64_i8 v[176:179], v[16:19], v[232:235], v[176:179]\n"
        "v_mfma_i32_16x16x64_i8 v[180:183], v[20:23], v[232:235], v[180:183]\n"
        "ds_read_b128 v[224:227], v248 offset:24832\n"
        "s_waitcnt lgkmcnt(7)\n"
        "v_mfma_i32_16x16x64_i8 v[184:187], v[16:19], v[236:239], v[184:187]\n"
        "global_load_dwordx4 v[48:51], v240, s[60:61] offset:0 sc1\n"
        "v_mfma_i32_16x16x64_i8 v[188:191], v[20:23], v[236:239], v[188:191]\n"
        "ds_read_b128 v[228:231], v248 offset:25856\n"
        "s_waitcnt vmcnt(5)\n"
        "s_waitcnt lgkmcnt(7)\n"
        "v_mfma_i32_16x16x64_i8 v[128:131], v[24:27], v[208:211], v[128:131]\n"
        "v_mfma_i32_16x16x64_i8 v[132:135], v[28:31], v[208:211], v[132:135]\n"
        "ds_read_b128 v[232:235], v248 offset:25344\n"
        "s_waitcnt lgkmcnt(6)\n"
        "v_mfma_i32_16x16x64_i8 v[136:139], v[24:27], v[212:215], v[136:139]\n"
        "v_mfma_i32_16x16x64_i8 v[140:143], v[28:31], v[212:215], v[140:143]\n"
        "ds_read_b128 v[236:239], v248 offset:26368\n"
        "v_cmp_gt_u32 vcc, 5, v254\n"
        "s_waitcnt lgkmcnt(5)\n"
        "v_mfma_i32_16x16x64_i8 v[144:147], v[24:27], v[216:219], v[144:147]\n"
        "s_cbranch_vccnz W8_s4\n"
        "W8_c4:\n"
        "v_mfma_i32_16x16x64_i8 v[148:151], v[28:31], v[216:219], v[148:151]\n"
        "v_cmp_gt_u32 vcc, 8, v241\n"
        "ds_read_b128 v[208:211], v248 offset:32768\n"
        "s_waitcnt lgkmcnt(5)\n"
        "v_mfma_i32_16x16x64_i8 v[152:155], v[24:27], v[220:223], v[152:155]\n"
        "s_cbranch_vccnz W8_sR3\n"
        "W8_cR3:\n"
        "ds_read_b32 v254, v250 offset:0\n"
        "v_mfma_i32_16x16x64_i8 v[156:159], v[28:31], v[220:223], v[156:159]\n"
        "ds_read_b128 v[212:215], v248 offset:33792\n"
        "s_waitcnt lgkmcnt(6)\n"
        "v_mfma_i32_16x16x64_i8 v[160:163], v[24:27], v[224:227], v[160:163]\n"
        "ds_read_b32 v241, v250 offset:4\n"
        "s_mov_b64 exec, 1\n"
        "ds_add_u32 v250, v250 offset:516\n"
        "s_mov_b64 exec, -1\n"
        "v_mfma_i32_16x16x64_i8 v[164:167], v[28:31], v[224:227], v[164:167]\n"
        "ds_read_b128 v[216:219], v248 offset:33280\n"
        "s_waitcnt lgkmcnt(8)\n"
        "v_mfma_i32_16x16x64_i8 v[168:171], v[24:27], v[228:231], v[168:171]\n"
        "global_load_dwordx4 v[52:55], v240, s[60:61] offset:1024 sc1\n"
        "v_mfma_i32_16x16x64_i8 v[172:175], v[28:31], v[228:231], v[172:175]\n"
        "ds_read_b128 v[220:223], v248 offset:34304\n"
        "s_waitcnt lgkmcnt(8)\n"
        "v_mfma_i32_16x16x64_i8 v[176:179], v[24:27], v[232:235], v[176:179]\n"
        "v_mfma_i32_16x16x64_i8 v[180:183], v[28:31], v[232:235], v[180:183]\n"
        "ds_read_b128 v[224:227], v248 offset:33024\n"
        "s_waitcnt lgkmcnt(8)\n"
        "v_mfma_i32_16x16x64_i8 v[184:187], v[24:27], v[236:239], v[184:187]\n"
        "v_mfma_i32_16x16x64_i8 v[188:191], v[28:31], v[236:239], v[188:191]\n"
        "ds_read_b128 v[228:231], v248 offset:34048\n"
        "s_waitcnt vmcnt(4)\n"
        "s_waitcnt lgkmcnt(8)\n"
        "v_mfma_i32_16x16x64_i8 v[128:131], v[32:35], v[208:211], v[128:131]\n"
        "global_load_dwordx4 v[56:59], v240, s[62:63] offset:0 sc1\n"
        "v_mfma_i32_16x16x64_i8 v[132:135], v[36:39], v[208:211], v[132:135]\n"
        "ds_read_b128 v[232:235], v248 offset:33536\n"
        "s_waitcnt lgkmcnt(7)\n"
        "v_mfma_i32_16x16x64_i8 v[136:139], v[32:35], v[212:215], v[136:139]\n"
        "v_mfma_i32_16x16x64_i8 v[140:143], v[36:39], v[212:215], v[140:143]\n"
        "ds_read_b128 v[236:239], v248 offset:34560\n"
        "v_cmp_gt_u32 vcc, 6, v254\n"
        "s_waitcnt lgkmcnt(5)\n"
        "v_mfma_i32_16x16x64_i8 v[144:147], v[32:35], v[216:219], v[144:147]\n"
        "s_cbranch_vccnz W8_s5\n"
        "W8_c5:\n"
        "v_mfma_i32_16x16x64_i8 v[148:151], v[36:39], v[216:219], v[148:151]\n"
        "v_cmp_gt_u32 vcc, 9, v241\n"
        "ds_read_b128 v[208:211], v248 offset:40960\n"
        "s_waitcnt lgkmcnt(5)\n"
        "v_mfma_i32_16x16x64_i8 v[152:155], v[32:35], v[220:223], v[152:155]\n"
        "s_cbranch_vccnz W8_sR4\n"
        "W8_cR4:\n"
        "ds_read_b32 v254, v250 offset:0\n"
        "v_mfma_i32_16x16x64_i8 v[156:159], v[36:39], v[220:223], v[156:159]\n"
        "ds_read_b128 v[212:215], v248 offset:41984\n"
        "s_waitcnt lgkmcnt(6)\n"
        "v_mfma_i32_16x16x64_i8 v[160:163], v[32:35], v[224:227], v[160:163]\n"
        "ds_read_b32 v241, v250 offset:4\n"
        "global_load_dwordx4 v[60:63], v240, s[62:63] offset:1024 sc1\n"
        "v_add_u32 v240, 8192, v240\n"
        "v_mfma_i32_16x16x64_i8 v[164:167], v[36:39], v[224:227], v[164:167]\n"
        "ds_read_b128 v[216:219], v248 offset:41472\n"
        "s_waitcnt lgkmcnt(7)\n"
        "v_mfma_i32_16x16x64_i8 v[168:171], v[32:35], v[228:231], v[168:171]\n"
        "v_mfma_i32_16x16x64_i8 v[172:175], v[36:39], v[228:231], v[172:175]\n"
        "ds_read_b128 v[220:223], v248 offset:42496\n"
        "s_waitcnt lgkmcnt(7)\n"
        "v_mfma_i32_16x16x64_i8 v[176:179], v[32:35], v[232:235], v[176:179]\n"
        "v_mfma_i32_16x16x64_i8 v[180:183], v[36:39], v[232:235], v[180:183]\n"
        "ds_read_b128 v[224:227], v248 offset:41216\n"
        "s_waitcnt lgkmcnt(7)\n"
        "v_mfma_i32_16x16x64_i8 v[184:187], v[32:35], v[236:239], v[184:187]\n"
        "global_load_dwordx4 v[64:67], v240, s[56:57] offset:0 sc1\n"
        "v_mfma_i32_16x16x64_i8 v[188:191], v[36:39], v[236:239], v[188:191]\n"
        "ds_read_b128 v[228:231], v248 offset:42240\n"
        "s_waitcnt vmcnt(5)\n"
        "s_waitcnt lgkmcnt(7)\n"
        "v_mfma_i32_16x16x64_i8 v[128:131], v[40:43], v[208:211], v[128:131]\n"
        "v_mfma_i32_16x16x64_i8 v[132:135], v[44:47], v[208:211], v[132:135]\n"
        "ds_read_b128 v[232:235], v248 offset:41728\n"
        "s_waitcnt lgkmcnt(6)\n"
        "v_mfma_i32_16x16x64_i8 v[136:139], v[40:43], v[212:215], v[136:139]\n"
        "v_mfma_i32_16x16x64_i8 v[140:143], v[44:47], v[212:215], v[140:143]\n"
        "ds_read_b128 v[236:239], v248 offset:42752\n"
        "v_cmp_gt_u32 vcc, 7, v254\n"
        "s_waitcnt lgkmcnt(5)\n"
        "v_mfma_i32_16x16x64_i8 v[144:147], v[40:43], v[216:219], v[144:147]\n"
        "s_cbranch_vccnz W8_s6\n"
        "W8_c6:\n"
        "v_mfma_i32_16x16x64_i8 v[148:151], v[44:47], v[216:219], v[148:151]\n"
        "v_cmp_gt_u32 vcc, 10, v241\n"
        "ds_read_b128 v[208:211], v248 offset:49152\n"
        "s_waitcnt lgkmcnt(5)\n"
        "v_mfma_i32_16x16x64_i8 v[152:155], v[40:43], v[220:223], v[152:155]\n"
        "s_cbranch_vccnz W8_sR5\n"
        "W8_cR5:\n"
        "ds_read_b32 v254, v250 offset:0\n"
        "v_mfma_i32_16x16x64_i8 v[156:159], v[44:47], v[220:223], v[156:159]\n"
        "ds_read_b128 v[212:215], v248 offset:50176\n"
        "s_waitcnt lgkmcnt(6)\n"
        "v_mfma_i32_16x16x64_i8 v[160:163], v[40:43], v[224:227], v[160:163]\n"
        "ds_read_b32 v241, v250 offset:4\n"
        "global_load_dwordx4 v[68:71], v240, s[56:57] offset:1024 sc1\n"
        "v_mfma_i32_16x16x64_i8 v[164:167], v[44:47], v[224:227], v[164:167]\n"
        "ds_read_b128 v[216:219], v248 offset:49664\n"
        "s_waitcnt lgkmcnt(7)\n"
        "v_mfma_i32_16x16x64_i8 v[168:171], v[40:43], v[228:231], v[168:171]\n"
        "v_mfma_i32_16x16x64_i8 v[172:175], v[44:47], v[228:231], v[172:175]\n"
        "ds_read_b128 v[220:223], v248 offset:50688\n"
        "s_waitcnt lgkmcnt(7)\n"
        "v_mfma_i32_16x16x64_i8 v[176:179], v[40:43], v[232:235], v[176:179]\n"
        "v_mfma_i32_16x16x64_i8 v[180:183], v[44:47], v[232:235], v[180:183]\n"
        "ds_read_b128 v[224:227], v248 offset:49408\n"
        "s_waitcnt lgkmcnt(7)\n"
        "v_mfma_i32_16x16x64_i8 v[184:187], v[40:43], v[236:239], v[184:187]\n"
        "global_load_dwordx4 v[72:75], v240, s[58:59] offset:0 sc1\n"
        "v_mfma_i32_16x16x64_i8 v[188:191], v[44:47], v[236:239], v[188:191]\n"
        "ds_read_b128 v[228:231], v248 offset:50432\n"
        "s_waitcnt vmcnt(5)\n"
        "s_waitcnt lgkmcnt(7)\n"
        "v_mfma_i32_16x16x64_i8 v[128:131], v[48:51], v[208:211], v[128:131]\n"
        "v_mfma_i32_16x16x64_i8 v[132:135], v[52:55], v[208:211], v[132:135]\n"
        "ds_read_b128 v[232:235], v248 offset:49920\n"
        "s_waitcnt lgkmcnt(6)\n"
        "v_mfma_i32_16x16x64_i8 v[136:139], v[48:51], v[212:215], v[136:139]\n"
        "v_mfma_i32_16x16x64_i8 v[140:143], v[52:55], v[212:215], v[140:143]\n"
        "ds_read_b128 v[236:239], v248 offset:50944\n"
        "v_cmp_gt_u32 vcc, 8, v254\n"
        "s_waitcnt lgkmcnt(5)\n"
        "v_mfma_i32_16x16x64_i8 v[144:147], v[48:51], v[216:219], v[144:147]\n"
        "s_cbranch_vccnz W8_s7\n"
        "W8_c7:\n"
        "v_mfma_i32_16x16x64_i8 v[148:151], v[52:55], v[216:219], v[148:151]\n"
        "v_cmp_gt_u32 vcc, 11, v241\n"
        "ds_read_b128 v[208:211], v248 offset:57344\n"
        "s_waitcnt lgkmcnt(5)\n"
        "v_mfma_i32_16x16x64_i8 v[152:155], v[48:51], v[220:223], v[152:155]\n"
        "s_cbranch_vccnz W8_sR6\n"
        "W8_cR6:\n"
        "ds_read_b32 v254, v250 offset:0\n"
        "v_mfma_i32_16x16x64_i8 v[156:159], v[52:55], v[220:223], v[156:159]\n"
        "ds_read_b128 v[212:215], v248 offset:58368\n"
        "s_waitcnt lgkmcnt(6)\n"
        "v_mfma_i32_16x16x64_i8 v[160:163], v[48:51], v[224:227], v[160:163]\n"
        "ds_read_b32 v241, v250 offset:4\n"
        "global_load_dwordx4 v[76:79], v240, s[58:59] offset:1024 sc1\n"
        "v_mfma_i32_16x16x64_i8 v[164:167], v[52:55], v[224:227], v[164:167]\n"
        "ds_read_b128 v[216:219], v248 offset:57856\n"
        "s_waitcnt lgkmcnt(7)\n"
        "v_mfma_i32_16x16x64_i8 v[168:171], v[48:51], v[228:231], v[168:171]\n"
        "v_mfma_i32_16x16x64_i8 v[172:175], v[52:55], v[228:231], v[172:175]\n"
        "ds_read_b128 v[220:223], v248 offset:58880\n"
        "s_waitcnt lgkmcnt(7)\n"
        "v_mfma_i32_16x16x64_i8 v[176:179], v[48:51], v[232:235], v[176:179]\n"
        "v_mfma_i32_16x16x64_i8 v[180:183], v[52:55], v[232:235], v[180:183]\n"
        "ds_read_b128 v[224:227], v248 offset:57600\n"
        "s_waitcnt lgkmcnt(7)\n"
        "v_mfma_i32_16x16x64_i8 v[184:187], v[48:51], v[236:239], v[184:187]\n"
        "global_load_dwordx4 v[80:83], v240, s[60:61] offset:0 sc1\n"
        "v_mfma_i32_16x16x64_i8 v[188:191], v[52:55], v[236:239], v[188:191]\n"
        "ds_read_b128 v[228:231], v248 offset:58624\n"
        "s_waitcnt vmcnt(5)\n"
        "s_waitcnt lgkmcnt(7)\n"
        "v_mfma_i32_16x16x64_i8 v[128:131], v[56:59], v[208:211], v[128:131]\n"
        "v_mfma_i32_16x16x64_i8 v[132:135], v[60:63], v[208:211], v[132:135]\n"
        "ds_read_b128 v[232:235], v248 offset:58112\n"
        "s_waitcnt lgkmcnt(6)\n"
        "v_mfma_i32_16x16x64_i8 v[136:139], v[56:59], v[212:215], v[136:139]\n"
        "v_mfma_i32_16x16x64_i8 v[140:143], v[60:63], v[212:215], v[140:143]\n"
        "ds_read_b128 v[236:239], v248 offset:59136\n"
        "v_cmp_gt_u32 vcc, 9, v254\n"
        "s_waitcnt lgkmcnt(5)\n"
        "v_mfma_i32_16x16x64_i8 v[144:147], v[56:59], v[216:219], v[144:147]\n"
        "s_cbranch_vccnz W8_s8\n"
        "W8_c8:\n"
        "v_mfma_i32_16x16x64_i8 v[148:151], v[60:63], v[216:219], v[148:151]\n"
        "v_cmp_gt_u32 vcc, 12, v241\n"
        "ds_read_b128 v[208:211], v249 offset:0\n"
        "s_waitcnt lgkmcnt(5)\n"
        "v_mfma_i32_16x16x64_i8 v[152:155], v[56:59], v[220:223], v[152:155]\n"
        "s_cbranch_vccnz W8_sR7\n"
        "W8_cR7:\n"
        "ds_read_b32 v254, v250 offset:0\n"
        "v_mfma_i32_16x16x64_i8 v[156:159], v[60:63], v[220:223], v[156:159]\n"
        "ds_read_b128 v[212:215], v249 offset:1024\n"
        "s_waitcnt lgkmcnt(6)\n"
        "v_mfma_i32_16x16x64_i8 v[160:163], v[56:59], v[224:227], v[160:163]\n"
        "ds_read_b32 v241, v250 offset:4\n"
        "s_mov_b64 exec, 1\n"
        "ds_add_u32 v250, v250 offset:520\n"
        "s_mov_b64 exec, -1\n"
        "v_mfma_i32_16x16x64_i8 v[164:167], v[60:63], v[224:227], v[164:167]\n"
        "ds_read_b128 v[216:219], v249 offset:512\n"
        "s_waitcnt lgkmcnt(8)\n"
        "v_mfma_i32_16x16x64_i8 v[168:171], v[56:59], v[228:231], v[168:171]\n"
        "global_load_dwordx4 v[84:87], v240, s[60:61] offset:1024 sc1\n"
        "v_mfma_i32_16x16x64_i8 v[172:175], v[60:63], v[228:231], v[172:175]\n"
        "ds_read_b128 v[220:223], v249 offset:1536\n"
        "s_waitcnt lgkmcnt(8)\n"
        "v_mfma_i32_16x16x64_i8 v[176:179], v[56:59], v[232:235], v[176:179]\n"
        "v_mfma_i32_16x16x64_i8 v[180:183], v[60:63], v[232:235], v[180:183]\n"
        "ds_read_b128 v[224:227], v249 offset:256\n"
        "s_waitcnt lgkmcnt(8)\n"
        "v_mfma_i32_16x16x64_i8 v[184:187], v[56:59], v[236:239], v[184:187]\n"
        "v_mfma_i32_16x16x64_i8 v[188:191], v[60:63], v[236:239], v[188:191]\n"
        "ds_read_b128 v[228:231], v249 offset:1280\n"
        "s_waitcnt vmcnt(4)\n"
        "s_waitcnt lgkmcnt(8)\n"
        "v_mfma_i32_16x16x64_i8 v[128:131], v[64:67], v[208:211], v[128:131]\n"
        "global_load_dwordx4 v[88:91], v240, s[62:63] offset:0 sc1\n"
        "v_mfma_i32_16x16x64_i8 v[132:135], v[68:71], v[208:211], v[132:135]\n"
        "ds_read_b128 v[232:235], v249 offset:768\n"
        "s_waitcnt lgkmcnt(7)\n"
        "v_mfma_i32_16x16x64_i8 v[136:139], v[64:67], v[212:215], v[136:139]\n"
        "v_mfma_i32_16x16x64_i8 v[140:143], v[68:71], v[212:215], v[140:143]\n"
        "ds_read_b128 v[236:239], v249 offset:1792\n"
        "v_cmp_gt_u32 vcc, 10, v254\n"
        "s_waitcnt lgkmcnt(5)\n"
        "v_mfma_i32_16x16x64_i8 v[144:147], v[64:67], v[216:219], v[144:147]\n"
        "s_cbranch_vccnz W8_s9\n"
        "W8_c9:\n"
        "v_mfma_i32_16x16x64_i8 v[148:151], v[68:71], v[216:219], v[148:151]\n"
        "v_cmp_gt_u32 vcc, 13, v241\n"
        "ds_read_b128 v[208:211], v249 offset:8192\n"
        "s_waitcnt lgkmcnt(5)\n"
        "v_mfma_i32_16x16x64_i8 v[152:155], v[64:67], v[220:223], v[152:155]\n"
        "s_cbranch_vccnz W8_sR8\n"
        "W8_cR8:\n"
        "ds_read_b32 v254, v250 offset:0\n"
        "v_mfma_i32_16x16x64_i8 v[156:159], v[68:71], v[220:223], v[156:159]\n"
        "ds_read_b128 v[212:215], v249 offset:9216\n"
        "s_waitcnt lgkmcnt(6)\n"
        "v_mfma_i32_16x16x64_i8 v[160:163], v[64:67], v[224:227], v[160:163]\n"
        "ds_read_b32 v241, v250 offset:4\n"
        "global_load_dwordx4 v[92:95], v240, s[62:63] offset:1024 sc1\n"
        "v_add_u32 v240, 8192, v240\n"
        "v_mfma_i32_16x16x64_i8 v[164:167], v[68:71], v[224:227], v[164:167]\n"
        "ds_read_b128 v[216:219], v249 offset:8704\n"
        "s_waitcnt lgkmcnt(7)\n"
        "v_mfma_i32_16x16x64_i8 v[168:171], v[64:67], v[228:231], v[168:171]\n"
        "v_mfma_i32_16x16x64_i8 v[172:175], v[68:71], v[228:231], v[172:175]\n"
        "ds_read_b128 v[220:223], v249 offset:9728\n"
        "s_waitcnt lgkmcnt(7)\n"
        "v_mfma_i32_16x16x64_i8 v[176:179], v[64:67], v[232:235], v[176:179]\n"
        "v_mfma_i32_16x16x64_i8 v[180:183], v[68:71], v[232:235], v[180:183]\n"
        "ds_read_b128 v[224:227], v249 offset:8448\n"
        "s_waitcnt lgkmcnt(7)\n"
        "v_mfma_i32_16x16x64_i8 v[184:187], v[64:67], v[236:239], v[184:187]\n"
        "global_load_dwordx4 v[96:99], v240, s[56:57] offset:0 sc1\n"
        "v_mfma_i32_16x16x64_i8 v[188:191], v[68:71], v[236:239], v[188:191]\n"
        "ds_read_b128 v[228:231], v249 offset:9472\n"
        "s_waitcnt vmcnt(5)\n"
        "s_waitcnt lgkmcnt(7)\n"
        "v_mfma_i32_16x16x64_i8 v[128:131], v[72:75], v[208:211], v[128:131]\n"
        "v_mfma_i32_16x16x64_i8 v[132:135], v[76:79], v[208:211], v[132:135]\n"
        "ds_read_b128 v[232:235], v249 offset:8960\n"
        "s_waitcnt lgkmcnt(6)\n"
        "v_mfma_i32_16x16x64_i8 v[136:139], v[72:75], v[212:215], v[136:139]\n"
        "v_mfma_i32_16x16x64_i8 v[140:143], v[76:79], v[212:215], v[140:143]\n"
        "ds_read_b128 v[236:239], v249 offset:9984\n"
        "v_cmp_gt_u32 vcc, 11, v254\n"
        "s_waitcnt lgkmcnt(5)\n"
        "v_mfma_i32_16x16x64_i8 v[144:147], v[72:75], v[216:219], v[144:147]\n"
        "s_cbranch_vccnz W8_s10\n"
        "W8_c10:\n"
        "v_mfma_i32_16x16x64_i8 v[148:151], v[76:79], v[216:219], v[148:151]\n"
        "v_cmp_gt_u32 vcc, 14, v241\n"
        "ds_read_b128 v[208:211], v249 offset:16384\n"
        "s_waitcnt lgkmcnt(5)\n"
        "v_mfma_i32_16x16x64_i8 v[152:155], v[72:75], v[220:223], v[152:155]\n"
        "s_cbranch_vccnz W8_sR9\n"
        "W8_cR9:\n"
        "ds_read_b32 v254, v250 offset:0\n"
        "v_mfma_i32_16x16x64_i8 v[156:159], v[76:79], v[220:223], v[156:159]\n"
        "ds_read_b128 v[212:215], v249 offset:17408\n"
        "s_waitcnt lgkmcnt(6)\n"
        "v_mfma_i32_16x16x64_i8 v[160:163], v[72:75], v[224:227], v[160:163]\n"
        "ds_read_b32 v241, v250 offset:4\n"
        "global_load_dwordx4 v[100:103], v240, s[56:57] offset:1024 sc1\n"
        "v_mfma_i32_16x16x64_i8 v[164:167], v[76:79], v[224:227], v[164:167]\n"
        "ds_read_b128 v[216:219], v249 offset:16896\n"
        "s_waitcnt lgkmcnt(7)\n"
        "v_mfma_i32_16x16x64_i8 v[168:171], v[72:75], v[228:231], v[168:171]\n"
        "v_mfma_i32_16x16x64_i8 v[172:175], v[76:79], v[228:231], v[172:175]\n"
        "ds_read_b128 v[220:223], v249 offset:17920\n"
        "s_waitcnt lgkmcnt(7)\n"
        "v_mfma_i32_16x16x64_i8 v[176:179], v[72:75], v[232:235], v[176:179]\n"
        "v_mfma_i32_16x16x64_i8 v[180:183], v[76:79], v[232:235], v[180:183]\n"
        "ds_read_b128 v[224:227], v249 offset:16640\n"
        "s_waitcnt lgkmcnt(7)\n"
        "v_mfma_i32_16x16x64_i8 v[184:187], v[72:75], v[236:239], v[184:187]\n"
        "global_load_dwordx4 v[104:107], v240, s[58:59] offset:0 sc1\n"
        "v_mfma_i32_16x16x64_i8 v[188:191], v[76:79], v[236:239], v[188:191]\n"
        "ds_read_b128 v[228:231], v249 offset:17664\n"
        "s_waitcnt vmcnt(5)\n"
        "s_waitcnt lgkmcnt(7)\n"
        "v_mfma_i32_16x16x64_i8 v[128:131], v[80:83], v[208:211], v[128:131]\n"
        "v_mfma_i32_16x16x64_i8 v[132:135], v[84:87], v[208:211], v[132:135]\n"
        "ds_read_b128 v[232:235], v249 offset:17152\n"
        "s_waitcnt lgkmcnt(6)\n"
        "v_mfma_i32_16x16x64_i8 v[136:139], v[80:83], v[212:215], v[136:139]\n"
        "v_mfma_i32_16x16x64_i8 v[140:143], v[84:87], v[212:215], v[140:143]\n"
        "ds_read_b128 v[236:239], v249 offset:18176\n"
        "v_cmp_gt_u32 vcc, 12, v254\n"
        "s_waitcnt lgkmcnt(5)\n"
        "v_mfma_i32_16x16x64_i8 v[144:147], v[80:83], v[216:219], v[144:147]\n"
        "s_cbranch_vccnz W8_s11\n"
        "W8_c11:\n"
        "v_mfma_i32_16x16x64_i8 v[148:151], v[84:87], v[216:219], v[148:151]\n"
        "v_cmp_gt_u32 vcc, 15, v241\n"
        "ds_read_b128 v[208:211], v249 offset:24576\n"
        "s_waitcnt lgkmcnt(5)\n"
        "v_mfma_i32_16x16x64_i8 v[152:155], v[80:83], v[220:223], v[152:155]\n"
        "s_cbranch_vccnz W8_sR10\n"
        "W8_cR10:\n"
        "ds_read_b32 v254, v250 offset:0\n"
        "v_mfma_i32_16x16x64_i8 v[156:159], v[84:87], v[220:223], v[156:159]\n"
        "ds_read_b128 v[212:215], v249 offset:25600\n"
        "s_waitcnt lgkmcnt(6)\n"
        "v_mfma_i32_16x16x64_i8 v[160:163], v[80:83], v[224:227], v[160:163]\n"
        "ds_read_b32 v241, v250 offset:4\n"
        "global_load_dwordx4 v[108:111], v240, s[58:59] offset:1024 sc1\n"
        "v_mfma_i32_16x16x64_i8 v[164:167], v[84:87], v[224:227], v[164:167]\n"
        "ds_read_b128 v[216:219], v249 offset:25088\n"
        "s_waitcnt lgkmcnt(7)\n"
        "v_mfma_i32_16x16x64_i8 v[168:171], v[80:83], v[228:231], v[168:171]\n"
        "v_mfma_i32_16x16x64_i8 v[172:175], v[84:87], v[228:231], v[172:175]\n"
        "ds_read_b128 v[220:223], v249 offset:26112\n"
        "s_waitcnt lgkmcnt(7)\n"
        "v_mfma_i32_16x16x64_i8 v[176:179], v[80:83], v[232:235], v[176:179]\n"
        "v_mfma_i32_16x16x64_i8 v[180:183], v[84:87], v[232:235], v[180:183]\n"
        "ds_read_b128 v[224:227], v249 offset:24832\n"
        "s_waitcnt lgkmcnt(7)\n"
        "v_mfma_i32_16x16x64_i8 v[184:187], v[80:83], v[236:239], v[184:187]\n"
        "global_load_dwordx4 v[112:115], v240, s[60:61] offset:0 sc1\n"
        "v_mfma_i32_16x16x64_i8 v[188:191], v[84:87], v[236:239], v[188:191]\n"
        "ds_read_b128 v[228:231], v249 offset:25856\n"
        "s_waitcnt vmcnt(5)\n"
        "s_waitcnt lgkmcnt(7)\n"
        "v_mfma_i32_16x16x64_i8 v[128:131], v[88:91], v[208:211], v[128:131]\n"
        "v_mfma_i32_16x16x64_i8 v[132:135], v[92:95], v[208:211], v[132:135]\n"
        "ds_read_b128 v[232:235], v249 offset:25344\n"
        "s_waitcnt lgkmcnt(6)\n"
        "v_mfma_i32_16x16x64_i8 v[136:139], v[88:91], v[212:215], v[136:139]\n"
        "v_mfma_i32_16x16x64_i8 v[140:143], v[92:95], v[212:215], v[140:143]\n"
        "ds_read_b128 v[236:239], v249 offset:26368\n"
        "v_cmp_gt_u32 vcc, 13, v254\n"
        "s_waitcnt lgkmcnt(5)\n"
        "v_mfma_i32_16x16x64_i8 v[144:147], v[88:91], v[216:219], v[144:147]\n"
        "s_cbranch_vccnz W8_s12\n"
        "W8_c12:\n"
        "v_mfma_i32_16x16x64_i8 v[148:151], v[92:95], v[216:219], v[148:151]\n"
        "v_cmp_gt_u32 vcc, 16, v241\n"
        "ds_read_b128 v[208:211], v249 offset:32768\n"
        "s_waitcnt lgkmcnt(5)\n"
        "v_mfma_i32_16x16x64_i8 v[152:155], v[88:91], v[220:223], v[152:155]\n"
        "s_cbranch_vccnz W8_sR11\n"
        "W8_cR11:\n"
        "ds_read_b32 v254, v250 offset:0\n"
        "v_mfma_i32_16x16x64_i8 v[156:159], v[92:95], v[220:223], v[156:159]\n"
        "ds_read_b128 v[212:215], v249 offset:33792\n"
        "s_waitcnt lgkmcnt(6)\n"
        "v_mfma_i32_16x16x64_i8 v[160:163], v[88:91], v[224:227], v[160:163]\n"
        "s_mov_b64 exec, 1\n"
        "ds_add_u32 v250, v250 offset:524\n"
        "s_mov_b64 exec, -1\n"
        "v_mfma_i32_16x16x64_i8 v[164:167], v[92:95], v[224:227], v[164:167]\n"
        "ds_read_b128 v[216:219], v249 offset:33280\n"
        "s_waitcnt lgkmcnt(7)\n"
        "v_mfma_i32_16x16x64_i8 v[168:171], v[88:91], v[228:231], v[168:171]\n"
        "global_load_dwordx4 v[116:119], v240, s[60:61] offset:1024 sc1\n"
        "v_mfma_i32_16x16x64_i8 v[172:175], v[92:95], v[228:231], v[172:175]\n"
        "ds_read_b128 v[220:223], v249 offset:34304\n"
        "s_waitcnt lgkmcnt(7)\n"
        "v_mfma_i32_16x16x64_i8 v[176:179], v[88:91], v[232:235], v[176:179]\n"
        "v_mfma_i32_16x16x64_i8 v[180:183], v[92:95], v[232:235], v[180:183]\n"
        "ds_read_b128 v[224:227], v249 offset:33024\n"
        "s_waitcnt lgkmcnt(7)\n"
        "v_mfma_i32_16x16x64_i8 v[184:187], v[88:91], v[236:239], v[184:187]\n"
        "v_mfma_i32_16x16x64_i8 v[188:191], v[92:95], v[236:239], v[188:191]\n"
        "ds_read_b128 v[228:231], v249 offset:34048\n"
        "s_waitcnt vmcnt(4)\n"
        "s_waitcnt lgkmcnt(7)\n"
        "v_mfma_i32_16x16x64_i8 v[128:131], v[96:99], v[208:211], v[128:131]\n"
        "global_load_dwordx4 v[120:123], v240, s[62:63] offset:0 sc1\n"
        "v_mfma_i32_16x16x64_i8 v[132:135], v[100:103], v[208:211], v[132:135]\n"
        "ds_read_b128 v[232:235], v249 offset:33536\n"
        "s_waitcnt lgkmcnt(6)\n"
        "v_mfma_i32_16x16x64_i8 v[136:139], v[96:99], v[212:215], v[136:139]\n"
        "v_and_b32 v248, 0x3ff, v240\n"
        "v_mfma_i32_16x16x64_i8 v[140:143], v[100:103], v[212:215], v[140:143]\n"
        "ds_read_b128 v[236:239], v249 offset:34560\n"
        "v_cmp_gt_u32 vcc, 14, v254\n"
        "s_waitcnt lgkmcnt(5)\n"
        "v_mfma_i32_16x16x64_i8 v[144:147], v[96:99], v[216:219], v[144:147]\n"
        "s_cbranch_vccnz W8_s13\n"
        "W8_c13:\n"
        "v_mfma_i32_16x16x64_i8 v[148:151], v[100:103], v[216:219], v[148:151]\n"
        "ds_read_b128 v[208:211], v249 offset:40960\n"
        "s_waitcnt lgkmcnt(5)\n"
        "v_mfma_i32_16x16x64_i8 v[152:155], v[96:99], v[220:223], v[152:155]\n"
        "ds_read_b32 v254, v250 offset:0\n"
        "v_mfma_i32_16x16x64_i8 v[156:159], v[100:103], v[220:223], v[156:159]\n"
        "ds_read_b128 v[212:215], v249 offset:41984\n"
        "s_waitcnt lgkmcnt(6)\n"
        "v_mfma_i32_16x16x64_i8 v[160:163], v[96:99], v[224:227], v[160:163]\n"
        "global_load_dwordx4 v[124:127], v240, s[62:63] offset:1024 sc1\n"
        "v_add_u32 v240, 8192, v240\n"
        "v_mfma_i32_16x16x64_i8 v[164:167], v[100:103], v[224:227], v[164:167]\n"
        "ds_read_b128 v[216:219], v249 offset:41472\n"
        "s_waitcnt lgkmcnt(6)\n"
        "v_mfma_i32_16x16x64_i8 v[168:171], v[96:99], v[228:231], v[168:171]\n"
        "v_mfma_i32_16x16x64_i8 v[172:175], v[100:103], v[228:231], v[172:175]\n"
        "ds_read_b128 v[220:223], v249 offset:42496\n"
        "s_waitcnt lgkmcnt(6)\n"
        "v_mfma_i32_16x16x64_i8 v[176:179], v[96:99], v[232:235], v[176:179]\n"
        "v_mfma_i32_16x16x64_i8 v[180:183], v[100:103], v[232:235], v[180:183]\n"
        "ds_read_b128 v[224:227], v249 offset:41216\n"
        "s_waitcnt lgkmcnt(6)\n"
        "v_mfma_i32_16x16x64_i8 v[184:187], v[96:99], v[236:239], v[184:187]\n"
        "v_mfma_i32_16x16x64_i8 v[188:191], v[100:103], v[236:239], v[188:191]\n"
        "ds_read_b128 v[228:231], v249 offset:42240\n"
        "s_waitcnt vmcnt(4)\n"
        "s_waitcnt lgkmcnt(6)\n"
        "v_mfma_i32_16x16x64_i8 v[128:131], v[104:107], v[208:211], v[128:131]\n"
        "v_mfma_i32_16x16x64_i8 v[132:135], v[108:111], v[208:211], v[132:135]\n"
        "ds_read_b128 v[232:235], v249 offset:41728\n"
        "s_waitcnt lgkmcnt(5)\n"
        "v_mfma_i32_16x16x64_i8 v[136:139], v[104:107], v[212:215], v[136:139]\n"
        "v_mfma_i32_16x16x64_i8 v[140:143], v[108:111], v[212:215], v[140:143]\n"
        "ds_read_b128 v[236:239], v249 offset:42752\n"
        "v_cmp_gt_u32 vcc, 15, v254\n"
        "s_waitcnt lgkmcnt(5)\n"
        "v_mfma_i32_16x16x64_i8 v[144:147], v[104:107], v[216:219], v[144:147]\n"
        "s_cbranch_vccnz W8_s14\n"
        "W8_c14:\n"
        "v_mfma_i32_16x16x64_i8 v[148:151], v[108:111], v[216:219], v[148:151]\n"
        "ds_read_b128 v[208:211], v249 offset:49152\n"
        "s_waitcnt lgkmcnt(5)\n"
        "v_mfma_i32_16x16x64_i8 v[152:155], v[104:107], v[220:223], v[152:155]\n"
        "ds_read_b32 v254, v250 offset:0\n"
        "v_mfma_i32_16x16x64_i8 v[156:159], v[108:111], v[220:223], v[156:159]\n"
        "ds_read_b128 v[212:215], v249 offset:50176\n"
        "s_waitcnt lgkmcnt(6)\n"
        "v_mfma_i32_16x16x64_i8 v[160:163], v[104:107], v[224:227], v[160:163]\n"
        "v_mfma_i32_16x16x64_i8 v[164:167], v[108:111], v[224:227], v[164:167]\n"
        "ds_read_b128 v[216:219], v249 offset:49664\n"
        "s_waitcnt lgkmcnt(6)\n"
        "v_mfma_i32_16x16x64_i8 v[168:171], v[104:107], v[228:231], v[168:171]\n"
        "v_mfma_i32_16x16x64_i8 v[172:175], v[108:111], v[228:231], v[172:175]\n"
        "ds_read_b128 v[220:223], v249 offset:50688\n"
        "s_waitcnt lgkmcnt(6)\n"
        "v_mfma_i32_16x16x64_i8 v[176:179], v[104:107], v[232:235], v[176:179]\n"
        "v_mfma_i32_16x16x64_i8 v[180:183], v[108:111], v[232:235], v[180:183]\n"
        "ds_read_b128 v[224:227], v249 offset:49408\n"
        "s_waitcnt lgkmcnt(6)\n"
        "v_mfma_i32_16x16x64_i8 v[184:187], v[104:107], v[236:239], v[184:187]\n"
        "v_mfma_i32_16x16x64_i8 v[188:191], v[108:111], v[236:239], v[188:191]\n"
        "ds_read_b128 v[228:231], v249 offset:50432\n"
        "s_waitcnt vmcnt(2)\n"
        "s_waitcnt lgkmcnt(6)\n"
        "v_mfma_i32_16x16x64_i8 v[128:131], v[112:115], v[208:211], v[128:131]\n"
        "v_mfma_i32_16x16x64_i8 v[132:135], v[116:119], v[208:211], v[132:135]\n"
        "ds_read_b128 v[232:235], v249 offset:49920\n"
        "s_waitcnt lgkmcnt(5)\n"
        "v_mfma_i32_16x16x64_i8 v[136:139], v[112:115], v[212:215], v[136:139]\n"
        "v_mfma_i32_16x16x64_i8 v[140:143], v[116:119], v[212:215], v[140:143]\n"
        "ds_read_b128 v[236:239], v249 offset:50944\n"
        "v_cmp_gt_u32 vcc, 16, v254\n"
        "s_waitcnt lgkmcnt(5)\n"
        "v_mfma_i32_16x16x64_i8 v[144:147], v[112:115], v[216:219], v[144:147]\n"
        "s_cbranch_vccnz W8_s15\n"
        "W8_c15:\n"
        "v_mfma_i32_16x16x64_i8 v[148:151], v[116:119], v[216:219], v[148:151]\n"
        "ds_read_b128 v[208:211], v249 offset:57344\n"
        "s_waitcnt lgkmcnt(5)\n"
        "v_mfma_i32_16x16x64_i8 v[152:155], v[112:115], v[220:223], v[152:155]\n"
        "ds_read_b32 v254, v250 offset:0\n"
        "v_mfma_i32_16x16x64_i8 v[156:159], v[116:119], v[220:223], v[156:159]\n"
        "ds_read_b128 v[212:215], v249 offset:58368\n"
        "s_waitcnt lgkmcnt(6)\n"
        "v_mfma_i32_16x16x64_i8 v[160:163], v[112:115], v[224:227], v[160:163]\n"
        "v_mfma_i32_16x16x64_i8 v[164:167], v[116:119], v[224:227], v[164:167]\n"
        "ds_read_b128 v[216:219], v249 offset:57856\n"
        "s_waitcnt lgkmcnt(6)\n"
        "v_mfma_i32_16x16x64_i8 v[168:171], v[112:115], v[228:231], v[168:171]\n"
        "ds_read_b32 v255, v250 offset:260\n"
        "v_mfma_i32_16x16x64_i8 v[172:175], v[116:119], v[228:231], v[172:175]\n"
        "ds_read_b128 v[220:223], v249 offset:58880\n"
        "s_waitcnt lgkmcnt(7)\n"
        "v_mfma_i32_16x16x64_i8 v[176:179], v[112:115], v[232:235], v[176:179]\n"
        "v_mfma_i32_16x16x64_i8 v[180:183], v[116:119], v[232:235], v[180:183]\n"
        "ds_read_b128 v[224:227], v249 offset:57600\n"
        "s_waitcnt lgkmcnt(7)\n"
        "v_mfma_i32_16x16x64_i8 v[184:187], v[112:115], v[236:239], v[184:187]\n"
        "v_mfma_i32_16x16x64_i8 v[188:191], v[116:119], v[236:239], v[188:191]\n"
        "ds_read_b128 v[228:231], v249 offset:58624\n"
        "s_waitcnt vmcnt(0)\n"
        "s_waitcnt lgkmcnt(5)\n"
        "v_mfma_i32_16x16x64_i8 v[128:131], v[120:123], v[208:211], v[128:131]\n"
        "v_mfma_i32_16x16x64_i8 v[132:135], v[124:127], v[208:211], v[132:135]\n"
        "ds_read_b128 v[232:235], v249 offset:58112\n"
        "v_mfma_i32_16x16x64_i8 v[136:139], v[120:123], v[212:215], v[136:139]\n"
        "v_mfma_i32_16x16x64_i8 v[140:143], v[124:127], v[212:215], v[140:143]\n"
        "ds_read_b128 v[236:239], v249 offset:59136\n"
        "v_cmp_gt_u32 vcc, 18, v254\n"
        "s_waitcnt lgkmcnt(4)\n"
        "v_mfma_i32_16x16x64_i8 v[144:147], v[120:123], v[216:219], v[144:147]\n"
        "s_cbranch_vccnz W8_s16\n"
        "W8_c16:\n"
        "v_mfma_i32_16x16x64_i8 v[148:151], v[124:127], v[216:219], v[148:151]\n"
        "ds_read_b128 v[208:211], v248 offset:0\n"
        "v_mfma_i32_16x16x64_i8 v[152:155], v[120:123], v[220:223], v[152:155]\n"
        "v_mfma_i32_16x16x64_i8 v[156:159], v[124:127], v[220:223], v[156:159]\n"
        "ds_read_b128 v[212:215], v248 offset:1024\n"
        "s_waitcnt lgkmcnt(4)\n"
        "v_mfma_i32_16x16x64_i8 v[160:163], v[120:123], v[224:227], v[160:163]\n"
        "v_mfma_i32_16x16x64_i8 v[164:167], v[124:127], v[224:227], v[164:167]\n"
        "ds_read_b128 v[216:219], v248 offset:2048\n"
        "v_cmp_gt_u32 vcc, 2, v255\n"
        "v_mfma_i32_16x16x64_i8 v[168:171], v[120:123], v[228:231], v[168:171]\n"
        "v_add_u32 v249, 65536, v248\n"
        "s_cbranch_vccnz W8_sT\n"
        "W8_cT:\n"
        "v_mfma_i32_16x16x64_i8 v[172:175], v[124:127], v[228:231], v[172:175]\n"
        "ds_read_b128 v[220:223], v248 offset:3072\n"
        "s_waitcnt lgkmcnt(4)\n"
        "v_mfma_i32_16x16x64_i8 v[176:179], v[120:123], v[232:235], v[176:179]\n"
        "ds_read_b128 v[240:243], v253\n"
        "ds_read_b128 v[244:247], v253 offset:16\n"
        "v_mfma_i32_16x16x64_i8 v[180:183], v[124:127], v[232:235], v[180:183]\n"
        "ds_read_b128 v[224:227], v248 offset:4096\n"
        "v_mfma_i32_16x16x64_i8 v[184:187], v[120:123], v[236:239], v[184:187]\n"
        "v_cvt_f32_i32 v128, v128\n"
        "v_cvt_f32_i32 v129, v129\n"
        "v_cvt_f32_i32 v130, v130\n"
        "v_mfma_i32_16x16x64_i8 v[188:191], v[124:127], v[236:239], v[188:191]\n"
        "ds_read_b128 v[228:231], v248 offset:5120\n"
        "s_waitcnt lgkmcnt(6)\n"
        "v_mfma_i32_16x16x64_i8 v[192:195], v[0:3], v[208:211], 0\n"
        "v_cvt_f32_i32 v131, v131\n"
        "v_cvt_f32_i32 v136, v136\n"
        "v_mfma_i32_16x16x64_i8 v[196:199], v[4:7], v[208:211], 0\n"
        "ds_read_b128 v[232:235], v248 offset:6144\n"
        "v_cvt_f32_i32 v137, v137\n"
        "v_mfma_i32_16x16x64_i8 v[200:203], v[0:3], v[212:215], 0\n"
        "v_cvt_f32_i32 v138, v138\n"
        "v_cvt_f32_i32 v139, v139\n"
        "v_mfma_i32_16x16x64_i8 v[204:207], v[4:7], v[212:215], 0\n"
        "ds_read_b128 v[236:239], v248 offset:7168\n"
        "v_cvt_f32_i32 v144, v144\n"
        "s_waitcnt lgkmcnt(6)\n"
        "v_mfma_i32_16x16x64_i8 v[192:195], v[8:11], v[216:219], v[192:195]\n"
        "v_cvt_f32_i32 v145, v145\n"
        "v_cvt_f32_i32 v146, v146\n"
        "v_mfma_i32_16x16x64_i8 v[196:199], v[12:15], v[216:219], v[196:199]\n"
        "ds_read_b128 v[208:211], v248 offset:8192\n"
        "v_cvt_f32_i32 v147, v147\n"
        "v_mfma_i32_16x16x64_i8 v[200:203], v[8:11], v[220:223], v[200:203]\n"
        "ds_read_b32 v254, v250 offset:0\n"
        "v_cvt_f32_i32 v152, v152\n"
        "v_mfma_i32_16x16x64_i8 v[204:207], v[12:15], v[220:223], v[204:207]\n"
        "ds_read_b128 v[212:215], v248 offset:9216\n"
        "s_waitcnt lgkmcnt(5)\n"
        "v_mfma_i32_16x16x64_i8 v[192:195], v[16:19], v[224:227], v[192:195]\n"
        "v_cvt_f32_i32 v153, v153\n"
        "v_cvt_f32_i32 v154, v154\n"
        "v_cvt_f32_i32 v155, v155\n"
        "v_mfma_i32_16x16x64_i8 v[196:199], v[20:23], v[224:227], v[196:199]\n"
        "ds_read_b128 v[216:219], v248 offset:10240\n"
        "v_mfma_i32_16x16x64_i8 v[200:203], v[16:19], v[228:231], v[200:203]\n"
        "v_fma_f32 v128, v128, v240, v241\n"
        "v_fma_f32 v129, v129, v240, v241\n"
        "v_mfma_i32_16x16x64_i8 v[204:207], v[20:23], v[228:231], v[204:207]\n"
        "ds_read_b128 v[220:223], v248 offset:11264\n"
        "s_waitcnt lgkmcnt(5)\n"
        "v_mfma_i32_16x16x64_i8 v[192:195], v[24:27], v[232:235], v[192:195]\n"
        "v_fma_f32 v130, v130, v240, v241\n"
        "v_fma_f32 v131, v131, v240, v241\n"
        "v_mfma_i32_16x16x64_i8 v[196:199], v[28:31], v[232:235], v[196:199]\n"
        "ds_read_b128 v[224:227], v248 offset:12288\n"
        "v_fma_f32 v136, v136, v242, v243\n"
        "v_fma_f32 v137, v137, v242, v243\n"
        "v_mfma_i32_16x16x64_i8 v[200:203], v[24:27], v[236:239], v[200:203]\n"
        "v_fma_f32 v138, v138, v242, v243\n"
        "v_fma_f32 v139, v139, v242, v243\n"
        "v_mfma_i32_16x16x64_i8 v[204:207], v[28:31], v[236:239], v[204:207]\n"
        "ds_read_b128 v[228:231], v248 offset:13312\n"
        "s_waitcnt lgkmcnt(4)\n"
        "v_mfma_i32_16x16x64_i8 v[192:195], v[32:35], v[208:211], v[192:195]\n"
        "v_fma_f32 v144, v144, v244, v245\n"
        "v_fma_f32 v145, v145, v244, v245\n"
        "v_mfma_i32_16x16x64_i8 v[196:199], v[36:39], v[208:211], v[196:199]\n"
        "ds_read_b128 v[232:235], v248 offset:14336\n"
        "v_fma_f32 v146, v146, v244, v245\n"
        "v_mfma_i32_16x16x64_i8 v[200:203], v[32:35], v[212:215], v[200:203]\n"
        "v_fma_f32 v147, v147, v244, v245\n"
        "v_fma_f32 v152, v152, v246, v247\n"
        "v_fma_f32 v153, v153, v246, v247\n"
        "v_mfma_i32_16x16x64_i8 v[204:207], v[36:39], v[212:215], v[204:207]\n"
        "ds_read_b128 v[236:239], v248 offset:15360\n"
        "v_cmp_gt_u32 vcc, 20, v254\n"
        "s_waitcnt lgkmcnt(4)\n"
        "v_mfma_i32_16x16x64_i8 v[192:195], v[40:43], v[216:219], v[192:195]\n"
        "s_cbranch_vccnz W8_s18\n"
        "W8_c18:\n"
        "v_fma_f32 v154, v154, v246, v247\n"
        "v_mfma_i32_16x16x64_i8 v[196:199], v[44:47], v[216:219], v[196:199]\n"
        "ds_read_b128 v[208:211], v248 offset:16384\n"
        "v_fma_f32 v155, v155, v246, v247\n"
        "v_mfma_i32_16x16x64_i8 v[200:203], v[40:43], v[220:223], v[200:203]\n"
        "v_cvt_pk_bf16_f32 v128, v128, v136\n"
        "v_cvt_pk_bf16_f32 v136, v129, v137\n"
        "v_mfma_i32_16x16x64_i8 v[204:207], v[44:47], v[220:223], v[204:207]\n"
        "ds_read_b128 v[212:215], v248 offset:17408\n"
        "v_cvt_pk_bf16_f32 v130, v130, v138\n"
        "s_waitcnt lgkmcnt(4)\n"
        "v_mfma_i32_16x16x64_i8 v[192:195], v[48:51], v[224:227], v[192:195]\n"
        "v_cvt_pk_bf16_f32 v138, v131, v139\n"
        "v_cvt_pk_bf16_f32 v129, v144, v152\n"
        "v_mfma_i32_16x16x64_i8 v[196:199], v[52:55], v[224:227], v[196:199]\n"
        "ds_read_b128 v[216:219], v248 offset:18432\n"
        "global_store_dwordx2 v251, v[128:129], s[40:41] offset:0 sc1\n"
        "v_mfma_i32_16x16x64_i8 v[200:203], v[48:51], v[228:231], v[200:203]\n"
        "v_cvt_pk_bf16_f32 v137, v145, v153\n"
        "global_store_dwordx2 v251, v[136:137], s[42:43] offset:0 sc1\n"
        "v_mfma_i32_16x16x64_i8 v[204:207], v[52:55], v[228:231], v[204:207]\n"
        "ds_read_b128 v[220:223], v248 offset:19456\n"
        "s_waitcnt lgkmcnt(4)\n"
        "v_mfma_i32_16x16x64_i8 v[192:195], v[56:59], v[232:235], v[192:195]\n"
        "v_cvt_pk_bf16_f32 v131, v146, v154\n"
        "global_store_dwordx2 v251, v[130:131], s[44:45] offset:0 sc1\n"
        "v_cvt_pk_bf16_f32 v139, v147, v155\n"
        "v_mfma_i32_16x16x64_i8 v[196:199], v[60:63], v[232:235], v[196:199]\n"
        "ds_read_b128 v[224:227], v248 offset:20480\n"
        "global_store_dwordx2 v251, v[138:139], s[46:47] offset:0 sc1\n"
        "v_mfma_i32_16x16x64_i8 v[200:203], v[56:59], v[236:239], v[200:203]\n"
        "v_cvt_f32_i32 v132, v132\n"
        "v_cvt_f32_i32 v133, v133\n"
        "v_mfma_i32_16x16x64_i8 v[204:207], v[60:63], v[236:239], v[204:207]\n"
        "ds_read_b128 v[228:231], v248 offset:21504\n"
        "s_waitcnt lgkmcnt(4)\n"
        "v_mfma_i32_16x16x64_i8 v[192:195], v[64:67], v[208:211], v[192:195]\n"
        "v_cvt_f32_i32 v134, v134\n"
        "v_cvt_f32_i32 v135, v135\n"
        "v_mfma_i32_16x16x64_i8 v[196:199], v[68:71], v[208:211], v[196:199]\n"
        "ds_read_b128 v[232:235], v248 offset:22528\n"
        "v_cvt_f32_i32 v140, v140\n"
        "v_cvt_f32_i32 v141, v141\n"
        "v_mfma_i32_16x16x64_i8 v[200:203], v[64:67], v[212:215], v[200:203]\n"
        "v_cvt_f32_i32 v142, v142\n"
        "v_cvt_f32_i32 v143, v143\n"
        "v_mfma_i32_16x16x64_i8 v[204:207], v[68:71], v[212:215], v[204:207]\n"
        "ds_read_b128 v[236:239], v248 offset:23552\n"
        "s_waitcnt lgkmcnt(4)\n"
        "v_mfma_i32_16x16x64_i8 v[192:195], v[72:75], v[216:219], v[192:195]\n"
        "v_cvt_f32_i32 v148, v148\n"
        "v_cvt_f32_i32 v149, v149\n"
        "v_mfma_i32_16x16x64_i8 v[196:199], v[76:79], v[216:219], v[196:199]\n"
        "ds_read_b128 v[208:211], v248 offset:24576\n"
        "v_cvt_f32_i32 v150, v150\n"
        "v_mfma_i32_16x16x64_i8 v[200:203], v[72:75], v[220:223], v[200:203]\n"
        "ds_read_b32 v254, v250 offset:0\n"
        "v_cvt_f32_i32 v151, v151\n"
        "v_cvt_f32_i32 v156, v156\n"
        "v_mfma_i32_16x16x64_i8 v[204:207], v[76:79], v[220:223], v[204:207]\n"
        "ds_read_b128 v[212:215], v248 offset:25600\n"
        "s_waitcnt lgkmcnt(5)\n"
        "v_mfma_i32_16x16x64_i8 v[192:195], v[80:83], v[224:227], v[192:195]\n"
        "v_cvt_f32_i32 v157, v157\n"
        "v_cvt_f32_i32 v158, v158\n"
        "v_mfma_i32_16x16x64_i8 v[196:199], v[84:87], v[224:227], v[196:199]\n"
        "ds_read_b128 v[216:219], v248 offset:26624\n"
        "v_cvt_f32_i32 v159, v159\n"
        "v_mfma_i32_16x16x64_i8 v[200:203], v[80:83], v[228:231], v[200:203]\n"
        "v_fma_f32 v132, v132, v240, v241\n"
        "v_fma_f32 v133, v133, v240, v241\n"
        "v_mfma_i32_16x16x64_i8 v[204:207], v[84:87], v[228:231], v[204:207]\n"
        "ds_read_b128 v[220:223], v248 offset:27648\n"
        "v_fma_f32 v134, v134, v240, v241\n"
        "s_waitcnt lgkmcnt(5)\n"
        "v_mfma_i32_16x16x64_i8 v[192:195], v[88:91], v[232:235], v[192:195]\n"
        "v_fma_f32 v135, v135, v240, v241\n"
        "v_fma_f32 v140, v140, v242, v243\n"
        "v_mfma_i32_16x16x64_i8 v[196:199], v[92:95], v[232:235], v[196:199]\n"
        "ds_read_b128 v[224:227], v248 offset:28672\n"
        "v_fma_f32 v141, v141, v242, v243\n"
        "v_mfma_i32_16x16x64_i8 v[200:203], v[88:91], v[236:239], v[200:203]\n"
        "v_fma_f32 v142, v142, v242, v243\n"
        "v_fma_f32 v143, v143, v242, v243\n"
        "v_mfma_i32_16x16x64_i8 v[204:207], v[92:95], v[236:239], v[204:207]\n"
        "ds_read_b128 v[228:231], v248 offset:29696\n"
        "s_waitcnt lgkmcnt(4)\n"
        "v_mfma_i32_16x16x64_i8 v[192:195], v[96:99], v[208:211], v[192:195]\n"
        "v_fma_f32 v148, v148, v244, v245\n"
        "v_fma_f32 v149, v149, v244, v245\n"
        "v_fma_f32 v150, v150, v244, v245\n"
        "v_mfma_i32_16x16x64_i8 v[196:199], v[100:103], v[208:211], v[196:199]\n"
        "ds_read_b128 v[232:235], v248 offset:30720\n"
        "v_fma_f32 v151, v151, v244, v245\n"
        "v_mfma_i32_16x16x64_i8 v[200:203], v[96:99], v[212:215], v[200:203]\n"
        "v_fma_f32 v156, v156, v246, v247\n"
        "v_fma_f32 v157, v157, v246, v247\n"
        "v_mfma_i32_16x16x64_i8 v[204:207], v[100:103], v[212:215], v[204:207]\n"
        "ds_read_b128 v[236:239], v248 offset:31744\n"
        "v_cmp_gt_u32 vcc, 22, v254\n"
        "s_waitcnt lgkmcnt(4)\n"
        "v_mfma_i32_16x16x64_i8 v[192:195], v[104:107], v[216:219], v[192:195]\n"
        "s_cbranch_vccnz W8_s20\n"
        "W8_c20:\n"
        "v_fma_f32 v158, v158, v246, v247\n"
        "v_mfma_i32_16x16x64_i8 v[196:199], v[108:111], v[216:219], v[196:199]\n"
        "ds_read_b128 v[208:211], v248 offset:32768\n"
        "v_fma_f32 v159, v159, v246, v247\n"
        "v_cvt_pk_bf16_f32 v132, v132, v140\n"
        "v_mfma_i32_16x16x64_i8 v[200:203], v[104:107], v[220:223], v[200:203]\n"
        "v_cvt_pk_bf16_f32 v140, v133, v141\n"
        "v_cvt_pk_bf16_f32 v134, v134, v142\n"
        "v_mfma_i32_16x16x64_i8 v[204:207], v[108:111], v[220:223], v[204:207]\n"
        "ds_read_b128 v[212:215], v248 offset:33792\n"
        "s_waitcnt lgkmcnt(4)\n"
        "v_mfma_i32_16x16x64_i8 v[192:195], v[112:115], v[224:227], v[192:195]\n"
        "v_cvt_pk_bf16_f32 v142, v135, v143\n"
        "v_cvt_pk_bf16_f32 v133, v148, v156\n"
        "v_mfma_i32_16x16x64_i8 v[196:199], v[116:119], v[224:227], v[196:199]\n"
        "ds_read_b128 v[216:219], v248 offset:34816\n"
        "global_store_dwordx2 v251, v[132:133], s[48:49] offset:0 sc1\n"
        "v_mfma_i32_16x16x64_i8 v[200:203], v[112:115], v[228:231], v[200:203]\n"
        "v_cvt_pk_bf16_f32 v141, v149, v157\n"
        "global_store_dwordx2 v251, v[140:141], s[50:51] offset:0 sc1\n"
        "v_cvt_pk_bf16_f32 v135, v150, v158\n"
        "v_mfma_i32_16x16x64_i8 v[204:207], v[116:119], v[228:231], v[204:207]\n"
        "ds_read_b128 v[220:223], v248 offset:35840\n"
        "s_waitcnt lgkmcnt(4)\n"
        "v_mfma_i32_16x16x64_i8 v[192:195], v[120:123], v[232:235], v[192:195]\n"
        "global_store_dwordx2 v251, v[134:135], s[52:53] offset:0 sc1\n"
        "v_cvt_pk_bf16_f32 v143, v151, v159\n"
        "v_mfma_i32_16x16x64_i8 v[196:199], v[124:127], v[232:235], v[196:199]\n"
        "ds_read_b128 v[224:227], v248 offset:36864\n"
        "global_store_dwordx2 v251, v[142:143], s[54:55] offset:0 sc1\n"
        "v_mfma_i32_16x16x64_i8 v[200:203], v[120:123], v[236:239], v[200:203]\n"
        "ds_read_b128 v[240:243], v253 offset:512\n"
        "ds_read_b128 v[244:247], v253 offset:528\n"
        "v_mfma_i32_16x16x64_i8 v[204:207], v[124:127], v[236:239], v[204:207]\n"
        "ds_read_b128 v[228:231], v248 offset:37888\n"
        "v_cvt_f32_i32 v160, v160\n"
        "s_waitcnt lgkmcnt(6)\n"
        "v_mfma_i32_16x16x64_i8 v[128:131], v[0:3], v[208:211], 0\n"
        "v_cvt_f32_i32 v161, v161\n"
        "v_cvt_f32_i32 v162, v162\n"
        "v_mfma_i32_16x16x64_i8 v[132:135], v[4:7], v[208:211], 0\n"
        "ds_read_b128 v[232:235], v248 offset:38912\n"
        "v_cvt_f32_i32 v163, v163\n"
        "v_mfma_i32_16x16x64_i8 v[136:139], v[0:3], v[212:215], 0\n"
        "v_cvt_f32_i32 v168, v168\n"
        "v_cvt_f32_i32 v169, v169\n"
        "v_cvt_f32_i32 v170, v170\n"
        "v_mfma_i32_16x16x64_i8 v[140:143], v[4:7], v[212:215], 0\n"
        "ds_read_b128 v[236:239], v248 offset:39936\n"
        "s_waitcnt lgkmcnt(6)\n"
        "v_mfma_i32_16x16x64_i8 v[128:131], v[8:11], v[216:219], v[128:131]\n"
        "v_cvt_f32_i32 v171, v171\n"
        "v_cvt_f32_i32 v176, v176\n"
        "v_mfma_i32_16x16x64_i8 v[132:135], v[12:15], v[216:219], v[132:135]\n"
        "ds_read_b128 v[208:211], v248 offset:40960\n"
        "v_cvt_f32_i32 v177, v177\n"
        "v_mfma_i32_16x16x64_i8 v[136:139], v[8:11], v[220:223], v[136:139]\n"
        "ds_read_b32 v254, v250 offset:0\n"
        "v_cvt_f32_i32 v178, v178\n"
        "v_cvt_f32_i32 v179, v179\n"
        "v_mfma_i32_16x16x64_i8 v[140:143], v[12:15], v[220:223], v[140:143]\n"
        "ds_read_b128 v[212:215], v248 offset:41984\n"
        "s_waitcnt lgkmcnt(5)\n"
        "v_mfma_i32_16x16x64_i8 v[128:131], v[16:19], v[224:227], v[128:131]\n"
        "v_cvt_f32_i32 v184, v184\n"
        "v_cvt_f32_i32 v185, v185\n"
        "v_mfma_i32_16x16x64_i8 v[132:135], v[20:23], v[224:227], v[132:135]\n"
        "ds_read_b128 v[216:219], v248 offset:43008\n"
        "v_cvt_f32_i32 v186, v186\n"
        "v_mfma_i32_16x16x64_i8 v[136:139], v[16:19], v[228:231], v[136:139]\n"
        "v_cvt_f32_i32 v187, v187\n"
        "v_fma_f32 v160, v160, v240, v241\n"
        "v_mfma_i32_16x16x64_i8 v[140:143], v[20:23], v[228:231], v[140:143]\n"
        "ds_read_b128 v[220:223], v248 offset:44032\n"
        "s_waitcnt lgkmcnt(5)\n"
        "v_mfma_i32_16x16x64_i8 v[128:131], v[24:27], v[232:235], v[128:131]\n"
        "v_fma_f32 v161, v161, v240, v241\n"
        "v_fma_f32 v162, v162, v240, v241\n"
        "v_mfma_i32_16x16x64_i8 v[132:135], v[28:31], v[232:235], v[132:135]\n"
        "ds_read_b128 v[224:227], v248 offset:45056\n"
        "v_fma_f32 v163, v163, v240, v241\n"
        "v_mfma_i32_16x16x64_i8 v[136:139], v[24:27], v[236:239], v[136:139]\n"
        "v_fma_f32 v168, v168, v242, v243\n"
        "v_fma_f32 v169, v169, v242, v243\n"
        "v_fma_f32 v170, v170, v242, v243\n"
        "v_mfma_i32_16x16x64_i8 v[140:143], v[28:31], v[236:239], v[140:143]\n"
        "ds_read_b128 v[228:231], v248 offset:46080\n"
        "s_waitcnt lgkmcnt(4)\n"
        "v_mfma_i32_16x16x64_i8 v[128:131], v[32:35], v[208:211], v[128:131]\n"
        "v_fma_f32 v171, v171, v242, v243\n"
        "v_fma_f32 v176, v176, v244, v245\n"
        "v_mfma_i32_16x16x64_i8 v[132:135], v[36:39], v[208:211], v[132:135]\n"
        "ds_read_b128 v[232:235], v248 offset:47104\n"
        "v_fma_f32 v177, v177, v244, v245\n"
        "v_mfma_i32_16x16x64_i8 v[136:139], v[32:35], v[212:215], v[136:139]\n"
        "v_fma_f32 v178, v178, v244, v245\n"
        "v_fma_f32 v179, v179, v244, v245\n"
        "v_fma_f32 v184, v184, v246, v247\n"
        "v_mfma_i32_16x16x64_i8 v[140:143], v[36:39], v[212:215], v[140:143]\n"
        "ds_read_b128 v[236:239], v248 offset:48128\n"
        "v_cmp_gt_u32 vcc, 24, v254\n"
        "s_waitcnt lgkmcnt(4)\n"
        "v_mfma_i32_16x16x64_i8 v[128:131], v[40:43], v[216:219], v[128:131]\n"
        "s_cbranch_vccnz W8_s22\n"
        "W8_c22:\n"
        "v_fma_f32 v185, v185, v246, v247\n"
        "v_mfma_i32_16x16x64_i8 v[132:135], v[44:47], v[216:219], v[132:135]\n"
        "ds_read_b128 v[208:211], v248 offset:49152\n"
        "v_fma_f32 v186, v186, v246, v247\n"
        "v_mfma_i32_16x16x64_i8 v[136:139], v[40:43], v[220:223], v[136:139]\n"
        "v_fma_f32 v187, v187, v246, v247\n"
        "v_cvt_pk_bf16_f32 v160, v160, v168\n"
        "v_cvt_pk_bf16_f32 v168, v161, v169\n"
        "v_mfma_i32_16x16x64_i8 v[140:143], v[44:47], v[220:223], v[140:143]\n"
        "ds_read_b128 v[212:215], v248 offset:50176\n"
        "s_waitcnt lgkmcnt(4)\n"
        "v_mfma_i32_16x16x64_i8 v[128:131], v[48:51], v[224:227], v[128:131]\n"
        "v_cvt_pk_bf16_f32 v162, v162, v170\n"
        "v_cvt_pk_bf16_f32 v170, v163, v171\n"
        "v_mfma_i32_16x16x64_i8 v[132:135], v[52:55], v[224:227], v[132:135]\n"
        "ds_read_b128 v[216:219], v248 offset:51200\n"
        "v_cvt_pk_bf16_f32 v161, v176, v184\n"
        "v_mfma_i32_16x16x64_i8 v[136:139], v[48:51], v[228:231], v[136:139]\n"
        "global_store_dwordx2 v251, v[160:161], s[40:41] offset:128 sc1\n"
        "v_cvt_pk_bf16_f32 v169, v177, v185\n"
        "global_store_dwordx2 v251, v[168:169], s[42:43] offset:128 sc1\n"
        "v_mfma_i32_16x16x64_i8 v[140:143], v[52:55], v[228:231], v[140:143]\n"
        "ds_read_b128 v[220:223], v248 offset:52224\n"
        "s_waitcnt lgkmcnt(4)\n"
        "v_mfma_i32_16x16x64_i8 v[128:131], v[56:59], v[232:235], v[128:131]\n"
        "v_cvt_pk_bf16_f32 v163, v178, v186\n"
        "global_store_dwordx2 v251, v[162:163], s[44:45] offset:128 sc1\n"
        "v_mfma_i32_16x16x64_i8 v[132:135], v[60:63], v[232:235], v[132:135]\n"
        "ds_read_b128 v[224:227], v248 offset:53248\n"
        "v_cvt_pk_bf16_f32 v171, v179, v187\n"
        "v_mfma_i32_16x16x64_i8 v[136:139], v[56:59], v[236:239], v[136:139]\n"
        "global_store_dwordx2 v251, v[170:171], s[46:47] offset:128 sc1\n"
        "v_cvt_f32_i32 v164, v164\n"
        "v_cvt_f32_i32 v165, v165\n"
        "v_mfma_i32_16x16x64_i8 v[140:143], v[60:63], v[236:239], v[140:143]\n"
        "ds_read_b128 v[228:231], v248 offset:54272\n"
        "s_waitcnt lgkmcnt(4)\n"
        "v_mfma_i32_16x16x64_i8 v[128:131], v[64:67], v[208:211], v[128:131]\n"
        "v_cvt_f32_i32 v166, v166\n"
        "v_cvt_f32_i32 v167, v167\n"
        "v_mfma_i32_16x16x64_i8 v[132:135], v[68:71], v[208:211], v[132:135]\n"
        "ds_read_b128 v[232:235], v248 offset:55296\n"
        "v_cvt_f32_i32 v172, v172\n"
        "v_mfma_i32_16x16x64_i8 v[136:139], v[64:67], v[212:215], v[136:139]\n"
        "v_cvt_f32_i32 v173, v173\n"
        "v_cvt_f32_i32 v174, v174\n"
        "v_cvt_f32_i32 v175, v175\n"
        "v_mfma_i32_16x16x64_i8 v[140:143], v[68:71], v[212:215], v[140:143]\n"
        "ds_read_b128 v[236:239], v248 offset:56320\n"
        "s_waitcnt lgkmcnt(4)\n"
        "v_mfma_i32_16x16x64_i8 v[128:131], v[72:75], v[216:219], v[128:131]\n"
        "v_cvt_f32_i32 v180, v180\n"
        "v_cvt_f32_i32 v181, v181\n"
        "v_mfma_i32_16x16x64_i8 v[132:135], v[76:79], v[216:219], v[132:135]\n"
        "ds_read_b128 v[208:211], v248 offset:57344\n"
        "v_cvt_f32_i32 v182, v182\n"
        "v_mfma_i32_16x16x64_i8 v[136:139], v[72:75], v[220:223], v[136:139]\n"
        "ds_read_b32 v254, v250 offset:0\n"
        "v_cvt_f32_i32 v183, v183\n"
        "v_cvt_f32_i32 v188, v188\n"
        "v_mfma_i32_16x16x64_i8 v[140:143], v[76:79], v[220:223], v[140:143]\n"
        "ds_read_b128 v[212:215], v248 offset:58368\n"
        "s_waitcnt lgkmcnt(5)\n"
        "v_mfma_i32_16x16x64_i8 v[128:131], v[80:83], v[224:227], v[128:131]\n"
        "v_cvt_f32_i32 v189, v189\n"
        "v_cvt_f32_i32 v190, v190\n"
        "v_mfma_i32_16x16x64_i8 v[132:135], v[84:87], v[224:227], v[132:135]\n"
        "ds_read_b128 v[216:219], v248 offset:59392\n"
        "v_cvt_f32_i32 v191, v191\n"
        "v_mfma_i32_16x16x64_i8 v[136:139], v[80:83], v[228:231], v[136:139]\n"
        "v_fma_f32 v164, v164, v240, v241\n"
        "v_fma_f32 v165, v165, v240, v241\n"
        "v_fma_f32 v166, v166, v240, v241\n"
        "v_mfma_i32_16x16x64_i8 v[140:143], v[84:87], v[228:231], v[140:143]\n"
        "ds_read_b128 v[220:223], v248 offset:60416\n"
        "s_waitcnt lgkmcnt(5)\n"
        "v_mfma_i32_16x16x64_i8 v[128:131], v[88:91], v[232:235], v[128:131]\n"
        "v_fma_f32 v167, v167, v240, v241\n"
        "v_fma_f32 v172, v172, v242, v243\n"
        "v_mfma_i32_16x16x64_i8 v[132:135], v[92:95], v[232:235], v[132:135]\n"
        "ds_read_b128 v[224:227], v248 offset:61440\n"
        "v_fma_f32 v173, v173, v242, v243\n"
        "v_mfma_i32_16x16x64_i8 v[136:139], v[88:91], v[236:239], v[136:139]\n"
        "v_fma_f32 v174, v174, v242, v243\n"
        "v_fma_f32 v175, v175, v242, v243\n"
        "v_fma_f32 v180, v180, v244, v245\n"
        "v_mfma_i32_16x16x64_i8 v[140:143], v[92:95], v[236:239], v[140:143]\n"
        "ds_read_b128 v[228:231], v248 offset:62464\n"
        "s_waitcnt lgkmcnt(4)\n"
        "v_mfma_i32_16x16x64_i8 v[128:131], v[96:99], v[208:211], v[128:131]\n"
        "v_fma_f32 v181, v181, v244, v245\n"
        "v_fma_f32 v182, v182, v244, v245\n"
        "v_mfma_i32_16x16x64_i8 v[132:135], v[100:103], v[208:211], v[132:135]\n"
        "ds_read_b128 v[232:235], v248 offset:63488\n"
        "v_fma_f32 v183, v183, v244, v245\n"
        "v_mfma_i32_16x16x64_i8 v[136:139], v[96:99], v[212:215], v[136:139]\n"
        "v_fma_f32 v188, v188, v246, v247\n"
        "v_fma_f32 v189, v189, v246, v247\n"
        "v_fma_f32 v190, v190, v246, v247\n"
        "v_mfma_i32_16x16x64_i8 v[140:143], v[100:103], v[212:215], v[140:143]\n"
        "ds_read_b128 v[236:239], v248 offset:64512\n"
        "v_cmp_gt_u32 vcc, 26, v254\n"
        "s_waitcnt lgkmcnt(4)\n"
        "v_mfma_i32_16x16x64_i8 v[128:131], v[104:107], v[216:219], v[128:131]\n"
        "s_cbranch_vccnz W8_s24\n"
        "W8_c24:\n"
        "v_fma_f32 v191, v191, v246, v247\n"
        "v_mfma_i32_16x16x64_i8 v[132:135], v[108:111], v[216:219], v[132:135]\n"
        "ds_read_b128 v[208:211], v249 offset:0\n"
        "v_cvt_pk_bf16_f32 v164, v164, v172\n"
        "v_mfma_i32_16x16x64_i8 v[136:139], v[104:107], v[220:223], v[136:139]\n"
        "v_cvt_pk_bf16_f32 v172, v165, v173\n"
        "v_cvt_pk_bf16_f32 v166, v166, v174\n"
        "v_cvt_pk_bf16_f32 v174, v167, v175\n"
        "v_mfma_i32_16x16x64_i8 v[140:143], v[108:111], v[220:223], v[140:143]\n"
        "ds_read_b128 v[212:215], v249 offset:1024\n"
        "s_waitcnt lgkmcnt(4)\n"
        "v_mfma_i32_16x16x64_i8 v[128:131], v[112:115], v[224:227], v[128:131]\n"
        "v_cvt_pk_bf16_f32 v165, v180, v188\n"
        "global_store_dwordx2 v251, v[164:165], s[48:49] offset:128 sc1\n"
        "v_mfma_i32_16x16x64_i8 v[132:135], v[116:119], v[224:227], v[132:135]\n"
        "ds_read_b128 v[216:219], v249 offset:2048\n"
        "v_cvt_pk_bf16_f32 v173, v181, v189\n"
        "v_mfma_i32_16x16x64_i8 v[136:139], v[112:115], v[228:231], v[136:139]\n"
        "global_store_dwordx2 v251, v[172:173], s[50:51] offset:128 sc1\n"
        "v_cvt_pk_bf16_f32 v167, v182, v190\n"
        "global_store_dwordx2 v251, v[166:167], s[52:53] offset:128 sc1\n"
        "v_mfma_i32_16x16x64_i8 v[140:143], v[116:119], v[228:231], v[140:143]\n"
        "ds_read_b128 v[220:223], v249 offset:3072\n"
        "s_waitcnt lgkmcnt(4)\n"
        "v_mfma_i32_16x16x64_i8 v[128:131], v[120:123], v[232:235], v[128:131]\n"
        "v_cvt_pk_bf16_f32 v175, v183, v191\n"
        "global_store_dwordx2 v251, v[174:175], s[54:55] offset:128 sc1\n"
        "v_mfma_i32_16x16x64_i8 v[132:135], v[124:127], v[232:235], v[132:135]\n"
        "ds_read_b128 v[224:227], v249 offset:4096\n"
        "v_add_u32 v251, s37, v251\n"
        "v_mfma_i32_16x16x64_i8 v[136:139], v[120:123], v[236:239], v[136:139]\n"
        "v_mfma_i32_16x16x64_i8 v[140:143], v[124:127], v[236:239], v[140:143]\n"
        "ds_read_b128 v[228:231], v249 offset:5120\n"
        "v_and_b32 v255, 0xfffffeff, v253\n"
        "s_waitcnt lgkmcnt(4)\n"
        "v_mfma_i32_16x16x64_i8 v[144:147], v[0:3], v[208:211], 0\n"
        "v_bfe_u32 v240, v253, 8, 1\n"
        "v_cvt_f32_i32 v192, v192\n"
        "v_mfma_i32_16x16x64_i8 v[148:151], v[4:7], v[208:211], 0\n"
        "ds_read_b128 v[232:235], v249 offset:6144\n"
        "v_lshl_add_u32 v255, v240, 4, v255\n"
        "v_mfma_i32_16x16x64_i8 v[152:155], v[0:3], v[212:215], 0\n"
        "ds_read_b128 v[244:247], v255 offset:1536\n"
        "ds_read_b128 v[240:243], v253 offset:1024\n"
        "v_mfma_i32_16x16x64_i8 v[156:159], v[4:7], v[212:215], 0\n"
        "ds_read_b128 v[236:239], v249 offset:7168\n"
        "s_waitcnt lgkmcnt(6)\n"
        "v_mfma_i32_16x16x64_i8 v[144:147], v[8:11], v[216:219], v[144:147]\n"
        "v_cvt_f32_i32 v193, v193\n"
        "v_cvt_f32_i32 v194, v194\n"
        "v_mfma_i32_16x16x64_i8 v[148:151], v[12:15], v[216:219], v[148:151]\n"
        "ds_read_b128 v[208:211], v249 offset:8192\n"
        "v_cvt_f32_i32 v195, v195\n"
        "v_cvt_f32_i32 v200, v200\n"
        "v_mfma_i32_16x16x64_i8 v[152:155], v[8:11], v[220:223], v[152:155]\n"
        "ds_read_b32 v254, v250 offset:0\n"
        "v_cvt_f32_i32 v201, v201\n"
        "v_mfma_i32_16x16x64_i8 v[156:159], v[12:15], v[220:223], v[156:159]\n"
        "ds_read_b128 v[212:215], v249 offset:9216\n"
        "s_waitcnt lgkmcnt(7)\n"
        "v_mfma_i32_16x16x64_i8 v[144:147], v[16:19], v[224:227], v[144:147]\n"
        "v_cvt_f32_i32 v202, v202\n"
        "v_cvt_f32_i32 v203, v203\n"
        "v_mfma_i32_16x16x64_i8 v[148:151], v[20:23], v[224:227], v[148:151]\n"
        "ds_read_b128 v[216:219], v249 offset:10240\n"
        "v_cvt_f32_i32 v196, v196\n"
        "v_mfma_i32_16x16x64_i8 v[152:155], v[16:19], v[228:231], v[152:155]\n"
        "v_cvt_f32_i32 v197, v197\n"
        "v_cvt_f32_i32 v198, v198\n"
        "v_mfma_i32_16x16x64_i8 v[156:159], v[20:23], v[228:231], v[156:159]\n"
        "ds_read_b128 v[220:223], v249 offset:11264\n"
        "s_waitcnt lgkmcnt(5)\n"
        "v_mfma_i32_16x16x64_i8 v[144:147], v[24:27], v[232:235], v[144:147]\n"
        "v_cvt_f32_i32 v199, v199\n"
        "v_cvt_f32_i32 v204, v204\n"
        "v_cvt_f32_i32 v205, v205\n"
        "v_mfma_i32_16x16x64_i8 v[148:151], v[28:31], v[232:235], v[148:151]\n"
        "ds_read_b128 v[224:227], v249 offset:12288\n"
        "v_cvt_f32_i32 v206, v206\n"
        "v_mfma_i32_16x16x64_i8 v[152:155], v[24:27], v[236:239], v[152:155]\n"
        "v_cvt_f32_i32 v207, v207\n"
        "v_mfma_i32_16x16x64_i8 v[156:159], v[28:31], v[236:239], v[156:159]\n"
        "ds_read_b128 v[228:231], v249 offset:13312\n"
        "s_waitcnt lgkmcnt(4)\n"
        "v_mfma_i32_16x16x64_i8 v[144:147], v[32:35], v[208:211], v[144:147]\n"
        "v_fma_f32 v192, v192, v244, v245\n"
        "v_fma_f32 v193, v193, v244, v245\n"
        "v_mfma_i32_16x16x64_i8 v[148:151], v[36:39], v[208:211], v[148:151]\n"
        "ds_read_b128 v[232:235], v249 offset:14336\n"
        "v_fma_f32 v194, v194, v244, v245\n"
        "v_mfma_i32_16x16x64_i8 v[152:155], v[32:35], v[212:215], v[152:155]\n"
        "v_fma_f32 v195, v195, v244, v245\n"
        "v_fma_f32 v200, v200, v246, v247\n"
        "v_fma_f32 v201, v201, v246, v247\n"
        "v_mfma_i32_16x16x64_i8 v[156:159], v[36:39], v[212:215], v[156:159]\n"
        "ds_read_b128 v[236:239], v249 offset:15360\n"
        "v_cmp_gt_u32 vcc, 28, v254\n"
        "s_waitcnt lgkmcnt(4)\n"
        "v_mfma_i32_16x16x64_i8 v[144:147], v[40:43], v[216:219], v[144:147]\n"
        "s_cbranch_vccnz W8_s26\n"
        "W8_c26:\n"
        "v_fma_f32 v202, v202, v246, v247\n"
        "v_mfma_i32_16x16x64_i8 v[148:151], v[44:47], v[216:219], v[148:151]\n"
        "ds_read_b128 v[208:211], v249 offset:16384\n"
        "v_fma_f32 v203, v203, v246, v247\n"
        "v_mfma_i32_16x16x64_i8 v[152:155], v[40:43], v[220:223], v[152:155]\n"
        "v_cvt_pk_bf16_f32 v192, v192, v200\n"
        "v_cvt_pk_bf16_f32 v200, v193, v201\n"
        "v_mfma_i32_16x16x64_i8 v[156:159], v[44:47], v[220:223], v[156:159]\n"
        "ds_read_b128 v[212:215], v249 offset:17408\n"
        "s_waitcnt lgkmcnt(4)\n"
        "v_mfma_i32_16x16x64_i8 v[144:147], v[48:51], v[224:227], v[144:147]\n"
        "v_cvt_pk_bf16_f32 v194, v194, v202\n"
        "v_cvt_pk_bf16_f32 v202, v195, v203\n"
        "v_mfma_i32_16x16x64_i8 v[148:151], v[52:55], v[224:227], v[148:151]\n"
        "ds_read_b128 v[216:219], v249 offset:18432\n"
        "global_store_dword v252, v192, s[40:41] sc1\n"
        "global_store_dword v252, v200, s[42:43] sc1\n"
        "v_mfma_i32_16x16x64_i8 v[152:155], v[48:51], v[228:231], v[152:155]\n"
        "global_store_dword v252, v194, s[44:45] sc1\n"
        "global_store_dword v252, v202, s[46:47] sc1\n"
        "v_mfma_i32_16x16x64_i8 v[156:159], v[52:55], v[228:231], v[156:159]\n"
        "ds_read_b128 v[220:223], v249 offset:19456\n"
        "s_waitcnt lgkmcnt(4)\n"
        "v_mfma_i32_16x16x64_i8 v[144:147], v[56:59], v[232:235], v[144:147]\n"
        "v_fma_f32 v196, v196, v244, v245\n"
        "v_fma_f32 v197, v197, v244, v245\n"
        "v_mfma_i32_16x16x64_i8 v[148:151], v[60:63], v[232:235], v[148:151]\n"
        "ds_read_b128 v[224:227], v249 offset:20480\n"
        "v_fma_f32 v198, v198, v244, v245\n"
        "v_mfma_i32_16x16x64_i8 v[152:155], v[56:59], v[236:239], v[152:155]\n"
        "v_fma_f32 v199, v199, v244, v245\n"
        "v_fma_f32 v204, v204, v246, v247\n"
        "v_mfma_i32_16x16x64_i8 v[156:159], v[60:63], v[236:239], v[156:159]\n"
        "ds_read_b128 v[228:231], v249 offset:21504\n"
        "v_fma_f32 v205, v205, v246, v247\n"
        "s_waitcnt lgkmcnt(4)\n"
        "v_mfma_i32_16x16x64_i8 v[144:147], v[64:67], v[208:211], v[144:147]\n"
        "v_fma_f32 v206, v206, v246, v247\n"
        "v_fma_f32 v207, v207, v246, v247\n"
        "v_mfma_i32_16x16x64_i8 v[148:151], v[68:71], v[208:211], v[148:151]\n"
        "ds_read_b128 v[232:235], v249 offset:22528\n"
        "v_cvt_pk_bf16_f32 v196, v196, v204\n"
        "v_mfma_i32_16x16x64_i8 v[152:155], v[64:67], v[212:215], v[152:155]\n"
        "v_cvt_pk_bf16_f32 v204, v197, v205\n"
        "v_cvt_pk_bf16_f32 v198, v198, v206\n"
        "v_mfma_i32_16x16x64_i8 v[156:159], v[68:71], v[212:215], v[156:159]\n"
        "ds_read_b128 v[236:239], v249 offset:23552\n"
        "s_waitcnt lgkmcnt(4)\n"
        "v_mfma_i32_16x16x64_i8 v[144:147], v[72:75], v[216:219], v[144:147]\n"
        "v_cvt_pk_bf16_f32 v206, v199, v207\n"
        "global_store_dword v252, v196, s[48:49] sc1\n"
        "v_mfma_i32_16x16x64_i8 v[148:151], v[76:79], v[216:219], v[148:151]\n"
        "ds_read_b128 v[208:211], v249 offset:24576\n"
        "global_store_dword v252, v204, s[50:51] sc1\n"
        "v_mfma_i32_16x16x64_i8 v[152:155], v[72:75], v[220:223], v[152:155]\n"
        "global_store_dword v252, v198, s[52:53] sc1\n"
        "global_store_dword v252, v206, s[54:55] sc1\n"
        "ds_read_b128 v[244:247], v253 offset:1040\n"
        "v_mfma_i32_16x16x64_i8 v[156:159], v[76:79], v[220:223], v[156:159]\n"
        "ds_read_b128 v[212:215], v249 offset:25600\n"
        "s_waitcnt lgkmcnt(5)\n"
        "v_mfma_i32_16x16x64_i8 v[144:147], v[80:83], v[224:227], v[144:147]\n"
        "v_cvt_f32_i32 v128, v128\n"
        "v_cvt_f32_i32 v129, v129\n"
        "v_mfma_i32_16x16x64_i8 v[148:151], v[84:87], v[224:227], v[148:151]\n"
        "ds_read_b128 v[216:219], v249 offset:26624\n"
        "v_cvt_f32_i32 v130, v130\n"
        "v_mfma_i32_16x16x64_i8 v[152:155], v[80:83], v[228:231], v[152:155]\n"
        "v_cvt_f32_i32 v131, v131\n"
        "v_cvt_f32_i32 v136, v136\n"
        "v_mfma_i32_16x16x64_i8 v[156:159], v[84:87], v[228:231], v[156:159]\n"
        "ds_read_b128 v[220:223], v249 offset:27648\n"
        "s_waitcnt lgkmcnt(5)\n"
        "v_mfma_i32_16x16x64_i8 v[144:147], v[88:91], v[232:235], v[144:147]\n"
        "v_cvt_f32_i32 v137, v137\n"
        "v_cvt_f32_i32 v138, v138\n"
        "v_cvt_f32_i32 v139, v139\n"
        "v_mfma_i32_16x16x64_i8 v[148:151], v[92:95], v[232:235], v[148:151]\n"
        "ds_read_b128 v[224:227], v249 offset:28672\n"
        "v_fma_f32 v128, v128, v240, v241\n"
        "v_mfma_i32_16x16x64_i8 v[152:155], v[88:91], v[236:239], v[152:155]\n"
        "v_fma_f32 v129, v129, v240, v241\n"
        "v_fma_f32 v130, v130, v240, v241\n"
        "v_mfma_i32_16x16x64_i8 v[156:159], v[92:95], v[236:239], v[156:159]\n"
        "ds_read_b128 v[228:231], v249 offset:29696\n"
        "s_waitcnt lgkmcnt(4)\n"
        "v_mfma_i32_16x16x64_i8 v[144:147], v[96:99], v[208:211], v[144:147]\n"
        "v_fma_f32 v131, v131, v240, v241\n"
        "v_fma_f32 v136, v136, v242, v243\n"
        "v_mfma_i32_16x16x64_i8 v[148:151], v[100:103], v[208:211], v[148:151]\n"
        "ds_read_b128 v[232:235], v249 offset:30720\n"
        "v_fma_f32 v137, v137, v242, v243\n"
        "v_mfma_i32_16x16x64_i8 v[152:155], v[96:99], v[212:215], v[152:155]\n"
        "v_fma_f32 v138, v138, v242, v243\n"
        "v_fma_f32 v139, v139, v242, v243\n"
        "v_mfma_i32_16x16x64_i8 v[156:159], v[100:103], v[212:215], v[156:159]\n"
        "ds_read_b128 v[236:239], v249 offset:31744\n"
        "v_cvt_pk_bf16_f32 v128, v128, v136\n"
        "s_waitcnt lgkmcnt(4)\n"
        "v_mfma_i32_16x16x64_i8 v[144:147], v[104:107], v[216:219], v[144:147]\n"
        "v_cvt_pk_bf16_f32 v136, v129, v137\n"
        "v_cvt_pk_bf16_f32 v130, v130, v138\n"
        "v_mfma_i32_16x16x64_i8 v[148:151], v[108:111], v[216:219], v[148:151]\n"
        "v_cvt_pk_bf16_f32 v138, v131, v139\n"
        "v_cvt_f32_i32 v132, v132\n"
        "v_mfma_i32_16x16x64_i8 v[152:155], v[104:107], v[220:223], v[152:155]\n"
        "v_cvt_f32_i32 v133, v133\n"
        "v_cvt_f32_i32 v134, v134\n"
        "v_mfma_i32_16x16x64_i8 v[156:159], v[108:111], v[220:223], v[156:159]\n"
        "v_cvt_f32_i32 v135, v135\n"
        "s_waitcnt lgkmcnt(2)\n"
        "v_mfma_i32_16x16x64_i8 v[144:147], v[112:115], v[224:227], v[144:147]\n"
        "v_cvt_f32_i32 v140, v140\n"
        "v_cvt_f32_i32 v141, v141\n"
        "v_mfma_i32_16x16x64_i8 v[148:151], v[116:119], v[224:227], v[148:151]\n"
        "v_cvt_f32_i32 v142, v142\n"
        "v_cvt_f32_i32 v143, v143\n"
        "v_fma_f32 v132, v132, v240, v241\n"
        "v_mfma_i32_16x16x64_i8 v[152:155], v[112:115], v[228:231], v[152:155]\n"
        "v_fma_f32 v133, v133, v240, v241\n"
        "v_fma_f32 v134, v134, v240, v241\n"
        "v_mfma_i32_16x16x64_i8 v[156:159], v[116:119], v[228:231], v[156:159]\n"
        "v_fma_f32 v135, v135, v240, v241\n"
        "s_waitcnt lgkmcnt(0)\n"
        "v_mfma_i32_16x16x64_i8 v[144:147], v[120:123], v[232:235], v[144:147]\n"
        "v_fma_f32 v140, v140, v242, v243\n"
        "v_fma_f32 v141, v141, v242, v243\n"
        "v_mfma_i32_16x16x64_i8 v[148:151], v[124:127], v[232:235], v[148:151]\n"
        "v_fma_f32 v142, v142, v242, v243\n"
        "v_fma_f32 v143, v143, v242, v243\n"
        "v_mfma_i32_16x16x64_i8 v[152:155], v[120:123], v[236:239], v[152:155]\n"
        "v_cvt_pk_bf16_f32 v132, v132, v140\n"
        "v_cvt_pk_bf16_f32 v140, v133, v141\n"
        "v_mfma_i32_16x16x64_i8 v[156:159], v[124:127], v[236:239], v[156:159]\n"
        "v_cvt_pk_bf16_f32 v134, v134, v142\n"
        "v_cvt_pk_bf16_f32 v142, v135, v143\n"
        "s_nop 5\n"
        "v_cvt_f32_i32 v144, v144\n"
        "v_cvt_f32_i32 v145, v145\n"
        "v_cvt_f32_i32 v146, v146\n"
        "v_cvt_f32_i32 v147, v147\n"
        "v_cvt_f32_i32 v152, v152\n"
        "v_cvt_f32_i32 v153, v153\n"
        "v_cvt_f32_i32 v154, v154\n"
        "v_cvt_f32_i32 v155, v155\n"
        "v_fma_f32 v144, v144, v244, v245\n"
        "v_fma_f32 v145, v145, v244, v245\n"
        "v_fma_f32 v146, v146, v244, v245\n"
        "v_fma_f32 v147, v147, v244, v245\n"
        "v_fma_f32 v152, v152, v246, v247\n"
        "v_fma_f32 v153, v153, v246, v247\n"
        "v_fma_f32 v154, v154, v246, v247\n"
        "v_fma_f32 v155, v155, v246, v247\n"
        "v_cvt_pk_bf16_f32 v129, v144, v152\n"
        "global_store_dwordx2 v251, v[128:129], s[40:41] offset:256 sc1\n"
        "v_cvt_pk_bf16_f32 v137, v145, v153\n"
        "global_store_dwordx2 v251, v[136:137], s[42:43] offset:256 sc1\n"
        "v_cvt_pk_bf16_f32 v131, v146, v154\n"
        "global_store_dwordx2 v251, v[130:131], s[44:45] offset:256 sc1\n"
        "v_cvt_pk_bf16_f32 v139, v147, v155\n"
        "global_store_dwordx2 v251, v[138:139], s[46:47] offset:256 sc1\n"
        "v_cvt_f32_i32 v148, v148\n"
        "v_cvt_f32_i32 v149, v149\n"
        "v_cvt_f32_i32 v150, v150\n"
        "v_cvt_f32_i32 v151, v151\n"
        "v_cvt_f32_i32 v156, v156\n"
        "v_cvt_f32_i32 v157, v157\n"
        "v_cvt_f32_i32 v158, v158\n"
        "v_cvt_f32_i32 v159, v159\n"
        "v_fma_f32 v148, v148, v244, v245\n"
        "v_fma_f32 v149, v149, v244, v245\n"
        "v_fma_f32 v150, v150, v244, v245\n"
        "v_fma_f32 v151, v151, v244, v245\n"
        "v_fma_f32 v156, v156, v246, v247\n"
        "v_fma_f32 v157, v157, v246, v247\n"
        "v_fma_f32 v158, v158, v246, v247\n"
        "v_fma_f32 v159, v159, v246, v247\n"
        "v_cvt_pk_bf16_f32 v133, v148, v156\n"
        "global_store_dwordx2 v251, v[132:133], s[48:49] offset:256 sc1\n"
        "v_cvt_pk_bf16_f32 v141, v149, v157\n"
        "global_store_dwordx2 v251, v[140:141], s[50:51] offset:256 sc1\n"
        "v_cvt_pk_bf16_f32 v135, v150, v158\n"
        "global_store_dwordx2 v251, v[134:135], s[52:53] offset:256 sc1\n"
        "v_cvt_pk_bf16_f32 v143, v151, v159\n"
        "global_store_dwordx2 v251, v[142:143], s[54:55] offset:256 sc1\n"
        "W8_fin:\n"
        "s_endpgm\n"
        "W8_s0:\n"
        "s_mov_b32 s38, 8388608\n"
        "W8_r0:\n"
        "ds_read_b32 v254, v250 offset:0\n"
        "s_sub_u32 s38, s38, 1\n"
        "s_waitcnt lgkmcnt(0)\n"
        "s_cbranch_scc1 W8_c0\n"
        "v_readfirstlane_b32 s39, v254\n"
        "s_cmp_lt_u32 s39, 1\n"
        "s_cbranch_scc1 W8_r0\n"
        "s_branch W8_c0\n"
        "W8_sZ:\n"
        "s_mov_b32 s38, 8388608\n"
        "W8_rZ:\n"
        "ds_read_b32 v241, v250 offset:4\n"
        "s_sub_u32 s38, s38, 1\n"
        "s_waitcnt lgkmcnt(0)\n"
        "s_cbranch_scc1 W8_cZ\n"
        "v_readfirstlane_b32 s39, v241\n"
        "s_cmp_lt_u32 s39, 4\n"
        "s_cbranch_scc1 W8_rZ\n"
        "s_branch W8_cZ\n"
        "W8_s1:\n"
        "s_mov_b32 s38, 8388608\n"
        "W8_r1:\n"
        "ds_read_b32 v254, v250 offset:0\n"
        "s_sub_u32 s38, s38, 1\n"
        "s_waitcnt lgkmcnt(0)\n"
        "s_cbranch_scc1 W8_c1\n"
        "v_readfirstlane_b32 s39, v254\n"
        "s_cmp_lt_u32 s39, 2\n"
        "s_cbranch_scc1 W8_r1\n"
        "s_branch W8_c1\n"
        "W8_sR0:\n"
        "s_mov_b32 s38, 8388608\n"
        "W8_rR0:\n"
        "ds_read_b32 v241, v250 offset:4\n"
        "s_sub_u32 s38, s38, 1\n"
        "s_waitcnt lgkmcnt(0)\n"
        "s_cbranch_scc1 W8_cR0\n"
        "v_readfirstlane_b32 s39, v241\n"
        "s_cmp_lt_u32 s39, 5\n"
        "s_cbranch_scc1 W8_rR0\n"
        "s_branch W8_cR0\n"
        "W8_s2:\n"
        "s_mov_b32 s38, 8388608\n"
        "W8_r2:\n"
        "ds_read_b32 v254, v250 offset:0\n"
        "s_sub_u32 s38, s38, 1\n"
        "s_waitcnt lgkmcnt(0)\n"
        "s_cbranch_scc1 W8_c2\n"
        "v_readfirstlane_b32 s39, v254\n"
        "s_cmp_lt_u32 s39, 3\n"
        "s_cbranch_scc1 W8_r2\n"
        "s_branch W8_c2\n"
        "W8_sR1:\n"
        "s_mov_b32 s38, 8388608\n"
        "W8_rR1:\n"
        "ds_read_b32 v241, v250 offset:4\n"
        "s_sub_u32 s38, s38, 1\n"
        "s_waitcnt lgkmcnt(0)\n"
        "s_cbranch_scc1 W8_cR1\n"
        "v_readfirstlane_b32 s39, v241\n"
        "s_cmp_lt_u32 s39, 6\n"
        "s_cbranch_scc1 W8_rR1\n"
        "s_branch W8_cR1\n"
        "W8_s3:\n"
        "s_mov_b32 s38, 8388608\n"
        "W8_r3:\n"
        "ds_read_b32 v254, v250 offset:0\n"
        "s_sub_u32 s38, s38, 1\n"
        "s_waitcnt lgkmcnt(0)\n"
        "s_cbranch_scc1 W8_c3\n"
        "v_readfirstlane_b32 s39, v254\n"
        "s_cmp_lt_u32 s39, 4\n"
        "s_cbranch_scc1 W8_r3\n"
        "s_branch W8_c3\n"
        "W8_sR2:\n"
        "s_mov_b32 s38, 8388608\n"
        "W8_rR2:\n"
        "ds_read_b32 v241, v250 offset:4\n"
        "s_sub_u32 s38, s38, 1\n"
        "s_waitcnt lgkmcnt(0)\n"
        "s_cbranch_scc1 W8_cR2\n"
        "v_readfirstlane_b32 s39, v241\n"
        "s_cmp_lt_u32 s39, 7\n"
        "s_cbranch_scc1 W8_rR2\n"
        "s_branch W8_cR2\n"
        "W8_s4:\n"
        "s_mov_b32 s38, 8388608\n"
        "W8_r4:\n"
        "ds_read_b32 v254, v250 offset:0\n"
        "s_sub_u32 s38, s38, 1\n"
        "s_waitcnt lgkmcnt(0)\n"
        "s_cbranch_scc1 W8_c4\n"
        "v_readfirstlane_b32 s39, v254\n"
        "s_cmp_lt_u32 s39, 5\n"
        "s_cbranch_scc1 W8_r4\n"
        "s_branch W8_c4\n"
        "W8_sR3:\n"
        "s_mov_b32 s38, 8388608\n"
        "W8_rR3:\n"
        "ds_read_b32 v241, v250 offset:4\n"
        "s_sub_u32 s38, s38, 1\n"
        "s_waitcnt lgkmcnt(0)\n"
        "s_cbranch_scc1 W8_cR3\n"
        "v_readfirstlane_b32 s39, v241\n"
        "s_cmp_lt_u32 s39, 8\n"
        "s_cbranch_scc1 W8_rR3\n"
        "s_branch W8_cR3\n"
        "W8_s5:\n"
        "s_mov_b32 s38, 8388608\n"
        "W8_r5:\n"
        "ds_read_b32 v254, v250 offset:0\n"
        "s_sub_u32 s38, s38, 1\n"
        "s_waitcnt lgkmcnt(0)\n"
        "s_cbranch_scc1 W8_c5\n"
        "v_readfirstlane_b32 s39, v254\n"
        "s_cmp_lt_u32 s39, 6\n"
        "s_cbranch_scc1 W8_r5\n"
        "s_branch W8_c5\n"
        "W8_sR4:\n"
        "s_mov_b32 s38, 8388608\n"
        "W8_rR4:\n"
        "ds_read_b32 v241, v250 offset:4\n"
        "s_sub_u32 s38, s38, 1\n"
        "s_waitcnt lgkmcnt(0)\n"
        "s_cbranch_scc1 W8_cR4\n"
        "v_readfirstlane_b32 s39, v241\n"
        "s_cmp_lt_u32 s39, 9\n"
        "s_cbranch_scc1 W8_rR4\n"
        "s_branch W8_cR4\n"
        "W8_s6:\n"
        "s_mov_b32 s38, 8388608\n"
        "W8_r6:\n"
        "ds_read_b32 v254, v250 offset:0\n"
        "s_sub_u32 s38, s38, 1\n"
        "s_waitcnt lgkmcnt(0)\n"
        "s_cbranch_scc1 W8_c6\n"
        "v_readfirstlane_b32 s39, v254\n"
        "s_cmp_lt_u32 s39, 7\n"
        "s_cbranch_scc1 W8_r6\n"
        "s_branch W8_c6\n"
        "W8_sR5:\n"
        "s_mov_b32 s38, 8388608\n"
        "W8_rR5:\n"
        "ds_read_b32 v241, v250 offset:4\n"
        "s_sub_u32 s38, s38, 1\n"
        "s_waitcnt lgkmcnt(0)\n"
        "s_cbranch_scc1 W8_cR5\n"
        "v_readfirstlane_b32 s39, v241\n"
        "s_cmp_lt_u32 s39, 10\n"
        "s_cbranch_scc1 W8_rR5\n"
        "s_branch W8_cR5\n"
        "W8_s7:\n"
        "s_mov_b32 s38, 8388608\n"
        "W8_r7:\n"
        "ds_read_b32 v254, v250 offset:0\n"
        "s_sub_u32 s38, s38, 1\n"
        "s_waitcnt lgkmcnt(0)\n"
        "s_cbranch_scc1 W8_c7\n"
        "v_readfirstlane_b32 s39, v254\n"
        "s_cmp_lt_u32 s39, 8\n"
        "s_cbranch_scc1 W8_r7\n"
        "s_branch W8_c7\n"
        "W8_sR6:\n"
        "s_mov_b32 s38, 8388608\n"
        "W8_rR6:\n"
        "ds_read_b32 v241, v250 offset:4\n"
        "s_sub_u32 s38, s38, 1\n"
        "s_waitcnt lgkmcnt(0)\n"
        "s_cbranch_scc1 W8_cR6\n"
        "v_readfirstlane_b32 s39, v241\n"
        "s_cmp_lt_u32 s39, 11\n"
        "s_cbranch_scc1 W8_rR6\n"
        "s_branch W8_cR6\n"
        "W8_s8:\n"
        "s_mov_b32 s38, 8388608\n"
        "W8_r8:\n"
        "ds_read_b32 v254, v250 offset:0\n"
        "s_sub_u32 s38, s38, 1\n"
        "s_waitcnt lgkmcnt(0)\n"
        "s_cbranch_scc1 W8_c8\n"
        "v_readfirstlane_b32 s39, v254\n"
        "s_cmp_lt_u32 s39, 9\n"
        "s_cbranch_scc1 W8_r8\n"
        "s_branch W8_c8\n"
        "W8_sR7:\n"
        "s_mov_b32 s38, 8388608\n"
        "W8_rR7:\n"
        "ds_read_b32 v241, v250 offset:4\n"
        "s_sub_u32 s38, s38, 1\n"
        "s_waitcnt lgkmcnt(0)\n"
        "s_cbranch_scc1 W8_cR7\n"
        "v_readfirstlane_b32 s39, v241\n"
        "s_cmp_lt_u32 s39, 12\n"
        "s_cbranch_scc1 W8_rR7\n"
        "s_branch W8_cR7\n"
        "W8_s9:\n"
        "s_mov_b32 s38, 8388608\n"
        "W8_r9:\n"
        "ds_read_b32 v254, v250 offset:0\n"
        "s_sub_u32 s38, s38, 1\n"
        "s_waitcnt lgkmcnt(0)\n"
        "s_cbranch_scc1 W8_c9\n"
        "v_readfirstlane_b32 s39, v254\n"
        "s_cmp_lt_u32 s39, 10\n"
        "s_cbranch_scc1 W8_r9\n"
        "s_branch W8_c9\n"
        "W8_sR8:\n"
        "s_mov_b32 s38, 8388608\n"
        "W8_rR8:\n"
        "ds_read_b32 v241, v250 offset:4\n"
        "s_sub_u32 s38, s38, 1\n"
        "s_waitcnt lgkmcnt(0)\n"
        "s_cbranch_scc1 W8_cR8\n"
        "v_readfirstlane_b32 s39, v241\n"
        "s_cmp_lt_u32 s39, 13\n"
        "s_cbranch_scc1 W8_rR8\n"
        "s_branch W8_cR8\n"
        "W8_s10:\n"
        "s_mov_b32 s38, 8388608\n"
        "W8_r10:\n"
        "ds_read_b32 v254, v250 offset:0\n"
        "s_sub_u32 s38, s38, 1\n"
        "s_waitcnt lgkmcnt(0)\n"
        "s_cbranch_scc1 W8_c10\n"
        "v_readfirstlane_b32 s39, v254\n"
        "s_cmp_lt_u32 s39, 11\n"
        "s_cbranch_scc1 W8_r10\n"
        "s_branch W8_c10\n"
        "W8_sR9:\n"
        "s_mov_b32 s38, 8388608\n"
        "W8_rR9:\n"
        "ds_read_b32 v241, v250 offset:4\n"
        "s_sub_u32 s38, s38, 1\n"
        "s_waitcnt lgkmcnt(0)\n"
        "s_cbranch_scc1 W8_cR9\n"
        "v_readfirstlane_b32 s39, v241\n"
        "s_cmp_lt_u32 s39, 14\n"
        "s_cbranch_scc1 W8_rR9\n"
        "s_branch W8_cR9\n"
        "W8_s11:\n"
        "s_mov_b32 s38, 8388608\n"
        "W8_r11:\n"
        "ds_read_b32 v254, v250 offset:0\n"
        "s_sub_u32 s38, s38, 1\n"
        "s_waitcnt lgkmcnt(0)\n"
        "s_cbranch_scc1 W8_c11\n"
        "v_readfirstlane_b32 s39, v254\n"
        "s_cmp_lt_u32 s39, 12\n"
        "s_cbranch_scc1 W8_r11\n"
        "s_branch W8_c11\n"
        "W8_sR10:\n"
        "s_mov_b32 s38, 8388608\n"
        "W8_rR10:\n"
        "ds_read_b32 v241, v250 offset:4\n"
        "s_sub_u32 s38, s38, 1\n"
        "s_waitcnt lgkmcnt(0)\n"
        "s_cbranch_scc1 W8_cR10\n"
        "v_readfirstlane_b32 s39, v241\n"
        "s_cmp_lt_u32 s39, 15\n"
        "s_cbranch_scc1 W8_rR10\n"
        "s_branch W8_cR10\n"
        "W8_s12:\n"
        "s_mov_b32 s38, 8388608\n"
        "W8_r12:\n"
        "ds_read_b32 v254, v250 offset:0\n"
        "s_sub_u32 s38, s38, 1\n"
        "s_waitcnt lgkmcnt(0)\n"
        "s_cbranch_scc1 W8_c12\n"
        "v_readfirstlane_b32 s39, v254\n"
        "s_cmp_lt_u32 s39, 13\n"
        "s_cbranch_scc1 W8_r12\n"
        "s_branch W8_c12\n"
        "W8_sR11:\n"
        "s_mov_b32 s38, 8388608\n"
        "W8_rR11:\n"
        "ds_read_b32 v241, v250 offset:4\n"
        "s_sub_u32 s38, s38, 1\n"
        "s_waitcnt lgkmcnt(0)\n"
        "s_cbranch_scc1 W8_cR11\n"
        "v_readfirstlane_b32 s39, v241\n"
        "s_cmp_lt_u32 s39, 16\n"
        "s_cbranch_scc1 W8_rR11\n"
        "s_branch W8_cR11\n"
        "W8_s13:\n"
        "s_mov_b32 s38, 8388608\n"
        "W8_r13:\n"
        "ds_read_b32 v254, v250 offset:0\n"
        "s_sub_u32 s38, s38, 1\n"
        "s_waitcnt lgkmcnt(0)\n"
        "s_cbranch_scc1 W8_c13\n"
        "v_readfirstlane_b32 s39, v254\n"
        "s_cmp_lt_u32 s39, 14\n"
        "s_cbranch_scc1 W8_r13\n"
        "s_branch W8_c13\n"
        "W8_s14:\n"
        "s_mov_b32 s38, 8388608\n"
        "W8_r14:\n"
        "ds_read_b32 v254, v250 offset:0\n"
        "s_sub_u32 s38, s38, 1\n"
        "s_waitcnt lgkmcnt(0)\n"
        "s_cbranch_scc1 W8_c14\n"
        "v_readfirstlane_b32 s39, v254\n"
        "s_cmp_lt_u32 s39, 15\n"
        "s_cbranch_scc1 W8_r14\n"
        "s_branch W8_c14\n"
        "W8_s15:\n"
        "s_mov_b32 s38, 8388608\n"
        "W8_r15:\n"
        "ds_read_b32 v254, v250 offset:0\n"
        "s_sub_u32 s38, s38, 1\n"
        "s_waitcnt lgkmcnt(0)\n"
        "s_cbranch_scc1 W8_c15\n"
        "v_readfirstlane_b32 s39, v254\n"
        "s_cmp_lt_u32 s39, 16\n"
        "s_cbranch_scc1 W8_r15\n"
        "s_branch W8_c15\n"
        "W8_s16:\n"
        "s_mov_b32 s38, 8388608\n"
        "W8_r16:\n"
        "ds_read_b32 v254, v250 offset:0\n"
        "s_sub_u32 s38, s38, 1\n"
        "s_waitcnt lgkmcnt(0)\n"
        "s_cbranch_scc1 W8_c16\n"
        "v_readfirstlane_b32 s39, v254\n"
        "s_cmp_lt_u32 s39, 18\n"
        "s_cbranch_scc1 W8_r16\n"
        "s_branch W8_c16\n"
        "W8_sT:\n"
        "s_mov_b32 s38, 8388608\n"
        "W8_rT:\n"
        "ds_read_b32 v255, v250 offset:260\n"
        "s_sub_u32 s38, s38, 1\n"
        "s_waitcnt lgkmcnt(0)\n"
        "s_cbranch_scc1 W8_cT\n"
        "v_readfirstlane_b32 s39, v255\n"
        "s_cmp_lt_u32 s39, 2\n"
        "s_cbranch_scc1 W8_rT\n"
        "s_branch W8_cT\n"
        "W8_s18:\n"
        "s_mov_b32 s38, 8388608\n"
        "W8_r18:\n"
        "ds_read_b32 v254, v250 offset:0\n"
        "s_sub_u32 s38, s38, 1\n"
        "s_waitcnt lgkmcnt(0)\n"
        "s_cbranch_scc1 W8_c18\n"
        "v_readfirstlane_b32 s39, v254\n"
        "s_cmp_lt_u32 s39, 20\n"
        "s_cbranch_scc1 W8_r18\n"
        "s_branch W8_c18\n"
        "W8_s20:\n"
        "s_mov_b32 s38, 8388608\n"
        "W8_r20:\n"
        "ds_read_b32 v254, v250 offset:0\n"
        "s_sub_u32 s38, s38, 1\n"
        "s_waitcnt lgkmcnt(0)\n"
        "s_cbranch_scc1 W8_c20\n"
        "v_readfirstlane_b32 s39, v254\n"
        "s_cmp_lt_u32 s39, 22\n"
        "s_cbranch_scc1 W8_r20\n"
        "s_branch W8_c20\n"
        "W8_s22:\n"
        "s_mov_b32 s38, 8388608\n"
        "W8_r22:\n"
        "ds_read_b32 v254, v250 offset:0\n"
        "s_sub_u32 s38, s38, 1\n"
        "s_waitcnt lgkmcnt(0)\n"
        "s_cbranch_scc1 W8_c22\n"
        "v_readfirstlane_b32 s39, v254\n"
        "s_cmp_lt_u32 s39, 24\n"
        "s_cbranch_scc1 W8_r22\n"
        "s_branch W8_c22\n"
        "W8_s24:\n"
        "s_mov_b32 s38, 8388608\n"
        "W8_r24:\n"
        "ds_read_b32 v254, v250 offset:0\n"
        "s_sub_u32 s38, s38, 1\n"
        "s_waitcnt lgkmcnt(0)\n"
        "s_cbranch_scc1 W8_c24\n"
        "v_readfirstlane_b32 s39, v254\n"
        "s_cmp_lt_u32 s39, 26\n"
        "s_cbranch_scc1 W8_r24\n"
        "s_branch W8_c24\n"
        "W8_s26:\n"
        "s_mov_b32 s38, 8388608\n"
        "W8_r26:\n"
        "ds_read_b32 v254, v250 offset:0\n"
        "s_sub_u32 s38, s38, 1\n"
        "s_waitcnt lgkmcnt(0)\n"
        "s_cbranch_scc1 W8_c26\n"
        "v_readfirstlane_b32 s39, v254\n"
        "s_cmp_lt_u32 s39, 28\n"
        "s_cbranch_scc1 W8_r26\n"
        "s_branch W8_c26\n"
        "W8_end:\n"
        : "+v"(tid)
        : "s"(__builtin_amdgcn_kernarg_segment_ptr()), "s"(blockIdx.x)
        : "v1", "v2", "v3", "v4", "v5", "v6", "v7", "v8", "v9", "v10", "v11", "v12", "v13", "v14", "v15", "v16", "v17", "v18", "v19", "v20", "v21", "v22", "v23", "v24", "v25", "v26", "v27", "v28", "v29", "v30", "v31", "v32", "v33", "v34", "v35", "v36", "v37", "v38", "v39", "v40", "v41", "v42", "v43", "v44", "v45", "v46", "v47", "v48", "v49", "v50", "v51", "v52", "v53", "v54", "v55", "v56", "v57", "v58", "v59", "v60", "v61", "v62", "v63", "v64", "v65", "v66", "v67", "v68", "v69", "v70", "v71", "v72", "v73", "v74", "v75", "v76", "v77", "v78", "v79", "v80", "v81", "v82", "v83", "v84", "v85", "v86", "v87", "v88", "v89", "v90", "v91", "v92", "v93", "v94", "v95", "v96", "v97", "v98", "v99", "v100", "v101", "v102", "v103", "v104", "v105", "v106", "v107", "v108", "v109", "v110", "v111", "v112", "v113", "v114", "v115", "v116", "v117", "v118", "v119", "v120", "v121", "v122", "v123", "v124", "v125", "v126", "v127", "v128", "v129", "v130", "v131", "v132", "v133", "v134", "v135", "v136", "v137", "v138", "v139", "v140", "v141", "v142", "v143", "v144", "v145", "v146", "v147", "v148", "v149", "v150", "v151", "v152", "v153", "v154", "v155", "v156", "v157", "v158", "v159", "v160", "v161", "v162", "v163", "v164", "v165", "v166", "v167", "v168", "v169", "v170", "v171", "v172", "v173", "v174", "v175", "v176", "v177", "v178", "v179", "v180", "v181", "v182", "v183", "v184", "v185", "v186", "v187", "v188", "v189", "v190", "v191", "v192", "v193", "v194", "v195", "v196", "v197", "v198", "v199", "v200", "v201", "v202", "v203", "v204", "v205", "v206", "v207", "v208", "v209", "v210", "v211", "v212", "v213", "v214", "v215", "v216", "v217", "v218", "v219", "v220", "v221", "v222", "v223", "v224", "v225", "v226", "v227", "v228", "v229", "v230", "v231", "v232", "v233", "v234", "v235", "v236", "v237", "v238", "v239", "v240", "v241", "v242", "v243", "v244", "v245", "v246", "v247", "v248", "v249", "v250", "v251", "v252", "v253", "v254", "v255", "s3", "s4", "s5", "s6", "s7", "s8", "s9", "s10", "s11", "s12", "s13", "s14", "s15", "s16", "s17", "s18", "s19", "s20", "s21", "s22", "s23", "s24", "s25", "s26", "s27", "s28", "s29", "s30", "s31", "s33", "s34", "s35", "s36", "s37", "s38", "s39", "s40", "s41", "s42", "s43", "s44", "s45", "s46", "s47", "s48", "s49", "s50", "s51", "s52", "s53", "s54", "s55", "s56", "s57", "s58", "s59", "s60", "s61", "s62", "s63", "s64", "s65", "s66", "s67", "s68", "s69", "s70", "s71", "s72", "s73", "s74", "s75", "s76", "s77", "s78", "s79", "s80", "s81", "s82", "s83", "s84", "s85", "s86", "s87", "s88", "s89", "s90", "s91", "s92", "s93", "s94", "s95", "s96", "s97", "s98", "s99", "vcc", "scc", "memory");
}

extern "C" void kernel_launch(void* const* d_in, const int* in_sizes, int n_in,
                              void* d_out, int out_size, void* d_ws, size_t ws_size,
                              hipStream_t stream) {
    (void)in_sizes; (void)n_in; (void)out_size; (void)ws_size;
    if (ws_size >= (size_t)(128u<<20) + (8u << 20)) d_ws = (char*)d_ws + (128u<<20);
    constexpr int lds_bytes = 155712;
    hipFuncSetAttribute((const void*)w8a8_fused, hipFuncAttributeMaxDynamicSharedMemorySize, lds_bytes);
    w8a8_fused<<<256, 512, lds_bytes, stream>>>(d_in[0], d_in[1], d_ws, d_out, d_in[4], d_in[5], d_in[2], d_in[3]);
}
```

```cpp
#include <hip/hip_runtime.h>
#include <stdint.h>

__global__ __launch_bounds__(512, 2) void w8a8_fused(const void* qx, const void* qw, void* ws, void* out,
                                                     const void* sw, const void* bias, const void* sx, const void* zx) {
    int tid = threadIdx.x;
    asm volatile(
        "W8_start:\n"
        "s_load_dwordx16 s[4:19], %1, 0x0\n"
        "s_mov_b32 s3, %2\n"
        "v_readfirstlane_b32 s20, v0\n"
        "s_and_b32 s24, s3, 1\n"
        "s_lshl_b32 s24, s24, 3\n"
        "s_bfe_u32 s21, s3, 0x30003\n"
        "s_or_b32 s21, s21, s24\n"
        "s_bfe_u32 s24, s3, 0x20001\n"
        "s_lshl_b32 s24, s24, 2\n"
        "s_lshr_b32 s22, s3, 6\n"
        "s_or_b32 s22, s22, s24\n"
        "v_lshlrev_b32 v2, 2, v0\n"
        "v_add_u32 v2, 133124, v2\n"
        "v_mov_b32 v3, 0\n"
        "ds_write_b32 v2, v3\n"
        "s_and_b32 s23, s22, 1\n"
        "s_lshr_b32 s20, s20, 6\n"
        "s_cbranch_scc0 W8_prod\n"
        "s_bfe_u32 s24, s3, 0x20003\n"
        "s_add_u32 s24, s24, 4\n"
        "s_cmp_lg_u32 s20, s24\n"
        "s_cbranch_scc1 W8_np\n"
        "s_getpc_b64 s[24:25]\n"
        "W8_pc:\n"
        "v_and_b32 v2, 255, v0\n"
        "v_lshlrev_b32 v2, 7, v2\n"
        "v_min_u32 v2, W8_end-W8_pc-4, v2\n"
        "global_load_dword v255, v2, s[24:25]\n"
        "W8_np:\n"
        "s_waitcnt lgkmcnt(0)\n"
        "s_barrier\n"
        "s_branch W8_other\n"
        "W8_prod:\n"
        "s_and_b32 s24, s22, 3\n"
        "s_lshl2_add_u32 s24, s21, s24\n"
        "s_lshl_b32 s24, s24, 17\n"
        "s_lshr_b32 s25, s22, 2\n"
        "s_lshl_b32 s25, s25, 8\n"
        "s_add_u32 s24, s24, s25\n"
        "v_lshrrev_b32 v11, 4, v0\n"
        "v_and_b32 v13, 15, v0\n"
        "v_lshl_add_u32 v4, v11, 12, s24\n"
        "v_lshl_add_u32 v4, v13, 4, v4\n"
        "s_lshl_b32 s25, s22, 9\n"
        "s_mul_i32 s26, s21, 229376\n"
        "s_add_u32 s27, s26, s25\n"
        "v_lshrrev_b32 v8, 5, v0\n"
        "v_mul_u32_u24 v9, 114688, v8\n"
        "v_and_b32 v10, 31, v0\n"
        "v_lshl_add_u32 v5, v10, 4, v9\n"
        "v_add_u32 v5, s27, v5\n"
        "s_movk_i32 s28, 16384\n"
        "s_movk_i32 s29, 14336\n"
        "v_bfe_u32 v60, v0, 4, 1\n"
        "s_add_u32 s27, s22, 16\n"
        "s_sub_u32 s27, s27, s21\n"
        "v_mul_lo_u32 v60, v60, s27\n"
        "v_add_lshl_u32 v60, v60, s21, 7\n"
        "v_add_u32 v60, 6815744, v60\n"
        "s_waitcnt lgkmcnt(0)\n"
        "global_load_dwordx4 v[64:67], v4, s[4:5] nt\n"
        "v_add_u32 v4, s28, v4\n"
        "global_load_dwordx4 v[96:99], v5, s[6:7] nt\n"
        "v_add_u32 v5, s29, v5\n"
        "global_load_dwordx2 v[62:63], v60, s[8:9] sc1\n"
        "global_load_dwordx4 v[68:71], v4, s[4:5] nt\n"
        "v_add_u32 v4, s28, v4\n"
        "global_load_dwordx4 v[72:75], v4, s[4:5] nt\n"
        "v_add_u32 v4, s28, v4\n"
        "global_load_dwordx4 v[76:79], v4, s[4:5] nt\n"
        "v_add_u32 v4, s28, v4\n"
        "global_load_dwordx4 v[80:83], v4, s[4:5] nt\n"
        "v_add_u32 v4, s28, v4\n"
        "global_load_dwordx4 v[84:87], v4, s[4:5] nt\n"
        "v_add_u32 v4, s28, v4\n"
        "global_load_dwordx4 v[88:91], v4, s[4:5] nt\n"
        "v_add_u32 v4, s28, v4\n"
        "global_load_dwordx4 v[92:95], v4, s[4:5] nt\n"
        "v_add_u32 v4, 4294853632, v4\n"
        "global_load_dwordx4 v[100:103], v5, s[6:7] nt\n"
        "v_add_u32 v5, s29, v5\n"
        "global_load_dwordx4 v[104:107], v5, s[6:7] nt\n"
        "v_add_u32 v5, s29, v5\n"
        "global_load_dwordx4 v[108:111], v5, s[6:7] nt\n"
        "v_add_u32 v5, s29, v5\n"
        "global_load_dwordx4 v[112:115], v5, s[6:7] nt\n"
        "v_add_u32 v5, s29, v5\n"
        "global_load_dwordx4 v[116:119], v5, s[6:7] nt\n"
        "v_add_u32 v5, s29, v5\n"
        "global_load_dwordx4 v[120:123], v5, s[6:7] nt\n"
        "v_add_u32 v5, s29, v5\n"
        "global_load_dwordx4 v[124:127], v5, s[6:7] nt\n"
        "v_add_u32 v5, 3569664, v5\n"
        "global_load_dwordx4 v[128:131], v4, s[4:5] nt\n"
        "v_add_u32 v4, s28, v4\n"
        "global_load_dwordx4 v[132:135], v4, s[4:5] nt\n"
        "v_add_u32 v4, s28, v4\n"
        "global_load_dwordx4 v[136:139], v4, s[4:5] nt\n"
        "v_add_u32 v4, s28, v4\n"
        "global_load_dwordx4 v[140:143], v4, s[4:5] nt\n"
        "v_add_u32 v4, s28, v4\n"
        "global_load_dwordx4 v[144:147], v4, s[4:5] nt\n"
        "v_add_u32 v4, s28, v4\n"
        "global_load_dwordx4 v[148:151], v4, s[4:5] nt\n"
        "v_add_u32 v4, s28, v4\n"
        "s_barrier\n"
        "s_lshl_b32 s26, s26, 2\n"
        "s_lshl_b32 s27, s22, 8\n"
        "s_add_u32 s27, s27, 8192\n"
        "s_add_u32 s27, s27, s26\n"
        "v_lshrrev_b32 v11, 4, v0\n"
        "v_mul_u32_u24 v12, 229376, v11\n"
        "v_and_b32 v13, 15, v0\n"
        "v_lshl_add_u32 v7, v13, 4, v12\n"
        "v_add_u32 v7, s27, v7\n"
        "s_lshl_b32 s27, s22, 7\n"
        "s_add_u32 s27, s27, 12288\n"
        "s_add_u32 s27, s27, s26\n"
        "v_bfe_u32 v14, v0, 3, 2\n"
        "v_mul_u32_u24 v12, 229376, v14\n"
        "v_and_b32 v15, 7, v0\n"
        "v_lshl_add_u32 v6, v15, 4, v12\n"
        "v_add3_u32 v6, v6, v9, s27\n"
        "v_lshlrev_b32 v16, 2, v0\n"
        "v_add_u32 v25, 133124, v16\n"
        "v_add_u32 v16, 135232, v16\n"
        "v_lshlrev_b32 v19, 4, v0\n"
        "s_lshr_b32 s24, s21, 2\n"
        "s_add_u32 s25, s21, s22\n"
        "s_xor_b32 s24, s24, s25\n"
        "s_and_b32 s24, s24, 3\n"
        "s_and_b32 s25, s21, 3\n"
        "s_lshl2_add_u32 s24, s24, s25\n"
        "s_lshl_b32 s24, s24, 11\n"
        "v_add_u32 v17, s24, v19\n"
        "v_lshlrev_b32 v21, 3, v0\n"
        "v_add_u32 v21, 135232, v21\n"
        "s_lshl_b32 s26, s22, 4\n"
        "s_add_u32 s26, s26, s21\n"
        "s_mul_i32 s24, s26, 6144\n"
        "s_add_u32 s24, s24, 4194304\n"
        "s_add_u32 s44, s8, s24\n"
        "s_addc_u32 s45, s9, 0\n"
        "s_lshl_b32 s24, s26, 9\n"
        "s_add_u32 s24, s24, 5767168\n"
        "s_add_u32 s46, s8, s24\n"
        "s_addc_u32 s47, s9, 0\n"
        "v_lshlrev_b32 v22, 4, v13\n"
        "v_lshl_add_u32 v22, v11, 2, v22\n"
        "v_lshlrev_b32 v23, 4, v15\n"
        "v_lshl_add_u32 v23, v11, 2, v23\n"
        "v_lshrrev_b32 v22, 1, v22\n"
        "v_lshrrev_b32 v23, 1, v23\n"
        "s_mul_i32 s24, s21, 512\n"
        "s_mul_i32 s25, s22, 8\n"
        "s_add_u32 s24, s24, s25\n"
        "s_add_u32 s24, s24, 6029312\n"
        "s_mul_i32 s25, s22, 1024\n"
        "s_mul_i32 s27, s21, 8\n"
        "s_add_u32 s25, s25, s27\n"
        "s_add_u32 s25, s25, 6291456\n"
        "s_sub_u32 s25, s25, s24\n"
        "v_mul_lo_u32 v24, v0, s25\n"
        "s_mul_i32 s25, 4, 128\n"
        "s_add_u32 s24, s24, s25\n"
        "v_add_u32 v24, s24, v24\n"
        "v_lshlrev_b32 v35, 8, v14\n"
        "v_lshl_add_u32 v35, v8, 7, v35\n"
        "v_lshl_add_u32 v35, v15, 4, v35\n"
        "v_add_u32 v35, 4096, v35\n"
        "v_mov_b32 v20, 1\n"
        "s_mov_b32 s30, 0x0c0c0400\n"
        "s_mov_b32 s31, 0x01010101\n"
        "v_mov_b32 v28, 0\n"
        "v_mov_b32 v29, 0\n"
        "v_mov_b32 v30, 0\n"
        "v_mov_b32 v31, 0\n"
        "v_mov_b32 v32, 0\n"
        "v_mov_b32 v33, 0\n"
        "v_lshlrev_b32 v12, 3, v0\n"
        "v_add_u32 v12, 134408, v12\n"
        "s_waitcnt vmcnt(20)\n"
        "v_add_co_u32 v62, vcc, 1, v62\n"
        "s_nop 1\n"
        "v_addc_co_u32 v63, vcc, 0, v63, vcc\n"
        "s_mov_b32 exec_hi, 0\n"
        "ds_write_b64 v12, v[62:63]\n"
        "s_mov_b32 exec_hi, -1\n"
        "ds_write_b32 v25, v20 offset:1024\n"
        "s_waitcnt vmcnt(19)\n"
        "global_load_dwordx4 v[152:155], v4, s[4:5] nt\n"
        "v_add_u32 v4, s28, v4\n"
        "global_load_dwordx4 v[156:159], v4, s[4:5] nt\n"
        "v_add_u32 v4, 4294853632, v4\n"
        "v_perm_b32 v64, v65, v64, s30\n"
        "v_perm_b32 v66, v67, v66, s30\n"
        "v_lshl_or_b32 v64, v66, 16, v64\n"
        "ds_write_b32 v16, v64 offset:0\n"
        "v_perm_b32 v68, v69, v68, s30\n"
        "v_perm_b32 v70, v71, v70, s30\n"
        "v_lshl_or_b32 v68, v70, 16, v68\n"
        "ds_write_b32 v16, v68 offset:256\n"
        "s_waitcnt vmcnt(19)\n"
        "global_load_dwordx4 v[160:163], v5, s[6:7] nt\n"
        "v_add_u32 v5, s29, v5\n"
        "global_load_dwordx4 v[164:167], v5, s[6:7] nt\n"
        "v_add_u32 v5, s29, v5\n"
        "v_perm_b32 v72, v73, v72, s30\n"
        "v_perm_b32 v74, v75, v74, s30\n"
        "v_lshl_or_b32 v72, v74, 16, v72\n"
        "ds_write_b32 v16, v72 offset:512\n"
        "v_perm_b32 v76, v77, v76, s30\n"
        "v_perm_b32 v78, v79, v78, s30\n"
        "v_lshl_or_b32 v76, v78, 16, v76\n"
        "ds_write_b32 v16, v76 offset:768\n"
        "s_waitcnt vmcnt(19)\n"
        "global_load_dwordx4 v[168:171], v5, s[6:7] nt\n"
        "v_add_u32 v5, s29, v5\n"
        "global_load_dwordx4 v[172:175], v5, s[6:7] nt\n"
        "v_add_u32 v5, s29, v5\n"
        "v_perm_b32 v80, v81, v80, s30\n"
        "v_perm_b32 v82, v83, v82, s30\n"
        "v_lshl_or_b32 v80, v82, 16, v80\n"
        "ds_write_b32 v16, v80 offset:1024\n"
        "v_perm_b32 v84, v85, v84, s30\n"
        "v_perm_b32 v86, v87, v86, s30\n"
        "v_lshl_or_b32 v84, v86, 16, v84\n"
        "ds_write_b32 v16, v84 offset:1280\n"
        "s_waitcnt vmcnt(19)\n"
        "global_load_dwordx4 v[176:179], v5, s[6:7] nt\n"
        "v_add_u32 v5, s29, v5\n"
        "global_load_dwordx4 v[180:183], v5, s[6:7] nt\n"
        "v_add_u32 v5, s29, v5\n"
        "v_perm_b32 v88, v89, v88, s30\n"
        "v_perm_b32 v90, v91, v90, s30\n"
        "v_lshl_or_b32 v88, v90, 16, v88\n"
        "ds_write_b32 v16, v88 offset:1536\n"
        "v_perm_b32 v92, v93, v92, s30\n"
        "v_perm_b32 v94, v95, v94, s30\n"
        "v_lshl_or_b32 v92, v94, 16, v92\n"
        "ds_write_b32 v16, v92 offset:1792\n"
        "s_waitcnt vmcnt(20)\n"
        "global_load_dwordx4 v[184:187], v5, s[6:7] nt\n"
        "v_add_u32 v5, s29, v5\n"
        "global_load_dwordx4 v[188:191], v5, s[6:7] nt\n"
        "v_add_u32 v5, 3569664, v5\n"
        "s_waitcnt vmcnt(20)\n"
        "global_load_dwordx4 v[192:195], v4, s[4:5] nt\n"
        "v_add_u32 v4, s28, v4\n"
        "global_load_dwordx4 v[196:199], v4, s[4:5] nt\n"
        "v_add_u32 v4, s28, v4\n"
        "v_perm_b32 v96, v100, v96, s30\n"
        "v_perm_b32 v104, v108, v104, s30\n"
        "v_perm_b32 v97, v101, v97, s30\n"
        "v_perm_b32 v105, v109, v105, s30\n"
        "v_perm_b32 v98, v102, v98, s30\n"
        "v_perm_b32 v106, v110, v106, s30\n"
        "v_perm_b32 v99, v103, v99, s30\n"
        "v_perm_b32 v107, v111, v107, s30\n"
        "v_lshl_or_b32 v36, v104, 16, v96\n"
        "v_lshl_or_b32 v40, v105, 16, v97\n"
        "v_lshl_or_b32 v38, v106, 16, v98\n"
        "v_lshl_or_b32 v42, v107, 16, v99\n"
        "s_waitcnt vmcnt(20)\n"
        "global_load_dwordx4 v[200:203], v4, s[4:5] nt\n"
        "v_add_u32 v4, s28, v4\n"
        "global_load_dwordx4 v[204:207], v4, s[4:5] nt\n"
        "v_add_u32 v4, s28, v4\n"
        "s_waitcnt vmcnt(20)\n"
        "global_load_dwordx4 v[208:211], v4, s[4:5] nt\n"
        "v_add_u32 v4, s28, v4\n"
        "global_load_dwordx4 v[212:215], v4, s[4:5] nt\n"
        "v_add_u32 v4, s28, v4\n"
        "v_perm_b32 v112, v116, v112, s30\n"
        "v_perm_b32 v120, v124, v120, s30\n"
        "v_perm_b32 v113, v117, v113, s30\n"
        "v_perm_b32 v121, v125, v121, s30\n"
        "v_perm_b32 v114, v118, v114, s30\n"
        "v_perm_b32 v122, v126, v122, s30\n"
        "v_perm_b32 v115, v119, v115, s30\n"
        "v_perm_b32 v123, v127, v123, s30\n"
        "v_lshl_or_b32 v37, v120, 16, v112\n"
        "v_lshl_or_b32 v41, v121, 16, v113\n"
        "v_lshl_or_b32 v39, v122, 16, v114\n"
        "v_lshl_or_b32 v43, v123, 16, v115\n"
        "s_nop 0\n"
        "v_permlane32_swap_b32 v36, v38\n"
        "v_permlane32_swap_b32 v37, v39\n"
        "v_permlane32_swap_b32 v40, v42\n"
        "v_permlane32_swap_b32 v41, v43\n"
        "s_nop 0\n"
        "ds_write_b128 v17, v[36:39] offset:0\n"
        "ds_write_b128 v17, v[40:43] offset:1024\n"
        "ds_write_b32 v25, v20 offset:1544\n"
        "v_add_u32 v20, 1, v20\n"
        "v_dot4_i32_i8 v28, v36, s31, v28\n"
        "v_dot4_i32_i8 v29, v40, s31, v29\n"
        "v_dot4_i32_i8 v28, v37, s31, v28\n"
        "v_dot4_i32_i8 v29, v41, s31, v29\n"
        "v_dot4_i32_i8 v28, v38, s31, v28\n"
        "v_dot4_i32_i8 v29, v42, s31, v29\n"
        "v_dot4_i32_i8 v28, v39, s31, v28\n"
        "v_dot4_i32_i8 v29, v43, s31, v29\n"
        "s_waitcnt vmcnt(20)\n"
        "global_load_dwordx4 v[216:219], v4, s[4:5] nt\n"
        "v_add_u32 v4, s28, v4\n"
        "global_load_dwordx4 v[220:223], v4, s[4:5] nt\n"
        "v_add_u32 v4, 4294853632, v4\n"
        "v_perm_b32 v128, v129, v128, s30\n"
        "v_perm_b32 v130, v131, v130, s30\n"
        "v_lshl_or_b32 v128, v130, 16, v128\n"
        "ds_write_b32 v16, v128 offset:5120\n"
        "v_perm_b32 v132, v133, v132, s30\n"
        "v_perm_b32 v134, v135, v134, s30\n"
        "v_lshl_or_b32 v132, v134, 16, v132\n"
        "ds_write_b32 v16, v132 offset:5376\n"
        "s_waitcnt vmcnt(20)\n"
        "global_load_dwordx4 v[224:227], v5, s[6:7] nt\n"
        "v_add_u32 v5, s29, v5\n"
        "global_load_dwordx4 v[228:231], v5, s[6:7] nt\n"
        "v_add_u32 v5, s29, v5\n"
        "v_perm_b32 v136, v137, v136, s30\n"
        "v_perm_b32 v138, v139, v138, s30\n"
        "v_lshl_or_b32 v136, v138, 16, v136\n"
        "ds_write_b32 v16, v136 offset:5632\n"
        "v_perm_b32 v140, v141, v140, s30\n"
        "v_perm_b32 v142, v143, v142, s30\n"
        "v_lshl_or_b32 v140, v142, 16, v140\n"
        "ds_write_b32 v16, v140 offset:5888\n"
        "s_waitcnt vmcnt(20)\n"
        "global_load_dwordx4 v[232:235], v5, s[6:7] nt\n"
        "v_add_u32 v5, s29, v5\n"
        "global_load_dwordx4 v[236:239], v5, s[6:7] nt\n"
        "v_add_u32 v5, s29, v5\n"
        "v_perm_b32 v144, v145, v144, s30\n"
        "v_perm_b32 v146, v147, v146, s30\n"
        "v_lshl_or_b32 v144, v146, 16, v144\n"
        "ds_write_b32 v16, v144 offset:6144\n"
        "v_perm_b32 v148, v149, v148, s30\n"
        "v_perm_b32 v150, v151, v150, s30\n"
        "v_lshl_or_b32 v148, v150, 16, v148\n"
        "ds_write_b32 v16, v148 offset:6400\n"
        "s_waitcnt vmcnt(20)\n"
        "global_load_dwordx4 v[240:243], v5, s[6:7] nt\n"
        "v_add_u32 v5, s29, v5\n"
        "global_load_dwordx4 v[244:247], v5, s[6:7] nt\n"
        "v_add_u32 v5, s29, v5\n"
        "v_perm_b32 v152, v153, v152, s30\n"
        "v_perm_b32 v154, v155, v154, s30\n"
        "v_lshl_or_b32 v152, v154, 16, v152\n"
        "ds_write_b32 v16, v152 offset:6656\n"
        "v_perm_b32 v156, v157, v156, s30\n"
        "v_perm_b32 v158, v159, v158, s30\n"
        "v_lshl_or_b32 v156, v158, 16, v156\n"
        "ds_write_b32 v16, v156 offset:6912\n"
        "s_waitcnt vmcnt(20)\n"
        "global_load_dwordx4 v[64:67], v5, s[6:7] nt\n"
        "v_add_u32 v5, s29, v5\n"
        "global_load_dwordx4 v[68:71], v5, s[6:7] nt\n"
        "v_add_u32 v5, 3569664, v5\n"
        "s_waitcnt vmcnt(20)\n"
        "global_load_dwordx4 v[72:75], v4, s[4:5] nt\n"
        "v_add_u32 v4, s28, v4\n"
        "global_load_dwordx4 v[76:79], v4, s[4:5] nt\n"
        "v_add_u32 v4, s28, v4\n"
        "v_perm_b32 v160, v164, v160, s30\n"
        "v_perm_b32 v168, v172, v168, s30\n"
        "v_perm_b32 v161, v165, v161, s30\n"
        "v_perm_b32 v169, v173, v169, s30\n"
        "v_perm_b32 v162, v166, v162, s30\n"
        "v_perm_b32 v170, v174, v170, s30\n"
        "v_perm_b32 v163, v167, v163, s30\n"
        "v_perm_b32 v171, v175, v171, s30\n"
        "v_lshl_or_b32 v36, v168, 16, v160\n"
        "v_lshl_or_b32 v40, v169, 16, v161\n"
        "v_lshl_or_b32 v38, v170, 16, v162\n"
        "v_lshl_or_b32 v42, v171, 16, v163\n"
        "s_waitcnt vmcnt(20)\n"
        "global_load_dwordx4 v[80:83], v4, s[4:5] nt\n"
        "v_add_u32 v4, s28, v4\n"
        "global_load_dwordx4 v[84:87], v4, s[4:5] nt\n"
        "v_add_u32 v4, s28, v4\n"
        "s_waitcnt vmcnt(20)\n"
        "global_load_dwordx4 v[88:91], v4, s[4:5] nt\n"
        "v_add_u32 v4, s28, v4\n"
        "global_load_dwordx4 v[92:95], v4, s[4:5] nt\n"
        "v_add_u32 v4, s28, v4\n"
        "v_perm_b32 v176, v180, v176, s30\n"
        "v_perm_b32 v184, v188, v184, s30\n"
        "v_perm_b32 v177, v181, v177, s30\n"
        "v_perm_b32 v185, v189, v185, s30\n"
        "v_perm_b32 v178, v182, v178, s30\n"
        "v_perm_b32 v186, v190, v186, s30\n"
        "v_perm_b32 v179, v183, v179, s30\n"
        "v_perm_b32 v187, v191, v187, s30\n"
        "v_lshl_or_b32 v37, v184, 16, v176\n"
        "v_lshl_or_b32 v41, v185, 16, v177\n"
        "v_lshl_or_b32 v39, v186, 16, v178\n"
        "v_lshl_or_b32 v43, v187, 16, v179\n"
        "s_nop 0\n"
        "v_permlane32_swap_b32 v36, v38\n"
        "v_permlane32_swap_b32 v37, v39\n"
        "v_permlane32_swap_b32 v40, v42\n"
        "v_permlane32_swap_b32 v41, v43\n"
        "s_nop 0\n"
        "ds_write_b128 v17, v[36:39] offset:32768\n"
        "ds_write_b128 v17, v[40:43] offset:33792\n"
        "v_add_u32 v17, 65536, v17\n"
        "ds_write_b32 v25, v20 offset:1544\n"
        "v_add_u32 v20, 1, v20\n"
        "v_dot4_i32_i8 v28, v36, s31, v28\n"
        "v_dot4_i32_i8 v29, v40, s31, v29\n"
        "v_dot4_i32_i8 v28, v37, s31, v28\n"
        "v_dot4_i32_i8 v29, v41, s31, v29\n"
        "v_dot4_i32_i8 v28, v38, s31, v28\n"
        "v_dot4_i32_i8 v29, v42, s31, v29\n"
        "v_dot4_i32_i8 v28, v39, s31, v28\n"
        "v_dot4_i32_i8 v29, v43, s31, v29\n"
        "s_waitcnt vmcnt(20)\n"
        "global_load_dwordx4 v[96:99], v4, s[4:5] nt\n"
        "v_add_u32 v4, s28, v4\n"
        "global_load_dwordx4 v[100:103], v4, s[4:5] nt\n"
        "v_perm_b32 v192, v193, v192, s30\n"
        "v_perm_b32 v194, v195, v194, s30\n"
        "v_lshl_or_b32 v192, v194, 16, v192\n"
        "ds_write_b32 v16, v192 offset:10240\n"
        "v_perm_b32 v196, v197, v196, s30\n"
        "v_perm_b32 v198, v199, v198, s30\n"
        "v_lshl_or_b32 v196, v198, 16, v196\n"
        "ds_write_b32 v16, v196 offset:10496\n"
        "s_waitcnt vmcnt(20)\n"
        "global_load_dwordx4 v[104:107], v5, s[6:7] nt\n"
        "v_add_u32 v5, s29, v5\n"
        "global_load_dwordx4 v[108:111], v5, s[6:7] nt\n"
        "v_add_u32 v5, s29, v5\n"
        "v_perm_b32 v200, v201, v200, s30\n"
        "v_perm_b32 v202, v203, v202, s30\n"
        "v_lshl_or_b32 v200, v202, 16, v200\n"
        "ds_write_b32 v16, v200 offset:10752\n"
        "v_perm_b32 v204, v205, v204, s30\n"
        "v_perm_b32 v206, v207, v206, s30\n"
        "v_lshl_or_b32 v204, v206, 16, v204\n"
        "ds_write_b32 v16, v204 offset:11008\n"
        "s_waitcnt vmcnt(20)\n"
        "global_load_dwordx4 v[112:115], v5, s[6:7] nt\n"
        "v_add_u32 v5, s29, v5\n"
        "global_load_dwordx4 v[116:119], v5, s[6:7] nt\n"
        "v_add_u32 v5, s29, v5\n"
        "v_perm_b32 v208, v209, v208, s30\n"
        "v_perm_b32 v210, v211, v210, s30\n"
        "v_lshl_or_b32 v208, v210, 16, v208\n"
        "ds_write_b32 v16, v208 offset:11264\n"
        "v_perm_b32 v212, v213, v212, s30\n"
        "v_perm_b32 v214, v215, v214, s30\n"
        "v_lshl_or_b32 v212, v214, 16, v212\n"
        "ds_write_b32 v16, v212 offset:11520\n"
        "s_waitcnt vmcnt(20)\n"
        "global_load_dwordx4 v[120:123], v5, s[6:7] nt\n"
        "v_add_u32 v5, s29, v5\n"
        "global_load_dwordx4 v[124:127], v5, s[6:7] nt\n"
        "v_add_u32 v5, s29, v5\n"
        "v_perm_b32 v216, v217, v216, s30\n"
        "v_perm_b32 v218, v219, v218, s30\n"
        "v_lshl_or_b32 v216, v218, 16, v216\n"
        "ds_write_b32 v16, v216 offset:11776\n"
        "v_perm_b32 v220, v221, v220, s30\n"
        "v_perm_b32 v222, v223, v222, s30\n"
        "v_lshl_or_b32 v220, v222, 16, v220\n"
        "ds_write_b32 v16, v220 offset:12032\n"
        "s_waitcnt vmcnt(20)\n"
        "global_load_dwordx4 v[128:131], v5, s[6:7] nt\n"
        "v_add_u32 v5, s29, v5\n"
        "global_load_dwordx4 v[132:135], v5, s[6:7] nt\n"
        "s_waitcnt vmcnt(20)\n"
        "global_load_dwordx4 v[136:139], v6, s[6:7] nt\n"
        "v_add_u32 v6, s29, v6\n"
        "global_load_dwordx4 v[140:143], v6, s[6:7] nt\n"
        "v_add_u32 v6, s29, v6\n"
        "v_perm_b32 v224, v228, v224, s30\n"
        "v_perm_b32 v232, v236, v232, s30\n"
        "v_perm_b32 v225, v229, v225, s30\n"
        "v_perm_b32 v233, v237, v233, s30\n"
        "v_perm_b32 v226, v230, v226, s30\n"
        "v_perm_b32 v234, v238, v234, s30\n"
        "v_perm_b32 v227, v231, v227, s30\n"
        "v_perm_b32 v235, v239, v235, s30\n"
        "v_lshl_or_b32 v36, v232, 16, v224\n"
        "v_lshl_or_b32 v40, v233, 16, v225\n"
        "v_lshl_or_b32 v38, v234, 16, v226\n"
        "v_lshl_or_b32 v42, v235, 16, v227\n"
        "s_waitcnt vmcnt(20)\n"
        "global_load_dwordx4 v[144:147], v6, s[6:7] nt\n"
        "v_add_u32 v6, s29, v6\n"
        "global_load_dwordx4 v[148:151], v6, s[6:7] nt\n"
        "v_add_u32 v6, s29, v6\n"
        "s_waitcnt vmcnt(20)\n"
        "global_load_dwordx4 v[152:155], v6, s[6:7] nt\n"
        "v_add_u32 v6, s29, v6\n"
        "global_load_dwordx4 v[156:159], v6, s[6:7] nt\n"
        "v_add_u32 v6, s29, v6\n"
        "v_lshl_add_u32 v61, v10, 1, v8\n"
        "v_mov_b32 v253, 0\n"
        "s_lshl_b32 s56, s22, 7\n"
        "s_lshl_b32 s57, s22, 6\n"
        "s_add_u32 s57, s57, 1920\n"
        "v_mov_b32 v254, s56\n"
        "v_mov_b32 v255, s57\n"
        "v_cmp_gt_u32 vcc, 32, v61\n"
        "s_lshl_b32 s56, s22, 5\n"
        "global_load_ushort v252, v253, s[16:17]\n"
        "v_cndmask_b32 v254, v255, v254, vcc\n"
        "s_add_u32 s56, s56, 2880\n"
        "v_mov_b32 v255, s56\n"
        "v_cmp_gt_u32 vcc, 48, v61\n"
        "s_nop 1\n"
        "v_cndmask_b32 v254, v255, v254, vcc\n"
        "v_lshl_add_u32 v254, v61, 2, v254\n"
        "v_lshlrev_b32 v254, 1, v254\n"
        "s_mov_b32 exec_lo, 0x0fffffff\n"
        "s_mov_b32 exec_hi, 268435455\n"
        "global_load_dwordx2 v[248:249], v254, s[12:13]\n"
        "global_load_dwordx2 v[250:251], v254, s[14:15]\n"
        "s_mov_b64 exec, -1\n"
        "v_perm_b32 v240, v244, v240, s30\n"
        "v_perm_b32 v64, v68, v64, s30\n"
        "v_perm_b32 v241, v245, v241, s30\n"
        "v_perm_b32 v65, v69, v65, s30\n"
        "v_perm_b32 v242, v246, v242, s30\n"
        "v_perm_b32 v66, v70, v66, s30\n"
        "v_perm_b32 v243, v247, v243, s30\n"
        "v_perm_b32 v67, v71, v67, s30\n"
        "v_lshl_or_b32 v37, v64, 16, v240\n"
        "v_lshl_or_b32 v41, v65, 16, v241\n"
        "v_lshl_or_b32 v39, v66, 16, v242\n"
        "v_lshl_or_b32 v43, v67, 16, v243\n"
        "s_nop 0\n"
        "v_permlane32_swap_b32 v36, v38\n"
        "v_permlane32_swap_b32 v37, v39\n"
        "v_permlane32_swap_b32 v40, v42\n"
        "v_permlane32_swap_b32 v41, v43\n"
        "s_nop 0\n"
        "ds_write_b128 v17, v[36:39] offset:0\n"
        "ds_write_b128 v17, v[40:43] offset:1024\n"
        "ds_write_b32 v25, v20 offset:1544\n"
        "v_add_u32 v20, 1, v20\n"
        "v_dot4_i32_i8 v28, v36, s31, v28\n"
        "v_dot4_i32_i8 v29, v40, s31, v29\n"
        "v_dot4_i32_i8 v28, v37, s31, v28\n"
        "v_dot4_i32_i8 v29, v41, s31, v29\n"
        "v_dot4_i32_i8 v28, v38, s31, v28\n"
        "v_dot4_i32_i8 v29, v42, s31, v29\n"
        "v_dot4_i32_i8 v28, v39, s31, v28\n"
        "v_dot4_i32_i8 v29, v43, s31, v29\n"
        "s_waitcnt vmcnt(23)\n"
        "global_load_dwordx4 v[160:163], v6, s[6:7] nt\n"
        "v_add_u32 v6, s29, v6\n"
        "global_load_dwordx4 v[164:167], v6, s[6:7] nt\n"
        "v_perm_b32 v72, v73, v72, s30\n"
        "v_perm_b32 v74, v75, v74, s30\n"
        "v_lshl_or_b32 v72, v74, 16, v72\n"
        "ds_write_b32 v16, v72 offset:15360\n"
        "v_perm_b32 v76, v77, v76, s30\n"
        "v_perm_b32 v78, v79, v78, s30\n"
        "v_lshl_or_b32 v76, v78, 16, v76\n"
        "ds_write_b32 v16, v76 offset:15616\n"
        "s_waitcnt vmcnt(23)\n"
        "global_load_dwordx4 v[168:171], v7, s[6:7] nt\n"
        "v_add_u32 v7, s29, v7\n"
        "global_load_dwordx4 v[172:175], v7, s[6:7] nt\n"
        "v_add_u32 v7, s29, v7\n"
        "v_perm_b32 v80, v81, v80, s30\n"
        "v_perm_b32 v82, v83, v82, s30\n"
        "v_lshl_or_b32 v80, v82, 16, v80\n"
        "ds_write_b32 v16, v80 offset:15872\n"
        "v_perm_b32 v84, v85, v84, s30\n"
        "v_perm_b32 v86, v87, v86, s30\n"
        "v_lshl_or_b32 v84, v86, 16, v84\n"
        "ds_write_b32 v16, v84 offset:16128\n"
        "s_waitcnt vmcnt(23)\n"
        "global_load_dwordx4 v[176:179], v7, s[6:7] nt\n"
        "v_add_u32 v7, s29, v7\n"
        "global_load_dwordx4 v[180:183], v7, s[6:7] nt\n"
        "v_add_u32 v7, s29, v7\n"
        "v_perm_b32 v88, v89, v88, s30\n"
        "v_perm_b32 v90, v91, v90, s30\n"
        "v_lshl_or_b32 v88, v90, 16, v88\n"
        "ds_write_b32 v16, v88 offset:16384\n"
        "v_perm_b32 v92, v93, v92, s30\n"
        "v_perm_b32 v94, v95, v94, s30\n"
        "v_lshl_or_b32 v92, v94, 16, v92\n"
        "ds_write_b32 v16, v92 offset:16640\n"
        "s_waitcnt vmcnt(23)\n"
        "global_load_dwordx4 v[184:187], v7, s[6:7] nt\n"
        "v_add_u32 v7, s29, v7\n"
        "global_load_dwordx4 v[188:191], v7, s[6:7] nt\n"
        "v_add_u32 v7, s29, v7\n"
        "v_perm_b32 v96, v97, v96, s30\n"
        "v_perm_b32 v98, v99, v98, s30\n"
        "v_lshl_or_b32 v96, v98, 16, v96\n"
        "ds_write_b32 v16, v96 offset:16896\n"
        "v_perm_b32 v100, v101, v100, s30\n"
        "v_perm_b32 v102, v103, v102, s30\n"
        "v_lshl_or_b32 v100, v102, 16, v100\n"
        "ds_write_b32 v16, v100 offset:17152\n"
        "s_waitcnt vmcnt(23)\n"
        "global_load_dwordx4 v[192:195], v7, s[6:7] nt\n"
        "v_add_u32 v7, s29, v7\n"
        "global_load_dwordx4 v[196:199], v7, s[6:7] nt\n"
        "v_add_u32 v7, s29, v7\n"
        "s_waitcnt vmcnt(23)\n"
        "global_load_dwordx4 v[200:203], v7, s[6:7] nt\n"
        "v_add_u32 v7, s29, v7\n"
        "global_load_dwordx4 v[204:207], v7, s[6:7] nt\n"
        "v_add_u32 v7, s29, v7\n"
        "v_perm_b32 v104, v108, v104, s30\n"
        "v_perm_b32 v112, v116, v112, s30\n"
        "v_perm_b32 v105, v109, v105, s30\n"
        "v_perm_b32 v113, v117, v113, s30\n"
        "v_perm_b32 v106, v110, v106, s30\n"
        "v_perm_b32 v114, v118, v114, s30\n"
        "v_perm_b32 v107, v111, v107, s30\n"
        "v_perm_b32 v115, v119, v115, s30\n"
        "v_lshl_or_b32 v36, v112, 16, v104\n"
        "v_lshl_or_b32 v40, v113, 16, v105\n"
        "v_lshl_or_b32 v38, v114, 16, v106\n"
        "v_lshl_or_b32 v42, v115, 16, v107\n"
        "s_waitcnt vmcnt(23)\n"
        "global_load_dwordx4 v[208:211], v7, s[6:7] nt\n"
        "v_add_u32 v7, s29, v7\n"
        "global_load_dwordx4 v[212:215], v7, s[6:7] nt\n"
        "v_add_u32 v7, s29, v7\n"
        "s_waitcnt vmcnt(23)\n"
        "global_load_dwordx4 v[216:219], v7, s[6:7] nt\n"
        "v_add_u32 v7, s29, v7\n"
        "global_load_dwordx4 v[220:223], v7, s[6:7] nt\n"
        "v_add_u32 v7, s29, v7\n"
        "v_perm_b32 v120, v124, v120, s30\n"
        "v_perm_b32 v128, v132, v128, s30\n"
        "v_perm_b32 v121, v125, v121, s30\n"
        "v_perm_b32 v129, v133, v129, s30\n"
        "v_perm_b32 v122, v126, v122, s30\n"
        "v_perm_b32 v130, v134, v130, s30\n"
        "v_perm_b32 v123, v127, v123, s30\n"
        "v_perm_b32 v131, v135, v131, s30\n"
        "v_lshl_or_b32 v37, v128, 16, v120\n"
        "v_lshl_or_b32 v41, v129, 16, v121\n"
        "v_lshl_or_b32 v39, v130, 16, v122\n"
        "v_lshl_or_b32 v43, v131, 16, v123\n"
        "s_nop 0\n"
        "v_permlane32_swap_b32 v36, v38\n"
        "v_permlane32_swap_b32 v37, v39\n"
        "v_permlane32_swap_b32 v40, v42\n"
        "v_permlane32_swap_b32 v41, v43\n"
        "s_nop 0\n"
        "ds_write_b128 v17, v[36:39] offset:32768\n"
        "ds_write_b128 v17, v[40:43] offset:33792\n"
        "v_dot4_i32_i8 v28, v36, s31, v28\n"
        "v_dot4_i32_i8 v29, v40, s31, v29\n"
        "v_dot4_i32_i8 v28, v37, s31, v28\n"
        "v_dot4_i32_i8 v29, v41, s31, v29\n"
        "v_dot4_i32_i8 v28, v38, s31, v28\n"
        "v_dot4_i32_i8 v29, v42, s31, v29\n"
        "v_dot4_i32_i8 v28, v39, s31, v28\n"
        "v_dot4_i32_i8 v29, v43, s31, v29\n"
        "s_nop 2\n"
        "v_and_b32 v28, 0xffff, v28\n"
        "v_lshl_or_b32 v28, v29, 16, v28\n"
        "ds_write_b64 v21, v[28:29] offset:19712\n"
        "ds_write_b32 v25, v20 offset:1544\n"
        "v_add_u32 v20, 1, v20\n"
        "s_waitcnt vmcnt(23)\n"
        "global_load_dwordx4 v[224:227], v7, s[6:7] nt\n"
        "v_add_u32 v7, s29, v7\n"
        "global_load_dwordx4 v[228:231], v7, s[6:7] nt\n"
        "v_mov_b32 v12, 1\n"
        "ds_write_b32 v25, v12 offset:512\n"
        "s_waitcnt vmcnt(23)\n"
        "v_perm_b32 v136, v140, v136, s30\n"
        "v_perm_b32 v144, v148, v144, s30\n"
        "v_perm_b32 v137, v141, v137, s30\n"
        "v_perm_b32 v145, v149, v145, s30\n"
        "v_perm_b32 v138, v142, v138, s30\n"
        "v_perm_b32 v146, v150, v146, s30\n"
        "v_perm_b32 v139, v143, v139, s30\n"
        "v_perm_b32 v147, v151, v147, s30\n"
        "v_lshl_or_b32 v36, v144, 16, v136\n"
        "v_lshl_or_b32 v40, v145, 16, v137\n"
        "v_lshl_or_b32 v38, v146, 16, v138\n"
        "v_lshl_or_b32 v42, v147, 16, v139\n"
        "s_waitcnt vmcnt(21)\n"
        "s_waitcnt vmcnt(16)\n"
        "v_perm_b32 v152, v156, v152, s30\n"
        "v_perm_b32 v160, v164, v160, s30\n"
        "v_perm_b32 v153, v157, v153, s30\n"
        "v_perm_b32 v161, v165, v161, s30\n"
        "v_perm_b32 v154, v158, v154, s30\n"
        "v_perm_b32 v162, v166, v162, s30\n"
        "v_perm_b32 v155, v159, v155, s30\n"
        "v_perm_b32 v163, v167, v163, s30\n"
        "v_lshl_or_b32 v37, v160, 16, v152\n"
        "v_lshl_or_b32 v41, v161, 16, v153\n"
        "v_lshl_or_b32 v39, v162, 16, v154\n"
        "v_lshl_or_b32 v43, v163, 16, v155\n"
        "s_nop 0\n"
        "v_permlane32_swap_b32 v36, v38\n"
        "v_permlane32_swap_b32 v37, v39\n"
        "v_permlane32_swap_b32 v40, v42\n"
        "v_permlane32_swap_b32 v41, v43\n"
        "s_nop 0\n"
        "global_store_dwordx4 v35, v[36:39], s[44:45] sc1\n"
        "global_store_dwordx4 v35, v[40:43], s[44:45] offset:1024 sc1\n"
        "v_dot4_i32_i8 v30, v36, s31, v30\n"
        "v_dot4_i32_i8 v31, v40, s31, v31\n"
        "v_dot4_i32_i8 v30, v37, s31, v30\n"
        "v_dot4_i32_i8 v31, v41, s31, v31\n"
        "v_dot4_i32_i8 v30, v38, s31, v30\n"
        "v_dot4_i32_i8 v31, v42, s31, v31\n"
        "v_dot4_i32_i8 v30, v39, s31, v30\n"
        "v_dot4_i32_i8 v31, v43, s31, v31\n"
        "s_nop 2\n"
        "v_permlane16_swap_b32 v30, v31\n"
        "s_nop 0\n"
        "v_add_u32 v30, v30, v31\n"
        "v_mov_b32 v31, 0\n"
        "s_nop 0\n"
        "v_add_u32_dpp v34, v30, v30 row_ror:8 row_mask:0xf bank_mask:0xf\n"
        "v_mov_b32 v30, 0\n"
        "s_mov_b32 s38, 8388608\n"
        "W8_tg0:\n"
        "ds_read_b32 v12, v25 offset:1024\n"
        "s_sub_u32 s38, s38, 1\n"
        "s_waitcnt lgkmcnt(0)\n"
        "s_cbranch_scc1 W8_tg0_x\n"
        "v_readfirstlane_b32 s39, v12\n"
        "s_cmp_ge_u32 s39, 1\n"
        "s_cbranch_scc1 W8_tg0_x\n"
        "s_sleep 1\n"
        "s_branch W8_tg0\n"
        "W8_tg0_x:\n"
        "v_lshrrev_b32 v12, 0, v0\n"
        "v_lshlrev_b32 v12, 7, v12\n"
        "v_add_u32 v12, 134408, v12\n"
        "s_mov_b64 exec, 3\n"
        "ds_read_b64 v[26:27], v12\n"
        "s_mov_b64 exec, -1\n"
        "s_waitcnt vmcnt(16)\n"
        "s_waitcnt vmcnt(14)\n"
        "v_perm_b32 v168, v172, v168, s30\n"
        "v_perm_b32 v176, v180, v176, s30\n"
        "v_perm_b32 v169, v173, v169, s30\n"
        "v_perm_b32 v177, v181, v177, s30\n"
        "v_perm_b32 v170, v174, v170, s30\n"
        "v_perm_b32 v178, v182, v178, s30\n"
        "v_perm_b32 v171, v175, v171, s30\n"
        "v_perm_b32 v179, v183, v179, s30\n"
        "v_lshl_or_b32 v36, v176, 16, v168\n"
        "v_lshl_or_b32 v40, v177, 16, v169\n"
        "v_lshl_or_b32 v44, v178, 16, v170\n"
        "v_lshl_or_b32 v48, v179, 16, v171\n"
        "v_dot4_i32_i8 v30, v36, s31, v30\n"
        "v_dot4_i32_i8 v31, v40, s31, v31\n"
        "v_dot4_i32_i8 v32, v44, s31, v32\n"
        "v_dot4_i32_i8 v33, v48, s31, v33\n"
        "s_waitcnt vmcnt(12)\n"
        "s_waitcnt vmcnt(10)\n"
        "v_perm_b32 v184, v188, v184, s30\n"
        "v_perm_b32 v192, v196, v192, s30\n"
        "v_perm_b32 v185, v189, v185, s30\n"
        "v_perm_b32 v193, v197, v193, s30\n"
        "v_perm_b32 v186, v190, v186, s30\n"
        "v_perm_b32 v194, v198, v194, s30\n"
        "v_perm_b32 v187, v191, v187, s30\n"
        "v_perm_b32 v195, v199, v195, s30\n"
        "v_lshl_or_b32 v37, v192, 16, v184\n"
        "v_lshl_or_b32 v41, v193, 16, v185\n"
        "v_lshl_or_b32 v45, v194, 16, v186\n"
        "v_lshl_or_b32 v49, v195, 16, v187\n"
        "v_dot4_i32_i8 v30, v37, s31, v30\n"
        "v_dot4_i32_i8 v31, v41, s31, v31\n"
        "v_dot4_i32_i8 v32, v45, s31, v32\n"
        "v_dot4_i32_i8 v33, v49, s31, v33\n"
        "s_waitcnt vmcnt(8)\n"
        "s_waitcnt vmcnt(6)\n"
        "v_perm_b32 v200, v204, v200, s30\n"
        "v_perm_b32 v208, v212, v208, s30\n"
        "v_perm_b32 v201, v205, v201, s30\n"
        "v_perm_b32 v209, v213, v209, s30\n"
        "v_perm_b32 v202, v206, v202, s30\n"
        "v_perm_b32 v210, v214, v210, s30\n"
        "v_perm_b32 v203, v207, v203, s30\n"
        "v_perm_b32 v211, v215, v211, s30\n"
        "v_lshl_or_b32 v38, v208, 16, v200\n"
        "v_lshl_or_b32 v42, v209, 16, v201\n"
        "v_lshl_or_b32 v46, v210, 16, v202\n"
        "v_lshl_or_b32 v50, v211, 16, v203\n"
        "v_dot4_i32_i8 v30, v38, s31, v30\n"
        "v_dot4_i32_i8 v31, v42, s31, v31\n"
        "v_dot4_i32_i8 v32, v46, s31, v32\n"
        "v_dot4_i32_i8 v33, v50, s31, v33\n"
        "s_waitcnt vmcnt(4)\n"
        "s_waitcnt vmcnt(2)\n"
        "v_perm_b32 v216, v220, v216, s30\n"
        "v_perm_b32 v224, v228, v224, s30\n"
        "v_perm_b32 v217, v221, v217, s30\n"
        "v_perm_b32 v225, v229, v225, s30\n"
        "v_perm_b32 v218, v222, v218, s30\n"
        "v_perm_b32 v226, v230, v226, s30\n"
        "v_perm_b32 v219, v223, v219, s30\n"
        "v_perm_b32 v227, v231, v227, s30\n"
        "v_lshl_or_b32 v39, v224, 16, v216\n"
        "v_lshl_or_b32 v43, v225, 16, v217\n"
        "v_lshl_or_b32 v47, v226, 16, v218\n"
        "v_lshl_or_b32 v51, v227, 16, v219\n"
        "ds_write_b32 v25, v20 offset:1544\n"
        "global_store_dwordx4 v19, v[36:39], s[44:45] offset:0 sc1\n"
        "global_store_dwordx4 v19, v[40:43], s[44:45] offset:1024 sc1\n"
        "global_store_dwordx4 v19, v[44:47], s[44:45] offset:2048 sc1\n"
        "global_store_dwordx4 v19, v[48:51], s[44:45] offset:3072 sc1\n"
        "v_dot4_i32_i8 v30, v39, s31, v30\n"
        "v_dot4_i32_i8 v31, v43, s31, v31\n"
        "v_dot4_i32_i8 v32, v47, s31, v32\n"
        "v_dot4_i32_i8 v33, v51, s31, v33\n"
        "s_nop 2\n"
        "v_permlane32_swap_b32 v30, v32\n"
        "v_permlane32_swap_b32 v31, v33\n"
        "s_nop 0\n"
        "v_add_u32 v30, v30, v32\n"
        "v_add_u32 v31, v31, v33\n"
        "s_nop 1\n"
        "v_permlane16_swap_b32 v30, v31\n"
        "s_nop 0\n"
        "v_add_u32 v30, v30, v31\n"
        "global_store_short v22, v30, s[46:47] offset:256 sc1\n"
        "s_mov_b32 exec_lo, 0x00ff00ff\n"
        "s_mov_b32 exec_hi, 0x00ff00ff\n"
        "global_store_short v23, v34, s[46:47] offset:384 sc1\n"
        "s_mov_b64 exec, 2\n"
        "s_waitcnt vmcnt(0) lgkmcnt(0)\n"
        "global_atomic_swap_x2 v24, v[26:27], s[8:9] sc1\n"
        "s_mov_b64 exec, -1\n"
        "s_load_dword s48, s[18:19], 0x0\n"
        "v_mov_b32 v32, v252\n"
        "v_mov_b32 v34, v248\n"
        "v_mov_b32 v35, v249\n"
        "v_mov_b32 v38, v250\n"
        "v_mov_b32 v39, v251\n"
        "s_lshl_b32 s24, s22, 13\n"
        "s_add_u32 s24, s24, 5771264\n"
        "v_add_u32 v21, s24, v19\n"
        "v_add_u32 v22, 8192, v21\n"
        "v_lshlrev_b32 v23, 5, v61\n"
        "v_add_u32 v23, 131072, v23\n"
        "s_waitcnt vmcnt(0)\n"
        "v_lshlrev_b32 v32, 16, v32\n"
        "v_and_b32 v37, 0xffff0000, v35\n"
        "v_lshlrev_b32 v36, 16, v35\n"
        "v_and_b32 v35, 0xffff0000, v34\n"
        "v_lshlrev_b32 v34, 16, v34\n"
        "v_and_b32 v41, 0xffff0000, v39\n"
        "v_lshlrev_b32 v40, 16, v39\n"
        "v_and_b32 v39, 0xffff0000, v38\n"
        "v_lshlrev_b32 v38, 16, v38\n"
        "v_mul_f32 v44, v32, v34\n"
        "v_mul_f32 v46, v32, v35\n"
        "v_mul_f32 v48, v32, v36\n"
        "v_mul_f32 v50, v32, v37\n"
        "s_mov_b32 s49, 6\n"
        "s_mov_b32 s50, 268435455\n"
        "s_mov_b32 s51, 268435455\n"
        "v_mov_b32 v33, 3\n"
        "W8_tab:\n"
        "s_mov_b64 exec, -1\n"
        "s_mov_b32 s38, 8388608\n"
        "W8_tr:\n"
        "ds_read_b32 v12, v25 offset:1024\n"
        "s_sub_u32 s38, s38, 1\n"
        "s_waitcnt lgkmcnt(0)\n"
        "s_cbranch_scc1 W8_tr_x\n"
        "v_readfirstlane_b32 s39, v12\n"
        "s_cmp_ge_u32 s39, 21\n"
        "s_cbranch_scc1 W8_tr_x\n"
        "s_sleep 2\n"
        "s_branch W8_tr\n"
        "W8_tr_x:\n"
        "s_mov_b32 s38, 8388608\n"
        "W8_tf:\n"
        "ds_read_b32 v12, v25 offset:0\n"
        "s_sub_u32 s38, s38, 1\n"
        "s_waitcnt lgkmcnt(0)\n"
        "s_cbranch_scc1 W8_tf_x\n"
        "v_readfirstlane_b32 s39, v12\n"
        "s_cmp_ge_u32 s39, 12\n"
        "s_cbranch_scc1 W8_tf_x\n"
        "s_sleep 2\n"
        "s_branch W8_tf\n"
        "W8_tf_x:\n"
        "s_mov_b64 exec, s[50:51]\n"
        "global_load_dwordx4 v[64:67], v21, s[8:9] offset:-4096 sc1\n"
        "global_load_dwordx4 v[68:71], v21, s[8:9] offset:-3072 sc1\n"
        "global_load_dwordx4 v[72:75], v21, s[8:9] offset:-2048 sc1\n"
        "global_load_dwordx4 v[76:79], v21, s[8:9] offset:-1024 sc1\n"
        "global_load_dwordx4 v[80:83], v21, s[8:9] offset:0 sc1\n"
        "global_load_dwordx4 v[84:87], v21, s[8:9] offset:1024 sc1\n"
        "global_load_dwordx4 v[88:91], v21, s[8:9] offset:2048 sc1\n"
        "global_load_dwordx4 v[92:95], v21, s[8:9] offset:3072 sc1\n"
        "s_waitcnt vmcnt(0)\n"
        "v_pk_add_i16 v64, v64, v68\n"
        "v_pk_add_i16 v65, v65, v69\n"
        "v_pk_add_i16 v66, v66, v70\n"
        "v_pk_add_i16 v67, v67, v71\n"
        "v_pk_add_i16 v72, v72, v76\n"
        "v_pk_add_i16 v73, v73, v77\n"
        "v_pk_add_i16 v74, v74, v78\n"
        "v_pk_add_i16 v75, v75, v79\n"
        "v_pk_add_i16 v80, v80, v84\n"
        "v_pk_add_i16 v81, v81, v85\n"
        "v_pk_add_i16 v82, v82, v86\n"
        "v_pk_add_i16 v83, v83, v87\n"
        "v_pk_add_i16 v88, v88, v92\n"
        "v_pk_add_i16 v89, v89, v93\n"
        "v_pk_add_i16 v90, v90, v94\n"
        "v_pk_add_i16 v91, v91, v95\n"
        "v_bfe_i32 v68, v64, 0, 16\n"
        "v_ashrrev_i32 v64, 16, v64\n"
        "v_bfe_i32 v69, v65, 0, 16\n"
        "v_ashrrev_i32 v65, 16, v65\n"
        "v_bfe_i32 v70, v66, 0, 16\n"
        "v_ashrrev_i32 v66, 16, v66\n"
        "v_bfe_i32 v71, v67, 0, 16\n"
        "v_ashrrev_i32 v67, 16, v67\n"
        "v_bfe_i32 v76, v72, 0, 16\n"
        "v_ashrrev_i32 v72, 16, v72\n"
        "v_bfe_i32 v77, v73, 0, 16\n"
        "v_ashrrev_i32 v73, 16, v73\n"
        "v_bfe_i32 v78, v74, 0, 16\n"
        "v_ashrrev_i32 v74, 16, v74\n"
        "v_bfe_i32 v79, v75, 0, 16\n"
        "v_ashrrev_i32 v75, 16, v75\n"
        "v_bfe_i32 v84, v80, 0, 16\n"
        "v_ashrrev_i32 v80, 16, v80\n"
        "v_bfe_i32 v85, v81, 0, 16\n"
        "v_ashrrev_i32 v81, 16, v81\n"
        "v_bfe_i32 v86, v82, 0, 16\n"
        "v_ashrrev_i32 v82, 16, v82\n"
        "v_bfe_i32 v87, v83, 0, 16\n"
        "v_ashrrev_i32 v83, 16, v83\n"
        "v_bfe_i32 v92, v88, 0, 16\n"
        "v_ashrrev_i32 v88, 16, v88\n"
        "v_bfe_i32 v93, v89, 0, 16\n"
        "v_ashrrev_i32 v89, 16, v89\n"
        "v_bfe_i32 v94, v90, 0, 16\n"
        "v_ashrrev_i32 v90, 16, v90\n"
        "v_bfe_i32 v95, v91, 0, 16\n"
        "v_ashrrev_i32 v91, 16, v91\n"
        "v_add3_u32 v100, v68, v76, v84\n"
        "v_add3_u32 v101, v64, v72, v80\n"
        "v_add3_u32 v102, v69, v77, v85\n"
        "v_add3_u32 v103, v65, v73, v81\n"
        "v_add3_u32 v104, v70, v78, v86\n"
        "v_add3_u32 v105, v66, v74, v82\n"
        "v_add3_u32 v106, v71, v79, v87\n"
        "v_add3_u32 v107, v67, v75, v83\n"
        "v_add_u32 v100, v100, v92\n"
        "v_add_u32 v101, v101, v88\n"
        "v_add_u32 v102, v102, v93\n"
        "v_add_u32 v103, v103, v89\n"
        "v_add_u32 v104, v104, v94\n"
        "v_add_u32 v105, v105, v90\n"
        "v_add_u32 v106, v106, v95\n"
        "v_add_u32 v107, v107, v91\n"
        "s_nop 1\n"
        "v_permlane32_swap_b32 v100, v104\n"
        "v_permlane32_swap_b32 v101, v105\n"
        "v_permlane32_swap_b32 v102, v106\n"
        "v_permlane32_swap_b32 v103, v107\n"
        "s_nop 0\n"
        "v_add_u32 v52, v100, v104\n"
        "v_add_u32 v53, v101, v105\n"
        "v_add_u32 v54, v102, v106\n"
        "v_add_u32 v55, v103, v107\n"
        "v_mul_lo_u32 v52, v52, s48\n"
        "v_mul_lo_u32 v53, v53, s48\n"
        "v_mul_lo_u32 v54, v54, s48\n"
        "v_mul_lo_u32 v55, v55, s48\n"
        "v_cvt_f32_i32 v52, v52\n"
        "v_cvt_f32_i32 v53, v53\n"
        "v_cvt_f32_i32 v54, v54\n"
        "v_cvt_f32_i32 v55, v55\n"
        "v_fma_f32 v45, -v52, v44, v38\n"
        "v_fma_f32 v47, -v53, v46, v39\n"
        "v_fma_f32 v49, -v54, v48, v40\n"
        "v_fma_f32 v51, -v55, v50, v41\n"
        "ds_write_b128 v23, v[44:47]\n"
        "ds_write_b128 v23, v[48:51] offset:16\n"
        "s_mov_b64 exec, -1\n"
        "ds_write_b32 v25, v33 offset:512\n"
        "v_add_u32 v33, 1, v33\n"
        "s_mov_b32 s50, 0\n"
        "s_mov_b32 s51, 0x00ffffff\n"
        "s_add_u32 s49, s49, 2\n"
        "s_cmp_eq_u32 s49, 6\n"
        "s_cbranch_scc1 W8_tab\n"
        "s_branch W8_fin\n"
        "W8_other:\n"
        "v_and_b32 v1, 63, v0\n"
        "s_add_u32 s33, s21, s22\n"
        "s_cmp_ge_u32 s20, 4\n"
        "s_cbranch_scc1 W8_comp\n"
        "v_lshlrev_b32 v25, 2, v1\n"
        "v_add_u32 v25, 133124, v25\n"
        "s_cmp_eq_u32 s20, 1\n"
        "s_cbranch_scc1 W8_load\n"
        "s_and_b32 s30, s20, 1\n"
        "s_lshl_b32 s24, s21, 9\n"
        "s_add_u32 s24, s24, 6029312\n"
        "s_lshl_b32 s25, s22, 10\n"
        "s_add_u32 s25, s25, 6291456\n"
        "s_add_u32 s26, s22, 16\n"
        "s_cmp_eq_u32 s30, 0\n"
        "s_cselect_b32 s24, s24, s25\n"
        "s_cselect_b32 s26, s21, s26\n"
        "s_cselect_b32 s31, s22, s21\n"
        "s_mul_i32 s27, s30, 768\n"
        "s_add_u32 s27, s27, 256\n"
        "s_lshl_b32 s26, s26, 7\n"
        "s_add_u32 s26, s26, 6815744\n"
        "v_mov_b32 v5, s26\n"
        "v_add_u32 v8, s27, v25\n"
        "s_lshl_b32 s26, s30, 7\n"
        "s_add_u32 s26, s26, 134408\n"
        "v_mov_b32 v12, s26\n"
        "s_add_u32 s24, s8, s24\n"
        "s_addc_u32 s25, s9, 0\n"
        "s_add_u32 s28, s30, 1\n"
        "v_mov_b32 v13, s28\n"
        "s_lshl_b32 s28, s30, 0\n"
        "s_add_u32 s28, s28, 4\n"
        "s_mov_b32 s38, 8388608\n"
        "W8_tg2:\n"
        "ds_read_b32 v14, v25 offset:1024\n"
        "s_sub_u32 s38, s38, 1\n"
        "s_waitcnt lgkmcnt(0)\n"
        "s_cbranch_scc1 W8_tg2_x\n"
        "v_readfirstlane_b32 s39, v14\n"
        "s_cmp_ge_u32 s39, 1\n"
        "s_cbranch_scc1 W8_tg2_x\n"
        "s_sleep 2\n"
        "s_branch W8_tg2\n"
        "W8_tg2_x:\n"
        "ds_read_b64 v[6:7], v12\n"
        "s_mov_b32 s38, 8388608\n"
        "W8_pst:\n"
        "ds_read_b32 v14, v25 offset:1544\n"
        "s_sub_u32 s38, s38, 1\n"
        "s_waitcnt lgkmcnt(0)\n"
        "s_cbranch_scc1 W8_pst_x\n"
        "v_readfirstlane_b32 s39, v14\n"
        "s_cmp_ge_u32 s39, 3\n"
        "s_cbranch_scc1 W8_pst_x\n"
        "s_sleep 4\n"
        "s_branch W8_pst\n"
        "W8_pst_x:\n"
        "s_add_u32 s14, s21, s22\n"
        "s_and_b32 s12, s14, 2\n"
        "s_lshl_b32 s12, s12, 5\n"
        "s_xor_b32 s13, s12, 64\n"
        "s_and_b32 s14, s14, 1\n"
        "v_add_u32 v16, 1, v13\n"
        "v_add_u32 v17, 2, v13\n"
        "v_add_u32 v18, 3, v13\n"
        "v_mov_b32 v15, v13\n"
        "s_mov_b32 s29, 2097152\n"
        "W8_pl:\n"
        "s_load_dwordx16 s[32:47], s[24:25], s12 glc\n"
        "s_load_dwordx16 s[48:63], s[24:25], s13 glc\n"
        "s_sub_u32 s29, s29, 1\n"
        "s_cselect_b32 s27, 1, 0\n"
        "s_cmp_eq_u32 s14, 1\n"
        "s_waitcnt lgkmcnt(0)\n"
        "s_cbranch_scc1 W8_plb\n"
        "v_cmpx_eq_u64 vcc, s[32:33], v[6:7]\n"
        "v_cmpx_eq_u64 vcc, s[34:35], v[6:7]\n"
        "v_cmpx_eq_u64 vcc, s[36:37], v[6:7]\n"
        "v_cmpx_eq_u64 vcc, s[38:39], v[6:7]\n"
        "ds_write_b32 v8, v15\n"
        "v_cmpx_eq_u64 vcc, s[40:41], v[6:7]\n"
        "v_cmpx_eq_u64 vcc, s[42:43], v[6:7]\n"
        "v_cmpx_eq_u64 vcc, s[44:45], v[6:7]\n"
        "v_cmpx_eq_u64 vcc, s[46:47], v[6:7]\n"
        "ds_write_b32 v8, v16\n"
        "v_cmpx_eq_u64 vcc, s[48:49], v[6:7]\n"
        "v_cmpx_eq_u64 vcc, s[50:51], v[6:7]\n"
        "v_cmpx_eq_u64 vcc, s[52:53], v[6:7]\n"
        "v_cmpx_eq_u64 vcc, s[54:55], v[6:7]\n"
        "ds_write_b32 v8, v17\n"
        "v_cmpx_eq_u64 vcc, s[56:57], v[6:7]\n"
        "v_cmpx_eq_u64 vcc, s[58:59], v[6:7]\n"
        "v_cmpx_eq_u64 vcc, s[60:61], v[6:7]\n"
        "v_cmpx_eq_u64 vcc, s[62:63], v[6:7]\n"
        "s_branch W8_plc\n"
        "W8_plb:\n"
        "v_cmpx_eq_u64 vcc, s[40:41], v[6:7]\n"
        "v_cmpx_eq_u64 vcc, s[42:43], v[6:7]\n"
        "v_cmpx_eq_u64 vcc, s[44:45], v[6:7]\n"
        "v_cmpx_eq_u64 vcc, s[46:47], v[6:7]\n"
        "ds_write_b32 v8, v15\n"
        "v_cmpx_eq_u64 vcc, s[32:33], v[6:7]\n"
        "v_cmpx_eq_u64 vcc, s[34:35], v[6:7]\n"
        "v_cmpx_eq_u64 vcc, s[36:37], v[6:7]\n"
        "v_cmpx_eq_u64 vcc, s[38:39], v[6:7]\n"
        "ds_write_b32 v8, v16\n"
        "v_cmpx_eq_u64 vcc, s[56:57], v[6:7]\n"
        "v_cmpx_eq_u64 vcc, s[58:59], v[6:7]\n"
        "v_cmpx_eq_u64 vcc, s[60:61], v[6:7]\n"
        "v_cmpx_eq_u64 vcc, s[62:63], v[6:7]\n"
        "ds_write_b32 v8, v17\n"
        "v_cmpx_eq_u64 vcc, s[48:49], v[6:7]\n"
        "v_cmpx_eq_u64 vcc, s[50:51], v[6:7]\n"
        "v_cmpx_eq_u64 vcc, s[52:53], v[6:7]\n"
        "v_cmpx_eq_u64 vcc, s[54:55], v[6:7]\n"
        "W8_plc:\n"
        "s_nop 4\n"
        "s_cbranch_execnz W8_pg\n"
        "s_mov_b64 exec, -1\n"
        "s_cmp_eq_u32 s27, 1\n"
        "s_cbranch_scc1 W8_pg\n"
        "s_sleep 1\n"
        "s_branch W8_pl\n"
        "W8_pg:\n"
        "ds_write_b32 v8, v18\n"
        "s_max_i32 s29, s29, 0\n"
        "v_add_u32 v15, 4, v15\n"
        "v_add_u32 v16, 4, v16\n"
        "v_add_u32 v17, 4, v17\n"
        "v_add_u32 v18, 4, v18\n"
        "s_add_u32 s24, s24, 128\n"
        "s_addc_u32 s25, s25, 0\n"
        "s_sub_u32 s28, s28, 1\n"
        "s_cmp_lg_u32 s28, 0\n"
        "s_cbranch_scc1 W8_pl\n"
        "s_cmp_lg_u32 s31, 0\n"
        "s_cbranch_scc1 W8_fin\n"
        "s_mov_b64 exec, 1\n"
        "global_store_dwordx2 v5, v[6:7], s[8:9] sc1\n"
        "s_branch W8_fin\n"
        "W8_load:\n"
        "v_lshlrev_b32 v2, 4, v1\n"
        "s_xor_b32 s24, s33, 0\n"
        "s_and_b32 s24, s24, 3\n"
        "s_lshl_b32 s25, s24, 13\n"
        "v_add_u32 v4, s25, v2\n"
        "v_add_u32 v8, 4096, v4\n"
        "s_mul_i32 s25, s24, 6144\n"
        "v_add_u32 v12, s25, v2\n"
        "s_xor_b32 s24, s33, 1\n"
        "s_and_b32 s24, s24, 3\n"
        "s_lshl_b32 s25, s24, 13\n"
        "v_add_u32 v5, s25, v2\n"
        "v_add_u32 v9, 4096, v5\n"
        "s_mul_i32 s25, s24, 6144\n"
        "v_add_u32 v13, s25, v2\n"
        "s_xor_b32 s24, s33, 2\n"
        "s_and_b32 s24, s24, 3\n"
        "s_lshl_b32 s25, s24, 13\n"
        "v_add_u32 v6, s25, v2\n"
        "v_add_u32 v10, 4096, v6\n"
        "s_mul_i32 s25, s24, 6144\n"
        "v_add_u32 v14, s25, v2\n"
        "s_xor_b32 s24, s33, 3\n"
        "s_and_b32 s24, s24, 3\n"
        "s_lshl_b32 s25, s24, 13\n"
        "v_add_u32 v7, s25, v2\n"
        "v_add_u32 v11, 4096, v7\n"
        "s_mul_i32 s25, s24, 6144\n"
        "v_add_u32 v15, s25, v2\n"
        "s_waitcnt lgkmcnt(0)\n"
        "s_lshl_b32 s24, s22, 17\n"
        "s_add_u32 s24, s24, 2097152\n"
        "s_add_u32 s40, s8, s24\n"
        "s_addc_u32 s41, s9, 0\n"
        "s_mul_i32 s24, s22, 98304\n"
        "s_add_u32 s24, s24, 4194304\n"
        "s_add_u32 s42, s8, s24\n"
        "s_addc_u32 s43, s9, 0\n"
        "s_lshr_b32 s37, s21, 2\n"
        "s_add_u32 s24, s21, s22\n"
        "s_xor_b32 s37, s37, s24\n"
        "s_and_b32 s37, s37, 3\n"
        "s_and_b32 s24, s21, 3\n"
        "s_lshl2_add_u32 s37, s37, s24\n"
        "s_lshl_b32 s37, 1, s37\n"
        "s_mov_b32 s34, 0\n"
        "s_mov_b32 s35, 2\n"
        "v_mov_b32 v3, -2\n"
        "s_mov_b32 s38, 8388608\n"
        "W8_lgo:\n"
        "ds_read_b32 v20, v25 offset:512\n"
        "s_sub_u32 s38, s38, 1\n"
        "s_waitcnt lgkmcnt(0)\n"
        "s_cbranch_scc1 W8_lgo_x\n"
        "v_readfirstlane_b32 s39, v20\n"
        "s_cmp_ge_u32 s39, 1\n"
        "s_cbranch_scc1 W8_lgo_x\n"
        "s_sleep 2\n"
        "s_branch W8_lgo\n"
        "W8_lgo_x:\n"
        "s_mov_b32 s38, 8388608\n"
        "W8_lr00:\n"
        "ds_read_b32 v20, v25 offset:1024\n"
        "s_sub_u32 s38, s38, 1\n"
        "s_waitcnt lgkmcnt(0)\n"
        "s_cbranch_scc1 W8_lr00_x\n"
        "v_readfirstlane_b32 s39, v20\n"
        "s_cmp_ge_u32 s39, 2\n"
        "s_cbranch_scc1 W8_lr00_x\n"
        "s_sleep 1\n"
        "s_branch W8_lr00\n"
        "W8_lr00_x:\n"
        "s_mov_b32 m0, s34\n"
        "s_add_u32 s34, s34, 4096\n"
        "s_bitcmp1_b32 s37, 0\n"
        "s_cselect_b64 exec, 0, -1\n"
        "global_load_lds_dwordx4 v4, s[40:41] offset:0 sc1\n"
        "global_load_lds_dwordx4 v4, s[40:41] offset:1024 sc1\n"
        "s_bitcmp1_b32 s37, 1\n"
        "s_cselect_b64 exec, 0, -1\n"
        "global_load_lds_dwordx4 v4, s[40:41] offset:2048 sc1\n"
        "global_load_lds_dwordx4 v4, s[40:41] offset:3072 sc1\n"
        "s_mov_b32 m0, s34\n"
        "s_add_u32 s34, s34, 4096\n"
        "s_bitcmp1_b32 s37, 2\n"
        "s_cselect_b64 exec, 0, -1\n"
        "global_load_lds_dwordx4 v8, s[40:41] offset:0 sc1\n"
        "global_load_lds_dwordx4 v8, s[40:41] offset:1024 sc1\n"
        "s_bitcmp1_b32 s37, 3\n"
        "s_cselect_b64 exec, 0, -1\n"
        "global_load_lds_dwordx4 v8, s[40:41] offset:2048 sc1\n"
        "global_load_lds_dwordx4 v8, s[40:41] offset:3072 sc1\n"
        "s_mov_b64 exec, -1\n"
        "s_cmp_ge_u32 s39, 3\n"
        "s_cbranch_scc1 W8_lg1_x\n"
        "ds_read_b32 v20, v25 offset:1024\n"
        "s_waitcnt lgkmcnt(0)\n"
        "v_readfirstlane_b32 s39, v20\n"
        "s_cmp_ge_u32 s39, 3\n"
        "s_cbranch_scc1 W8_lg1_x\n"
        "v_mov_b32 v21, 1\n"
        "s_waitcnt vmcnt(0)\n"
        "ds_max_i32 v25, v21 offset:0\n"
        "s_mov_b32 s38, 8388608\n"
        "W8_lg1:\n"
        "ds_read_b32 v20, v25 offset:1024\n"
        "s_sub_u32 s38, s38, 1\n"
        "s_waitcnt lgkmcnt(0)\n"
        "s_cbranch_scc1 W8_lg1_x\n"
        "v_readfirstlane_b32 s39, v20\n"
        "s_cmp_ge_u32 s39, 3\n"
        "s_cbranch_scc1 W8_lg1_x\n"
        "s_sleep 1\n"
        "s_branch W8_lg1\n"
        "W8_lg1_x:\n"
        "s_mov_b32 m0, s34\n"
        "s_add_u32 s34, s34, 4096\n"
        "s_bitcmp1_b32 s37, 4\n"
        "s_cselect_b64 exec, 0, -1\n"
        "global_load_lds_dwordx4 v5, s[40:41] offset:0 sc1\n"
        "global_load_lds_dwordx4 v5, s[40:41] offset:1024 sc1\n"
        "s_bitcmp1_b32 s37, 5\n"
        "s_cselect_b64 exec, 0, -1\n"
        "global_load_lds_dwordx4 v5, s[40:41] offset:2048 sc1\n"
        "global_load_lds_dwordx4 v5, s[40:41] offset:3072 sc1\n"
        "s_mov_b32 m0, s34\n"
        "s_add_u32 s34, s34, 4096\n"
        "s_bitcmp1_b32 s37, 6\n"
        "s_cselect_b64 exec, 0, -1\n"
        "global_load_lds_dwordx4 v9, s[40:41] offset:0 sc1\n"
        "global_load_lds_dwordx4 v9, s[40:41] offset:1024 sc1\n"
        "s_bitcmp1_b32 s37, 7\n"
        "s_cselect_b64 exec, 0, -1\n"
        "global_load_lds_dwordx4 v9, s[40:41] offset:2048 sc1\n"
        "global_load_lds_dwordx4 v9, s[40:41] offset:3072 sc1\n"
        "s_mov_b64 exec, -1\n"
        "v_mov_b32 v21, 1\n"
        "s_waitcnt vmcnt(8)\n"
        "ds_max_i32 v25, v21 offset:0\n"
        "s_cmp_ge_u32 s39, 4\n"
        "s_cbranch_scc1 W8_lg2_x\n"
        "ds_read_b32 v20, v25 offset:1024\n"
        "s_waitcnt lgkmcnt(0)\n"
        "v_readfirstlane_b32 s39, v20\n"
        "s_cmp_ge_u32 s39, 4\n"
        "s_cbranch_scc1 W8_lg2_x\n"
        "v_mov_b32 v21, 2\n"
        "s_waitcnt vmcnt(0)\n"
        "ds_max_i32 v25, v21 offset:0\n"
        "s_mov_b32 s38, 8388608\n"
        "W8_lg2:\n"
        "ds_read_b32 v20, v25 offset:1024\n"
        "s_sub_u32 s38, s38, 1\n"
        "s_waitcnt lgkmcnt(0)\n"
        "s_cbranch_scc1 W8_lg2_x\n"
        "v_readfirstlane_b32 s39, v20\n"
        "s_cmp_ge_u32 s39, 4\n"
        "s_cbranch_scc1 W8_lg2_x\n"
        "s_sleep 1\n"
        "s_branch W8_lg2\n"
        "W8_lg2_x:\n"
        "s_mov_b32 m0, s34\n"
        "s_add_u32 s34, s34, 4096\n"
        "s_bitcmp1_b32 s37, 8\n"
        "s_cselect_b64 exec, 0, -1\n"
        "global_load_lds_dwordx4 v6, s[40:41] offset:0 sc1\n"
        "global_load_lds_dwordx4 v6, s[40:41] offset:1024 sc1\n"
        "s_bitcmp1_b32 s37, 9\n"
        "s_cselect_b64 exec, 0, -1\n"
        "global_load_lds_dwordx4 v6, s[40:41] offset:2048 sc1\n"
        "global_load_lds_dwordx4 v6, s[40:41] offset:3072 sc1\n"
        "s_mov_b32 m0, s34\n"
        "s_add_u32 s34, s34, 4096\n"
        "s_bitcmp1_b32 s37, 10\n"
        "s_cselect_b64 exec, 0, -1\n"
        "global_load_lds_dwordx4 v10, s[40:41] offset:0 sc1\n"
        "global_load_lds_dwordx4 v10, s[40:41] offset:1024 sc1\n"
        "s_bitcmp1_b32 s37, 11\n"
        "s_cselect_b64 exec, 0, -1\n"
        "global_load_lds_dwordx4 v10, s[40:41] offset:2048 sc1\n"
        "global_load_lds_dwordx4 v10, s[40:41] offset:3072 sc1\n"
        "s_mov_b64 exec, -1\n"
        "s_cmp_ge_u32 s39, 5\n"
        "s_cbranch_scc1 W8_lg3_x\n"
        "ds_read_b32 v20, v25 offset:1024\n"
        "s_waitcnt lgkmcnt(0)\n"
        "v_readfirstlane_b32 s39, v20\n"
        "s_cmp_ge_u32 s39, 5\n"
        "s_cbranch_scc1 W8_lg3_x\n"
        "v_mov_b32 v21, 2\n"
        "s_waitcnt vmcnt(8)\n"
        "ds_max_i32 v25, v21 offset:0\n"
        "v_mov_b32 v21, 3\n"
        "s_waitcnt vmcnt(0)\n"
        "ds_max_i32 v25, v21 offset:0\n"
        "s_mov_b32 s38, 8388608\n"
        "W8_lg3:\n"
        "ds_read_b32 v20, v25 offset:1024\n"
        "s_sub_u32 s38, s38, 1\n"
        "s_waitcnt lgkmcnt(0)\n"
        "s_cbranch_scc1 W8_lg3_x\n"
        "v_readfirstlane_b32 s39, v20\n"
        "s_cmp_ge_u32 s39, 5\n"
        "s_cbranch_scc1 W8_lg3_x\n"
        "s_sleep 1\n"
        "s_branch W8_lg3\n"
        "W8_lg3_x:\n"
        "s_mov_b32 m0, s34\n"
        "s_add_u32 s34, s34, 4096\n"
        "s_bitcmp1_b32 s37, 12\n"
        "s_cselect_b64 exec, 0, -1\n"
        "global_load_lds_dwordx4 v7, s[40:41] offset:0 sc1\n"
        "global_load_lds_dwordx4 v7, s[40:41] offset:1024 sc1\n"
        "s_bitcmp1_b32 s37, 13\n"
        "s_cselect_b64 exec, 0, -1\n"
        "global_load_lds_dwordx4 v7, s[40:41] offset:2048 sc1\n"
        "global_load_lds_dwordx4 v7, s[40:41] offset:3072 sc1\n"
        "s_mov_b32 m0, s34\n"
        "s_add_u32 s34, s34, 4096\n"
        "s_bitcmp1_b32 s37, 14\n"
        "s_cselect_b64 exec, 0, -1\n"
        "global_load_lds_dwordx4 v11, s[40:41] offset:0 sc1\n"
        "global_load_lds_dwordx4 v11, s[40:41] offset:1024 sc1\n"
        "s_bitcmp1_b32 s37, 15\n"
        "s_cselect_b64 exec, 0, -1\n"
        "global_load_lds_dwordx4 v11, s[40:41] offset:2048 sc1\n"
        "global_load_lds_dwordx4 v11, s[40:41] offset:3072 sc1\n"
        "s_mov_b64 exec, -1\n"
        "v_mov_b32 v21, 2\n"
        "s_waitcnt vmcnt(16)\n"
        "ds_max_i32 v25, v21 offset:0\n"
        "v_mov_b32 v3, 2\n"
        "s_add_u32 s40, s40, 32768\n"
        "s_addc_u32 s41, s41, 0\n"
        "s_mov_b32 s35, 3\n"
        "s_mov_b32 s36, 6\n"
        "W8_lq:\n"
        "ds_read_b32 v20, v25 offset:1024\n"
        "s_waitcnt lgkmcnt(0)\n"
        "v_readfirstlane_b32 s39, v20\n"
        "s_cmp_ge_u32 s39, s36\n"
        "s_cbranch_scc1 W8_lr_x\n"
        "v_add_u32 v21, 1, v3\n"
        "s_waitcnt vmcnt(8)\n"
        "ds_max_i32 v25, v21 offset:0\n"
        "v_add_u32 v21, 2, v3\n"
        "s_waitcnt vmcnt(0)\n"
        "ds_max_i32 v25, v21 offset:0\n"
        "s_mov_b32 s38, 8388608\n"
        "W8_lr:\n"
        "ds_read_b32 v20, v25 offset:1024\n"
        "s_sub_u32 s38, s38, 1\n"
        "s_waitcnt lgkmcnt(0)\n"
        "s_cbranch_scc1 W8_lr_x\n"
        "v_readfirstlane_b32 s39, v20\n"
        "s_cmp_ge_u32 s39, s36\n"
        "s_cbranch_scc1 W8_lr_x\n"
        "s_sleep 1\n"
        "s_branch W8_lr\n"
        "W8_lr_x:\n"
        "s_mov_b32 m0, s34\n"
        "s_add_u32 s34, s34, 4096\n"
        "s_bitcmp1_b32 s37, 0\n"
        "s_cselect_b64 exec, 0, -1\n"
        "global_load_lds_dwordx4 v4, s[40:41] offset:0 sc1\n"
        "global_load_lds_dwordx4 v4, s[40:41] offset:1024 sc1\n"
        "s_bitcmp1_b32 s37, 1\n"
        "s_cselect_b64 exec, 0, -1\n"
        "global_load_lds_dwordx4 v4, s[40:41] offset:2048 sc1\n"
        "global_load_lds_dwordx4 v4, s[40:41] offset:3072 sc1\n"
        "s_mov_b32 m0, s34\n"
        "s_add_u32 s34, s34, 4096\n"
        "s_bitcmp1_b32 s37, 2\n"
        "s_cselect_b64 exec, 0, -1\n"
        "global_load_lds_dwordx4 v8, s[40:41] offset:0 sc1\n"
        "global_load_lds_dwordx4 v8, s[40:41] offset:1024 sc1\n"
        "s_bitcmp1_b32 s37, 3\n"
        "s_cselect_b64 exec, 0, -1\n"
        "global_load_lds_dwordx4 v8, s[40:41] offset:2048 sc1\n"
        "global_load_lds_dwordx4 v8, s[40:41] offset:3072 sc1\n"
        "s_mov_b64 exec, -1\n"
        "v_add_u32 v3, 1, v3\n"
        "s_waitcnt vmcnt(16)\n"
        "ds_max_i32 v25, v3 offset:0\n"
        "s_add_u32 s36, s36, 1\n"
        "s_cmp_ge_u32 s39, s36\n"
        "s_cbranch_scc1 W8_lu1_x\n"
        "ds_read_b32 v20, v25 offset:1024\n"
        "s_waitcnt lgkmcnt(0)\n"
        "v_readfirstlane_b32 s39, v20\n"
        "s_cmp_ge_u32 s39, s36\n"
        "s_cbranch_scc1 W8_lu1_x\n"
        "v_add_u32 v21, 1, v3\n"
        "s_waitcnt vmcnt(8)\n"
        "ds_max_i32 v25, v21 offset:0\n"
        "v_add_u32 v21, 2, v3\n"
        "s_waitcnt vmcnt(0)\n"
        "ds_max_i32 v25, v21 offset:0\n"
        "s_mov_b32 s38, 8388608\n"
        "W8_lu1:\n"
        "ds_read_b32 v20, v25 offset:1024\n"
        "s_sub_u32 s38, s38, 1\n"
        "s_waitcnt lgkmcnt(0)\n"
        "s_cbranch_scc1 W8_lu1_x\n"
        "v_readfirstlane_b32 s39, v20\n"
        "s_cmp_ge_u32 s39, s36\n"
        "s_cbranch_scc1 W8_lu1_x\n"
        "s_sleep 1\n"
        "s_branch W8_lu1\n"
        "W8_lu1_x:\n"
        "s_mov_b32 m0, s34\n"
        "s_add_u32 s34, s34, 4096\n"
        "s_bitcmp1_b32 s37, 4\n"
        "s_cselect_b64 exec, 0, -1\n"
        "global_load_lds_dwordx4 v5, s[40:41] offset:0 sc1\n"
        "global_load_lds_dwordx4 v5, s[40:41] offset:1024 sc1\n"
        "s_bitcmp1_b32 s37, 5\n"
        "s_cselect_b64 exec, 0, -1\n"
        "global_load_lds_dwordx4 v5, s[40:41] offset:2048 sc1\n"
        "global_load_lds_dwordx4 v5, s[40:41] offset:3072 sc1\n"
        "s_mov_b32 m0, s34\n"
        "s_add_u32 s34, s34, 4096\n"
        "s_bitcmp1_b32 s37, 6\n"
        "s_cselect_b64 exec, 0, -1\n"
        "global_load_lds_dwordx4 v9, s[40:41] offset:0 sc1\n"
        "global_load_lds_dwordx4 v9, s[40:41] offset:1024 sc1\n"
        "s_bitcmp1_b32 s37, 7\n"
        "s_cselect_b64 exec, 0, -1\n"
        "global_load_lds_dwordx4 v9, s[40:41] offset:2048 sc1\n"
        "global_load_lds_dwordx4 v9, s[40:41] offset:3072 sc1\n"
        "s_mov_b64 exec, -1\n"
        "v_add_u32 v3, 1, v3\n"
        "s_waitcnt vmcnt(16)\n"
        "ds_max_i32 v25, v3 offset:0\n"
        "s_add_u32 s36, s36, 1\n"
        "s_cmp_ge_u32 s39, s36\n"
        "s_cbranch_scc1 W8_lu2_x\n"
        "ds_read_b32 v20, v25 offset:1024\n"
        "s_waitcnt lgkmcnt(0)\n"
        "v_readfirstlane_b32 s39, v20\n"
        "s_cmp_ge_u32 s39, s36\n"
        "s_cbranch_scc1 W8_lu2_x\n"
        "v_add_u32 v21, 1, v3\n"
        "s_waitcnt vmcnt(8)\n"
        "ds_max_i32 v25, v21 offset:0\n"
        "v_add_u32 v21, 2, v3\n"
        "s_waitcnt vmcnt(0)\n"
        "ds_max_i32 v25, v21 offset:0\n"
        "s_mov_b32 s38, 8388608\n"
        "W8_lu2:\n"
        "ds_read_b32 v20, v25 offset:1024\n"
        "s_sub_u32 s38, s38, 1\n"
        "s_waitcnt lgkmcnt(0)\n"
        "s_cbranch_scc1 W8_lu2_x\n"
        "v_readfirstlane_b32 s39, v20\n"
        "s_cmp_ge_u32 s39, s36\n"
        "s_cbranch_scc1 W8_lu2_x\n"
        "s_sleep 1\n"
        "s_branch W8_lu2\n"
        "W8_lu2_x:\n"
        "s_mov_b32 m0, s34\n"
        "s_add_u32 s34, s34, 4096\n"
        "s_bitcmp1_b32 s37, 8\n"
        "s_cselect_b64 exec, 0, -1\n"
        "global_load_lds_dwordx4 v6, s[40:41] offset:0 sc1\n"
        "global_load_lds_dwordx4 v6, s[40:41] offset:1024 sc1\n"
        "s_bitcmp1_b32 s37, 9\n"
        "s_cselect_b64 exec, 0, -1\n"
        "global_load_lds_dwordx4 v6, s[40:41] offset:2048 sc1\n"
        "global_load_lds_dwordx4 v6, s[40:41] offset:3072 sc1\n"
        "s_mov_b32 m0, s34\n"
        "s_add_u32 s34, s34, 4096\n"
        "s_bitcmp1_b32 s37, 10\n"
        "s_cselect_b64 exec, 0, -1\n"
        "global_load_lds_dwordx4 v10, s[40:41] offset:0 sc1\n"
        "global_load_lds_dwordx4 v10, s[40:41] offset:1024 sc1\n"
        "s_bitcmp1_b32 s37, 11\n"
        "s_cselect_b64 exec, 0, -1\n"
        "global_load_lds_dwordx4 v10, s[40:41] offset:2048 sc1\n"
        "global_load_lds_dwordx4 v10, s[40:41] offset:3072 sc1\n"
        "s_mov_b64 exec, -1\n"
        "v_add_u32 v3, 1, v3\n"
        "s_waitcnt vmcnt(16)\n"
        "ds_max_i32 v25, v3 offset:0\n"
        "s_add_u32 s36, s36, 1\n"
        "s_cmp_ge_u32 s39, s36\n"
        "s_cbranch_scc1 W8_lu3_x\n"
        "ds_read_b32 v20, v25 offset:1024\n"
        "s_waitcnt lgkmcnt(0)\n"
        "v_readfirstlane_b32 s39, v20\n"
        "s_cmp_ge_u32 s39, s36\n"
        "s_cbranch_scc1 W8_lu3_x\n"
        "v_add_u32 v21, 1, v3\n"
        "s_waitcnt vmcnt(8)\n"
        "ds_max_i32 v25, v21 offset:0\n"
        "v_add_u32 v21, 2, v3\n"
        "s_waitcnt vmcnt(0)\n"
        "ds_max_i32 v25, v21 offset:0\n"
        "s_mov_b32 s38, 8388608\n"
        "W8_lu3:\n"
        "ds_read_b32 v20, v25 offset:1024\n"
        "s_sub_u32 s38, s38, 1\n"
        "s_waitcnt lgkmcnt(0)\n"
        "s_cbranch_scc1 W8_lu3_x\n"
        "v_readfirstlane_b32 s39, v20\n"
        "s_cmp_ge_u32 s39, s36\n"
        "s_cbranch_scc1 W8_lu3_x\n"
        "s_sleep 1\n"
        "s_branch W8_lu3\n"
        "W8_lu3_x:\n"
        "s_mov_b32 m0, s34\n"
        "s_add_u32 s34, s34, 4096\n"
        "s_bitcmp1_b32 s37, 12\n"
        "s_cselect_b64 exec, 0, -1\n"
        "global_load_lds_dwordx4 v7, s[40:41] offset:0 sc1\n"
        "global_load_lds_dwordx4 v7, s[40:41] offset:1024 sc1\n"
        "s_bitcmp1_b32 s37, 13\n"
        "s_cselect_b64 exec, 0, -1\n"
        "global_load_lds_dwordx4 v7, s[40:41] offset:2048 sc1\n"
        "global_load_lds_dwordx4 v7, s[40:41] offset:3072 sc1\n"
        "s_mov_b32 m0, s34\n"
        "s_add_u32 s34, s34, 4096\n"
        "s_bitcmp1_b32 s37, 14\n"
        "s_cselect_b64 exec, 0, -1\n"
        "global_load_lds_dwordx4 v11, s[40:41] offset:0 sc1\n"
        "global_load_lds_dwordx4 v11, s[40:41] offset:1024 sc1\n"
        "s_bitcmp1_b32 s37, 15\n"
        "s_cselect_b64 exec, 0, -1\n"
        "global_load_lds_dwordx4 v11, s[40:41] offset:2048 sc1\n"
        "global_load_lds_dwordx4 v11, s[40:41] offset:3072 sc1\n"
        "s_mov_b64 exec, -1\n"
        "v_add_u32 v3, 1, v3\n"
        "s_waitcnt vmcnt(16)\n"
        "ds_max_i32 v25, v3 offset:0\n"
        "s_add_u32 s40, s40, 32768\n"
        "s_addc_u32 s41, s41, 0\n"
        "s_add_u32 s36, s36, 1\n"
        "s_add_u32 s35, s35, 1\n"
        "s_cmp_lt_u32 s35, 6\n"
        "s_cbranch_scc1 W8_lq\n"
        "s_mov_b32 s34, 0\n"
        "ds_read_b32 v20, v25 offset:1024\n"
        "s_waitcnt lgkmcnt(0)\n"
        "v_readfirstlane_b32 s39, v20\n"
        "s_cmp_ge_u32 s39, 21\n"
        "s_cbranch_scc1 W8_lr0_x\n"
        "v_add_u32 v21, 1, v3\n"
        "s_waitcnt vmcnt(8)\n"
        "ds_max_i32 v25, v21 offset:0\n"
        "v_add_u32 v21, 2, v3\n"
        "s_waitcnt vmcnt(0)\n"
        "ds_max_i32 v25, v21 offset:0\n"
        "s_mov_b32 s38, 8388608\n"
        "W8_lr0:\n"
        "ds_read_b32 v20, v25 offset:1024\n"
        "s_sub_u32 s38, s38, 1\n"
        "s_waitcnt lgkmcnt(0)\n"
        "s_cbranch_scc1 W8_lr0_x\n"
        "v_readfirstlane_b32 s39, v20\n"
        "s_cmp_ge_u32 s39, 21\n"
        "s_cbranch_scc1 W8_lr0_x\n"
        "s_sleep 1\n"
        "s_branch W8_lr0\n"
        "W8_lr0_x:\n"
        "ds_read_b32 v20, v25 offset:768\n"
        "s_waitcnt lgkmcnt(0)\n"
        "v_readfirstlane_b32 s39, v20\n"
        "s_cmp_ge_u32 s39, 533504\n"
        "s_cbranch_scc1 W8_lf0_x\n"
        "v_add_u32 v21, 1, v3\n"
        "s_waitcnt vmcnt(8)\n"
        "ds_max_i32 v25, v21 offset:0\n"
        "v_add_u32 v21, 2, v3\n"
        "s_waitcnt vmcnt(0)\n"
        "ds_max_i32 v25, v21 offset:0\n"
        "s_mov_b32 s38, 8388608\n"
        "W8_lf0:\n"
        "ds_read_b32 v20, v25 offset:768\n"
        "s_sub_u32 s38, s38, 1\n"
        "s_waitcnt lgkmcnt(0)\n"
        "s_cbranch_scc1 W8_lf0_x\n"
        "v_readfirstlane_b32 s39, v20\n"
        "s_cmp_ge_u32 s39, 533504\n"
        "s_cbranch_scc1 W8_lf0_x\n"
        "s_sleep 1\n"
        "s_branch W8_lf0\n"
        "W8_lf0_x:\n"
        "s_add_u32 s40, s42, 4096\n"
        "s_addc_u32 s41, s43, 0\n"
        "W8_ll0:\n"
        "s_mov_b32 m0, s34\n"
        "s_add_u32 s34, s34, 2048\n"
        "global_load_lds_dwordx4 v12, s[40:41] sc1\n"
        "global_load_lds_dwordx4 v12, s[40:41] offset:1024 sc1\n"
        "s_mov_b32 m0, s34\n"
        "s_add_u32 s34, s34, 2048\n"
        "global_load_lds_dwordx4 v13, s[40:41] sc1\n"
        "global_load_lds_dwordx4 v13, s[40:41] offset:1024 sc1\n"
        "s_mov_b32 m0, s34\n"
        "s_add_u32 s34, s34, 2048\n"
        "global_load_lds_dwordx4 v14, s[40:41] sc1\n"
        "global_load_lds_dwordx4 v14, s[40:41] offset:1024 sc1\n"
        "s_mov_b32 m0, s34\n"
        "s_add_u32 s34, s34, 2048\n"
        "global_load_lds_dwordx4 v15, s[40:41] sc1\n"
        "global_load_lds_dwordx4 v15, s[40:41] offset:1024 sc1\n"
        "s_add_u32 s40, s40, 24576\n"
        "s_addc_u32 s41, s41, 0\n"
        "v_add_u32 v3, 1, v3\n"
        "s_waitcnt vmcnt(16)\n"
        "ds_max_i32 v25, v3 offset:0\n"
        "s_cmp_lt_u32 s34, 32768\n"
        "s_cbranch_scc1 W8_ll0\n"
        "v_add_u32 v3, -1, v3\n"
        "ds_read_b32 v20, v25 offset:772\n"
        "s_waitcnt lgkmcnt(0)\n"
        "v_readfirstlane_b32 s39, v20\n"
        "s_cmp_ge_u32 s39, 533504\n"
        "s_cbranch_scc1 W8_lf1_x\n"
        "v_add_u32 v21, 1, v3\n"
        "s_waitcnt vmcnt(16)\n"
        "ds_max_i32 v25, v21 offset:0\n"
        "v_add_u32 v21, 2, v3\n"
        "s_waitcnt vmcnt(8)\n"
        "ds_max_i32 v25, v21 offset:0\n"
        "v_add_u32 v21, 3, v3\n"
        "s_waitcnt vmcnt(0)\n"
        "ds_max_i32 v25, v21 offset:0\n"
        "s_mov_b32 s38, 8388608\n"
        "W8_lf1:\n"
        "ds_read_b32 v20, v25 offset:772\n"
        "s_sub_u32 s38, s38, 1\n"
        "s_waitcnt lgkmcnt(0)\n"
        "s_cbranch_scc1 W8_lf1_x\n"
        "v_readfirstlane_b32 s39, v20\n"
        "s_cmp_ge_u32 s39, 533504\n"
        "s_cbranch_scc1 W8_lf1_x\n"
        "s_sleep 1\n"
        "s_branch W8_lf1\n"
        "W8_lf1_x:\n"
        "s_add_u32 s40, s42, 0\n"
        "s_addc_u32 s41, s43, 0\n"
        "W8_ll1:\n"
        "s_mov_b32 m0, s34\n"
        "s_add_u32 s34, s34, 2048\n"
        "global_load_lds_dwordx4 v12, s[40:41] sc1\n"
        "global_load_lds_dwordx4 v12, s[40:41] offset:1024 sc1\n"
        "s_mov_b32 m0, s34\n"
        "s_add_u32 s34, s34, 2048\n"
        "global_load_lds_dwordx4 v13, s[40:41] sc1\n"
        "global_load_lds_dwordx4 v13, s[40:41] offset:1024 sc1\n"
        "s_mov_b32 m0, s34\n"
        "s_add_u32 s34, s34, 2048\n"
        "global_load_lds_dwordx4 v14, s[40:41] sc1\n"
        "global_load_lds_dwordx4 v14, s[40:41] offset:1024 sc1\n"
        "s_mov_b32 m0, s34\n"
        "s_add_u32 s34, s34, 2048\n"
        "global_load_lds_dwordx4 v15, s[40:41] sc1\n"
        "global_load_lds_dwordx4 v15, s[40:41] offset:1024 sc1\n"
        "s_add_u32 s40, s40, 24576\n"
        "s_addc_u32 s41, s41, 0\n"
        "v_add_u32 v3, 1, v3\n"
        "s_waitcnt vmcnt(24)\n"
        "ds_max_i32 v25, v3 offset:0\n"
        "s_cmp_lt_u32 s34, 65536\n"
        "s_cbranch_scc1 W8_ll1\n"
        "v_add_u32 v3, -1, v3\n"
        "ds_read_b32 v20, v25 offset:776\n"
        "s_waitcnt lgkmcnt(0)\n"
        "v_readfirstlane_b32 s39, v20\n"
        "s_cmp_ge_u32 s39, 533504\n"
        "s_cbranch_scc1 W8_lf2_x\n"
        "v_add_u32 v21, 1, v3\n"
        "s_waitcnt vmcnt(24)\n"
        "ds_max_i32 v25, v21 offset:0\n"
        "v_add_u32 v21, 2, v3\n"
        "s_waitcnt vmcnt(16)\n"
        "ds_max_i32 v25, v21 offset:0\n"
        "v_add_u32 v21, 3, v3\n"
        "s_waitcnt vmcnt(8)\n"
        "ds_max_i32 v25, v21 offset:0\n"
        "v_add_u32 v21, 4, v3\n"
        "s_waitcnt vmcnt(0)\n"
        "ds_max_i32 v25, v21 offset:0\n"
        "s_mov_b32 s38, 8388608\n"
        "W8_lf2:\n"
        "ds_read_b32 v20, v25 offset:776\n"
        "s_sub_u32 s38, s38, 1\n"
        "s_waitcnt lgkmcnt(0)\n"
        "s_cbranch_scc1 W8_lf2_x\n"
        "v_readfirstlane_b32 s39, v20\n"
        "s_cmp_ge_u32 s39, 533504\n"
        "s_cbranch_scc1 W8_lf2_x\n"
        "s_sleep 1\n"
        "s_branch W8_lf2\n"
        "W8_lf2_x:\n"
        "s_add_u32 s40, s42, 2048\n"
        "s_addc_u32 s41, s43, 0\n"
        "W8_ll2:\n"
        "s_mov_b32 m0, s34\n"
        "s_add_u32 s34, s34, 2048\n"
        "global_load_lds_dwordx4 v12, s[40:41] sc1\n"
        "global_load_lds_dwordx4 v12, s[40:41] offset:1024 sc1\n"
        "s_mov_b32 m0, s34\n"
        "s_add_u32 s34, s34, 2048\n"
        "global_load_lds_dwordx4 v13, s[40:41] sc1\n"
        "global_load_lds_dwordx4 v13, s[40:41] offset:1024 sc1\n"
        "s_mov_b32 m0, s34\n"
        "s_add_u32 s34, s34, 2048\n"
        "global_load_lds_dwordx4 v14, s[40:41] sc1\n"
        "global_load_lds_dwordx4 v14, s[40:41] offset:1024 sc1\n"
        "s_mov_b32 m0, s34\n"
        "s_add_u32 s34, s34, 2048\n"
        "global_load_lds_dwordx4 v15, s[40:41] sc1\n"
        "global_load_lds_dwordx4 v15, s[40:41] offset:1024 sc1\n"
        "s_add_u32 s40, s40, 24576\n"
        "s_addc_u32 s41, s41, 0\n"
        "v_add_u32 v3, 1, v3\n"
        "s_waitcnt vmcnt(32)\n"
        "ds_max_i32 v25, v3 offset:0\n"
        "s_cmp_lt_u32 s34, 98304\n"
        "s_cbranch_scc1 W8_ll2\n"
        "v_add_u32 v3, 1, v3\n"
        "s_waitcnt vmcnt(24)\n"
        "ds_max_i32 v25, v3 offset:0\n"
        "v_add_u32 v3, 1, v3\n"
        "s_waitcnt vmcnt(16)\n"
        "ds_max_i32 v25, v3 offset:0\n"
        "v_add_u32 v3, 1, v3\n"
        "s_waitcnt vmcnt(8)\n"
        "ds_max_i32 v25, v3 offset:0\n"
        "v_add_u32 v3, 1, v3\n"
        "s_waitcnt vmcnt(0)\n"
        "ds_max_i32 v25, v3 offset:0\n"
        "s_branch W8_fin\n"
        "W8_comp:\n"
        "s_setprio 3\n"
        "s_lshl_b32 s26, s21, 2\n"
        "s_add_u32 s26, s26, s20\n"
        "s_sub_u32 s26, s26, 4\n"
        "v_lshrrev_b32 v2, 4, v1\n"
        "v_and_b32 v3, 15, v1\n"
        "v_lshlrev_b32 v248, 11, v2\n"
        "v_lshl_add_u32 v248, v3, 4, v248\n"
        "v_add_u32 v249, 65536, v248\n"
        "v_mov_b32 v250, 133376\n"
        "v_lshlrev_b32 v240, 4, v1\n"
        "v_mul_u32_u24 v2, 28672, v2\n"
        "v_lshl_add_u32 v251, v3, 3, v2\n"
        "v_and_b32 v4, 7, v1\n"
        "v_lshl_add_u32 v252, v4, 3, v2\n"
        "v_bfe_u32 v4, v1, 3, 1\n"
        "v_lshl_add_u32 v252, v4, 2, v252\n"
        "s_mul_i32 s24, s22, 192\n"
        "s_sub_u32 s24, 6144, s24\n"
        "s_lshl_b32 s25, s22, 7\n"
        "s_sub_u32 s37, 3840, s25\n"
        "v_add_u32 v252, s24, v252\n"
        "v_lshlrev_b32 v253, 5, v3\n"
        "v_add_u32 v253, 131072, v253\n"
        "s_waitcnt lgkmcnt(0)\n"
        "s_lshl_b32 s27, s26, 15\n"
        "s_xor_b32 s24, s33, 0\n"
        "s_and_b32 s24, s24, 3\n"
        "s_lshl_b32 s24, s24, 11\n"
        "s_add_u32 s24, s24, s27\n"
        "s_add_u32 s56, s8, s24\n"
        "s_addc_u32 s57, s9, 0\n"
        "s_xor_b32 s24, s33, 1\n"
        "s_and_b32 s24, s24, 3\n"
        "s_lshl_b32 s24, s24, 11\n"
        "s_add_u32 s24, s24, s27\n"
        "s_add_u32 s58, s8, s24\n"
        "s_addc_u32 s59, s9, 0\n"
        "s_xor_b32 s24, s33, 2\n"
        "s_and_b32 s24, s24, 3\n"
        "s_lshl_b32 s24, s24, 11\n"
        "s_add_u32 s24, s24, s27\n"
        "s_add_u32 s60, s8, s24\n"
        "s_addc_u32 s61, s9, 0\n"
        "s_xor_b32 s24, s33, 3\n"
        "s_and_b32 s24, s24, 3\n"
        "s_lshl_b32 s24, s24, 11\n"
        "s_add_u32 s24, s24, s27\n"
        "s_add_u32 s62, s8, s24\n"
        "s_addc_u32 s63, s9, 0\n"
        "s_mul_i32 s24, s26, 229376\n"
        "s_mul_i32 s25, s22, 256\n"
        "s_add_u32 s24, s24, s25\n"
        "s_add_u32 s40, s10, s24\n"
        "s_addc_u32 s41, s11, 0\n"
        "s_add_u32 s42, s40, 7168\n"
        "s_addc_u32 s43, s41, 0\n"
        "s_add_u32 s44, s40, 14336\n"
        "s_addc_u32 s45, s41, 0\n"
        "s_add_u32 s46, s40, 21504\n"
        "s_addc_u32 s47, s41, 0\n"
        "s_add_u32 s48, s40, 114688\n"
        "s_addc_u32 s49, s41, 0\n"
        "s_add_u32 s50, s40, 121856\n"
        "s_addc_u32 s51, s41, 0\n"
        "s_add_u32 s52, s40, 129024\n"
        "s_addc_u32 s53, s41, 0\n"
        "s_add_u32 s54, s40, 136192\n"
        "s_addc_u32 s55, s41, 0\n"
        "s_lshr_b32 s16, s22, 2\n"
        "s_xor_b32 s16, s16, s33\n"
        "s_and_b32 s16, s16, 3\n"
        "s_and_b32 s24, s22, 3\n"
        "s_add_u32 s24, s24, 4\n"
        "s_cmp_eq_u32 s24, s20\n"
        "s_cselect_b32 s16, s16, 4\n"
        "s_cmp_eq_u32 s16, 0\n"
        "s_cselect_b64 s[4:5], 0, -1\n"
        "s_cmp_eq_u32 s16, 1\n"
        "s_cselect_b64 s[6:7], 0, -1\n"
        "s_cmp_eq_u32 s16, 2\n"
        "s_cselect_b64 s[12:13], 0, -1\n"
        "s_cmp_eq_u32 s16, 3\n"
        "s_cselect_b64 s[14:15], 0, -1\n"
        "v_lshrrev_b32 v242, 4, v1\n"
        "v_lshlrev_b32 v242, 4, v242\n"
        "v_lshl_add_u32 v242, v3, 6, v242\n"
        "v_add_u32 v242, 135232, v242\n"
        "s_sub_u32 s28, s20, 4\n"
        "s_mul_i32 s24, s21, 512\n"
        "s_mul_i32 s25, s22, 8\n"
        "s_add_u32 s24, s24, s25\n"
        "s_add_u32 s24, s24, 6029312\n"
        "s_mul_i32 s25, s22, 1024\n"
        "s_mul_i32 s27, s21, 8\n"
        "s_add_u32 s25, s25, s27\n"
        "s_add_u32 s25, s25, 6291456\n"
        "s_sub_u32 s25, s25, s24\n"
        "v_mul_lo_u32 v24, v1, s25\n"
        "s_mul_i32 s25, s28, 128\n"
        "s_add_u32 s24, s24, s25\n"
        "v_add_u32 v24, s24, v24\n"
        "s_mul_i32 s29, s28, 5120\n"
        "s_add_u32 s29, s29, 135232\n"
        "v_and_b32 v6, 7, v1\n"
        "v_lshrrev_b32 v7, 3, v1\n"
        "v_lshrrev_b32 v8, 4, v1\n"
        "v_lshl_add_u32 v8, v8, 4, s29\n"
        "v_lshl_add_u32 v8, v3, 6, v8\n"
        "s_lshr_b32 s24, s21, 2\n"
        "s_add_u32 s25, s21, s22\n"
        "s_xor_b32 s24, s24, s25\n"
        "s_and_b32 s24, s24, 3\n"
        "s_and_b32 s25, s21, 3\n"
        "s_lshl2_add_u32 s24, s24, s25\n"
        "s_lshl4_add_u32 s24, s28, s24\n"
        "s_lshl_b32 s24, s24, 11\n"
        "v_add_u32 v9, s24, v240\n"
        "s_and_b32 s24, s22, 3\n"
        "s_lshl2_add_u32 s24, s21, s24\n"
        "s_lshl2_add_u32 s24, s24, s28\n"
        "s_lshr_b32 s25, s22, 2\n"
        "s_lshl2_add_u32 s24, s24, s25\n"
        "s_lshl_b32 s24, s24, 11\n"
        "v_add_u32 v11, s24, v240\n"
        "v_lshrrev_b32 v10, 5, v1\n"
        "s_lshl_b32 s24, s22, 2\n"
        "s_add_u32 s24, s24, s28\n"
        "s_lshl_b32 s24, s24, 4\n"
        "s_add_u32 s24, s24, s21\n"
        "s_lshl_b32 s24, s24, 11\n"
        "s_add_u32 s24, s24, 2097152\n"
        "v_add_u32 v13, s24, v240\n"
        "s_lshl_b32 s24, s22, 4\n"
        "s_add_u32 s24, s24, s21\n"
        "s_lshl_b32 s24, s24, 9\n"
        "s_add_u32 s24, s24, 5767168\n"
        "v_and_b32 v14, 31, v1\n"
        "v_lshl_add_u32 v14, v14, 3, s24\n"
        "v_lshl_add_u32 v14, v10, 2, v14\n"
        "v_lshlrev_b32 v15, 3, v1\n"
        "v_add_u32 v15, 154944, v15\n"
        "s_add_u32 s28, s28, 1\n"
        "s_mov_b32 s38, 8388608\n"
        "W8_tgc:\n"
        "ds_read_b32 v12, v250 offset:772\n"
        "s_sub_u32 s38, s38, 1\n"
        "s_waitcnt lgkmcnt(0)\n"
        "s_cbranch_scc1 W8_tgc_x\n"
        "v_readfirstlane_b32 s39, v12\n"
        "s_cmp_ge_u32 s39, 1\n"
        "s_cbranch_scc1 W8_tgc_x\n"
        "s_sleep 1\n"
        "s_branch W8_tgc\n"
        "W8_tgc_x:\n"
        "v_lshrrev_b32 v12, 0, v1\n"
        "v_lshlrev_b32 v12, 7, v12\n"
        "v_add_u32 v12, 134408, v12\n"
        "s_mov_b64 exec, 3\n"
        "ds_read_b64 v[26:27], v12\n"
        "s_mov_b64 exec, -1\n"
        "s_mov_b32 s38, 8388608\n"
        "W8_pkc:\n"
        "ds_read_b32 v12, v250 offset:1292\n"
        "s_sub_u32 s38, s38, 1\n"
        "s_waitcnt lgkmcnt(0)\n"
        "s_cbranch_scc1 W8_pkc_x\n"
        "v_readfirstlane_b32 s39, v12\n"
        "s_cmp_ge_u32 s39, s28\n"
        "s_cbranch_scc1 W8_pkc_x\n"
        "s_sleep 1\n"
        "s_branch W8_pkc\n"
        "W8_pkc_x:\n"
        "ds_read_b128 v[28:31], v8\n"
        "ds_read_b128 v[32:35], v8 offset:1024\n"
        "ds_read_b128 v[36:39], v9 offset:0\n"
        "ds_read_b128 v[40:43], v9 offset:1024\n"
        "ds_read_b64 v[44:45], v15\n"
        "s_waitcnt lgkmcnt(3)\n"
        "global_store_dwordx4 v11, v[28:31], s[8:9] offset:0 sc1\n"
        "global_store_dwordx4 v11, v[32:35], s[8:9] offset:1024 sc1\n"
        "s_waitcnt lgkmcnt(0)\n"
        "global_store_dwordx4 v13, v[36:39], s[8:9] sc1\n"
        "global_store_dwordx4 v13, v[40:43], s[8:9] offset:1024 sc1\n"
        "s_cmp_lg_u32 s28, 4\n"
        "s_cbranch_scc1 W8_ncs\n"
        "global_store_dword v14, v44, s[8:9] sc1\n"
        "W8_ncs:\n"
        "s_mov_b64 exec, 3\n"
        "s_waitcnt vmcnt(0)\n"
        "global_atomic_swap_x2 v24, v[26:27], s[8:9] sc1\n"
        "s_mov_b64 exec, -1\n"
        "s_mov_b32 s38, 8388608\n"
        "W8_cgo:\n"
        "ds_read_b32 v254, v250 offset:260\n"
        "s_sub_u32 s38, s38, 1\n"
        "s_waitcnt lgkmcnt(0)\n"
        "s_cbranch_scc1 W8_cgo_x\n"
        "v_readfirstlane_b32 s39, v254\n"
        "s_cmp_ge_u32 s39, 1\n"
        "s_cbranch_scc1 W8_cgo_x\n"
        "s_sleep 2\n"
        "s_branch W8_cgo\n"
        "W8_cgo_x:\n"
        "s_cmp_lg_u32 s16, 0\n"
        "s_cbranch_scc1 W8_oa0\n"
        "ds_read_b128 v[0:3], v242 offset:0\n"
        "ds_read_b128 v[4:7], v242 offset:1024\n"
        "ds_read_b128 v[32:35], v242 offset:5120\n"
        "ds_read_b128 v[36:39], v242 offset:6144\n"
        "ds_read_b128 v[64:67], v242 offset:10240\n"
        "ds_read_b128 v[68:71], v242 offset:11264\n"
        "ds_read_b128 v[96:99], v242 offset:15360\n"
        "ds_read_b128 v[100:103], v242 offset:16384\n"
        "W8_oa0:\n"
        "s_cmp_lg_u32 s16, 1\n"
        "s_cbranch_scc1 W8_oa1\n"
        "ds_read_b128 v[8:11], v242 offset:0\n"
        "ds_read_b128 v[12:15], v242 offset:1024\n"
        "ds_read_b128 v[40:43], v242 offset:5120\n"
        "ds_read_b128 v[44:47], v242 offset:6144\n"
        "ds_read_b128 v[72:75], v242 offset:10240\n"
        "ds_read_b128 v[76:79], v242 offset:11264\n"
        "ds_read_b128 v[104:107], v242 offset:15360\n"
        "ds_read_b128 v[108:111], v242 offset:16384\n"
        "W8_oa1:\n"
        "s_cmp_lg_u32 s16, 2\n"
        "s_cbranch_scc1 W8_oa2\n"
        "ds_read_b128 v[16:19], v242 offset:0\n"
        "ds_read_b128 v[20:23], v242 offset:1024\n"
        "ds_read_b128 v[48:51], v242 offset:5120\n"
        "ds_read_b128 v[52:55], v242 offset:6144\n"
        "ds_read_b128 v[80:83], v242 offset:10240\n"
        "ds_read_b128 v[84:87], v242 offset:11264\n"
        "ds_read_b128 v[112:115], v242 offset:15360\n"
        "ds_read_b128 v[116:119], v242 offset:16384\n"
        "W8_oa2:\n"
        "s_cmp_lg_u32 s16, 3\n"
        "s_cbranch_scc1 W8_oa3\n"
        "ds_read_b128 v[24:27], v242 offset:0\n"
        "ds_read_b128 v[28:31], v242 offset:1024\n"
        "ds_read_b128 v[56:59], v242 offset:5120\n"
        "ds_read_b128 v[60:63], v242 offset:6144\n"
        "ds_read_b128 v[88:91], v242 offset:10240\n"
        "ds_read_b128 v[92:95], v242 offset:11264\n"
        "ds_read_b128 v[120:123], v242 offset:15360\n"
        "ds_read_b128 v[124:127], v242 offset:16384\n"
        "W8_oa3:\n"
        "s_mov_b32 s38, 8388608\n"
        "W8_rd0:\n"
        "ds_read_b32 v254, v250 offset:4\n"
        "s_sub_u32 s38, s38, 1\n"
        "s_waitcnt lgkmcnt(0)\n"
        "s_cbranch_scc1 W8_rd0_x\n"
        "v_readfirstlane_b32 s39, v254\n"
        "s_cmp_ge_u32 s39, 1\n"
        "s_cbranch_scc1 W8_rd0_x\n"
        "s_sleep 1\n"
        "s_branch W8_rd0\n"
        "W8_rd0_x:\n"
        "s_mov_b64 exec, s[4:5]\n"
        "global_load_dwordx4 v[0:3], v240, s[56:57] offset:0 sc1\n"
        "s_mov_b64 exec, -1\n"
        "s_mov_b64 exec, s[4:5]\n"
        "global_load_dwordx4 v[4:7], v240, s[56:57] offset:1024 sc1\n"
        "s_mov_b64 exec, -1\n"
        "s_sleep 2\n"
        "s_cmp_ge_u32 s39, 2\n"
        "s_cbranch_scc1 W8_rd1_x\n"
        "s_mov_b32 s38, 8388608\n"
        "W8_rd1:\n"
        "ds_read_b32 v254, v250 offset:4\n"
        "s_sub_u32 s38, s38, 1\n"
        "s_waitcnt lgkmcnt(0)\n"
        "s_cbranch_scc1 W8_rd1_x\n"
        "v_readfirstlane_b32 s39, v254\n"
        "s_cmp_ge_u32 s39, 2\n"
        "s_cbranch_scc1 W8_rd1_x\n"
        "s_sleep 1\n"
        "s_branch W8_rd1\n"
        "W8_rd1_x:\n"
        "s_mov_b64 exec, s[6:7]\n"
        "global_load_dwordx4 v[8:11], v240, s[58:59] offset:0 sc1\n"
        "s_mov_b64 exec, -1\n"
        "s_mov_b64 exec, s[6:7]\n"
        "global_load_dwordx4 v[12:15], v240, s[58:59] offset:1024 sc1\n"
        "s_mov_b64 exec, -1\n"
        "W8_dwx:\n"
        "ds_read_b32 v254, v250 offset:0\n"
        "s_waitcnt lgkmcnt(0)\n"
        "v_cmp_gt_u32 vcc, 1, v254\n"
        "s_cbranch_vccnz W8_s0\n"
        "W8_c0:\n"
        "ds_read_b32 v241, v250 offset:4\n"
        "ds_read_b128 v[208:211], v248 offset:0\n"
        "ds_read_b128 v[212:215], v248 offset:1024\n"
        "ds_read_b128 v[216:219], v248 offset:512\n"
        "ds_read_b128 v[220:223], v248 offset:1536\n"
        "ds_read_b128 v[224:227], v248 offset:256\n"
        "ds_read_b128 v[228:231], v248 offset:1280\n"
        "s_waitcnt vmcnt(2)\n"
        "s_waitcnt lgkmcnt(5)\n"
        "v_mfma_i32_16x16x64_i8 v[128:131], v[0:3], v[208:211], 0\n"
        "v_cmp_gt_u32 vcc, 4, v241\n"
        "s_cbranch_vccnz W8_sZ\n"
        "W8_cZ:\n"
        "ds_read_b32 v254, v250 offset:0\n"
        "v_mfma_i32_16x16x64_i8 v[132:135], v[4:7], v[208:211], 0\n"
        "ds_read_b128 v[232:235], v248 offset:768\n"
        "s_waitcnt lgkmcnt(6)\n"
        "v_mfma_i32_16x16x64_i8 v[136:139], v[0:3], v[212:215], 0\n"
        "s_mov_b64 exec, s[12:13]\n"
        "global_load_dwordx4 v[16:19], v240, s[60:61] offset:0 sc1\n"
        "s_mov_b64 exec, -1\n"
        "v_mfma_i32_16x16x64_i8 v[140:143], v[4:7], v[212:215], 0\n"
        "ds_read_b128 v[236:239], v248 offset:1792\n"
        "s_waitcnt lgkmcnt(2)\n"
        "v_cmp_gt_u32 vcc, 2, v254\n"
        "v_mfma_i32_16x16x64_i8 v[144:147], v[0:3], v[216:219], 0\n"
        "s_cbranch_vccnz W8_s1\n"
        "W8_c1:\n"
        "v_mfma_i32_16x16x64_i8 v[148:151], v[4:7], v[216:219], 0\n"
        "v_cmp_gt_u32 vcc, 5, v241\n"
        "ds_read_b128 v[208:211], v248 offset:8192\n"
        "v_mfma_i32_16x16x64_i8 v[152:155], v[0:3], v[220:223], 0\n"
        "s_cbranch_vccnz W8_sR0\n"
        "W8_cR0:\n"
        "ds_read_b32 v254, v250 offset:0\n"
        "v_mfma_i32_16x16x64_i8 v[156:159], v[4:7], v[220:223], 0\n"
        "ds_read_b128 v[212:215], v248 offset:9216\n"
        "v_mfma_i32_16x16x64_i8 v[160:163], v[0:3], v[224:227], 0\n"
        "ds_read_b32 v241, v250 offset:4\n"
        "s_mov_b64 exec, s[12:13]\n"
        "global_load_dwordx4 v[20:23], v240, s[60:61] offset:1024 sc1\n"
        "s_mov_b64 exec, -1\n"
        "v_mfma_i32_16x16x64_i8 v[164:167], v[4:7], v[224:227], 0\n"
        "ds_read_b128 v[216:219], v248 offset:8704\n"
        "v_mfma_i32_16x16x64_i8 v[168:171], v[0:3], v[228:231], 0\n"
        "s_mov_b64 exec, s[14:15]\n"
        "global_load_dwordx4 v[24:27], v240, s[62:63] offset:0 sc1\n"
        "s_mov_b64 exec, -1\n"
        "v_mfma_i32_16x16x64_i8 v[172:175], v[4:7], v[228:231], 0\n"
        "ds_read_b128 v[220:223], v248 offset:9728\n"
        "s_waitcnt lgkmcnt(7)\n"
        "v_mfma_i32_16x16x64_i8 v[176:179], v[0:3], v[232:235], 0\n"
        "s_mov_b64 exec, s[14:15]\n"
        "global_load_dwordx4 v[28:31], v240, s[62:63] offset:1024 sc1\n"
        "s_mov_b64 exec, -1\n"
        "v_add_u32 v240, 8192, v240\n"
        "v_mfma_i32_16x16x64_i8 v[180:183], v[4:7], v[232:235], 0\n"
        "ds_read_b128 v[224:227], v248 offset:8448\n"
        "s_waitcnt lgkmcnt(7)\n"
        "v_mfma_i32_16x16x64_i8 v[184:187], v[0:3], v[236:239], 0\n"
        "v_mfma_i32_16x16x64_i8 v[188:191], v[4:7], v[236:239], 0\n"
        "ds_read_b128 v[228:231], v248 offset:9472\n"
        "s_waitcnt vmcnt(4)\n"
        "s_waitcnt lgkmcnt(7)\n"
        "v_mfma_i32_16x16x64_i8 v[128:131], v[8:11], v[208:211], v[128:131]\n"
        "v_mfma_i32_16x16x64_i8 v[132:135], v[12:15], v[208:211], v[132:135]\n"
        "ds_read_b128 v[232:235], v248 offset:8960\n"
        "s_waitcnt lgkmcnt(6)\n"
        "v_mfma_i32_16x16x64_i8 v[136:139], v[8:11], v[212:215], v[136:139]\n"
        "s_mov_b64 exec, s[4:5]\n"
        "global_load_dwordx4 v[32:35], v240, s[56:57] offset:0 sc1\n"
        "s_mov_b64 exec, -1\n"
        "v_mfma_i32_16x16x64_i8 v[140:143], v[12:15], v[212:215], v[140:143]\n"
        "ds_read_b128 v[236:239], v248 offset:9984\n"
        "v_cmp_gt_u32 vcc, 3, v254\n"
        "s_waitcnt lgkmcnt(5)\n"
        "v_mfma_i32_16x16x64_i8 v[144:147], v[8:11], v[216:219], v[144:147]\n"
        "s_cbranch_vccnz W8_s2\n"
        "W8_c2:\n"
        "v_mfma_i32_16x16x64_i8 v[148:151], v[12:15], v[216:219], v[148:151]\n"
        "v_cmp_gt_u32 vcc, 6, v241\n"
        "ds_read_b128 v[208:211], v248 offset:16384\n"
        "s_waitcnt lgkmcnt(5)\n"
        "v_mfma_i32_16x16x64_i8 v[152:155], v[8:11], v[220:223], v[152:155]\n"
        "s_cbranch_vccnz W8_sR1\n"
        "W8_cR1:\n"
        "ds_read_b32 v254, v250 offset:0\n"
        "v_mfma_i32_16x16x64_i8 v[156:159], v[12:15], v[220:223], v[156:159]\n"
        "ds_read_b128 v[212:215], v248 offset:17408\n"
        "s_waitcnt lgkmcnt(6)\n"
        "v_mfma_i32_16x16x64_i8 v[160:163], v[8:11], v[224:227], v[160:163]\n"
        "ds_read_b32 v241, v250 offset:4\n"
        "s_mov_b64 exec, s[4:5]\n"
        "global_load_dwordx4 v[36:39], v240, s[56:57] offset:1024 sc1\n"
        "s_mov_b64 exec, -1\n"
        "v_mfma_i32_16x16x64_i8 v[164:167], v[12:15], v[224:227], v[164:167]\n"
        "ds_read_b128 v[216:219], v248 offset:16896\n"
        "s_waitcnt lgkmcnt(7)\n"
        "v_mfma_i32_16x16x64_i8 v[168:171], v[8:11], v[228:231], v[168:171]\n"
        "v_mfma_i32_16x16x64_i8 v[172:175], v[12:15], v[228:231], v[172:175]\n"
        "ds_read_b128 v[220:223], v248 offset:17920\n"
        "s_waitcnt lgkmcnt(7)\n"
        "v_mfma_i32_16x16x64_i8 v[176:179], v[8:11], v[232:235], v[176:179]\n"
        "v_mfma_i32_16x16x64_i8 v[180:183], v[12:15], v[232:235], v[180:183]\n"
        "ds_read_b128 v[224:227], v248 offset:16640\n"
        "s_waitcnt lgkmcnt(7)\n"
        "v_mfma_i32_16x16x64_i8 v[184:187], v[8:11], v[236:239], v[184:187]\n"
        "s_mov_b64 exec, s[6:7]\n"
        "global_load_dwordx4 v[40:43], v240, s[58:59] offset:0 sc1\n"
        "s_mov_b64 exec, -1\n"
        "v_mfma_i32_16x16x64_i8 v[188:191], v[12:15], v[236:239], v[188:191]\n"
        "ds_read_b128 v[228:231], v248 offset:17664\n"
        "s_waitcnt vmcnt(5)\n"
        "s_waitcnt lgkmcnt(7)\n"
        "v_mfma_i32_16x16x64_i8 v[128:131], v[16:19], v[208:211], v[128:131]\n"
        "v_mfma_i32_16x16x64_i8 v[132:135], v[20:23], v[208:211], v[132:135]\n"
        "ds_read_b128 v[232:235], v248 offset:17152\n"
        "s_waitcnt lgkmcnt(6)\n"
        "v_mfma_i32_16x16x64_i8 v[136:139], v[16:19], v[212:215], v[136:139]\n"
        "v_mfma_i32_16x16x64_i8 v[140:143], v[20:23], v[212:215], v[140:143]\n"
        "ds_read_b128 v[236:239], v248 offset:18176\n"
        "v_cmp_gt_u32 vcc, 4, v254\n"
        "s_waitcnt lgkmcnt(5)\n"
        "v_mfma_i32_16x16x64_i8 v[144:147], v[16:19], v[216:219], v[144:147]\n"
        "s_cbranch_vccnz W8_s3\n"
        "W8_c3:\n"
        "v_mfma_i32_16x16x64_i8 v[148:151], v[20:23], v[216:219], v[148:151]\n"
        "v_cmp_gt_u32 vcc, 7, v241\n"
        "ds_read_b128 v[208:211], v248 offset:24576\n"
        "s_waitcnt lgkmcnt(5)\n"
        "v_mfma_i32_16x16x64_i8 v[152:155], v[16:19], v[220:223], v[152:155]\n"
        "s_cbranch_vccnz W8_sR2\n"
        "W8_cR2:\n"
        "ds_read_b32 v254, v250 offset:0\n"
        "v_mfma_i32_16x16x64_i8 v[156:159], v[20:23], v[220:223], v[156:159]\n"
        "ds_read_b128 v[212:215], v248 offset:25600\n"
        "s_waitcnt lgkmcnt(6)\n"
        "v_mfma_i32_16x16x64_i8 v[160:163], v[16:19], v[224:227], v[160:163]\n"
        "ds_read_b32 v241, v250 offset:4\n"
        "s_mov_b64 exec, s[6:7]\n"
        "global_load_dwordx4 v[44:47], v240, s[58:59] offset:1024 sc1\n"
        "s_mov_b64 exec, -1\n"
        "v_mfma_i32_16x16x64_i8 v[164:167], v[20:23], v[224:227], v[164:167]\n"
        "ds_read_b128 v[216:219], v248 offset:25088\n"
        "s_waitcnt lgkmcnt(7)\n"
        "v_mfma_i32_16x16x64_i8 v[168:171], v[16:19], v[228:231], v[168:171]\n"
        "v_mfma_i32_16x16x64_i8 v[172:175], v[20:23], v[228:231], v[172:175]\n"
        "ds_read_b128 v[220:223], v248 offset:26112\n"
        "s_waitcnt lgkmcnt(7)\n"
        "v_mfma_i32_16x16x64_i8 v[176:179], v[16:19], v[232:235], v[176:179]\n"
        "v_mfma_i32_16x16x64_i8 v[180:183], v[20:23], v[232:235], v[180:183]\n"
        "ds_read_b128 v[224:227], v248 offset:24832\n"
        "s_waitcnt lgkmcnt(7)\n"
        "v_mfma_i32_16x16x64_i8 v[184:187], v[16:19], v[236:239], v[184:187]\n"
        "s_mov_b64 exec, s[12:13]\n"
        "global_load_dwordx4 v[48:51], v240, s[60:61] offset:0 sc1\n"
        "s_mov_b64 exec, -1\n"
        "v_mfma_i32_16x16x64_i8 v[188:191], v[20:23], v[236:239], v[188:191]\n"
        "ds_read_b128 v[228:231], v248 offset:25856\n"
        "s_waitcnt vmcnt(5)\n"
        "s_waitcnt lgkmcnt(7)\n"
        "v_mfma_i32_16x16x64_i8 v[128:131], v[24:27], v[208:211], v[128:131]\n"
        "v_mfma_i32_16x16x64_i8 v[132:135], v[28:31], v[208:211], v[132:135]\n"
        "ds_read_b128 v[232:235], v248 offset:25344\n"
        "s_waitcnt lgkmcnt(6)\n"
        "v_mfma_i32_16x16x64_i8 v[136:139], v[24:27], v[212:215], v[136:139]\n"
        "v_mfma_i32_16x16x64_i8 v[140:143], v[28:31], v[212:215], v[140:143]\n"
        "ds_read_b128 v[236:239], v248 offset:26368\n"
        "v_cmp_gt_u32 vcc, 5, v254\n"
        "s_waitcnt lgkmcnt(5)\n"
        "v_mfma_i32_16x16x64_i8 v[144:147], v[24:27], v[216:219], v[144:147]\n"
        "s_cbranch_vccnz W8_s4\n"
        "W8_c4:\n"
        "v_mfma_i32_16x16x64_i8 v[148:151], v[28:31], v[216:219], v[148:151]\n"
        "v_cmp_gt_u32 vcc, 8, v241\n"
        "ds_read_b128 v[208:211], v248 offset:32768\n"
        "s_waitcnt lgkmcnt(5)\n"
        "v_mfma_i32_16x16x64_i8 v[152:155], v[24:27], v[220:223], v[152:155]\n"
        "s_cbranch_vccnz W8_sR3\n"
        "W8_cR3:\n"
        "ds_read_b32 v254, v250 offset:0\n"
        "v_mfma_i32_16x16x64_i8 v[156:159], v[28:31], v[220:223], v[156:159]\n"
        "ds_read_b128 v[212:215], v248 offset:33792\n"
        "s_waitcnt lgkmcnt(6)\n"
        "v_mfma_i32_16x16x64_i8 v[160:163], v[24:27], v[224:227], v[160:163]\n"
        "ds_read_b32 v241, v250 offset:4\n"
        "s_mov_b64 exec, 1\n"
        "ds_add_u32 v250, v250 offset:516\n"
        "s_mov_b64 exec, -1\n"
        "v_mfma_i32_16x16x64_i8 v[164:167], v[28:31], v[224:227], v[164:167]\n"
        "ds_read_b128 v[216:219], v248 offset:33280\n"
        "s_waitcnt lgkmcnt(8)\n"
        "v_mfma_i32_16x16x64_i8 v[168:171], v[24:27], v[228:231], v[168:171]\n"
        "s_mov_b64 exec, s[12:13]\n"
        "global_load_dwordx4 v[52:55], v240, s[60:61] offset:1024 sc1\n"
        "s_mov_b64 exec, -1\n"
        "v_mfma_i32_16x16x64_i8 v[172:175], v[28:31], v[228:231], v[172:175]\n"
        "ds_read_b128 v[220:223], v248 offset:34304\n"
        "s_waitcnt lgkmcnt(8)\n"
        "v_mfma_i32_16x16x64_i8 v[176:179], v[24:27], v[232:235], v[176:179]\n"
        "v_mfma_i32_16x16x64_i8 v[180:183], v[28:31], v[232:235], v[180:183]\n"
        "ds_read_b128 v[224:227], v248 offset:33024\n"
        "s_waitcnt lgkmcnt(8)\n"
        "v_mfma_i32_16x16x64_i8 v[184:187], v[24:27], v[236:239], v[184:187]\n"
        "v_mfma_i32_16x16x64_i8 v[188:191], v[28:31], v[236:239], v[188:191]\n"
        "ds_read_b128 v[228:231], v248 offset:34048\n"
        "s_waitcnt vmcnt(4)\n"
        "s_waitcnt lgkmcnt(8)\n"
        "v_mfma_i32_16x16x64_i8 v[128:131], v[32:35], v[208:211], v[128:131]\n"
        "s_mov_b64 exec, s[14:15]\n"
        "global_load_dwordx4 v[56:59], v240, s[62:63] offset:0 sc1\n"
        "s_mov_b64 exec, -1\n"
        "v_mfma_i32_16x16x64_i8 v[132:135], v[36:39], v[208:211], v[132:135]\n"
        "ds_read_b128 v[232:235], v248 offset:33536\n"
        "s_waitcnt lgkmcnt(7)\n"
        "v_mfma_i32_16x16x64_i8 v[136:139], v[32:35], v[212:215], v[136:139]\n"
        "v_mfma_i32_16x16x64_i8 v[140:143], v[36:39], v[212:215], v[140:143]\n"
        "ds_read_b128 v[236:239], v248 offset:34560\n"
        "v_cmp_gt_u32 vcc, 6, v254\n"
        "s_waitcnt lgkmcnt(5)\n"
        "v_mfma_i32_16x16x64_i8 v[144:147], v[32:35], v[216:219], v[144:147]\n"
        "s_cbranch_vccnz W8_s5\n"
        "W8_c5:\n"
        "v_mfma_i32_16x16x64_i8 v[148:151], v[36:39], v[216:219], v[148:151]\n"
        "v_cmp_gt_u32 vcc, 9, v241\n"
        "ds_read_b128 v[208:211], v248 offset:40960\n"
        "s_waitcnt lgkmcnt(5)\n"
        "v_mfma_i32_16x16x64_i8 v[152:155], v[32:35], v[220:223], v[152:155]\n"
        "s_cbranch_vccnz W8_sR4\n"
        "W8_cR4:\n"
        "ds_read_b32 v254, v250 offset:0\n"
        "v_mfma_i32_16x16x64_i8 v[156:159], v[36:39], v[220:223], v[156:159]\n"
        "ds_read_b128 v[212:215], v248 offset:41984\n"
        "s_waitcnt lgkmcnt(6)\n"
        "v_mfma_i32_16x16x64_i8 v[160:163], v[32:35], v[224:227], v[160:163]\n"
        "ds_read_b32 v241, v250 offset:4\n"
        "s_mov_b64 exec, s[14:15]\n"
        "global_load_dwordx4 v[60:63], v240, s[62:63] offset:1024 sc1\n"
        "s_mov_b64 exec, -1\n"
        "v_add_u32 v240, 8192, v240\n"
        "v_mfma_i32_16x16x64_i8 v[164:167], v[36:39], v[224:227], v[164:167]\n"
        "ds_read_b128 v[216:219], v248 offset:41472\n"
        "s_waitcnt lgkmcnt(7)\n"
        "v_mfma_i32_16x16x64_i8 v[168:171], v[32:35], v[228:231], v[168:171]\n"
        "v_mfma_i32_16x16x64_i8 v[172:175], v[36:39], v[228:231], v[172:175]\n"
        "ds_read_b128 v[220:223], v248 offset:42496\n"
        "s_waitcnt lgkmcnt(7)\n"
        "v_mfma_i32_16x16x64_i8 v[176:179], v[32:35], v[232:235], v[176:179]\n"
        "v_mfma_i32_16x16x64_i8 v[180:183], v[36:39], v[232:235], v[180:183]\n"
        "ds_read_b128 v[224:227], v248 offset:41216\n"
        "s_waitcnt lgkmcnt(7)\n"
        "v_mfma_i32_16x16x64_i8 v[184:187], v[32:35], v[236:239], v[184:187]\n"
        "s_mov_b64 exec, s[4:5]\n"
        "global_load_dwordx4 v[64:67], v240, s[56:57] offset:0 sc1\n"
        "s_mov_b64 exec, -1\n"
        "v_mfma_i32_16x16x64_i8 v[188:191], v[36:39], v[236:239], v[188:191]\n"
        "ds_read_b128 v[228:231], v248 offset:42240\n"
        "s_waitcnt vmcnt(5)\n"
        "s_waitcnt lgkmcnt(7)\n"
        "v_mfma_i32_16x16x64_i8 v[128:131], v[40:43], v[208:211], v[128:131]\n"
        "v_mfma_i32_16x16x64_i8 v[132:135], v[44:47], v[208:211], v[132:135]\n"
        "ds_read_b128 v[232:235], v248 offset:41728\n"
        "s_waitcnt lgkmcnt(6)\n"
        "v_mfma_i32_16x16x64_i8 v[136:139], v[40:43], v[212:215], v[136:139]\n"
        "v_mfma_i32_16x16x64_i8 v[140:143], v[44:47], v[212:215], v[140:143]\n"
        "ds_read_b128 v[236:239], v248 offset:42752\n"
        "v_cmp_gt_u32 vcc, 7, v254\n"
        "s_waitcnt lgkmcnt(5)\n"
        "v_mfma_i32_16x16x64_i8 v[144:147], v[40:43], v[216:219], v[144:147]\n"
        "s_cbranch_vccnz W8_s6\n"
        "W8_c6:\n"
        "v_mfma_i32_16x16x64_i8 v[148:151], v[44:47], v[216:219], v[148:151]\n"
        "v_cmp_gt_u32 vcc, 10, v241\n"
        "ds_read_b128 v[208:211], v248 offset:49152\n"
        "s_waitcnt lgkmcnt(5)\n"
        "v_mfma_i32_16x16x64_i8 v[152:155], v[40:43], v[220:223], v[152:155]\n"
        "s_cbranch_vccnz W8_sR5\n"
        "W8_cR5:\n"
        "ds_read_b32 v254, v250 offset:0\n"
        "v_mfma_i32_16x16x64_i8 v[156:159], v[44:47], v[220:223], v[156:159]\n"
        "ds_read_b128 v[212:215], v248 offset:50176\n"
        "s_waitcnt lgkmcnt(6)\n"
        "v_mfma_i32_16x16x64_i8 v[160:163], v[40:43], v[224:227], v[160:163]\n"
        "ds_read_b32 v241, v250 offset:4\n"
        "s_mov_b64 exec, s[4:5]\n"
        "global_load_dwordx4 v[68:71], v240, s[56:57] offset:1024 sc1\n"
        "s_mov_b64 exec, -1\n"
        "v_mfma_i32_16x16x64_i8 v[164:167], v[44:47], v[224:227], v[164:167]\n"
        "ds_read_b128 v[216:219], v248 offset:49664\n"
        "s_waitcnt lgkmcnt(7)\n"
        "v_mfma_i32_16x16x64_i8 v[168:171], v[40:43], v[228:231], v[168:171]\n"
        "v_mfma_i32_16x16x64_i8 v[172:175], v[44:47], v[228:231], v[172:175]\n"
        "ds_read_b128 v[220:223], v248 offset:50688\n"
        "s_waitcnt lgkmcnt(7)\n"
        "v_mfma_i32_16x16x64_i8 v[176:179], v[40:43], v[232:235], v[176:179]\n"
        "v_mfma_i32_16x16x64_i8 v[180:183], v[44:47], v[232:235], v[180:183]\n"
        "ds_read_b128 v[224:227], v248 offset:49408\n"
        "s_waitcnt lgkmcnt(7)\n"
        "v_mfma_i32_16x16x64_i8 v[184:187], v[40:43], v[236:239], v[184:187]\n"
        "s_mov_b64 exec, s[6:7]\n"
        "global_load_dwordx4 v[72:75], v240, s[58:59] offset:0 sc1\n"
        "s_mov_b64 exec, -1\n"
        "v_mfma_i32_16x16x64_i8 v[188:191], v[44:47], v[236:239], v[188:191]\n"
        "ds_read_b128 v[228:231], v248 offset:50432\n"
        "s_waitcnt vmcnt(5)\n"
        "s_waitcnt lgkmcnt(7)\n"
        "v_mfma_i32_16x16x64_i8 v[128:131], v[48:51], v[208:211], v[128:131]\n"
        "v_mfma_i32_16x16x64_i8 v[132:135], v[52:55], v[208:211], v[132:135]\n"
        "ds_read_b128 v[232:235], v248 offset:49920\n"
        "s_waitcnt lgkmcnt(6)\n"
        "v_mfma_i32_16x16x64_i8 v[136:139], v[48:51], v[212:215], v[136:139]\n"
        "v_mfma_i32_16x16x64_i8 v[140:143], v[52:55], v[212:215], v[140:143]\n"
        "ds_read_b128 v[236:239], v248 offset:50944\n"
        "v_cmp_gt_u32 vcc, 8, v254\n"
        "s_waitcnt lgkmcnt(5)\n"
        "v_mfma_i32_16x16x64_i8 v[144:147], v[48:51], v[216:219], v[144:147]\n"
        "s_cbranch_vccnz W8_s7\n"
        "W8_c7:\n"
        "v_mfma_i32_16x16x64_i8 v[148:151], v[52:55], v[216:219], v[148:151]\n"
        "v_cmp_gt_u32 vcc, 11, v241\n"
        "ds_read_b128 v[208:211], v248 offset:57344\n"
        "s_waitcnt lgkmcnt(5)\n"
        "v_mfma_i32_16x16x64_i8 v[152:155], v[48:51], v[220:223], v[152:155]\n"
        "s_cbranch_vccnz W8_sR6\n"
        "W8_cR6:\n"
        "ds_read_b32 v254, v250 offset:0\n"
        "v_mfma_i32_16x16x64_i8 v[156:159], v[52:55], v[220:223], v[156:159]\n"
        "ds_read_b128 v[212:215], v248 offset:58368\n"
        "s_waitcnt lgkmcnt(6)\n"
        "v_mfma_i32_16x16x64_i8 v[160:163], v[48:51], v[224:227], v[160:163]\n"
        "ds_read_b32 v241, v250 offset:4\n"
        "s_mov_b64 exec, s[6:7]\n"
        "global_load_dwordx4 v[76:79], v240, s[58:59] offset:1024 sc1\n"
        "s_mov_b64 exec, -1\n"
        "v_mfma_i32_16x16x64_i8 v[164:167], v[52:55], v[224:227], v[164:167]\n"
        "ds_read_b128 v[216:219], v248 offset:57856\n"
        "s_waitcnt lgkmcnt(7)\n"
        "v_mfma_i32_16x16x64_i8 v[168:171], v[48:51], v[228:231], v[168:171]\n"
        "v_mfma_i32_16x16x64_i8 v[172:175], v[52:55], v[228:231], v[172:175]\n"
        "ds_read_b128 v[220:223], v248 offset:58880\n"
        "s_waitcnt lgkmcnt(7)\n"
        "v_mfma_i32_16x16x64_i8 v[176:179], v[48:51], v[232:235], v[176:179]\n"
        "v_mfma_i32_16x16x64_i8 v[180:183], v[52:55], v[232:235], v[180:183]\n"
        "ds_read_b128 v[224:227], v248 offset:57600\n"
        "s_waitcnt lgkmcnt(7)\n"
        "v_mfma_i32_16x16x64_i8 v[184:187], v[48:51], v[236:239], v[184:187]\n"
        "s_mov_b64 exec, s[12:13]\n"
        "global_load_dwordx4 v[80:83], v240, s[60:61] offset:0 sc1\n"
        "s_mov_b64 exec, -1\n"
        "v_mfma_i32_16x16x64_i8 v[188:191], v[52:55], v[236:239], v[188:191]\n"
        "ds_read_b128 v[228:231], v248 offset:58624\n"
        "s_waitcnt vmcnt(5)\n"
        "s_waitcnt lgkmcnt(7)\n"
        "v_mfma_i32_16x16x64_i8 v[128:131], v[56:59], v[208:211], v[128:131]\n"
        "v_mfma_i32_16x16x64_i8 v[132:135], v[60:63], v[208:211], v[132:135]\n"
        "ds_read_b128 v[232:235], v248 offset:58112\n"
        "s_waitcnt lgkmcnt(6)\n"
        "v_mfma_i32_16x16x64_i8 v[136:139], v[56:59], v[212:215], v[136:139]\n"
        "v_mfma_i32_16x16x64_i8 v[140:143], v[60:63], v[212:215], v[140:143]\n"
        "ds_read_b128 v[236:239], v248 offset:59136\n"
        "v_cmp_gt_u32 vcc, 9, v254\n"
        "s_waitcnt lgkmcnt(5)\n"
        "v_mfma_i32_16x16x64_i8 v[144:147], v[56:59], v[216:219], v[144:147]\n"
        "s_cbranch_vccnz W8_s8\n"
        "W8_c8:\n"
        "v_mfma_i32_16x16x64_i8 v[148:151], v[60:63], v[216:219], v[148:151]\n"
        "v_cmp_gt_u32 vcc, 12, v241\n"
        "ds_read_b128 v[208:211], v249 offset:0\n"
        "s_waitcnt lgkmcnt(5)\n"
        "v_mfma_i32_16x16x64_i8 v[152:155], v[56:59], v[220:223], v[152:155]\n"
        "s_cbranch_vccnz W8_sR7\n"
        "W8_cR7:\n"
        "ds_read_b32 v254, v250 offset:0\n"
        "v_mfma_i32_16x16x64_i8 v[156:159], v[60:63], v[220:223], v[156:159]\n"
        "ds_read_b128 v[212:215], v249 offset:1024\n"
        "s_waitcnt lgkmcnt(6)\n"
        "v_mfma_i32_16x16x64_i8 v[160:163], v[56:59], v[224:227], v[160:163]\n"
        "ds_read_b32 v241, v250 offset:4\n"
        "s_mov_b64 exec, 1\n"
        "ds_add_u32 v250, v250 offset:520\n"
        "s_mov_b64 exec, -1\n"
        "v_mfma_i32_16x16x64_i8 v[164:167], v[60:63], v[224:227], v[164:167]\n"
        "ds_read_b128 v[216:219], v249 offset:512\n"
        "s_waitcnt lgkmcnt(8)\n"
        "v_mfma_i32_16x16x64_i8 v[168:171], v[56:59], v[228:231], v[168:171]\n"
        "s_mov_b64 exec, s[12:13]\n"
        "global_load_dwordx4 v[84:87], v240, s[60:61] offset:1024 sc1\n"
        "s_mov_b64 exec, -1\n"
        "v_mfma_i32_16x16x64_i8 v[172:175], v[60:63], v[228:231], v[172:175]\n"
        "ds_read_b128 v[220:223], v249 offset:1536\n"
        "s_waitcnt lgkmcnt(8)\n"
        "v_mfma_i32_16x16x64_i8 v[176:179], v[56:59], v[232:235], v[176:179]\n"
        "v_mfma_i32_16x16x64_i8 v[180:183], v[60:63], v[232:235], v[180:183]\n"
        "ds_read_b128 v[224:227], v249 offset:256\n"
        "s_waitcnt lgkmcnt(8)\n"
        "v_mfma_i32_16x16x64_i8 v[184:187], v[56:59], v[236:239], v[184:187]\n"
        "v_mfma_i32_16x16x64_i8 v[188:191], v[60:63], v[236:239], v[188:191]\n"
        "ds_read_b128 v[228:231], v249 offset:1280\n"
        "s_waitcnt vmcnt(4)\n"
        "s_waitcnt lgkmcnt(8)\n"
        "v_mfma_i32_16x16x64_i8 v[128:131], v[64:67], v[208:211], v[128:131]\n"
        "s_mov_b64 exec, s[14:15]\n"
        "global_load_dwordx4 v[88:91], v240, s[62:63] offset:0 sc1\n"
        "s_mov_b64 exec, -1\n"
        "v_mfma_i32_16x16x64_i8 v[132:135], v[68:71], v[208:211], v[132:135]\n"
        "ds_read_b128 v[232:235], v249 offset:768\n"
        "s_waitcnt lgkmcnt(7)\n"
        "v_mfma_i32_16x16x64_i8 v[136:139], v[64:67], v[212:215], v[136:139]\n"
        "v_mfma_i32_16x16x64_i8 v[140:143], v[68:71], v[212:215], v[140:143]\n"
        "ds_read_b128 v[236:239], v249 offset:1792\n"
        "v_cmp_gt_u32 vcc, 10, v254\n"
        "s_waitcnt lgkmcnt(5)\n"
        "v_mfma_i32_16x16x64_i8 v[144:147], v[64:67], v[216:219], v[144:147]\n"
        "s_cbranch_vccnz W8_s9\n"
        "W8_c9:\n"
        "v_mfma_i32_16x16x64_i8 v[148:151], v[68:71], v[216:219], v[148:151]\n"
        "v_cmp_gt_u32 vcc, 13, v241\n"
        "ds_read_b128 v[208:211], v249 offset:8192\n"
        "s_waitcnt lgkmcnt(5)\n"
        "v_mfma_i32_16x16x64_i8 v[152:155], v[64:67], v[220:223], v[152:155]\n"
        "s_cbranch_vccnz W8_sR8\n"
        "W8_cR8:\n"
        "ds_read_b32 v254, v250 offset:0\n"
        "v_mfma_i32_16x16x64_i8 v[156:159], v[68:71], v[220:223], v[156:159]\n"
        "ds_read_b128 v[212:215], v249 offset:9216\n"
        "s_waitcnt lgkmcnt(6)\n"
        "v_mfma_i32_16x16x64_i8 v[160:163], v[64:67], v[224:227], v[160:163]\n"
        "ds_read_b32 v241, v250 offset:4\n"
        "s_mov_b64 exec, s[14:15]\n"
        "global_load_dwordx4 v[92:95], v240, s[62:63] offset:1024 sc1\n"
        "s_mov_b64 exec, -1\n"
        "v_add_u32 v240, 8192, v240\n"
        "v_mfma_i32_16x16x64_i8 v[164:167], v[68:71], v[224:227], v[164:167]\n"
        "ds_read_b128 v[216:219], v249 offset:8704\n"
        "s_waitcnt lgkmcnt(7)\n"
        "v_mfma_i32_16x16x64_i8 v[168:171], v[64:67], v[228:231], v[168:171]\n"
        "v_mfma_i32_16x16x64_i8 v[172:175], v[68:71], v[228:231], v[172:175]\n"
        "ds_read_b128 v[220:223], v249 offset:9728\n"
        "s_waitcnt lgkmcnt(7)\n"
        "v_mfma_i32_16x16x64_i8 v[176:179], v[64:67], v[232:235], v[176:179]\n"
        "v_mfma_i32_16x16x64_i8 v[180:183], v[68:71], v[232:235], v[180:183]\n"
        "ds_read_b128 v[224:227], v249 offset:8448\n"
        "s_waitcnt lgkmcnt(7)\n"
        "v_mfma_i32_16x16x64_i8 v[184:187], v[64:67], v[236:239], v[184:187]\n"
        "s_mov_b64 exec, s[4:5]\n"
        "global_load_dwordx4 v[96:99], v240, s[56:57] offset:0 sc1\n"
        "s_mov_b64 exec, -1\n"
        "v_mfma_i32_16x16x64_i8 v[188:191], v[68:71], v[236:239], v[188:191]\n"
        "ds_read_b128 v[228:231], v249 offset:9472\n"
        "s_waitcnt vmcnt(5)\n"
        "s_waitcnt lgkmcnt(7)\n"
        "v_mfma_i32_16x16x64_i8 v[128:131], v[72:75], v[208:211], v[128:131]\n"
        "v_mfma_i32_16x16x64_i8 v[132:135], v[76:79], v[208:211], v[132:135]\n"
        "ds_read_b128 v[232:235], v249 offset:8960\n"
        "s_waitcnt lgkmcnt(6)\n"
        "v_mfma_i32_16x16x64_i8 v[136:139], v[72:75], v[212:215], v[136:139]\n"
        "v_mfma_i32_16x16x64_i8 v[140:143], v[76:79], v[212:215], v[140:143]\n"
        "ds_read_b128 v[236:239], v249 offset:9984\n"
        "v_cmp_gt_u32 vcc, 11, v254\n"
        "s_waitcnt lgkmcnt(5)\n"
        "v_mfma_i32_16x16x64_i8 v[144:147], v[72:75], v[216:219], v[144:147]\n"
        "s_cbranch_vccnz W8_s10\n"
        "W8_c10:\n"
        "v_mfma_i32_16x16x64_i8 v[148:151], v[76:79], v[216:219], v[148:151]\n"
        "v_cmp_gt_u32 vcc, 14, v241\n"
        "ds_read_b128 v[208:211], v249 offset:16384\n"
        "s_waitcnt lgkmcnt(5)\n"
        "v_mfma_i32_16x16x64_i8 v[152:155], v[72:75], v[220:223], v[152:155]\n"
        "s_cbranch_vccnz W8_sR9\n"
        "W8_cR9:\n"
        "ds_read_b32 v254, v250 offset:0\n"
        "v_mfma_i32_16x16x64_i8 v[156:159], v[76:79], v[220:223], v[156:159]\n"
        "ds_read_b128 v[212:215], v249 offset:17408\n"
        "s_waitcnt lgkmcnt(6)\n"
        "v_mfma_i32_16x16x64_i8 v[160:163], v[72:75], v[224:227], v[160:163]\n"
        "ds_read_b32 v241, v250 offset:4\n"
        "s_mov_b64 exec, s[4:5]\n"
        "global_load_dwordx4 v[100:103], v240, s[56:57] offset:1024 sc1\n"
        "s_mov_b64 exec, -1\n"
        "v_mfma_i32_16x16x64_i8 v[164:167], v[76:79], v[224:227], v[164:167]\n"
        "ds_read_b128 v[216:219], v249 offset:16896\n"
        "s_waitcnt lgkmcnt(7)\n"
        "v_mfma_i32_16x16x64_i8 v[168:171], v[72:75], v[228:231], v[168:171]\n"
        "v_mfma_i32_16x16x64_i8 v[172:175], v[76:79], v[228:231], v[172:175]\n"
        "ds_read_b128 v[220:223], v249 offset:17920\n"
        "s_waitcnt lgkmcnt(7)\n"
        "v_mfma_i32_16x16x64_i8 v[176:179], v[72:75], v[232:235], v[176:179]\n"
        "v_mfma_i32_16x16x64_i8 v[180:183], v[76:79], v[232:235], v[180:183]\n"
        "ds_read_b128 v[224:227], v249 offset:16640\n"
        "s_waitcnt lgkmcnt(7)\n"
        "v_mfma_i32_16x16x64_i8 v[184:187], v[72:75], v[236:239], v[184:187]\n"
        "s_mov_b64 exec, s[6:7]\n"
        "global_load_dwordx4 v[104:107], v240, s[58:59] offset:0 sc1\n"
        "s_mov_b64 exec, -1\n"
        "v_mfma_i32_16x16x64_i8 v[188:191], v[76:79], v[236:239], v[188:191]\n"
        "ds_read_b128 v[228:231], v249 offset:17664\n"
        "s_waitcnt vmcnt(5)\n"
        "s_waitcnt lgkmcnt(7)\n"
        "v_mfma_i32_16x16x64_i8 v[128:131], v[80:83], v[208:211], v[128:131]\n"
        "v_mfma_i32_16x16x64_i8 v[132:135], v[84:87], v[208:211], v[132:135]\n"
        "ds_read_b128 v[232:235], v249 offset:17152\n"
        "s_waitcnt lgkmcnt(6)\n"
        "v_mfma_i32_16x16x64_i8 v[136:139], v[80:83], v[212:215], v[136:139]\n"
        "v_mfma_i32_16x16x64_i8 v[140:143], v[84:87], v[212:215], v[140:143]\n"
        "ds_read_b128 v[236:239], v249 offset:18176\n"
        "v_cmp_gt_u32 vcc, 12, v254\n"
        "s_waitcnt lgkmcnt(5)\n"
        "v_mfma_i32_16x16x64_i8 v[144:147], v[80:83], v[216:219], v[144:147]\n"
        "s_cbranch_vccnz W8_s11\n"
        "W8_c11:\n"
        "v_mfma_i32_16x16x64_i8 v[148:151], v[84:87], v[216:219], v[148:151]\n"
        "v_cmp_gt_u32 vcc, 15, v241\n"
        "ds_read_b128 v[208:211], v249 offset:24576\n"
        "s_waitcnt lgkmcnt(5)\n"
        "v_mfma_i32_16x16x64_i8 v[152:155], v[80:83], v[220:223], v[152:155]\n"
        "s_cbranch_vccnz W8_sR10\n"
        "W8_cR10:\n"
        "ds_read_b32 v254, v250 offset:0\n"
        "v_mfma_i32_16x16x64_i8 v[156:159], v[84:87], v[220:223], v[156:159]\n"
        "ds_read_b128 v[212:215], v249 offset:25600\n"
        "s_waitcnt lgkmcnt(6)\n"
        "v_mfma_i32_16x16x64_i8 v[160:163], v[80:83], v[224:227], v[160:163]\n"
        "ds_read_b32 v241, v250 offset:4\n"
        "s_mov_b64 exec, s[6:7]\n"
        "global_load_dwordx4 v[108:111], v240, s[58:59] offset:1024 sc1\n"
        "s_mov_b64 exec, -1\n"
        "v_mfma_i32_16x16x64_i8 v[164:167], v[84:87], v[224:227], v[164:167]\n"
        "ds_read_b128 v[216:219], v249 offset:25088\n"
        "s_waitcnt lgkmcnt(7)\n"
        "v_mfma_i32_16x16x64_i8 v[168:171], v[80:83], v[228:231], v[168:171]\n"
        "v_mfma_i32_16x16x64_i8 v[172:175], v[84:87], v[228:231], v[172:175]\n"
        "ds_read_b128 v[220:223], v249 offset:26112\n"
        "s_waitcnt lgkmcnt(7)\n"
        "v_mfma_i32_16x16x64_i8 v[176:179], v[80:83], v[232:235], v[176:179]\n"
        "v_mfma_i32_16x16x64_i8 v[180:183], v[84:87], v[232:235], v[180:183]\n"
        "ds_read_b128 v[224:227], v249 offset:24832\n"
        "s_waitcnt lgkmcnt(7)\n"
        "v_mfma_i32_16x16x64_i8 v[184:187], v[80:83], v[236:239], v[184:187]\n"
        "s_mov_b64 exec, s[12:13]\n"
        "global_load_dwordx4 v[112:115], v240, s[60:61] offset:0 sc1\n"
        "s_mov_b64 exec, -1\n"
        "v_mfma_i32_16x16x64_i8 v[188:191], v[84:87], v[236:239], v[188:191]\n"
        "ds_read_b128 v[228:231], v249 offset:25856\n"
        "s_waitcnt vmcnt(5)\n"
        "s_waitcnt lgkmcnt(7)\n"
        "v_mfma_i32_16x16x64_i8 v[128:131], v[88:91], v[208:211], v[128:131]\n"
        "v_mfma_i32_16x16x64_i8 v[132:135], v[92:95], v[208:211], v[132:135]\n"
        "ds_read_b128 v[232:235], v249 offset:25344\n"
        "s_waitcnt lgkmcnt(6)\n"
        "v_mfma_i32_16x16x64_i8 v[136:139], v[88:91], v[212:215], v[136:139]\n"
        "v_mfma_i32_16x16x64_i8 v[140:143], v[92:95], v[212:215], v[140:143]\n"
        "ds_read_b128 v[236:239], v249 offset:26368\n"
        "v_cmp_gt_u32 vcc, 13, v254\n"
        "s_waitcnt lgkmcnt(5)\n"
        "v_mfma_i32_16x16x64_i8 v[144:147], v[88:91], v[216:219], v[144:147]\n"
        "s_cbranch_vccnz W8_s12\n"
        "W8_c12:\n"
        "v_mfma_i32_16x16x64_i8 v[148:151], v[92:95], v[216:219], v[148:151]\n"
        "v_cmp_gt_u32 vcc, 16, v241\n"
        "ds_read_b128 v[208:211], v249 offset:32768\n"
        "s_waitcnt lgkmcnt(5)\n"
        "v_mfma_i32_16x16x64_i8 v[152:155], v[88:91], v[220:223], v[152:155]\n"
        "s_cbranch_vccnz W8_sR11\n"
        "W8_cR11:\n"
        "ds_read_b32 v254, v250 offset:0\n"
        "v_mfma_i32_16x16x64_i8 v[156:159], v[92:95], v[220:223], v[156:159]\n"
        "ds_read_b128 v[212:215], v249 offset:33792\n"
        "s_waitcnt lgkmcnt(6)\n"
        "v_mfma_i32_16x16x64_i8 v[160:163], v[88:91], v[224:227], v[160:163]\n"
        "s_mov_b64 exec, 1\n"
        "ds_add_u32 v250, v250 offset:524\n"
        "s_mov_b64 exec, -1\n"
        "v_mfma_i32_16x16x64_i8 v[164:167], v[92:95], v[224:227], v[164:167]\n"
        "ds_read_b128 v[216:219], v249 offset:33280\n"
        "s_waitcnt lgkmcnt(7)\n"
        "v_mfma_i32_16x16x64_i8 v[168:171], v[88:91], v[228:231], v[168:171]\n"
        "s_mov_b64 exec, s[12:13]\n"
        "global_load_dwordx4 v[116:119], v240, s[60:61] offset:1024 sc1\n"
        "s_mov_b64 exec, -1\n"
        "v_mfma_i32_16x16x64_i8 v[172:175], v[92:95], v[228:231], v[172:175]\n"
        "ds_read_b128 v[220:223], v249 offset:34304\n"
        "s_waitcnt lgkmcnt(7)\n"
        "v_mfma_i32_16x16x64_i8 v[176:179], v[88:91], v[232:235], v[176:179]\n"
        "v_mfma_i32_16x16x64_i8 v[180:183], v[92:95], v[232:235], v[180:183]\n"
        "ds_read_b128 v[224:227], v249 offset:33024\n"
        "s_waitcnt lgkmcnt(7)\n"
        "v_mfma_i32_16x16x64_i8 v[184:187], v[88:91], v[236:239], v[184:187]\n"
        "v_mfma_i32_16x16x64_i8 v[188:191], v[92:95], v[236:239], v[188:191]\n"
        "ds_read_b128 v[228:231], v249 offset:34048\n"
        "s_waitcnt vmcnt(4)\n"
        "s_waitcnt lgkmcnt(7)\n"
        "v_mfma_i32_16x16x64_i8 v[128:131], v[96:99], v[208:211], v[128:131]\n"
        "s_mov_b64 exec, s[14:15]\n"
        "global_load_dwordx4 v[120:123], v240, s[62:63] offset:0 sc1\n"
        "s_mov_b64 exec, -1\n"
        "v_mfma_i32_16x16x64_i8 v[132:135], v[100:103], v[208:211], v[132:135]\n"
        "ds_read_b128 v[232:235], v249 offset:33536\n"
        "s_waitcnt lgkmcnt(6)\n"
        "v_mfma_i32_16x16x64_i8 v[136:139], v[96:99], v[212:215], v[136:139]\n"
        "v_and_b32 v248, 0x3ff, v240\n"
        "v_mfma_i32_16x16x64_i8 v[140:143], v[100:103], v[212:215], v[140:143]\n"
        "ds_read_b128 v[236:239], v249 offset:34560\n"
        "v_cmp_gt_u32 vcc, 14, v254\n"
        "s_waitcnt lgkmcnt(5)\n"
        "v_mfma_i32_16x16x64_i8 v[144:147], v[96:99], v[216:219], v[144:147]\n"
        "s_cbranch_vccnz W8_s13\n"
        "W8_c13:\n"
        "v_mfma_i32_16x16x64_i8 v[148:151], v[100:103], v[216:219], v[148:151]\n"
        "ds_read_b128 v[208:211], v249 offset:40960\n"
        "s_waitcnt lgkmcnt(5)\n"
        "v_mfma_i32_16x16x64_i8 v[152:155], v[96:99], v[220:223], v[152:155]\n"
        "ds_read_b32 v254, v250 offset:0\n"
        "v_mfma_i32_16x16x64_i8 v[156:159], v[100:103], v[220:223], v[156:159]\n"
        "ds_read_b128 v[212:215], v249 offset:41984\n"
        "s_waitcnt lgkmcnt(6)\n"
        "v_mfma_i32_16x16x64_i8 v[160:163], v[96:99], v[224:227], v[160:163]\n"
        "s_mov_b64 exec, s[14:15]\n"
        "global_load_dwordx4 v[124:127], v240, s[62:63] offset:1024 sc1\n"
        "s_mov_b64 exec, -1\n"
        "v_add_u32 v240, 8192, v240\n"
        "v_mfma_i32_16x16x64_i8 v[164:167], v[100:103], v[224:227], v[164:167]\n"
        "ds_read_b128 v[216:219], v249 offset:41472\n"
        "s_waitcnt lgkmcnt(6)\n"
        "v_mfma_i32_16x16x64_i8 v[168:171], v[96:99], v[228:231], v[168:171]\n"
        "v_mfma_i32_16x16x64_i8 v[172:175], v[100:103], v[228:231], v[172:175]\n"
        "ds_read_b128 v[220:223], v249 offset:42496\n"
        "s_waitcnt lgkmcnt(6)\n"
        "v_mfma_i32_16x16x64_i8 v[176:179], v[96:99], v[232:235], v[176:179]\n"
        "v_mfma_i32_16x16x64_i8 v[180:183], v[100:103], v[232:235], v[180:183]\n"
        "ds_read_b128 v[224:227], v249 offset:41216\n"
        "s_waitcnt lgkmcnt(6)\n"
        "v_mfma_i32_16x16x64_i8 v[184:187], v[96:99], v[236:239], v[184:187]\n"
        "v_mfma_i32_16x16x64_i8 v[188:191], v[100:103], v[236:239], v[188:191]\n"
        "ds_read_b128 v[228:231], v249 offset:42240\n"
        "s_waitcnt vmcnt(4)\n"
        "s_waitcnt lgkmcnt(6)\n"
        "v_mfma_i32_16x16x64_i8 v[128:131], v[104:107], v[208:211], v[128:131]\n"
        "v_mfma_i32_16x16x64_i8 v[132:135], v[108:111], v[208:211], v[132:135]\n"
        "ds_read_b128 v[232:235], v249 offset:41728\n"
        "s_waitcnt lgkmcnt(5)\n"
        "v_mfma_i32_16x16x64_i8 v[136:139], v[104:107], v[212:215], v[136:139]\n"
        "v_mfma_i32_16x16x64_i8 v[140:143], v[108:111], v[212:215], v[140:143]\n"
        "ds_read_b128 v[236:239], v249 offset:42752\n"
        "v_cmp_gt_u32 vcc, 15, v254\n"
        "s_waitcnt lgkmcnt(5)\n"
        "v_mfma_i32_16x16x64_i8 v[144:147], v[104:107], v[216:219], v[144:147]\n"
        "s_cbranch_vccnz W8_s14\n"
        "W8_c14:\n"
        "v_mfma_i32_16x16x64_i8 v[148:151], v[108:111], v[216:219], v[148:151]\n"
        "ds_read_b128 v[208:211], v249 offset:49152\n"
        "s_waitcnt lgkmcnt(5)\n"
        "v_mfma_i32_16x16x64_i8 v[152:155], v[104:107], v[220:223], v[152:155]\n"
        "ds_read_b32 v254, v250 offset:0\n"
        "v_mfma_i32_16x16x64_i8 v[156:159], v[108:111], v[220:223], v[156:159]\n"
        "ds_read_b128 v[212:215], v249 offset:50176\n"
        "s_waitcnt lgkmcnt(6)\n"
        "v_mfma_i32_16x16x64_i8 v[160:163], v[104:107], v[224:227], v[160:163]\n"
        "v_mfma_i32_16x16x64_i8 v[164:167], v[108:111], v[224:227], v[164:167]\n"
        "ds_read_b128 v[216:219], v249 offset:49664\n"
        "s_waitcnt lgkmcnt(6)\n"
        "v_mfma_i32_16x16x64_i8 v[168:171], v[104:107], v[228:231], v[168:171]\n"
        "v_mfma_i32_16x16x64_i8 v[172:175], v[108:111], v[228:231], v[172:175]\n"
        "ds_read_b128 v[220:223], v249 offset:50688\n"
        "s_waitcnt lgkmcnt(6)\n"
        "v_mfma_i32_16x16x64_i8 v[176:179], v[104:107], v[232:235], v[176:179]\n"
        "v_mfma_i32_16x16x64_i8 v[180:183], v[108:111], v[232:235], v[180:183]\n"
        "ds_read_b128 v[224:227], v249 offset:49408\n"
        "s_waitcnt lgkmcnt(6)\n"
        "v_mfma_i32_16x16x64_i8 v[184:187], v[104:107], v[236:239], v[184:187]\n"
        "v_mfma_i32_16x16x64_i8 v[188:191], v[108:111], v[236:239], v[188:191]\n"
        "ds_read_b128 v[228:231], v249 offset:50432\n"
        "s_waitcnt vmcnt(2)\n"
        "s_waitcnt lgkmcnt(6)\n"
        "v_mfma_i32_16x16x64_i8 v[128:131], v[112:115], v[208:211], v[128:131]\n"
        "v_mfma_i32_16x16x64_i8 v[132:135], v[116:119], v[208:211], v[132:135]\n"
        "ds_read_b128 v[232:235], v249 offset:49920\n"
        "s_waitcnt lgkmcnt(5)\n"
        "v_mfma_i32_16x16x64_i8 v[136:139], v[112:115], v[212:215], v[136:139]\n"
        "v_mfma_i32_16x16x64_i8 v[140:143], v[116:119], v[212:215], v[140:143]\n"
        "ds_read_b128 v[236:239], v249 offset:50944\n"
        "v_cmp_gt_u32 vcc, 16, v254\n"
        "s_waitcnt lgkmcnt(5)\n"
        "v_mfma_i32_16x16x64_i8 v[144:147], v[112:115], v[216:219], v[144:147]\n"
        "s_cbranch_vccnz W8_s15\n"
        "W8_c15:\n"
        "v_mfma_i32_16x16x64_i8 v[148:151], v[116:119], v[216:219], v[148:151]\n"
        "ds_read_b128 v[208:211], v249 offset:57344\n"
        "s_waitcnt lgkmcnt(5)\n"
        "v_mfma_i32_16x16x64_i8 v[152:155], v[112:115], v[220:223], v[152:155]\n"
        "ds_read_b32 v254, v250 offset:0\n"
        "v_mfma_i32_16x16x64_i8 v[156:159], v[116:119], v[220:223], v[156:159]\n"
        "ds_read_b128 v[212:215], v249 offset:58368\n"
        "s_waitcnt lgkmcnt(6)\n"
        "v_mfma_i32_16x16x64_i8 v[160:163], v[112:115], v[224:227], v[160:163]\n"
        "v_mfma_i32_16x16x64_i8 v[164:167], v[116:119], v[224:227], v[164:167]\n"
        "ds_read_b128 v[216:219], v249 offset:57856\n"
        "s_waitcnt lgkmcnt(6)\n"
        "v_mfma_i32_16x16x64_i8 v[168:171], v[112:115], v[228:231], v[168:171]\n"
        "ds_read_b32 v255, v250 offset:260\n"
        "v_mfma_i32_16x16x64_i8 v[172:175], v[116:119], v[228:231], v[172:175]\n"
        "ds_read_b128 v[220:223], v249 offset:58880\n"
        "s_waitcnt lgkmcnt(7)\n"
        "v_mfma_i32_16x16x64_i8 v[176:179], v[112:115], v[232:235], v[176:179]\n"
        "v_mfma_i32_16x16x64_i8 v[180:183], v[116:119], v[232:235], v[180:183]\n"
        "ds_read_b128 v[224:227], v249 offset:57600\n"
        "s_waitcnt lgkmcnt(7)\n"
        "v_mfma_i32_16x16x64_i8 v[184:187], v[112:115], v[236:239], v[184:187]\n"
        "v_mfma_i32_16x16x64_i8 v[188:191], v[116:119], v[236:239], v[188:191]\n"
        "ds_read_b128 v[228:231], v249 offset:58624\n"
        "s_waitcnt vmcnt(0)\n"
        "s_waitcnt lgkmcnt(5)\n"
        "v_mfma_i32_16x16x64_i8 v[128:131], v[120:123], v[208:211], v[128:131]\n"
        "v_mfma_i32_16x16x64_i8 v[132:135], v[124:127], v[208:211], v[132:135]\n"
        "ds_read_b128 v[232:235], v249 offset:58112\n"
        "v_mfma_i32_16x16x64_i8 v[136:139], v[120:123], v[212:215], v[136:139]\n"
        "v_mfma_i32_16x16x64_i8 v[140:143], v[124:127], v[212:215], v[140:143]\n"
        "ds_read_b128 v[236:239], v249 offset:59136\n"
        "v_cmp_gt_u32 vcc, 18, v254\n"
        "s_waitcnt lgkmcnt(4)\n"
        "v_mfma_i32_16x16x64_i8 v[144:147], v[120:123], v[216:219], v[144:147]\n"
        "s_cbranch_vccnz W8_s16\n"
        "W8_c16:\n"
        "v_mfma_i32_16x16x64_i8 v[148:151], v[124:127], v[216:219], v[148:151]\n"
        "ds_read_b128 v[208:211], v248 offset:0\n"
        "v_mfma_i32_16x16x64_i8 v[152:155], v[120:123], v[220:223], v[152:155]\n"
        "v_mfma_i32_16x16x64_i8 v[156:159], v[124:127], v[220:223], v[156:159]\n"
        "ds_read_b128 v[212:215], v248 offset:1024\n"
        "s_waitcnt lgkmcnt(4)\n"
        "v_mfma_i32_16x16x64_i8 v[160:163], v[120:123], v[224:227], v[160:163]\n"
        "v_mfma_i32_16x16x64_i8 v[164:167], v[124:127], v[224:227], v[164:167]\n"
        "ds_read_b128 v[216:219], v248 offset:2048\n"
        "v_cmp_gt_u32 vcc, 2, v255\n"
        "v_mfma_i32_16x16x64_i8 v[168:171], v[120:123], v[228:231], v[168:171]\n"
        "v_add_u32 v249, 65536, v248\n"
        "s_cbranch_vccnz W8_sT\n"
        "W8_cT:\n"
        "v_mfma_i32_16x16x64_i8 v[172:175], v[124:127], v[228:231], v[172:175]\n"
        "ds_read_b128 v[220:223], v248 offset:3072\n"
        "s_waitcnt lgkmcnt(4)\n"
        "v_mfma_i32_16x16x64_i8 v[176:179], v[120:123], v[232:235], v[176:179]\n"
        "ds_read_b128 v[240:243], v253\n"
        "ds_read_b128 v[244:247], v253 offset:16\n"
        "v_mfma_i32_16x16x64_i8 v[180:183], v[124:127], v[232:235], v[180:183]\n"
        "ds_read_b128 v[224:227], v248 offset:4096\n"
        "v_mfma_i32_16x16x64_i8 v[184:187], v[120:123], v[236:239], v[184:187]\n"
        "v_cvt_f32_i32 v128, v128\n"
        "v_cvt_f32_i32 v129, v129\n"
        "v_cvt_f32_i32 v130, v130\n"
        "v_mfma_i32_16x16x64_i8 v[188:191], v[124:127], v[236:239], v[188:191]\n"
        "ds_read_b128 v[228:231], v248 offset:5120\n"
        "s_waitcnt lgkmcnt(6)\n"
        "v_mfma_i32_16x16x64_i8 v[192:195], v[0:3], v[208:211], 0\n"
        "v_cvt_f32_i32 v131, v131\n"
        "v_cvt_f32_i32 v136, v136\n"
        "v_mfma_i32_16x16x64_i8 v[196:199], v[4:7], v[208:211], 0\n"
        "ds_read_b128 v[232:235], v248 offset:6144\n"
        "v_cvt_f32_i32 v137, v137\n"
        "v_mfma_i32_16x16x64_i8 v[200:203], v[0:3], v[212:215], 0\n"
        "v_cvt_f32_i32 v138, v138\n"
        "v_cvt_f32_i32 v139, v139\n"
        "v_mfma_i32_16x16x64_i8 v[204:207], v[4:7], v[212:215], 0\n"
        "ds_read_b128 v[236:239], v248 offset:7168\n"
        "v_cvt_f32_i32 v144, v144\n"
        "s_waitcnt lgkmcnt(6)\n"
        "v_mfma_i32_16x16x64_i8 v[192:195], v[8:11], v[216:219], v[192:195]\n"
        "v_cvt_f32_i32 v145, v145\n"
        "v_cvt_f32_i32 v146, v146\n"
        "v_mfma_i32_16x16x64_i8 v[196:199], v[12:15], v[216:219], v[196:199]\n"
        "ds_read_b128 v[208:211], v248 offset:8192\n"
        "v_cvt_f32_i32 v147, v147\n"
        "v_mfma_i32_16x16x64_i8 v[200:203], v[8:11], v[220:223], v[200:203]\n"
        "ds_read_b32 v254, v250 offset:0\n"
        "v_cvt_f32_i32 v152, v152\n"
        "v_mfma_i32_16x16x64_i8 v[204:207], v[12:15], v[220:223], v[204:207]\n"
        "ds_read_b128 v[212:215], v248 offset:9216\n"
        "s_waitcnt lgkmcnt(5)\n"
        "v_mfma_i32_16x16x64_i8 v[192:195], v[16:19], v[224:227], v[192:195]\n"
        "v_cvt_f32_i32 v153, v153\n"
        "v_cvt_f32_i32 v154, v154\n"
        "v_cvt_f32_i32 v155, v155\n"
        "v_mfma_i32_16x16x64_i8 v[196:199], v[20:23], v[224:227], v[196:199]\n"
        "ds_read_b128 v[216:219], v248 offset:10240\n"
        "v_mfma_i32_16x16x64_i8 v[200:203], v[16:19], v[228:231], v[200:203]\n"
        "v_fma_f32 v128, v128, v240, v241\n"
        "v_fma_f32 v129, v129, v240, v241\n"
        "v_mfma_i32_16x16x64_i8 v[204:207], v[20:23], v[228:231], v[204:207]\n"
        "ds_read_b128 v[220:223], v248 offset:11264\n"
        "s_waitcnt lgkmcnt(5)\n"
        "v_mfma_i32_16x16x64_i8 v[192:195], v[24:27], v[232:235], v[192:195]\n"
        "v_fma_f32 v130, v130, v240, v241\n"
        "v_fma_f32 v131, v131, v240, v241\n"
        "v_mfma_i32_16x16x64_i8 v[196:199], v[28:31], v[232:235], v[196:199]\n"
        "ds_read_b128 v[224:227], v248 offset:12288\n"
        "v_fma_f32 v136, v136, v242, v243\n"
        "v_fma_f32 v137, v137, v242, v243\n"
        "v_mfma_i32_16x16x64_i8 v[200:203], v[24:27], v[236:239], v[200:203]\n"
        "v_fma_f32 v138, v138, v242, v243\n"
        "v_fma_f32 v139, v139, v242, v243\n"
        "v_mfma_i32_16x16x64_i8 v[204:207], v[28:31], v[236:239], v[204:207]\n"
        "ds_read_b128 v[228:231], v248 offset:13312\n"
        "s_waitcnt lgkmcnt(4)\n"
        "v_mfma_i32_16x16x64_i8 v[192:195], v[32:35], v[208:211], v[192:195]\n"
        "v_fma_f32 v144, v144, v244, v245\n"
        "v_fma_f32 v145, v145, v244, v245\n"
        "v_mfma_i32_16x16x64_i8 v[196:199], v[36:39], v[208:211], v[196:199]\n"
        "ds_read_b128 v[232:235], v248 offset:14336\n"
        "v_fma_f32 v146, v146, v244, v245\n"
        "v_mfma_i32_16x16x64_i8 v[200:203], v[32:35], v[212:215], v[200:203]\n"
        "v_fma_f32 v147, v147, v244, v245\n"
        "v_fma_f32 v152, v152, v246, v247\n"
        "v_fma_f32 v153, v153, v246, v247\n"
        "v_mfma_i32_16x16x64_i8 v[204:207], v[36:39], v[212:215], v[204:207]\n"
        "ds_read_b128 v[236:239], v248 offset:15360\n"
        "v_cmp_gt_u32 vcc, 20, v254\n"
        "s_waitcnt lgkmcnt(4)\n"
        "v_mfma_i32_16x16x64_i8 v[192:195], v[40:43], v[216:219], v[192:195]\n"
        "s_cbranch_vccnz W8_s18\n"
        "W8_c18:\n"
        "v_fma_f32 v154, v154, v246, v247\n"
        "v_mfma_i32_16x16x64_i8 v[196:199], v[44:47], v[216:219], v[196:199]\n"
        "ds_read_b128 v[208:211], v248 offset:16384\n"
        "v_fma_f32 v155, v155, v246, v247\n"
        "v_mfma_i32_16x16x64_i8 v[200:203], v[40:43], v[220:223], v[200:203]\n"
        "v_cvt_pk_bf16_f32 v128, v128, v136\n"
        "v_cvt_pk_bf16_f32 v136, v129, v137\n"
        "v_mfma_i32_16x16x64_i8 v[204:207], v[44:47], v[220:223], v[204:207]\n"
        "ds_read_b128 v[212:215], v248 offset:17408\n"
        "v_cvt_pk_bf16_f32 v130, v130, v138\n"
        "s_waitcnt lgkmcnt(4)\n"
        "v_mfma_i32_16x16x64_i8 v[192:195], v[48:51], v[224:227], v[192:195]\n"
        "v_cvt_pk_bf16_f32 v138, v131, v139\n"
        "v_cvt_pk_bf16_f32 v129, v144, v152\n"
        "v_mfma_i32_16x16x64_i8 v[196:199], v[52:55], v[224:227], v[196:199]\n"
        "ds_read_b128 v[216:219], v248 offset:18432\n"
        "global_store_dwordx2 v251, v[128:129], s[40:41] offset:0 sc1\n"
        "v_mfma_i32_16x16x64_i8 v[200:203], v[48:51], v[228:231], v[200:203]\n"
        "v_cvt_pk_bf16_f32 v137, v145, v153\n"
        "global_store_dwordx2 v251, v[136:137], s[42:43] offset:0 sc1\n"
        "v_mfma_i32_16x16x64_i8 v[204:207], v[52:55], v[228:231], v[204:207]\n"
        "ds_read_b128 v[220:223], v248 offset:19456\n"
        "s_waitcnt lgkmcnt(4)\n"
        "v_mfma_i32_16x16x64_i8 v[192:195], v[56:59], v[232:235], v[192:195]\n"
        "v_cvt_pk_bf16_f32 v131, v146, v154\n"
        "global_store_dwordx2 v251, v[130:131], s[44:45] offset:0 sc1\n"
        "v_cvt_pk_bf16_f32 v139, v147, v155\n"
        "v_mfma_i32_16x16x64_i8 v[196:199], v[60:63], v[232:235], v[196:199]\n"
        "ds_read_b128 v[224:227], v248 offset:20480\n"
        "global_store_dwordx2 v251, v[138:139], s[46:47] offset:0 sc1\n"
        "v_mfma_i32_16x16x64_i8 v[200:203], v[56:59], v[236:239], v[200:203]\n"
        "v_cvt_f32_i32 v132, v132\n"
        "v_cvt_f32_i32 v133, v133\n"
        "v_mfma_i32_16x16x64_i8 v[204:207], v[60:63], v[236:239], v[204:207]\n"
        "ds_read_b128 v[228:231], v248 offset:21504\n"
        "s_waitcnt lgkmcnt(4)\n"
        "v_mfma_i32_16x16x64_i8 v[192:195], v[64:67], v[208:211], v[192:195]\n"
        "v_cvt_f32_i32 v134, v134\n"
        "v_cvt_f32_i32 v135, v135\n"
        "v_mfma_i32_16x16x64_i8 v[196:199], v[68:71], v[208:211], v[196:199]\n"
        "ds_read_b128 v[232:235], v248 offset:22528\n"
        "v_cvt_f32_i32 v140, v140\n"
        "v_cvt_f32_i32 v141, v141\n"
        "v_mfma_i32_16x16x64_i8 v[200:203], v[64:67], v[212:215], v[200:203]\n"
        "v_cvt_f32_i32 v142, v142\n"
        "v_cvt_f32_i32 v143, v143\n"
        "v_mfma_i32_16x16x64_i8 v[204:207], v[68:71], v[212:215], v[204:207]\n"
        "ds_read_b128 v[236:239], v248 offset:23552\n"
        "s_waitcnt lgkmcnt(4)\n"
        "v_mfma_i32_16x16x64_i8 v[192:195], v[72:75], v[216:219], v[192:195]\n"
        "v_cvt_f32_i32 v148, v148\n"
        "v_cvt_f32_i32 v149, v149\n"
        "v_mfma_i32_16x16x64_i8 v[196:199], v[76:79], v[216:219], v[196:199]\n"
        "ds_read_b128 v[208:211], v248 offset:24576\n"
        "v_cvt_f32_i32 v150, v150\n"
        "v_mfma_i32_16x16x64_i8 v[200:203], v[72:75], v[220:223], v[200:203]\n"
        "ds_read_b32 v254, v250 offset:0\n"
        "v_cvt_f32_i32 v151, v151\n"
        "v_cvt_f32_i32 v156, v156\n"
        "v_mfma_i32_16x16x64_i8 v[204:207], v[76:79], v[220:223], v[204:207]\n"
        "ds_read_b128 v[212:215], v248 offset:25600\n"
        "s_waitcnt lgkmcnt(5)\n"
        "v_mfma_i32_16x16x64_i8 v[192:195], v[80:83], v[224:227], v[192:195]\n"
        "v_cvt_f32_i32 v157, v157\n"
        "v_cvt_f32_i32 v158, v158\n"
        "v_mfma_i32_16x16x64_i8 v[196:199], v[84:87], v[224:227], v[196:199]\n"
        "ds_read_b128 v[216:219], v248 offset:26624\n"
        "v_cvt_f32_i32 v159, v159\n"
        "v_mfma_i32_16x16x64_i8 v[200:203], v[80:83], v[228:231], v[200:203]\n"
        "v_fma_f32 v132, v132, v240, v241\n"
        "v_fma_f32 v133, v133, v240, v241\n"
        "v_mfma_i32_16x16x64_i8 v[204:207], v[84:87], v[228:231], v[204:207]\n"
        "ds_read_b128 v[220:223], v248 offset:27648\n"
        "v_fma_f32 v134, v134, v240, v241\n"
        "s_waitcnt lgkmcnt(5)\n"
        "v_mfma_i32_16x16x64_i8 v[192:195], v[88:91], v[232:235], v[192:195]\n"
        "v_fma_f32 v135, v135, v240, v241\n"
        "v_fma_f32 v140, v140, v242, v243\n"
        "v_mfma_i32_16x16x64_i8 v[196:199], v[92:95], v[232:235], v[196:199]\n"
        "ds_read_b128 v[224:227], v248 offset:28672\n"
        "v_fma_f32 v141, v141, v242, v243\n"
        "v_mfma_i32_16x16x64_i8 v[200:203], v[88:91], v[236:239], v[200:203]\n"
        "v_fma_f32 v142, v142, v242, v243\n"
        "v_fma_f32 v143, v143, v242, v243\n"
        "v_mfma_i32_16x16x64_i8 v[204:207], v[92:95], v[236:239], v[204:207]\n"
        "ds_read_b128 v[228:231], v248 offset:29696\n"
        "s_waitcnt lgkmcnt(4)\n"
        "v_mfma_i32_16x16x64_i8 v[192:195], v[96:99], v[208:211], v[192:195]\n"
        "v_fma_f32 v148, v148, v244, v245\n"
        "v_fma_f32 v149, v149, v244, v245\n"
        "v_fma_f32 v150, v150, v244, v245\n"
        "v_mfma_i32_16x16x64_i8 v[196:199], v[100:103], v[208:211], v[196:199]\n"
        "ds_read_b128 v[232:235], v248 offset:30720\n"
        "v_fma_f32 v151, v151, v244, v245\n"
        "v_mfma_i32_16x16x64_i8 v[200:203], v[96:99], v[212:215], v[200:203]\n"
        "v_fma_f32 v156, v156, v246, v247\n"
        "v_fma_f32 v157, v157, v246, v247\n"
        "v_mfma_i32_16x16x64_i8 v[204:207], v[100:103], v[212:215], v[204:207]\n"
        "ds_read_b128 v[236:239], v248 offset:31744\n"
        "v_cmp_gt_u32 vcc, 22, v254\n"
        "s_waitcnt lgkmcnt(4)\n"
        "v_mfma_i32_16x16x64_i8 v[192:195], v[104:107], v[216:219], v[192:195]\n"
        "s_cbranch_vccnz W8_s20\n"
        "W8_c20:\n"
        "v_fma_f32 v158, v158, v246, v247\n"
        "v_mfma_i32_16x16x64_i8 v[196:199], v[108:111], v[216:219], v[196:199]\n"
        "ds_read_b128 v[208:211], v248 offset:32768\n"
        "v_fma_f32 v159, v159, v246, v247\n"
        "v_cvt_pk_bf16_f32 v132, v132, v140\n"
        "v_mfma_i32_16x16x64_i8 v[200:203], v[104:107], v[220:223], v[200:203]\n"
        "v_cvt_pk_bf16_f32 v140, v133, v141\n"
        "v_cvt_pk_bf16_f32 v134, v134, v142\n"
        "v_mfma_i32_16x16x64_i8 v[204:207], v[108:111], v[220:223], v[204:207]\n"
        "ds_read_b128 v[212:215], v248 offset:33792\n"
        "s_waitcnt lgkmcnt(4)\n"
        "v_mfma_i32_16x16x64_i8 v[192:195], v[112:115], v[224:227], v[192:195]\n"
        "v_cvt_pk_bf16_f32 v142, v135, v143\n"
        "v_cvt_pk_bf16_f32 v133, v148, v156\n"
        "v_mfma_i32_16x16x64_i8 v[196:199], v[116:119], v[224:227], v[196:199]\n"
        "ds_read_b128 v[216:219], v248 offset:34816\n"
        "global_store_dwordx2 v251, v[132:133], s[48:49] offset:0 sc1\n"
        "v_mfma_i32_16x16x64_i8 v[200:203], v[112:115], v[228:231], v[200:203]\n"
        "v_cvt_pk_bf16_f32 v141, v149, v157\n"
        "global_store_dwordx2 v251, v[140:141], s[50:51] offset:0 sc1\n"
        "v_cvt_pk_bf16_f32 v135, v150, v158\n"
        "v_mfma_i32_16x16x64_i8 v[204:207], v[116:119], v[228:231], v[204:207]\n"
        "ds_read_b128 v[220:223], v248 offset:35840\n"
        "s_waitcnt lgkmcnt(4)\n"
        "v_mfma_i32_16x16x64_i8 v[192:195], v[120:123], v[232:235], v[192:195]\n"
        "global_store_dwordx2 v251, v[134:135], s[52:53] offset:0 sc1\n"
        "v_cvt_pk_bf16_f32 v143, v151, v159\n"
        "v_mfma_i32_16x16x64_i8 v[196:199], v[124:127], v[232:235], v[196:199]\n"
        "ds_read_b128 v[224:227], v248 offset:36864\n"
        "global_store_dwordx2 v251, v[142:143], s[54:55] offset:0 sc1\n"
        "v_mfma_i32_16x16x64_i8 v[200:203], v[120:123], v[236:239], v[200:203]\n"
        "ds_read_b128 v[240:243], v253 offset:512\n"
        "ds_read_b128 v[244:247], v253 offset:528\n"
        "v_mfma_i32_16x16x64_i8 v[204:207], v[124:127], v[236:239], v[204:207]\n"
        "ds_read_b128 v[228:231], v248 offset:37888\n"
        "v_cvt_f32_i32 v160, v160\n"
        "s_waitcnt lgkmcnt(6)\n"
        "v_mfma_i32_16x16x64_i8 v[128:131], v[0:3], v[208:211], 0\n"
        "v_cvt_f32_i32 v161, v161\n"
        "v_cvt_f32_i32 v162, v162\n"
        "v_mfma_i32_16x16x64_i8 v[132:135], v[4:7], v[208:211], 0\n"
        "ds_read_b128 v[232:235], v248 offset:38912\n"
        "v_cvt_f32_i32 v163, v163\n"
        "v_mfma_i32_16x16x64_i8 v[136:139], v[0:3], v[212:215], 0\n"
        "v_cvt_f32_i32 v168, v168\n"
        "v_cvt_f32_i32 v169, v169\n"
        "v_cvt_f32_i32 v170, v170\n"
        "v_mfma_i32_16x16x64_i8 v[140:143], v[4:7], v[212:215], 0\n"
        "ds_read_b128 v[236:239], v248 offset:39936\n"
        "s_waitcnt lgkmcnt(6)\n"
        "v_mfma_i32_16x16x64_i8 v[128:131], v[8:11], v[216:219], v[128:131]\n"
        "v_cvt_f32_i32 v171, v171\n"
        "v_cvt_f32_i32 v176, v176\n"
        "v_mfma_i32_16x16x64_i8 v[132:135], v[12:15], v[216:219], v[132:135]\n"
        "ds_read_b128 v[208:211], v248 offset:40960\n"
        "v_cvt_f32_i32 v177, v177\n"
        "v_mfma_i32_16x16x64_i8 v[136:139], v[8:11], v[220:223], v[136:139]\n"
        "ds_read_b32 v254, v250 offset:0\n"
        "v_cvt_f32_i32 v178, v178\n"
        "v_cvt_f32_i32 v179, v179\n"
        "v_mfma_i32_16x16x64_i8 v[140:143], v[12:15], v[220:223], v[140:143]\n"
        "ds_read_b128 v[212:215], v248 offset:41984\n"
        "s_waitcnt lgkmcnt(5)\n"
        "v_mfma_i32_16x16x64_i8 v[128:131], v[16:19], v[224:227], v[128:131]\n"
        "v_cvt_f32_i32 v184, v184\n"
        "v_cvt_f32_i32 v185, v185\n"
        "v_mfma_i32_16x16x64_i8 v[132:135], v[20:23], v[224:227], v[132:135]\n"
        "ds_read_b128 v[216:219], v248 offset:43008\n"
        "v_cvt_f32_i32 v186, v186\n"
        "v_mfma_i32_16x16x64_i8 v[136:139], v[16:19], v[228:231], v[136:139]\n"
        "v_cvt_f32_i32 v187, v187\n"
        "v_fma_f32 v160, v160, v240, v241\n"
        "v_mfma_i32_16x16x64_i8 v[140:143], v[20:23], v[228:231], v[140:143]\n"
        "ds_read_b128 v[220:223], v248 offset:44032\n"
        "s_waitcnt lgkmcnt(5)\n"
        "v_mfma_i32_16x16x64_i8 v[128:131], v[24:27], v[232:235], v[128:131]\n"
        "v_fma_f32 v161, v161, v240, v241\n"
        "v_fma_f32 v162, v162, v240, v241\n"
        "v_mfma_i32_16x16x64_i8 v[132:135], v[28:31], v[232:235], v[132:135]\n"
        "ds_read_b128 v[224:227], v248 offset:45056\n"
        "v_fma_f32 v163, v163, v240, v241\n"
        "v_mfma_i32_16x16x64_i8 v[136:139], v[24:27], v[236:239], v[136:139]\n"
        "v_fma_f32 v168, v168, v242, v243\n"
        "v_fma_f32 v169, v169, v242, v243\n"
        "v_fma_f32 v170, v170, v242, v243\n"
        "v_mfma_i32_16x16x64_i8 v[140:143], v[28:31], v[236:239], v[140:143]\n"
        "ds_read_b128 v[228:231], v248 offset:46080\n"
        "s_waitcnt lgkmcnt(4)\n"
        "v_mfma_i32_16x16x64_i8 v[128:131], v[32:35], v[208:211], v[128:131]\n"
        "v_fma_f32 v171, v171, v242, v243\n"
        "v_fma_f32 v176, v176, v244, v245\n"
        "v_mfma_i32_16x16x64_i8 v[132:135], v[36:39], v[208:211], v[132:135]\n"
        "ds_read_b128 v[232:235], v248 offset:47104\n"
        "v_fma_f32 v177, v177, v244, v245\n"
        "v_mfma_i32_16x16x64_i8 v[136:139], v[32:35], v[212:215], v[136:139]\n"
        "v_fma_f32 v178, v178, v244, v245\n"
        "v_fma_f32 v179, v179, v244, v245\n"
        "v_fma_f32 v184, v184, v246, v247\n"
        "v_mfma_i32_16x16x64_i8 v[140:143], v[36:39], v[212:215], v[140:143]\n"
        "ds_read_b128 v[236:239], v248 offset:48128\n"
        "v_cmp_gt_u32 vcc, 24, v254\n"
        "s_waitcnt lgkmcnt(4)\n"
        "v_mfma_i32_16x16x64_i8 v[128:131], v[40:43], v[216:219], v[128:131]\n"
        "s_cbranch_vccnz W8_s22\n"
        "W8_c22:\n"
        "v_fma_f32 v185, v185, v246, v247\n"
        "v_mfma_i32_16x16x64_i8 v[132:135], v[44:47], v[216:219], v[132:135]\n"
        "ds_read_b128 v[208:211], v248 offset:49152\n"
        "v_fma_f32 v186, v186, v246, v247\n"
        "v_mfma_i32_16x16x64_i8 v[136:139], v[40:43], v[220:223], v[136:139]\n"
        "v_fma_f32 v187, v187, v246, v247\n"
        "v_cvt_pk_bf16_f32 v160, v160, v168\n"
        "v_cvt_pk_bf16_f32 v168, v161, v169\n"
        "v_mfma_i32_16x16x64_i8 v[140:143], v[44:47], v[220:223], v[140:143]\n"
        "ds_read_b128 v[212:215], v248 offset:50176\n"
        "s_waitcnt lgkmcnt(4)\n"
        "v_mfma_i32_16x16x64_i8 v[128:131], v[48:51], v[224:227], v[128:131]\n"
        "v_cvt_pk_bf16_f32 v162, v162, v170\n"
        "v_cvt_pk_bf16_f32 v170, v163, v171\n"
        "v_mfma_i32_16x16x64_i8 v[132:135], v[52:55], v[224:227], v[132:135]\n"
        "ds_read_b128 v[216:219], v248 offset:51200\n"
        "v_cvt_pk_bf16_f32 v161, v176, v184\n"
        "v_mfma_i32_16x16x64_i8 v[136:139], v[48:51], v[228:231], v[136:139]\n"
        "global_store_dwordx2 v251, v[160:161], s[40:41] offset:128 sc1\n"
        "v_cvt_pk_bf16_f32 v169, v177, v185\n"
        "global_store_dwordx2 v251, v[168:169], s[42:43] offset:128 sc1\n"
        "v_mfma_i32_16x16x64_i8 v[140:143], v[52:55], v[228:231], v[140:143]\n"
        "ds_read_b128 v[220:223], v248 offset:52224\n"
        "s_waitcnt lgkmcnt(4)\n"
        "v_mfma_i32_16x16x64_i8 v[128:131], v[56:59], v[232:235], v[128:131]\n"
        "v_cvt_pk_bf16_f32 v163, v178, v186\n"
        "global_store_dwordx2 v251, v[162:163], s[44:45] offset:128 sc1\n"
        "v_mfma_i32_16x16x64_i8 v[132:135], v[60:63], v[232:235], v[132:135]\n"
        "ds_read_b128 v[224:227], v248 offset:53248\n"
        "v_cvt_pk_bf16_f32 v171, v179, v187\n"
        "v_mfma_i32_16x16x64_i8 v[136:139], v[56:59], v[236:239], v[136:139]\n"
        "global_store_dwordx2 v251, v[170:171], s[46:47] offset:128 sc1\n"
        "v_cvt_f32_i32 v164, v164\n"
        "v_cvt_f32_i32 v165, v165\n"
        "v_mfma_i32_16x16x64_i8 v[140:143], v[60:63], v[236:239], v[140:143]\n"
        "ds_read_b128 v[228:231], v248 offset:54272\n"
        "s_waitcnt lgkmcnt(4)\n"
        "v_mfma_i32_16x16x64_i8 v[128:131], v[64:67], v[208:211], v[128:131]\n"
        "v_cvt_f32_i32 v166, v166\n"
        "v_cvt_f32_i32 v167, v167\n"
        "v_mfma_i32_16x16x64_i8 v[132:135], v[68:71], v[208:211], v[132:135]\n"
        "ds_read_b128 v[232:235], v248 offset:55296\n"
        "v_cvt_f32_i32 v172, v172\n"
        "v_mfma_i32_16x16x64_i8 v[136:139], v[64:67], v[212:215], v[136:139]\n"
        "v_cvt_f32_i32 v173, v173\n"
        "v_cvt_f32_i32 v174, v174\n"
        "v_cvt_f32_i32 v175, v175\n"
        "v_mfma_i32_16x16x64_i8 v[140:143], v[68:71], v[212:215], v[140:143]\n"
        "ds_read_b128 v[236:239], v248 offset:56320\n"
        "s_waitcnt lgkmcnt(4)\n"
        "v_mfma_i32_16x16x64_i8 v[128:131], v[72:75], v[216:219], v[128:131]\n"
        "v_cvt_f32_i32 v180, v180\n"
        "v_cvt_f32_i32 v181, v181\n"
        "v_mfma_i32_16x16x64_i8 v[132:135], v[76:79], v[216:219], v[132:135]\n"
        "ds_read_b128 v[208:211], v248 offset:57344\n"
        "v_cvt_f32_i32 v182, v182\n"
        "v_mfma_i32_16x16x64_i8 v[136:139], v[72:75], v[220:223], v[136:139]\n"
        "ds_read_b32 v254, v250 offset:0\n"
        "v_cvt_f32_i32 v183, v183\n"
        "v_cvt_f32_i32 v188, v188\n"
        "v_mfma_i32_16x16x64_i8 v[140:143], v[76:79], v[220:223], v[140:143]\n"
        "ds_read_b128 v[212:215], v248 offset:58368\n"
        "s_waitcnt lgkmcnt(5)\n"
        "v_mfma_i32_16x16x64_i8 v[128:131], v[80:83], v[224:227], v[128:131]\n"
        "v_cvt_f32_i32 v189, v189\n"
        "v_cvt_f32_i32 v190, v190\n"
        "v_mfma_i32_16x16x64_i8 v[132:135], v[84:87], v[224:227], v[132:135]\n"
        "ds_read_b128 v[216:219], v248 offset:59392\n"
        "v_cvt_f32_i32 v191, v191\n"
        "v_mfma_i32_16x16x64_i8 v[136:139], v[80:83], v[228:231], v[136:139]\n"
        "v_fma_f32 v164, v164, v240, v241\n"
        "v_fma_f32 v165, v165, v240, v241\n"
        "v_fma_f32 v166, v166, v240, v241\n"
        "v_mfma_i32_16x16x64_i8 v[140:143], v[84:87], v[228:231], v[140:143]\n"
        "ds_read_b128 v[220:223], v248 offset:60416\n"
        "s_waitcnt lgkmcnt(5)\n"
        "v_mfma_i32_16x16x64_i8 v[128:131], v[88:91], v[232:235], v[128:131]\n"
        "v_fma_f32 v167, v167, v240, v241\n"
        "v_fma_f32 v172, v172, v242, v243\n"
        "v_mfma_i32_16x16x64_i8 v[132:135], v[92:95], v[232:235], v[132:135]\n"
        "ds_read_b128 v[224:227], v248 offset:61440\n"
        "v_fma_f32 v173, v173, v242, v243\n"
        "v_mfma_i32_16x16x64_i8 v[136:139], v[88:91], v[236:239], v[136:139]\n"
        "v_fma_f32 v174, v174, v242, v243\n"
        "v_fma_f32 v175, v175, v242, v243\n"
        "v_fma_f32 v180, v180, v244, v245\n"
        "v_mfma_i32_16x16x64_i8 v[140:143], v[92:95], v[236:239], v[140:143]\n"
        "ds_read_b128 v[228:231], v248 offset:62464\n"
        "s_waitcnt lgkmcnt(4)\n"
        "v_mfma_i32_16x16x64_i8 v[128:131], v[96:99], v[208:211], v[128:131]\n"
        "v_fma_f32 v181, v181, v244, v245\n"
        "v_fma_f32 v182, v182, v244, v245\n"
        "v_mfma_i32_16x16x64_i8 v[132:135], v[100:103], v[208:211], v[132:135]\n"
        "ds_read_b128 v[232:235], v248 offset:63488\n"
        "v_fma_f32 v183, v183, v244, v245\n"
        "v_mfma_i32_16x16x64_i8 v[136:139], v[96:99], v[212:215], v[136:139]\n"
        "v_fma_f32 v188, v188, v246, v247\n"
        "v_fma_f32 v189, v189, v246, v247\n"
        "v_fma_f32 v190, v190, v246, v247\n"
        "v_mfma_i32_16x16x64_i8 v[140:143], v[100:103], v[212:215], v[140:143]\n"
        "ds_read_b128 v[236:239], v248 offset:64512\n"
        "v_cmp_gt_u32 vcc, 26, v254\n"
        "s_waitcnt lgkmcnt(4)\n"
        "v_mfma_i32_16x16x64_i8 v[128:131], v[104:107], v[216:219], v[128:131]\n"
        "s_cbranch_vccnz W8_s24\n"
        "W8_c24:\n"
        "v_fma_f32 v191, v191, v246, v247\n"
        "v_mfma_i32_16x16x64_i8 v[132:135], v[108:111], v[216:219], v[132:135]\n"
        "ds_read_b128 v[208:211], v249 offset:0\n"
        "v_cvt_pk_bf16_f32 v164, v164, v172\n"
        "v_mfma_i32_16x16x64_i8 v[136:139], v[104:107], v[220:223], v[136:139]\n"
        "v_cvt_pk_bf16_f32 v172, v165, v173\n"
        "v_cvt_pk_bf16_f32 v166, v166, v174\n"
        "v_cvt_pk_bf16_f32 v174, v167, v175\n"
        "v_mfma_i32_16x16x64_i8 v[140:143], v[108:111], v[220:223], v[140:143]\n"
        "ds_read_b128 v[212:215], v249 offset:1024\n"
        "s_waitcnt lgkmcnt(4)\n"
        "v_mfma_i32_16x16x64_i8 v[128:131], v[112:115], v[224:227], v[128:131]\n"
        "v_cvt_pk_bf16_f32 v165, v180, v188\n"
        "global_store_dwordx2 v251, v[164:165], s[48:49] offset:128 sc1\n"
        "v_mfma_i32_16x16x64_i8 v[132:135], v[116:119], v[224:227], v[132:135]\n"
        "ds_read_b128 v[216:219], v249 offset:2048\n"
        "v_cvt_pk_bf16_f32 v173, v181, v189\n"
        "v_mfma_i32_16x16x64_i8 v[136:139], v[112:115], v[228:231], v[136:139]\n"
        "global_store_dwordx2 v251, v[172:173], s[50:51] offset:128 sc1\n"
        "v_cvt_pk_bf16_f32 v167, v182, v190\n"
        "global_store_dwordx2 v251, v[166:167], s[52:53] offset:128 sc1\n"
        "v_mfma_i32_16x16x64_i8 v[140:143], v[116:119], v[228:231], v[140:143]\n"
        "ds_read_b128 v[220:223], v249 offset:3072\n"
        "s_waitcnt lgkmcnt(4)\n"
        "v_mfma_i32_16x16x64_i8 v[128:131], v[120:123], v[232:235], v[128:131]\n"
        "v_cvt_pk_bf16_f32 v175, v183, v191\n"
        "global_store_dwordx2 v251, v[174:175], s[54:55] offset:128 sc1\n"
        "v_mfma_i32_16x16x64_i8 v[132:135], v[124:127], v[232:235], v[132:135]\n"
        "ds_read_b128 v[224:227], v249 offset:4096\n"
        "v_add_u32 v251, s37, v251\n"
        "v_mfma_i32_16x16x64_i8 v[136:139], v[120:123], v[236:239], v[136:139]\n"
        "v_mfma_i32_16x16x64_i8 v[140:143], v[124:127], v[236:239], v[140:143]\n"
        "ds_read_b128 v[228:231], v249 offset:5120\n"
        "v_and_b32 v255, 0xfffffeff, v253\n"
        "s_waitcnt lgkmcnt(4)\n"
        "v_mfma_i32_16x16x64_i8 v[144:147], v[0:3], v[208:211], 0\n"
        "v_bfe_u32 v240, v253, 8, 1\n"
        "v_cvt_f32_i32 v192, v192\n"
        "v_mfma_i32_16x16x64_i8 v[148:151], v[4:7], v[208:211], 0\n"
        "ds_read_b128 v[232:235], v249 offset:6144\n"
        "v_lshl_add_u32 v255, v240, 4, v255\n"
        "v_mfma_i32_16x16x64_i8 v[152:155], v[0:3], v[212:215], 0\n"
        "ds_read_b128 v[244:247], v255 offset:1536\n"
        "ds_read_b128 v[240:243], v253 offset:1024\n"
        "v_mfma_i32_16x16x64_i8 v[156:159], v[4:7], v[212:215], 0\n"
        "ds_read_b128 v[236:239], v249 offset:7168\n"
        "s_waitcnt lgkmcnt(6)\n"
        "v_mfma_i32_16x16x64_i8 v[144:147], v[8:11], v[216:219], v[144:147]\n"
        "v_cvt_f32_i32 v193, v193\n"
        "v_cvt_f32_i32 v194, v194\n"
        "v_mfma_i32_16x16x64_i8 v[148:151], v[12:15], v[216:219], v[148:151]\n"
        "ds_read_b128 v[208:211], v249 offset:8192\n"
        "v_cvt_f32_i32 v195, v195\n"
        "v_cvt_f32_i32 v200, v200\n"
        "v_mfma_i32_16x16x64_i8 v[152:155], v[8:11], v[220:223], v[152:155]\n"
        "ds_read_b32 v254, v250 offset:0\n"
        "v_cvt_f32_i32 v201, v201\n"
        "v_mfma_i32_16x16x64_i8 v[156:159], v[12:15], v[220:223], v[156:159]\n"
        "ds_read_b128 v[212:215], v249 offset:9216\n"
        "s_waitcnt lgkmcnt(7)\n"
        "v_mfma_i32_16x16x64_i8 v[144:147], v[16:19], v[224:227], v[144:147]\n"
        "v_cvt_f32_i32 v202, v202\n"
        "v_cvt_f32_i32 v203, v203\n"
        "v_mfma_i32_16x16x64_i8 v[148:151], v[20:23], v[224:227], v[148:151]\n"
        "ds_read_b128 v[216:219], v249 offset:10240\n"
        "v_cvt_f32_i32 v196, v196\n"
        "v_mfma_i32_16x16x64_i8 v[152:155], v[16:19], v[228:231], v[152:155]\n"
        "v_cvt_f32_i32 v197, v197\n"
        "v_cvt_f32_i32 v198, v198\n"
        "v_mfma_i32_16x16x64_i8 v[156:159], v[20:23], v[228:231], v[156:159]\n"
        "ds_read_b128 v[220:223], v249 offset:11264\n"
        "s_waitcnt lgkmcnt(5)\n"
        "v_mfma_i32_16x16x64_i8 v[144:147], v[24:27], v[232:235], v[144:147]\n"
        "v_cvt_f32_i32 v199, v199\n"
        "v_cvt_f32_i32 v204, v204\n"
        "v_cvt_f32_i32 v205, v205\n"
        "v_mfma_i32_16x16x64_i8 v[148:151], v[28:31], v[232:235], v[148:151]\n"
        "ds_read_b128 v[224:227], v249 offset:12288\n"
        "v_cvt_f32_i32 v206, v206\n"
        "v_mfma_i32_16x16x64_i8 v[152:155], v[24:27], v[236:239], v[152:155]\n"
        "v_cvt_f32_i32 v207, v207\n"
        "v_mfma_i32_16x16x64_i8 v[156:159], v[28:31], v[236:239], v[156:159]\n"
        "ds_read_b128 v[228:231], v249 offset:13312\n"
        "s_waitcnt lgkmcnt(4)\n"
        "v_mfma_i32_16x16x64_i8 v[144:147], v[32:35], v[208:211], v[144:147]\n"
        "v_fma_f32 v192, v192, v244, v245\n"
        "v_fma_f32 v193, v193, v244, v245\n"
        "v_mfma_i32_16x16x64_i8 v[148:151], v[36:39], v[208:211], v[148:151]\n"
        "ds_read_b128 v[232:235], v249 offset:14336\n"
        "v_fma_f32 v194, v194, v244, v245\n"
        "v_mfma_i32_16x16x64_i8 v[152:155], v[32:35], v[212:215], v[152:155]\n"
        "v_fma_f32 v195, v195, v244, v245\n"
        "v_fma_f32 v200, v200, v246, v247\n"
        "v_fma_f32 v201, v201, v246, v247\n"
        "v_mfma_i32_16x16x64_i8 v[156:159], v[36:39], v[212:215], v[156:159]\n"
        "ds_read_b128 v[236:239], v249 offset:15360\n"
        "v_cmp_gt_u32 vcc, 28, v254\n"
        "s_waitcnt lgkmcnt(4)\n"
        "v_mfma_i32_16x16x64_i8 v[144:147], v[40:43], v[216:219], v[144:147]\n"
        "s_cbranch_vccnz W8_s26\n"
        "W8_c26:\n"
        "v_fma_f32 v202, v202, v246, v247\n"
        "v_mfma_i32_16x16x64_i8 v[148:151], v[44:47], v[216:219], v[148:151]\n"
        "ds_read_b128 v[208:211], v249 offset:16384\n"
        "v_fma_f32 v203, v203, v246, v247\n"
        "v_mfma_i32_16x16x64_i8 v[152:155], v[40:43], v[220:223], v[152:155]\n"
        "v_cvt_pk_bf16_f32 v192, v192, v200\n"
        "v_cvt_pk_bf16_f32 v200, v193, v201\n"
        "v_mfma_i32_16x16x64_i8 v[156:159], v[44:47], v[220:223], v[156:159]\n"
        "ds_read_b128 v[212:215], v249 offset:17408\n"
        "s_waitcnt lgkmcnt(4)\n"
        "v_mfma_i32_16x16x64_i8 v[144:147], v[48:51], v[224:227], v[144:147]\n"
        "v_cvt_pk_bf16_f32 v194, v194, v202\n"
        "v_cvt_pk_bf16_f32 v202, v195, v203\n"
        "v_mfma_i32_16x16x64_i8 v[148:151], v[52:55], v[224:227], v[148:151]\n"
        "ds_read_b128 v[216:219], v249 offset:18432\n"
        "global_store_dword v252, v192, s[40:41] sc1\n"
        "global_store_dword v252, v200, s[42:43] sc1\n"
        "v_mfma_i32_16x16x64_i8 v[152:155], v[48:51], v[228:231], v[152:155]\n"
        "global_store_dword v252, v194, s[44:45] sc1\n"
        "global_store_dword v252, v202, s[46:47] sc1\n"
        "v_mfma_i32_16x16x64_i8 v[156:159], v[52:55], v[228:231], v[156:159]\n"
        "ds_read_b128 v[220:223], v249 offset:19456\n"
        "s_waitcnt lgkmcnt(4)\n"
        "v_mfma_i32_16x16x64_i8 v[144:147], v[56:59], v[232:235], v[144:147]\n"
        "v_fma_f32 v196, v196, v244, v245\n"
        "v_fma_f32 v197, v197, v244, v245\n"
        "v_mfma_i32_16x16x64_i8 v[148:151], v[60:63], v[232:235], v[148:151]\n"
        "ds_read_b128 v[224:227], v249 offset:20480\n"
        "v_fma_f32 v198, v198, v244, v245\n"
        "v_mfma_i32_16x16x64_i8 v[152:155], v[56:59], v[236:239], v[152:155]\n"
        "v_fma_f32 v199, v199, v244, v245\n"
        "v_fma_f32 v204, v204, v246, v247\n"
        "v_mfma_i32_16x16x64_i8 v[156:159], v[60:63], v[236:239], v[156:159]\n"
        "ds_read_b128 v[228:231], v249 offset:21504\n"
        "v_fma_f32 v205, v205, v246, v247\n"
        "s_waitcnt lgkmcnt(4)\n"
        "v_mfma_i32_16x16x64_i8 v[144:147], v[64:67], v[208:211], v[144:147]\n"
        "v_fma_f32 v206, v206, v246, v247\n"
        "v_fma_f32 v207, v207, v246, v247\n"
        "v_mfma_i32_16x16x64_i8 v[148:151], v[68:71], v[208:211], v[148:151]\n"
        "ds_read_b128 v[232:235], v249 offset:22528\n"
        "v_cvt_pk_bf16_f32 v196, v196, v204\n"
        "v_mfma_i32_16x16x64_i8 v[152:155], v[64:67], v[212:215], v[152:155]\n"
        "v_cvt_pk_bf16_f32 v204, v197, v205\n"
        "v_cvt_pk_bf16_f32 v198, v198, v206\n"
        "v_mfma_i32_16x16x64_i8 v[156:159], v[68:71], v[212:215], v[156:159]\n"
        "ds_read_b128 v[236:239], v249 offset:23552\n"
        "s_waitcnt lgkmcnt(4)\n"
        "v_mfma_i32_16x16x64_i8 v[144:147], v[72:75], v[216:219], v[144:147]\n"
        "v_cvt_pk_bf16_f32 v206, v199, v207\n"
        "global_store_dword v252, v196, s[48:49] sc1\n"
        "v_mfma_i32_16x16x64_i8 v[148:151], v[76:79], v[216:219], v[148:151]\n"
        "ds_read_b128 v[208:211], v249 offset:24576\n"
        "global_store_dword v252, v204, s[50:51] sc1\n"
        "v_mfma_i32_16x16x64_i8 v[152:155], v[72:75], v[220:223], v[152:155]\n"
        "global_store_dword v252, v198, s[52:53] sc1\n"
        "global_store_dword v252, v206, s[54:55] sc1\n"
        "ds_read_b128 v[244:247], v253 offset:1040\n"
        "v_mfma_i32_16x16x64_i8 v[156:159], v[76:79], v[220:223], v[156:159]\n"
        "ds_read_b128 v[212:215], v249 offset:25600\n"
        "s_waitcnt lgkmcnt(5)\n"
        "v_mfma_i32_16x16x64_i8 v[144:147], v[80:83], v[224:227], v[144:147]\n"
        "v_cvt_f32_i32 v128, v128\n"
        "v_cvt_f32_i32 v129, v129\n"
        "v_mfma_i32_16x16x64_i8 v[148:151], v[84:87], v[224:227], v[148:151]\n"
        "ds_read_b128 v[216:219], v249 offset:26624\n"
        "v_cvt_f32_i32 v130, v130\n"
        "v_mfma_i32_16x16x64_i8 v[152:155], v[80:83], v[228:231], v[152:155]\n"
        "v_cvt_f32_i32 v131, v131\n"
        "v_cvt_f32_i32 v136, v136\n"
        "v_mfma_i32_16x16x64_i8 v[156:159], v[84:87], v[228:231], v[156:159]\n"
        "ds_read_b128 v[220:223], v249 offset:27648\n"
        "s_waitcnt lgkmcnt(5)\n"
        "v_mfma_i32_16x16x64_i8 v[144:147], v[88:91], v[232:235], v[144:147]\n"
        "v_cvt_f32_i32 v137, v137\n"
        "v_cvt_f32_i32 v138, v138\n"
        "v_cvt_f32_i32 v139, v139\n"
        "v_mfma_i32_16x16x64_i8 v[148:151], v[92:95], v[232:235], v[148:151]\n"
        "ds_read_b128 v[224:227], v249 offset:28672\n"
        "v_fma_f32 v128, v128, v240, v241\n"
        "v_mfma_i32_16x16x64_i8 v[152:155], v[88:91], v[236:239], v[152:155]\n"
        "v_fma_f32 v129, v129, v240, v241\n"
        "v_fma_f32 v130, v130, v240, v241\n"
        "v_mfma_i32_16x16x64_i8 v[156:159], v[92:95], v[236:239], v[156:159]\n"
        "ds_read_b128 v[228:231], v249 offset:29696\n"
        "s_waitcnt lgkmcnt(4)\n"
        "v_mfma_i32_16x16x64_i8 v[144:147], v[96:99], v[208:211], v[144:147]\n"
        "v_fma_f32 v131, v131, v240, v241\n"
        "v_fma_f32 v136, v136, v242, v243\n"
        "v_mfma_i32_16x16x64_i8 v[148:151], v[100:103], v[208:211], v[148:151]\n"
        "ds_read_b128 v[232:235], v249 offset:30720\n"
        "v_fma_f32 v137, v137, v242, v243\n"
        "v_mfma_i32_16x16x64_i8 v[152:155], v[96:99], v[212:215], v[152:155]\n"
        "v_fma_f32 v138, v138, v242, v243\n"
        "v_fma_f32 v139, v139, v242, v243\n"
        "v_mfma_i32_16x16x64_i8 v[156:159], v[100:103], v[212:215], v[156:159]\n"
        "ds_read_b128 v[236:239], v249 offset:31744\n"
        "v_cvt_pk_bf16_f32 v128, v128, v136\n"
        "s_waitcnt lgkmcnt(4)\n"
        "v_mfma_i32_16x16x64_i8 v[144:147], v[104:107], v[216:219], v[144:147]\n"
        "v_cvt_pk_bf16_f32 v136, v129, v137\n"
        "v_cvt_pk_bf16_f32 v130, v130, v138\n"
        "v_mfma_i32_16x16x64_i8 v[148:151], v[108:111], v[216:219], v[148:151]\n"
        "v_cvt_pk_bf16_f32 v138, v131, v139\n"
        "v_cvt_f32_i32 v132, v132\n"
        "v_mfma_i32_16x16x64_i8 v[152:155], v[104:107], v[220:223], v[152:155]\n"
        "v_cvt_f32_i32 v133, v133\n"
        "v_cvt_f32_i32 v134, v134\n"
        "v_mfma_i32_16x16x64_i8 v[156:159], v[108:111], v[220:223], v[156:159]\n"
        "v_cvt_f32_i32 v135, v135\n"
        "s_waitcnt lgkmcnt(2)\n"
        "v_mfma_i32_16x16x64_i8 v[144:147], v[112:115], v[224:227], v[144:147]\n"
        "v_cvt_f32_i32 v140, v140\n"
        "v_cvt_f32_i32 v141, v141\n"
        "v_mfma_i32_16x16x64_i8 v[148:151], v[116:119], v[224:227], v[148:151]\n"
        "v_cvt_f32_i32 v142, v142\n"
        "v_cvt_f32_i32 v143, v143\n"
        "v_fma_f32 v132, v132, v240, v241\n"
        "v_mfma_i32_16x16x64_i8 v[152:155], v[112:115], v[228:231], v[152:155]\n"
        "v_fma_f32 v133, v133, v240, v241\n"
        "v_fma_f32 v134, v134, v240, v241\n"
        "v_mfma_i32_16x16x64_i8 v[156:159], v[116:119], v[228:231], v[156:159]\n"
        "v_fma_f32 v135, v135, v240, v241\n"
        "s_waitcnt lgkmcnt(0)\n"
        "v_mfma_i32_16x16x64_i8 v[144:147], v[120:123], v[232:235], v[144:147]\n"
        "v_fma_f32 v140, v140, v242, v243\n"
        "v_fma_f32 v141, v141, v242, v243\n"
        "v_mfma_i32_16x16x64_i8 v[148:151], v[124:127], v[232:235], v[148:151]\n"
        "v_fma_f32 v142, v142, v242, v243\n"
        "v_fma_f32 v143, v143, v242, v243\n"
        "v_mfma_i32_16x16x64_i8 v[152:155], v[120:123], v[236:239], v[152:155]\n"
        "v_cvt_pk_bf16_f32 v132, v132, v140\n"
        "v_cvt_pk_bf16_f32 v140, v133, v141\n"
        "v_mfma_i32_16x16x64_i8 v[156:159], v[124:127], v[236:239], v[156:159]\n"
        "v_cvt_pk_bf16_f32 v134, v134, v142\n"
        "v_cvt_pk_bf16_f32 v142, v135, v143\n"
        "s_nop 5\n"
        "v_cvt_f32_i32 v144, v144\n"
        "v_cvt_f32_i32 v145, v145\n"
        "v_cvt_f32_i32 v146, v146\n"
        "v_cvt_f32_i32 v147, v147\n"
        "v_cvt_f32_i32 v152, v152\n"
        "v_cvt_f32_i32 v153, v153\n"
        "v_cvt_f32_i32 v154, v154\n"
        "v_cvt_f32_i32 v155, v155\n"
        "v_fma_f32 v144, v144, v244, v245\n"
        "v_fma_f32 v145, v145, v244, v245\n"
        "v_fma_f32 v146, v146, v244, v245\n"
        "v_fma_f32 v147, v147, v244, v245\n"
        "v_fma_f32 v152, v152, v246, v247\n"
        "v_fma_f32 v153, v153, v246, v247\n"
        "v_fma_f32 v154, v154, v246, v247\n"
        "v_fma_f32 v155, v155, v246, v247\n"
        "v_cvt_pk_bf16_f32 v129, v144, v152\n"
        "global_store_dwordx2 v251, v[128:129], s[40:41] offset:256 sc1\n"
        "v_cvt_pk_bf16_f32 v137, v145, v153\n"
        "global_store_dwordx2 v251, v[136:137], s[42:43] offset:256 sc1\n"
        "v_cvt_pk_bf16_f32 v131, v146, v154\n"
        "global_store_dwordx2 v251, v[130:131], s[44:45] offset:256 sc1\n"
        "v_cvt_pk_bf16_f32 v139, v147, v155\n"
        "global_store_dwordx2 v251, v[138:139], s[46:47] offset:256 sc1\n"
        "v_cvt_f32_i32 v148, v148\n"
        "v_cvt_f32_i32 v149, v149\n"
        "v_cvt_f32_i32 v150, v150\n"
        "v_cvt_f32_i32 v151, v151\n"
        "v_cvt_f32_i32 v156, v156\n"
        "v_cvt_f32_i32 v157, v157\n"
        "v_cvt_f32_i32 v158, v158\n"
        "v_cvt_f32_i32 v159, v159\n"
        "v_fma_f32 v148, v148, v244, v245\n"
        "v_fma_f32 v149, v149, v244, v245\n"
        "v_fma_f32 v150, v150, v244, v245\n"
        "v_fma_f32 v151, v151, v244, v245\n"
        "v_fma_f32 v156, v156, v246, v247\n"
        "v_fma_f32 v157, v157, v246, v247\n"
        "v_fma_f32 v158, v158, v246, v247\n"
        "v_fma_f32 v159, v159, v246, v247\n"
        "v_cvt_pk_bf16_f32 v133, v148, v156\n"
        "global_store_dwordx2 v251, v[132:133], s[48:49] offset:256 sc1\n"
        "v_cvt_pk_bf16_f32 v141, v149, v157\n"
        "global_store_dwordx2 v251, v[140:141], s[50:51] offset:256 sc1\n"
        "v_cvt_pk_bf16_f32 v135, v150, v158\n"
        "global_store_dwordx2 v251, v[134:135], s[52:53] offset:256 sc1\n"
        "v_cvt_pk_bf16_f32 v143, v151, v159\n"
        "global_store_dwordx2 v251, v[142:143], s[54:55] offset:256 sc1\n"
        "W8_fin:\n"
        "s_endpgm\n"
        "W8_s0:\n"
        "s_mov_b32 s38, 8388608\n"
        "W8_r0:\n"
        "ds_read_b32 v254, v250 offset:0\n"
        "s_sub_u32 s38, s38, 1\n"
        "s_waitcnt lgkmcnt(0)\n"
        "s_cbranch_scc1 W8_c0\n"
        "v_readfirstlane_b32 s39, v254\n"
        "s_cmp_lt_u32 s39, 1\n"
        "s_cbranch_scc1 W8_r0\n"
        "s_branch W8_c0\n"
        "W8_sZ:\n"
        "s_mov_b32 s38, 8388608\n"
        "W8_rZ:\n"
        "ds_read_b32 v241, v250 offset:4\n"
        "s_sub_u32 s38, s38, 1\n"
        "s_waitcnt lgkmcnt(0)\n"
        "s_cbranch_scc1 W8_cZ\n"
        "v_readfirstlane_b32 s39, v241\n"
        "s_cmp_lt_u32 s39, 4\n"
        "s_cbranch_scc1 W8_rZ\n"
        "s_branch W8_cZ\n"
        "W8_s1:\n"
        "s_mov_b32 s38, 8388608\n"
        "W8_r1:\n"
        "ds_read_b32 v254, v250 offset:0\n"
        "s_sub_u32 s38, s38, 1\n"
        "s_waitcnt lgkmcnt(0)\n"
        "s_cbranch_scc1 W8_c1\n"
        "v_readfirstlane_b32 s39, v254\n"
        "s_cmp_lt_u32 s39, 2\n"
        "s_cbranch_scc1 W8_r1\n"
        "s_branch W8_c1\n"
        "W8_sR0:\n"
        "s_mov_b32 s38, 8388608\n"
        "W8_rR0:\n"
        "ds_read_b32 v241, v250 offset:4\n"
        "s_sub_u32 s38, s38, 1\n"
        "s_waitcnt lgkmcnt(0)\n"
        "s_cbranch_scc1 W8_cR0\n"
        "v_readfirstlane_b32 s39, v241\n"
        "s_cmp_lt_u32 s39, 5\n"
        "s_cbranch_scc1 W8_rR0\n"
        "s_branch W8_cR0\n"
        "W8_s2:\n"
        "s_mov_b32 s38, 8388608\n"
        "W8_r2:\n"
        "ds_read_b32 v254, v250 offset:0\n"
        "s_sub_u32 s38, s38, 1\n"
        "s_waitcnt lgkmcnt(0)\n"
        "s_cbranch_scc1 W8_c2\n"
        "v_readfirstlane_b32 s39, v254\n"
        "s_cmp_lt_u32 s39, 3\n"
        "s_cbranch_scc1 W8_r2\n"
        "s_branch W8_c2\n"
        "W8_sR1:\n"
        "s_mov_b32 s38, 8388608\n"
        "W8_rR1:\n"
        "ds_read_b32 v241, v250 offset:4\n"
        "s_sub_u32 s38, s38, 1\n"
        "s_waitcnt lgkmcnt(0)\n"
        "s_cbranch_scc1 W8_cR1\n"
        "v_readfirstlane_b32 s39, v241\n"
        "s_cmp_lt_u32 s39, 6\n"
        "s_cbranch_scc1 W8_rR1\n"
        "s_branch W8_cR1\n"
        "W8_s3:\n"
        "s_mov_b32 s38, 8388608\n"
        "W8_r3:\n"
        "ds_read_b32 v254, v250 offset:0\n"
        "s_sub_u32 s38, s38, 1\n"
        "s_waitcnt lgkmcnt(0)\n"
        "s_cbranch_scc1 W8_c3\n"
        "v_readfirstlane_b32 s39, v254\n"
        "s_cmp_lt_u32 s39, 4\n"
        "s_cbranch_scc1 W8_r3\n"
        "s_branch W8_c3\n"
        "W8_sR2:\n"
        "s_mov_b32 s38, 8388608\n"
        "W8_rR2:\n"
        "ds_read_b32 v241, v250 offset:4\n"
        "s_sub_u32 s38, s38, 1\n"
        "s_waitcnt lgkmcnt(0)\n"
        "s_cbranch_scc1 W8_cR2\n"
        "v_readfirstlane_b32 s39, v241\n"
        "s_cmp_lt_u32 s39, 7\n"
        "s_cbranch_scc1 W8_rR2\n"
        "s_branch W8_cR2\n"
        "W8_s4:\n"
        "s_mov_b32 s38, 8388608\n"
        "W8_r4:\n"
        "ds_read_b32 v254, v250 offset:0\n"
        "s_sub_u32 s38, s38, 1\n"
        "s_waitcnt lgkmcnt(0)\n"
        "s_cbranch_scc1 W8_c4\n"
        "v_readfirstlane_b32 s39, v254\n"
        "s_cmp_lt_u32 s39, 5\n"
        "s_cbranch_scc1 W8_r4\n"
        "s_branch W8_c4\n"
        "W8_sR3:\n"
        "s_mov_b32 s38, 8388608\n"
        "W8_rR3:\n"
        "ds_read_b32 v241, v250 offset:4\n"
        "s_sub_u32 s38, s38, 1\n"
        "s_waitcnt lgkmcnt(0)\n"
        "s_cbranch_scc1 W8_cR3\n"
        "v_readfirstlane_b32 s39, v241\n"
        "s_cmp_lt_u32 s39, 8\n"
        "s_cbranch_scc1 W8_rR3\n"
        "s_branch W8_cR3\n"
        "W8_s5:\n"
        "s_mov_b32 s38, 8388608\n"
        "W8_r5:\n"
        "ds_read_b32 v254, v250 offset:0\n"
        "s_sub_u32 s38, s38, 1\n"
        "s_waitcnt lgkmcnt(0)\n"
        "s_cbranch_scc1 W8_c5\n"
        "v_readfirstlane_b32 s39, v254\n"
        "s_cmp_lt_u32 s39, 6\n"
        "s_cbranch_scc1 W8_r5\n"
        "s_branch W8_c5\n"
        "W8_sR4:\n"
        "s_mov_b32 s38, 8388608\n"
        "W8_rR4:\n"
        "ds_read_b32 v241, v250 offset:4\n"
        "s_sub_u32 s38, s38, 1\n"
        "s_waitcnt lgkmcnt(0)\n"
        "s_cbranch_scc1 W8_cR4\n"
        "v_readfirstlane_b32 s39, v241\n"
        "s_cmp_lt_u32 s39, 9\n"
        "s_cbranch_scc1 W8_rR4\n"
        "s_branch W8_cR4\n"
        "W8_s6:\n"
        "s_mov_b32 s38, 8388608\n"
        "W8_r6:\n"
        "ds_read_b32 v254, v250 offset:0\n"
        "s_sub_u32 s38, s38, 1\n"
        "s_waitcnt lgkmcnt(0)\n"
        "s_cbranch_scc1 W8_c6\n"
        "v_readfirstlane_b32 s39, v254\n"
        "s_cmp_lt_u32 s39, 7\n"
        "s_cbranch_scc1 W8_r6\n"
        "s_branch W8_c6\n"
        "W8_sR5:\n"
        "s_mov_b32 s38, 8388608\n"
        "W8_rR5:\n"
        "ds_read_b32 v241, v250 offset:4\n"
        "s_sub_u32 s38, s38, 1\n"
        "s_waitcnt lgkmcnt(0)\n"
        "s_cbranch_scc1 W8_cR5\n"
        "v_readfirstlane_b32 s39, v241\n"
        "s_cmp_lt_u32 s39, 10\n"
        "s_cbranch_scc1 W8_rR5\n"
        "s_branch W8_cR5\n"
        "W8_s7:\n"
        "s_mov_b32 s38, 8388608\n"
        "W8_r7:\n"
        "ds_read_b32 v254, v250 offset:0\n"
        "s_sub_u32 s38, s38, 1\n"
        "s_waitcnt lgkmcnt(0)\n"
        "s_cbranch_scc1 W8_c7\n"
        "v_readfirstlane_b32 s39, v254\n"
        "s_cmp_lt_u32 s39, 8\n"
        "s_cbranch_scc1 W8_r7\n"
        "s_branch W8_c7\n"
        "W8_sR6:\n"
        "s_mov_b32 s38, 8388608\n"
        "W8_rR6:\n"
        "ds_read_b32 v241, v250 offset:4\n"
        "s_sub_u32 s38, s38, 1\n"
        "s_waitcnt lgkmcnt(0)\n"
        "s_cbranch_scc1 W8_cR6\n"
        "v_readfirstlane_b32 s39, v241\n"
        "s_cmp_lt_u32 s39, 11\n"
        "s_cbranch_scc1 W8_rR6\n"
        "s_branch W8_cR6\n"
        "W8_s8:\n"
        "s_mov_b32 s38, 8388608\n"
        "W8_r8:\n"
        "ds_read_b32 v254, v250 offset:0\n"
        "s_sub_u32 s38, s38, 1\n"
        "s_waitcnt lgkmcnt(0)\n"
        "s_cbranch_scc1 W8_c8\n"
        "v_readfirstlane_b32 s39, v254\n"
        "s_cmp_lt_u32 s39, 9\n"
        "s_cbranch_scc1 W8_r8\n"
        "s_branch W8_c8\n"
        "W8_sR7:\n"
        "s_mov_b32 s38, 8388608\n"
        "W8_rR7:\n"
        "ds_read_b32 v241, v250 offset:4\n"
        "s_sub_u32 s38, s38, 1\n"
        "s_waitcnt lgkmcnt(0)\n"
        "s_cbranch_scc1 W8_cR7\n"
        "v_readfirstlane_b32 s39, v241\n"
        "s_cmp_lt_u32 s39, 12\n"
        "s_cbranch_scc1 W8_rR7\n"
        "s_branch W8_cR7\n"
        "W8_s9:\n"
        "s_mov_b32 s38, 8388608\n"
        "W8_r9:\n"
        "ds_read_b32 v254, v250 offset:0\n"
        "s_sub_u32 s38, s38, 1\n"
        "s_waitcnt lgkmcnt(0)\n"
        "s_cbranch_scc1 W8_c9\n"
        "v_readfirstlane_b32 s39, v254\n"
        "s_cmp_lt_u32 s39, 10\n"
        "s_cbranch_scc1 W8_r9\n"
        "s_branch W8_c9\n"
        "W8_sR8:\n"
        "s_mov_b32 s38, 8388608\n"
        "W8_rR8:\n"
        "ds_read_b32 v241, v250 offset:4\n"
        "s_sub_u32 s38, s38, 1\n"
        "s_waitcnt lgkmcnt(0)\n"
        "s_cbranch_scc1 W8_cR8\n"
        "v_readfirstlane_b32 s39, v241\n"
        "s_cmp_lt_u32 s39, 13\n"
        "s_cbranch_scc1 W8_rR8\n"
        "s_branch W8_cR8\n"
        "W8_s10:\n"
        "s_mov_b32 s38, 8388608\n"
        "W8_r10:\n"
        "ds_read_b32 v254, v250 offset:0\n"
        "s_sub_u32 s38, s38, 1\n"
        "s_waitcnt lgkmcnt(0)\n"
        "s_cbranch_scc1 W8_c10\n"
        "v_readfirstlane_b32 s39, v254\n"
        "s_cmp_lt_u32 s39, 11\n"
        "s_cbranch_scc1 W8_r10\n"
        "s_branch W8_c10\n"
        "W8_sR9:\n"
        "s_mov_b32 s38, 8388608\n"
        "W8_rR9:\n"
        "ds_read_b32 v241, v250 offset:4\n"
        "s_sub_u32 s38, s38, 1\n"
        "s_waitcnt lgkmcnt(0)\n"
        "s_cbranch_scc1 W8_cR9\n"
        "v_readfirstlane_b32 s39, v241\n"
        "s_cmp_lt_u32 s39, 14\n"
        "s_cbranch_scc1 W8_rR9\n"
        "s_branch W8_cR9\n"
        "W8_s11:\n"
        "s_mov_b32 s38, 8388608\n"
        "W8_r11:\n"
        "ds_read_b32 v254, v250 offset:0\n"
        "s_sub_u32 s38, s38, 1\n"
        "s_waitcnt lgkmcnt(0)\n"
        "s_cbranch_scc1 W8_c11\n"
        "v_readfirstlane_b32 s39, v254\n"
        "s_cmp_lt_u32 s39, 12\n"
        "s_cbranch_scc1 W8_r11\n"
        "s_branch W8_c11\n"
        "W8_sR10:\n"
        "s_mov_b32 s38, 8388608\n"
        "W8_rR10:\n"
        "ds_read_b32 v241, v250 offset:4\n"
        "s_sub_u32 s38, s38, 1\n"
        "s_waitcnt lgkmcnt(0)\n"
        "s_cbranch_scc1 W8_cR10\n"
        "v_readfirstlane_b32 s39, v241\n"
        "s_cmp_lt_u32 s39, 15\n"
        "s_cbranch_scc1 W8_rR10\n"
        "s_branch W8_cR10\n"
        "W8_s12:\n"
        "s_mov_b32 s38, 8388608\n"
        "W8_r12:\n"
        "ds_read_b32 v254, v250 offset:0\n"
        "s_sub_u32 s38, s38, 1\n"
        "s_waitcnt lgkmcnt(0)\n"
        "s_cbranch_scc1 W8_c12\n"
        "v_readfirstlane_b32 s39, v254\n"
        "s_cmp_lt_u32 s39, 13\n"
        "s_cbranch_scc1 W8_r12\n"
        "s_branch W8_c12\n"
        "W8_sR11:\n"
        "s_mov_b32 s38, 8388608\n"
        "W8_rR11:\n"
        "ds_read_b32 v241, v250 offset:4\n"
        "s_sub_u32 s38, s38, 1\n"
        "s_waitcnt lgkmcnt(0)\n"
        "s_cbranch_scc1 W8_cR11\n"
        "v_readfirstlane_b32 s39, v241\n"
        "s_cmp_lt_u32 s39, 16\n"
        "s_cbranch_scc1 W8_rR11\n"
        "s_branch W8_cR11\n"
        "W8_s13:\n"
        "s_mov_b32 s38, 8388608\n"
        "W8_r13:\n"
        "ds_read_b32 v254, v250 offset:0\n"
        "s_sub_u32 s38, s38, 1\n"
        "s_waitcnt lgkmcnt(0)\n"
        "s_cbranch_scc1 W8_c13\n"
        "v_readfirstlane_b32 s39, v254\n"
        "s_cmp_lt_u32 s39, 14\n"
        "s_cbranch_scc1 W8_r13\n"
        "s_branch W8_c13\n"
        "W8_s14:\n"
        "s_mov_b32 s38, 8388608\n"
        "W8_r14:\n"
        "ds_read_b32 v254, v250 offset:0\n"
        "s_sub_u32 s38, s38, 1\n"
        "s_waitcnt lgkmcnt(0)\n"
        "s_cbranch_scc1 W8_c14\n"
        "v_readfirstlane_b32 s39, v254\n"
        "s_cmp_lt_u32 s39, 15\n"
        "s_cbranch_scc1 W8_r14\n"
        "s_branch W8_c14\n"
        "W8_s15:\n"
        "s_mov_b32 s38, 8388608\n"
        "W8_r15:\n"
        "ds_read_b32 v254, v250 offset:0\n"
        "s_sub_u32 s38, s38, 1\n"
        "s_waitcnt lgkmcnt(0)\n"
        "s_cbranch_scc1 W8_c15\n"
        "v_readfirstlane_b32 s39, v254\n"
        "s_cmp_lt_u32 s39, 16\n"
        "s_cbranch_scc1 W8_r15\n"
        "s_branch W8_c15\n"
        "W8_s16:\n"
        "s_mov_b32 s38, 8388608\n"
        "W8_r16:\n"
        "ds_read_b32 v254, v250 offset:0\n"
        "s_sub_u32 s38, s38, 1\n"
        "s_waitcnt lgkmcnt(0)\n"
        "s_cbranch_scc1 W8_c16\n"
        "v_readfirstlane_b32 s39, v254\n"
        "s_cmp_lt_u32 s39, 18\n"
        "s_cbranch_scc1 W8_r16\n"
        "s_branch W8_c16\n"
        "W8_sT:\n"
        "s_mov_b32 s38, 8388608\n"
        "W8_rT:\n"
        "ds_read_b32 v255, v250 offset:260\n"
        "s_sub_u32 s38, s38, 1\n"
        "s_waitcnt lgkmcnt(0)\n"
        "s_cbranch_scc1 W8_cT\n"
        "v_readfirstlane_b32 s39, v255\n"
        "s_cmp_lt_u32 s39, 2\n"
        "s_cbranch_scc1 W8_rT\n"
        "s_branch W8_cT\n"
        "W8_s18:\n"
        "s_mov_b32 s38, 8388608\n"
        "W8_r18:\n"
        "ds_read_b32 v254, v250 offset:0\n"
        "s_sub_u32 s38, s38, 1\n"
        "s_waitcnt lgkmcnt(0)\n"
        "s_cbranch_scc1 W8_c18\n"
        "v_readfirstlane_b32 s39, v254\n"
        "s_cmp_lt_u32 s39, 20\n"
        "s_cbranch_scc1 W8_r18\n"
        "s_branch W8_c18\n"
        "W8_s20:\n"
        "s_mov_b32 s38, 8388608\n"
        "W8_r20:\n"
        "ds_read_b32 v254, v250 offset:0\n"
        "s_sub_u32 s38, s38, 1\n"
        "s_waitcnt lgkmcnt(0)\n"
        "s_cbranch_scc1 W8_c20\n"
        "v_readfirstlane_b32 s39, v254\n"
        "s_cmp_lt_u32 s39, 22\n"
        "s_cbranch_scc1 W8_r20\n"
        "s_branch W8_c20\n"
        "W8_s22:\n"
        "s_mov_b32 s38, 8388608\n"
        "W8_r22:\n"
        "ds_read_b32 v254, v250 offset:0\n"
        "s_sub_u32 s38, s38, 1\n"
        "s_waitcnt lgkmcnt(0)\n"
        "s_cbranch_scc1 W8_c22\n"
        "v_readfirstlane_b32 s39, v254\n"
        "s_cmp_lt_u32 s39, 24\n"
        "s_cbranch_scc1 W8_r22\n"
        "s_branch W8_c22\n"
        "W8_s24:\n"
        "s_mov_b32 s38, 8388608\n"
        "W8_r24:\n"
        "ds_read_b32 v254, v250 offset:0\n"
        "s_sub_u32 s38, s38, 1\n"
        "s_waitcnt lgkmcnt(0)\n"
        "s_cbranch_scc1 W8_c24\n"
        "v_readfirstlane_b32 s39, v254\n"
        "s_cmp_lt_u32 s39, 26\n"
        "s_cbranch_scc1 W8_r24\n"
        "s_branch W8_c24\n"
        "W8_s26:\n"
        "s_mov_b32 s38, 8388608\n"
        "W8_r26:\n"
        "ds_read_b32 v254, v250 offset:0\n"
        "s_sub_u32 s38, s38, 1\n"
        "s_waitcnt lgkmcnt(0)\n"
        "s_cbranch_scc1 W8_c26\n"
        "v_readfirstlane_b32 s39, v254\n"
        "s_cmp_lt_u32 s39, 28\n"
        "s_cbranch_scc1 W8_r26\n"
        "s_branch W8_c26\n"
        "W8_end:\n"
        : "+v"(tid)
        : "s"(__builtin_amdgcn_kernarg_segment_ptr()), "s"(blockIdx.x)
        : "v1", "v2", "v3", "v4", "v5", "v6", "v7", "v8", "v9", "v10", "v11", "v12", "v13", "v14", "v15", "v16", "v17", "v18", "v19", "v20", "v21", "v22", "v23", "v24", "v25", "v26", "v27", "v28", "v29", "v30", "v31", "v32", "v33", "v34", "v35", "v36", "v37", "v38", "v39", "v40", "v41", "v42", "v43", "v44", "v45", "v46", "v47", "v48", "v49", "v50", "v51", "v52", "v53", "v54", "v55", "v56", "v57", "v58", "v59", "v60", "v61", "v62", "v63", "v64", "v65", "v66", "v67", "v68", "v69", "v70", "v71", "v72", "v73", "v74", "v75", "v76", "v77", "v78", "v79", "v80", "v81", "v82", "v83", "v84", "v85", "v86", "v87", "v88", "v89", "v90", "v91", "v92", "v93", "v94", "v95", "v96", "v97", "v98", "v99", "v100", "v101", "v102", "v103", "v104", "v105", "v106", "v107", "v108", "v109", "v110", "v111", "v112", "v113", "v114", "v115", "v116", "v117", "v118", "v119", "v120", "v121", "v122", "v123", "v124", "v125", "v126", "v127", "v128", "v129", "v130", "v131", "v132", "v133", "v134", "v135", "v136", "v137", "v138", "v139", "v140", "v141", "v142", "v143", "v144", "v145", "v146", "v147", "v148", "v149", "v150", "v151", "v152", "v153", "v154", "v155", "v156", "v157", "v158", "v159", "v160", "v161", "v162", "v163", "v164", "v165", "v166", "v167", "v168", "v169", "v170", "v171", "v172", "v173", "v174", "v175", "v176", "v177", "v178", "v179", "v180", "v181", "v182", "v183", "v184", "v185", "v186", "v187", "v188", "v189", "v190", "v191", "v192", "v193", "v194", "v195", "v196", "v197", "v198", "v199", "v200", "v201", "v202", "v203", "v204", "v205", "v206", "v207", "v208", "v209", "v210", "v211", "v212", "v213", "v214", "v215", "v216", "v217", "v218", "v219", "v220", "v221", "v222", "v223", "v224", "v225", "v226", "v227", "v228", "v229", "v230", "v231", "v232", "v233", "v234", "v235", "v236", "v237", "v238", "v239", "v240", "v241", "v242", "v243", "v244", "v245", "v246", "v247", "v248", "v249", "v250", "v251", "v252", "v253", "v254", "v255", "s3", "s4", "s5", "s6", "s7", "s8", "s9", "s10", "s11", "s12", "s13", "s14", "s15", "s16", "s17", "s18", "s19", "s20", "s21", "s22", "s23", "s24", "s25", "s26", "s27", "s28", "s29", "s30", "s31", "s33", "s34", "s35", "s36", "s37", "s38", "s39", "s40", "s41", "s42", "s43", "s44", "s45", "s46", "s47", "s48", "s49", "s50", "s51", "s52", "s53", "s54", "s55", "s56", "s57", "s58", "s59", "s60", "s61", "s62", "s63", "s64", "s65", "s66", "s67", "s68", "s69", "s70", "s71", "s72", "s73", "s74", "s75", "s76", "s77", "s78", "s79", "s80", "s81", "s82", "s83", "s84", "s85", "s86", "s87", "s88", "s89", "s90", "s91", "s92", "s93", "s94", "s95", "s96", "s97", "s98", "s99", "vcc", "scc", "memory");
}

extern "C" void kernel_launch(void* const* d_in, const int* in_sizes, int n_in,
                              void* d_out, int out_size, void* d_ws, size_t ws_size,
                              hipStream_t stream) {
    (void)in_sizes; (void)n_in; (void)out_size; (void)ws_size;
    if (ws_size >= (size_t)(128u<<20) + (8u << 20)) d_ws = (char*)d_ws + (128u<<20);
    constexpr int lds_bytes = 155712;
    hipFuncSetAttribute((const void*)w8a8_fused, hipFuncAttributeMaxDynamicSharedMemorySize, lds_bytes);
    w8a8_fused<<<256, 512, lds_bytes, stream>>>(d_in[0], d_in[1], d_ws, d_out, d_in[4], d_in[5], d_in[2], d_in[3]);
}
```

```cpp
#include <hip/hip_runtime.h>
#include <stdint.h>

__global__ __launch_bounds__(512, 2) void w8a8_fused(const void* qx, const void* qw, void* ws, void* out,
                                                     const void* sw, const void* bias, const void* sx, const void* zx) {
    int tid = threadIdx.x;
    asm volatile(
        "W8_start:\n"
        "s_load_dwordx16 s[4:19], %1, 0x0\n"
        "s_mov_b32 s3, %2\n"
        "v_readfirstlane_b32 s20, v0\n"
        "s_and_b32 s24, s3, 1\n"
        "s_lshl_b32 s24, s24, 3\n"
        "s_bfe_u32 s21, s3, 0x30003\n"
        "s_or_b32 s21, s21, s24\n"
        "s_bfe_u32 s24, s3, 0x20001\n"
        "s_lshl_b32 s24, s24, 2\n"
        "s_lshr_b32 s22, s3, 6\n"
        "s_or_b32 s22, s22, s24\n"
        "v_lshlrev_b32 v2, 2, v0\n"
        "v_add_u32 v2, 133124, v2\n"
        "v_mov_b32 v3, 0\n"
        "ds_write_b32 v2, v3\n"
        "s_and_b32 s23, s22, 1\n"
        "s_lshr_b32 s20, s20, 6\n"
        "s_cbranch_scc0 W8_prod\n"
        "s_bfe_u32 s24, s3, 0x20003\n"
        "s_add_u32 s24, s24, 4\n"
        "s_cmp_lg_u32 s20, s24\n"
        "s_cbranch_scc1 W8_np\n"
        "s_getpc_b64 s[24:25]\n"
        "W8_pc:\n"
        "v_and_b32 v2, 255, v0\n"
        "v_lshlrev_b32 v2, 7, v2\n"
        "v_min_u32 v2, W8_end-W8_pc-4, v2\n"
        "global_load_dword v255, v2, s[24:25]\n"
        "W8_np:\n"
        "s_waitcnt lgkmcnt(0)\n"
        "s_barrier\n"
        "s_branch W8_other\n"
        "W8_prod:\n"
        "s_and_b32 s24, s22, 3\n"
        "s_lshl2_add_u32 s24, s21, s24\n"
        "s_lshl_b32 s24, s24, 17\n"
        "s_lshr_b32 s25, s22, 2\n"
        "s_lshl_b32 s25, s25, 8\n"
        "s_add_u32 s24, s24, s25\n"
        "v_lshrrev_b32 v11, 4, v0\n"
        "v_and_b32 v13, 15, v0\n"
        "v_lshl_add_u32 v4, v11, 12, s24\n"
        "v_lshl_add_u32 v4, v13, 4, v4\n"
        "s_lshl_b32 s25, s22, 9\n"
        "s_mul_i32 s26, s21, 229376\n"
        "s_add_u32 s27, s26, s25\n"
        "v_lshrrev_b32 v8, 5, v0\n"
        "v_mul_u32_u24 v9, 114688, v8\n"
        "v_and_b32 v10, 31, v0\n"
        "v_lshl_add_u32 v5, v10, 4, v9\n"
        "v_add_u32 v5, s27, v5\n"
        "s_movk_i32 s28, 16384\n"
        "s_movk_i32 s29, 14336\n"
        "v_bfe_u32 v60, v0, 4, 1\n"
        "s_add_u32 s27, s22, 16\n"
        "s_sub_u32 s27, s27, s21\n"
        "v_mul_lo_u32 v60, v60, s27\n"
        "v_add_lshl_u32 v60, v60, s21, 7\n"
        "v_add_u32 v60, 6815744, v60\n"
        "s_waitcnt lgkmcnt(0)\n"
        "global_load_dwordx4 v[64:67], v4, s[4:5] nt\n"
        "v_add_u32 v4, s28, v4\n"
        "global_load_dwordx4 v[96:99], v5, s[6:7] nt\n"
        "v_add_u32 v5, s29, v5\n"
        "global_load_dwordx2 v[62:63], v60, s[8:9] sc1\n"
        "global_load_dwordx4 v[68:71], v4, s[4:5] nt\n"
        "v_add_u32 v4, s28, v4\n"
        "global_load_dwordx4 v[72:75], v4, s[4:5] nt\n"
        "v_add_u32 v4, s28, v4\n"
        "global_load_dwordx4 v[76:79], v4, s[4:5] nt\n"
        "v_add_u32 v4, s28, v4\n"
        "global_load_dwordx4 v[80:83], v4, s[4:5] nt\n"
        "v_add_u32 v4, s28, v4\n"
        "global_load_dwordx4 v[84:87], v4, s[4:5] nt\n"
        "v_add_u32 v4, s28, v4\n"
        "global_load_dwordx4 v[88:91], v4, s[4:5] nt\n"
        "v_add_u32 v4, s28, v4\n"
        "global_load_dwordx4 v[92:95], v4, s[4:5] nt\n"
        "v_add_u32 v4, 4294853632, v4\n"
        "global_load_dwordx4 v[100:103], v5, s[6:7] nt\n"
        "v_add_u32 v5, s29, v5\n"
        "global_load_dwordx4 v[104:107], v5, s[6:7] nt\n"
        "v_add_u32 v5, s29, v5\n"
        "global_load_dwordx4 v[108:111], v5, s[6:7] nt\n"
        "v_add_u32 v5, s29, v5\n"
        "global_load_dwordx4 v[112:115], v5, s[6:7] nt\n"
        "v_add_u32 v5, s29, v5\n"
        "global_load_dwordx4 v[116:119], v5, s[6:7] nt\n"
        "v_add_u32 v5, s29, v5\n"
        "global_load_dwordx4 v[120:123], v5, s[6:7] nt\n"
        "v_add_u32 v5, s29, v5\n"
        "global_load_dwordx4 v[124:127], v5, s[6:7] nt\n"
        "v_add_u32 v5, 3569664, v5\n"
        "global_load_dwordx4 v[128:131], v4, s[4:5] nt\n"
        "v_add_u32 v4, s28, v4\n"
        "global_load_dwordx4 v[132:135], v4, s[4:5] nt\n"
        "v_add_u32 v4, s28, v4\n"
        "global_load_dwordx4 v[136:139], v4, s[4:5] nt\n"
        "v_add_u32 v4, s28, v4\n"
        "global_load_dwordx4 v[140:143], v4, s[4:5] nt\n"
        "v_add_u32 v4, s28, v4\n"
        "global_load_dwordx4 v[144:147], v4, s[4:5] nt\n"
        "v_add_u32 v4, s28, v4\n"
        "global_load_dwordx4 v[148:151], v4, s[4:5] nt\n"
        "v_add_u32 v4, s28, v4\n"
        "s_barrier\n"
        "s_lshl_b32 s26, s26, 2\n"
        "s_lshl_b32 s27, s22, 8\n"
        "s_add_u32 s27, s27, 8192\n"
        "s_add_u32 s27, s27, s26\n"
        "v_lshrrev_b32 v11, 4, v0\n"
        "v_mul_u32_u24 v12, 229376, v11\n"
        "v_and_b32 v13, 15, v0\n"
        "v_lshl_add_u32 v7, v13, 4, v12\n"
        "v_add_u32 v7, s27, v7\n"
        "s_lshl_b32 s27, s22, 7\n"
        "s_add_u32 s27, s27, 12288\n"
        "s_add_u32 s27, s27, s26\n"
        "v_bfe_u32 v14, v0, 3, 2\n"
        "v_mul_u32_u24 v12, 229376, v14\n"
        "v_and_b32 v15, 7, v0\n"
        "v_lshl_add_u32 v6, v15, 4, v12\n"
        "v_add3_u32 v6, v6, v9, s27\n"
        "v_lshlrev_b32 v16, 2, v0\n"
        "v_add_u32 v25, 133124, v16\n"
        "v_add_u32 v16, 135232, v16\n"
        "v_lshlrev_b32 v19, 4, v0\n"
        "s_lshr_b32 s24, s21, 2\n"
        "s_add_u32 s25, s21, s22\n"
        "s_xor_b32 s24, s24, s25\n"
        "s_and_b32 s24, s24, 3\n"
        "s_and_b32 s25, s21, 3\n"
        "s_lshl2_add_u32 s24, s24, s25\n"
        "s_lshl_b32 s24, s24, 11\n"
        "v_add_u32 v17, s24, v19\n"
        "v_lshlrev_b32 v21, 3, v0\n"
        "v_add_u32 v21, 135232, v21\n"
        "s_lshl_b32 s26, s22, 4\n"
        "s_add_u32 s26, s26, s21\n"
        "s_mul_i32 s24, s26, 6144\n"
        "s_add_u32 s24, s24, 4194304\n"
        "s_add_u32 s44, s8, s24\n"
        "s_addc_u32 s45, s9, 0\n"
        "s_lshl_b32 s24, s26, 9\n"
        "s_add_u32 s24, s24, 5767168\n"
        "s_add_u32 s46, s8, s24\n"
        "s_addc_u32 s47, s9, 0\n"
        "v_lshlrev_b32 v22, 4, v13\n"
        "v_lshl_add_u32 v22, v11, 2, v22\n"
        "v_lshlrev_b32 v23, 4, v15\n"
        "v_lshl_add_u32 v23, v11, 2, v23\n"
        "v_lshrrev_b32 v22, 1, v22\n"
        "v_lshrrev_b32 v23, 1, v23\n"
        "s_mul_i32 s24, s21, 512\n"
        "s_mul_i32 s25, s22, 8\n"
        "s_add_u32 s24, s24, s25\n"
        "s_add_u32 s24, s24, 6029312\n"
        "s_mul_i32 s25, s22, 1024\n"
        "s_mul_i32 s27, s21, 8\n"
        "s_add_u32 s25, s25, s27\n"
        "s_add_u32 s25, s25, 6291456\n"
        "s_sub_u32 s25, s25, s24\n"
        "v_mul_lo_u32 v24, v0, s25\n"
        "s_mul_i32 s25, 4, 128\n"
        "s_add_u32 s24, s24, s25\n"
        "v_add_u32 v24, s24, v24\n"
        "v_lshlrev_b32 v35, 8, v14\n"
        "v_lshl_add_u32 v35, v8, 7, v35\n"
        "v_lshl_add_u32 v35, v15, 4, v35\n"
        "v_add_u32 v35, 4096, v35\n"
        "v_mov_b32 v20, 1\n"
        "s_mov_b32 s30, 0x0c0c0400\n"
        "s_mov_b32 s31, 0x01010101\n"
        "v_mov_b32 v28, 0\n"
        "v_mov_b32 v29, 0\n"
        "v_mov_b32 v30, 0\n"
        "v_mov_b32 v31, 0\n"
        "v_mov_b32 v32, 0\n"
        "v_mov_b32 v33, 0\n"
        "v_lshlrev_b32 v12, 3, v0\n"
        "v_add_u32 v12, 134408, v12\n"
        "s_waitcnt vmcnt(20)\n"
        "v_add_co_u32 v62, vcc, 1, v62\n"
        "s_nop 1\n"
        "v_addc_co_u32 v63, vcc, 0, v63, vcc\n"
        "s_mov_b32 exec_hi, 0\n"
        "ds_write_b64 v12, v[62:63]\n"
        "s_mov_b32 exec_hi, -1\n"
        "ds_write_b32 v25, v20 offset:1024\n"
        "s_waitcnt vmcnt(19)\n"
        "global_load_dwordx4 v[152:155], v4, s[4:5] nt\n"
        "v_add_u32 v4, s28, v4\n"
        "global_load_dwordx4 v[156:159], v4, s[4:5] nt\n"
        "v_add_u32 v4, 4294853632, v4\n"
        "v_perm_b32 v64, v65, v64, s30\n"
        "v_perm_b32 v66, v67, v66, s30\n"
        "v_lshl_or_b32 v64, v66, 16, v64\n"
        "ds_write_b32 v16, v64 offset:0\n"
        "v_perm_b32 v68, v69, v68, s30\n"
        "v_perm_b32 v70, v71, v70, s30\n"
        "v_lshl_or_b32 v68, v70, 16, v68\n"
        "ds_write_b32 v16, v68 offset:256\n"
        "s_waitcnt vmcnt(19)\n"
        "global_load_dwordx4 v[160:163], v5, s[6:7] nt\n"
        "v_add_u32 v5, s29, v5\n"
        "global_load_dwordx4 v[164:167], v5, s[6:7] nt\n"
        "v_add_u32 v5, s29, v5\n"
        "v_perm_b32 v72, v73, v72, s30\n"
        "v_perm_b32 v74, v75, v74, s30\n"
        "v_lshl_or_b32 v72, v74, 16, v72\n"
        "ds_write_b32 v16, v72 offset:512\n"
        "v_perm_b32 v76, v77, v76, s30\n"
        "v_perm_b32 v78, v79, v78, s30\n"
        "v_lshl_or_b32 v76, v78, 16, v76\n"
        "ds_write_b32 v16, v76 offset:768\n"
        "s_waitcnt vmcnt(19)\n"
        "global_load_dwordx4 v[168:171], v5, s[6:7] nt\n"
        "v_add_u32 v5, s29, v5\n"
        "global_load_dwordx4 v[172:175], v5, s[6:7] nt\n"
        "v_add_u32 v5, s29, v5\n"
        "v_perm_b32 v80, v81, v80, s30\n"
        "v_perm_b32 v82, v83, v82, s30\n"
        "v_lshl_or_b32 v80, v82, 16, v80\n"
        "ds_write_b32 v16, v80 offset:1024\n"
        "v_perm_b32 v84, v85, v84, s30\n"
        "v_perm_b32 v86, v87, v86, s30\n"
        "v_lshl_or_b32 v84, v86, 16, v84\n"
        "ds_write_b32 v16, v84 offset:1280\n"
        "s_waitcnt vmcnt(19)\n"
        "global_load_dwordx4 v[176:179], v5, s[6:7] nt\n"
        "v_add_u32 v5, s29, v5\n"
        "global_load_dwordx4 v[180:183], v5, s[6:7] nt\n"
        "v_add_u32 v5, s29, v5\n"
        "v_perm_b32 v88, v89, v88, s30\n"
        "v_perm_b32 v90, v91, v90, s30\n"
        "v_lshl_or_b32 v88, v90, 16, v88\n"
        "ds_write_b32 v16, v88 offset:1536\n"
        "v_perm_b32 v92, v93, v92, s30\n"
        "v_perm_b32 v94, v95, v94, s30\n"
        "v_lshl_or_b32 v92, v94, 16, v92\n"
        "ds_write_b32 v16, v92 offset:1792\n"
        "s_waitcnt vmcnt(20)\n"
        "global_load_dwordx4 v[184:187], v5, s[6:7] nt\n"
        "v_add_u32 v5, s29, v5\n"
        "global_load_dwordx4 v[188:191], v5, s[6:7] nt\n"
        "v_add_u32 v5, 3569664, v5\n"
        "s_waitcnt vmcnt(20)\n"
        "global_load_dwordx4 v[192:195], v4, s[4:5] nt\n"
        "v_add_u32 v4, s28, v4\n"
        "global_load_dwordx4 v[196:199], v4, s[4:5] nt\n"
        "v_add_u32 v4, s28, v4\n"
        "v_perm_b32 v96, v100, v96, s30\n"
        "v_perm_b32 v104, v108, v104, s30\n"
        "v_perm_b32 v97, v101, v97, s30\n"
        "v_perm_b32 v105, v109, v105, s30\n"
        "v_perm_b32 v98, v102, v98, s30\n"
        "v_perm_b32 v106, v110, v106, s30\n"
        "v_perm_b32 v99, v103, v99, s30\n"
        "v_perm_b32 v107, v111, v107, s30\n"
        "v_lshl_or_b32 v36, v104, 16, v96\n"
        "v_lshl_or_b32 v40, v105, 16, v97\n"
        "v_lshl_or_b32 v38, v106, 16, v98\n"
        "v_lshl_or_b32 v42, v107, 16, v99\n"
        "s_waitcnt vmcnt(20)\n"
        "global_load_dwordx4 v[200:203], v4, s[4:5] nt\n"
        "v_add_u32 v4, s28, v4\n"
        "global_load_dwordx4 v[204:207], v4, s[4:5] nt\n"
        "v_add_u32 v4, s28, v4\n"
        "s_waitcnt vmcnt(20)\n"
        "global_load_dwordx4 v[208:211], v4, s[4:5] nt\n"
        "v_add_u32 v4, s28, v4\n"
        "global_load_dwordx4 v[212:215], v4, s[4:5] nt\n"
        "v_add_u32 v4, s28, v4\n"
        "v_perm_b32 v112, v116, v112, s30\n"
        "v_perm_b32 v120, v124, v120, s30\n"
        "v_perm_b32 v113, v117, v113, s30\n"
        "v_perm_b32 v121, v125, v121, s30\n"
        "v_perm_b32 v114, v118, v114, s30\n"
        "v_perm_b32 v122, v126, v122, s30\n"
        "v_perm_b32 v115, v119, v115, s30\n"
        "v_perm_b32 v123, v127, v123, s30\n"
        "v_lshl_or_b32 v37, v120, 16, v112\n"
        "v_lshl_or_b32 v41, v121, 16, v113\n"
        "v_lshl_or_b32 v39, v122, 16, v114\n"
        "v_lshl_or_b32 v43, v123, 16, v115\n"
        "s_nop 0\n"
        "v_permlane32_swap_b32 v36, v38\n"
        "v_permlane32_swap_b32 v37, v39\n"
        "v_permlane32_swap_b32 v40, v42\n"
        "v_permlane32_swap_b32 v41, v43\n"
        "s_nop 0\n"
        "ds_write_b128 v17, v[36:39] offset:0\n"
        "ds_write_b128 v17, v[40:43] offset:1024\n"
        "ds_write_b32 v25, v20 offset:1544\n"
        "v_add_u32 v20, 1, v20\n"
        "v_dot4_i32_i8 v28, v36, s31, v28\n"
        "v_dot4_i32_i8 v29, v40, s31, v29\n"
        "v_dot4_i32_i8 v28, v37, s31, v28\n"
        "v_dot4_i32_i8 v29, v41, s31, v29\n"
        "v_dot4_i32_i8 v28, v38, s31, v28\n"
        "v_dot4_i32_i8 v29, v42, s31, v29\n"
        "v_dot4_i32_i8 v28, v39, s31, v28\n"
        "v_dot4_i32_i8 v29, v43, s31, v29\n"
        "s_waitcnt vmcnt(20)\n"
        "global_load_dwordx4 v[216:219], v4, s[4:5] nt\n"
        "v_add_u32 v4, s28, v4\n"
        "global_load_dwordx4 v[220:223], v4, s[4:5] nt\n"
        "v_add_u32 v4, 4294853632, v4\n"
        "v_perm_b32 v128, v129, v128, s30\n"
        "v_perm_b32 v130, v131, v130, s30\n"
        "v_lshl_or_b32 v128, v130, 16, v128\n"
        "ds_write_b32 v16, v128 offset:5120\n"
        "v_perm_b32 v132, v133, v132, s30\n"
        "v_perm_b32 v134, v135, v134, s30\n"
        "v_lshl_or_b32 v132, v134, 16, v132\n"
        "ds_write_b32 v16, v132 offset:5376\n"
        "s_waitcnt vmcnt(20)\n"
        "global_load_dwordx4 v[224:227], v5, s[6:7] nt\n"
        "v_add_u32 v5, s29, v5\n"
        "global_load_dwordx4 v[228:231], v5, s[6:7] nt\n"
        "v_add_u32 v5, s29, v5\n"
        "v_perm_b32 v136, v137, v136, s30\n"
        "v_perm_b32 v138, v139, v138, s30\n"
        "v_lshl_or_b32 v136, v138, 16, v136\n"
        "ds_write_b32 v16, v136 offset:5632\n"
        "v_perm_b32 v140, v141, v140, s30\n"
        "v_perm_b32 v142, v143, v142, s30\n"
        "v_lshl_or_b32 v140, v142, 16, v140\n"
        "ds_write_b32 v16, v140 offset:5888\n"
        "s_waitcnt vmcnt(20)\n"
        "global_load_dwordx4 v[232:235], v5, s[6:7] nt\n"
        "v_add_u32 v5, s29, v5\n"
        "global_load_dwordx4 v[236:239], v5, s[6:7] nt\n"
        "v_add_u32 v5, s29, v5\n"
        "v_perm_b32 v144, v145, v144, s30\n"
        "v_perm_b32 v146, v147, v146, s30\n"
        "v_lshl_or_b32 v144, v146, 16, v144\n"
        "ds_write_b32 v16, v144 offset:6144\n"
        "v_perm_b32 v148, v149, v148, s30\n"
        "v_perm_b32 v150, v151, v150, s30\n"
        "v_lshl_or_b32 v148, v150, 16, v148\n"
        "ds_write_b32 v16, v148 offset:6400\n"
        "s_waitcnt vmcnt(20)\n"
        "global_load_dwordx4 v[240:243], v5, s[6:7] nt\n"
        "v_add_u32 v5, s29, v5\n"
        "global_load_dwordx4 v[244:247], v5, s[6:7] nt\n"
        "v_add_u32 v5, s29, v5\n"
        "v_perm_b32 v152, v153, v152, s30\n"
        "v_perm_b32 v154, v155, v154, s30\n"
        "v_lshl_or_b32 v152, v154, 16, v152\n"
        "ds_write_b32 v16, v152 offset:6656\n"
        "v_perm_b32 v156, v157, v156, s30\n"
        "v_perm_b32 v158, v159, v158, s30\n"
        "v_lshl_or_b32 v156, v158, 16, v156\n"
        "ds_write_b32 v16, v156 offset:6912\n"
        "s_waitcnt vmcnt(20)\n"
        "global_load_dwordx4 v[64:67], v5, s[6:7] nt\n"
        "v_add_u32 v5, s29, v5\n"
        "global_load_dwordx4 v[68:71], v5, s[6:7] nt\n"
        "v_add_u32 v5, 3569664, v5\n"
        "s_waitcnt vmcnt(20)\n"
        "global_load_dwordx4 v[72:75], v4, s[4:5] nt\n"
        "v_add_u32 v4, s28, v4\n"
        "global_load_dwordx4 v[76:79], v4, s[4:5] nt\n"
        "v_add_u32 v4, s28, v4\n"
        "v_perm_b32 v160, v164, v160, s30\n"
        "v_perm_b32 v168, v172, v168, s30\n"
        "v_perm_b32 v161, v165, v161, s30\n"
        "v_perm_b32 v169, v173, v169, s30\n"
        "v_perm_b32 v162, v166, v162, s30\n"
        "v_perm_b32 v170, v174, v170, s30\n"
        "v_perm_b32 v163, v167, v163, s30\n"
        "v_perm_b32 v171, v175, v171, s30\n"
        "v_lshl_or_b32 v36, v168, 16, v160\n"
        "v_lshl_or_b32 v40, v169, 16, v161\n"
        "v_lshl_or_b32 v38, v170, 16, v162\n"
        "v_lshl_or_b32 v42, v171, 16, v163\n"
        "s_waitcnt vmcnt(20)\n"
        "global_load_dwordx4 v[80:83], v4, s[4:5] nt\n"
        "v_add_u32 v4, s28, v4\n"
        "global_load_dwordx4 v[84:87], v4, s[4:5] nt\n"
        "v_add_u32 v4, s28, v4\n"
        "s_waitcnt vmcnt(20)\n"
        "global_load_dwordx4 v[88:91], v4, s[4:5] nt\n"
        "v_add_u32 v4, s28, v4\n"
        "global_load_dwordx4 v[92:95], v4, s[4:5] nt\n"
        "v_add_u32 v4, s28, v4\n"
        "v_perm_b32 v176, v180, v176, s30\n"
        "v_perm_b32 v184, v188, v184, s30\n"
        "v_perm_b32 v177, v181, v177, s30\n"
        "v_perm_b32 v185, v189, v185, s30\n"
        "v_perm_b32 v178, v182, v178, s30\n"
        "v_perm_b32 v186, v190, v186, s30\n"
        "v_perm_b32 v179, v183, v179, s30\n"
        "v_perm_b32 v187, v191, v187, s30\n"
        "v_lshl_or_b32 v37, v184, 16, v176\n"
        "v_lshl_or_b32 v41, v185, 16, v177\n"
        "v_lshl_or_b32 v39, v186, 16, v178\n"
        "v_lshl_or_b32 v43, v187, 16, v179\n"
        "s_nop 0\n"
        "v_permlane32_swap_b32 v36, v38\n"
        "v_permlane32_swap_b32 v37, v39\n"
        "v_permlane32_swap_b32 v40, v42\n"
        "v_permlane32_swap_b32 v41, v43\n"
        "s_nop 0\n"
        "ds_write_b128 v17, v[36:39] offset:32768\n"
        "ds_write_b128 v17, v[40:43] offset:33792\n"
        "v_add_u32 v17, 65536, v17\n"
        "ds_write_b32 v25, v20 offset:1544\n"
        "v_add_u32 v20, 1, v20\n"
        "v_dot4_i32_i8 v28, v36, s31, v28\n"
        "v_dot4_i32_i8 v29, v40, s31, v29\n"
        "v_dot4_i32_i8 v28, v37, s31, v28\n"
        "v_dot4_i32_i8 v29, v41, s31, v29\n"
        "v_dot4_i32_i8 v28, v38, s31, v28\n"
        "v_dot4_i32_i8 v29, v42, s31, v29\n"
        "v_dot4_i32_i8 v28, v39, s31, v28\n"
        "v_dot4_i32_i8 v29, v43, s31, v29\n"
        "s_waitcnt vmcnt(20)\n"
        "global_load_dwordx4 v[96:99], v4, s[4:5] nt\n"
        "v_add_u32 v4, s28, v4\n"
        "global_load_dwordx4 v[100:103], v4, s[4:5] nt\n"
        "v_perm_b32 v192, v193, v192, s30\n"
        "v_perm_b32 v194, v195, v194, s30\n"
        "v_lshl_or_b32 v192, v194, 16, v192\n"
        "ds_write_b32 v16, v192 offset:10240\n"
        "v_perm_b32 v196, v197, v196, s30\n"
        "v_perm_b32 v198, v199, v198, s30\n"
        "v_lshl_or_b32 v196, v198, 16, v196\n"
        "ds_write_b32 v16, v196 offset:10496\n"
        "s_waitcnt vmcnt(20)\n"
        "global_load_dwordx4 v[104:107], v5, s[6:7] nt\n"
        "v_add_u32 v5, s29, v5\n"
        "global_load_dwordx4 v[108:111], v5, s[6:7] nt\n"
        "v_add_u32 v5, s29, v5\n"
        "v_perm_b32 v200, v201, v200, s30\n"
        "v_perm_b32 v202, v203, v202, s30\n"
        "v_lshl_or_b32 v200, v202, 16, v200\n"
        "ds_write_b32 v16, v200 offset:10752\n"
        "v_perm_b32 v204, v205, v204, s30\n"
        "v_perm_b32 v206, v207, v206, s30\n"
        "v_lshl_or_b32 v204, v206, 16, v204\n"
        "ds_write_b32 v16, v204 offset:11008\n"
        "s_waitcnt vmcnt(20)\n"
        "global_load_dwordx4 v[112:115], v5, s[6:7] nt\n"
        "v_add_u32 v5, s29, v5\n"
        "global_load_dwordx4 v[116:119], v5, s[6:7] nt\n"
        "v_add_u32 v5, s29, v5\n"
        "v_perm_b32 v208, v209, v208, s30\n"
        "v_perm_b32 v210, v211, v210, s30\n"
        "v_lshl_or_b32 v208, v210, 16, v208\n"
        "ds_write_b32 v16, v208 offset:11264\n"
        "v_perm_b32 v212, v213, v212, s30\n"
        "v_perm_b32 v214, v215, v214, s30\n"
        "v_lshl_or_b32 v212, v214, 16, v212\n"
        "ds_write_b32 v16, v212 offset:11520\n"
        "s_waitcnt vmcnt(20)\n"
        "global_load_dwordx4 v[120:123], v5, s[6:7] nt\n"
        "v_add_u32 v5, s29, v5\n"
        "global_load_dwordx4 v[124:127], v5, s[6:7] nt\n"
        "v_add_u32 v5, s29, v5\n"
        "v_perm_b32 v216, v217, v216, s30\n"
        "v_perm_b32 v218, v219, v218, s30\n"
        "v_lshl_or_b32 v216, v218, 16, v216\n"
        "ds_write_b32 v16, v216 offset:11776\n"
        "v_perm_b32 v220, v221, v220, s30\n"
        "v_perm_b32 v222, v223, v222, s30\n"
        "v_lshl_or_b32 v220, v222, 16, v220\n"
        "ds_write_b32 v16, v220 offset:12032\n"
        "s_waitcnt vmcnt(20)\n"
        "global_load_dwordx4 v[128:131], v5, s[6:7] nt\n"
        "v_add_u32 v5, s29, v5\n"
        "global_load_dwordx4 v[132:135], v5, s[6:7] nt\n"
        "s_waitcnt vmcnt(20)\n"
        "global_load_dwordx4 v[136:139], v6, s[6:7] nt\n"
        "v_add_u32 v6, s29, v6\n"
        "global_load_dwordx4 v[140:143], v6, s[6:7] nt\n"
        "v_add_u32 v6, s29, v6\n"
        "v_perm_b32 v224, v228, v224, s30\n"
        "v_perm_b32 v232, v236, v232, s30\n"
        "v_perm_b32 v225, v229, v225, s30\n"
        "v_perm_b32 v233, v237, v233, s30\n"
        "v_perm_b32 v226, v230, v226, s30\n"
        "v_perm_b32 v234, v238, v234, s30\n"
        "v_perm_b32 v227, v231, v227, s30\n"
        "v_perm_b32 v235, v239, v235, s30\n"
        "v_lshl_or_b32 v36, v232, 16, v224\n"
        "v_lshl_or_b32 v40, v233, 16, v225\n"
        "v_lshl_or_b32 v38, v234, 16, v226\n"
        "v_lshl_or_b32 v42, v235, 16, v227\n"
        "s_waitcnt vmcnt(20)\n"
        "global_load_dwordx4 v[144:147], v6, s[6:7] nt\n"
        "v_add_u32 v6, s29, v6\n"
        "global_load_dwordx4 v[148:151], v6, s[6:7] nt\n"
        "v_add_u32 v6, s29, v6\n"
        "s_waitcnt vmcnt(20)\n"
        "global_load_dwordx4 v[152:155], v6, s[6:7] nt\n"
        "v_add_u32 v6, s29, v6\n"
        "global_load_dwordx4 v[156:159], v6, s[6:7] nt\n"
        "v_add_u32 v6, s29, v6\n"
        "v_lshl_add_u32 v61, v10, 1, v8\n"
        "v_mov_b32 v253, 0\n"
        "s_lshl_b32 s56, s22, 7\n"
        "s_lshl_b32 s57, s22, 6\n"
        "s_add_u32 s57, s57, 1920\n"
        "v_mov_b32 v254, s56\n"
        "v_mov_b32 v255, s57\n"
        "v_cmp_gt_u32 vcc, 32, v61\n"
        "s_lshl_b32 s56, s22, 5\n"
        "global_load_ushort v252, v253, s[16:17]\n"
        "v_cndmask_b32 v254, v255, v254, vcc\n"
        "s_add_u32 s56, s56, 2880\n"
        "v_mov_b32 v255, s56\n"
        "v_cmp_gt_u32 vcc, 48, v61\n"
        "s_nop 1\n"
        "v_cndmask_b32 v254, v255, v254, vcc\n"
        "v_lshl_add_u32 v254, v61, 2, v254\n"
        "v_lshlrev_b32 v254, 1, v254\n"
        "s_mov_b32 exec_lo, 0x0fffffff\n"
        "s_mov_b32 exec_hi, 268435455\n"
        "global_load_dwordx2 v[248:249], v254, s[12:13]\n"
        "global_load_dwordx2 v[250:251], v254, s[14:15]\n"
        "s_mov_b64 exec, -1\n"
        "v_perm_b32 v240, v244, v240, s30\n"
        "v_perm_b32 v64, v68, v64, s30\n"
        "v_perm_b32 v241, v245, v241, s30\n"
        "v_perm_b32 v65, v69, v65, s30\n"
        "v_perm_b32 v242, v246, v242, s30\n"
        "v_perm_b32 v66, v70, v66, s30\n"
        "v_perm_b32 v243, v247, v243, s30\n"
        "v_perm_b32 v67, v71, v67, s30\n"
        "v_lshl_or_b32 v37, v64, 16, v240\n"
        "v_lshl_or_b32 v41, v65, 16, v241\n"
        "v_lshl_or_b32 v39, v66, 16, v242\n"
        "v_lshl_or_b32 v43, v67, 16, v243\n"
        "s_nop 0\n"
        "v_permlane32_swap_b32 v36, v38\n"
        "v_permlane32_swap_b32 v37, v39\n"
        "v_permlane32_swap_b32 v40, v42\n"
        "v_permlane32_swap_b32 v41, v43\n"
        "s_nop 0\n"
        "ds_write_b128 v17, v[36:39] offset:0\n"
        "ds_write_b128 v17, v[40:43] offset:1024\n"
        "ds_write_b32 v25, v20 offset:1544\n"
        "v_add_u32 v20, 1, v20\n"
        "v_dot4_i32_i8 v28, v36, s31, v28\n"
        "v_dot4_i32_i8 v29, v40, s31, v29\n"
        "v_dot4_i32_i8 v28, v37, s31, v28\n"
        "v_dot4_i32_i8 v29, v41, s31, v29\n"
        "v_dot4_i32_i8 v28, v38, s31, v28\n"
        "v_dot4_i32_i8 v29, v42, s31, v29\n"
        "v_dot4_i32_i8 v28, v39, s31, v28\n"
        "v_dot4_i32_i8 v29, v43, s31, v29\n"
        "s_waitcnt vmcnt(23)\n"
        "global_load_dwordx4 v[160:163], v6, s[6:7] nt\n"
        "v_add_u32 v6, s29, v6\n"
        "global_load_dwordx4 v[164:167], v6, s[6:7] nt\n"
        "v_perm_b32 v72, v73, v72, s30\n"
        "v_perm_b32 v74, v75, v74, s30\n"
        "v_lshl_or_b32 v72, v74, 16, v72\n"
        "ds_write_b32 v16, v72 offset:15360\n"
        "v_perm_b32 v76, v77, v76, s30\n"
        "v_perm_b32 v78, v79, v78, s30\n"
        "v_lshl_or_b32 v76, v78, 16, v76\n"
        "ds_write_b32 v16, v76 offset:15616\n"
        "s_waitcnt vmcnt(23)\n"
        "global_load_dwordx4 v[168:171], v7, s[6:7] nt\n"
        "v_add_u32 v7, s29, v7\n"
        "global_load_dwordx4 v[172:175], v7, s[6:7] nt\n"
        "v_add_u32 v7, s29, v7\n"
        "v_perm_b32 v80, v81, v80, s30\n"
        "v_perm_b32 v82, v83, v82, s30\n"
        "v_lshl_or_b32 v80, v82, 16, v80\n"
        "ds_write_b32 v16, v80 offset:15872\n"
        "v_perm_b32 v84, v85, v84, s30\n"
        "v_perm_b32 v86, v87, v86, s30\n"
        "v_lshl_or_b32 v84, v86, 16, v84\n"
        "ds_write_b32 v16, v84 offset:16128\n"
        "s_waitcnt vmcnt(23)\n"
        "global_load_dwordx4 v[176:179], v7, s[6:7] nt\n"
        "v_add_u32 v7, s29, v7\n"
        "global_load_dwordx4 v[180:183], v7, s[6:7] nt\n"
        "v_add_u32 v7, s29, v7\n"
        "v_perm_b32 v88, v89, v88, s30\n"
        "v_perm_b32 v90, v91, v90, s30\n"
        "v_lshl_or_b32 v88, v90, 16, v88\n"
        "ds_write_b32 v16, v88 offset:16384\n"
        "v_perm_b32 v92, v93, v92, s30\n"
        "v_perm_b32 v94, v95, v94, s30\n"
        "v_lshl_or_b32 v92, v94, 16, v92\n"
        "ds_write_b32 v16, v92 offset:16640\n"
        "s_waitcnt vmcnt(23)\n"
        "global_load_dwordx4 v[184:187], v7, s[6:7] nt\n"
        "v_add_u32 v7, s29, v7\n"
        "global_load_dwordx4 v[188:191], v7, s[6:7] nt\n"
        "v_add_u32 v7, s29, v7\n"
        "v_perm_b32 v96, v97, v96, s30\n"
        "v_perm_b32 v98, v99, v98, s30\n"
        "v_lshl_or_b32 v96, v98, 16, v96\n"
        "ds_write_b32 v16, v96 offset:16896\n"
        "v_perm_b32 v100, v101, v100, s30\n"
        "v_perm_b32 v102, v103, v102, s30\n"
        "v_lshl_or_b32 v100, v102, 16, v100\n"
        "ds_write_b32 v16, v100 offset:17152\n"
        "s_waitcnt vmcnt(23)\n"
        "global_load_dwordx4 v[192:195], v7, s[6:7] nt\n"
        "v_add_u32 v7, s29, v7\n"
        "global_load_dwordx4 v[196:199], v7, s[6:7] nt\n"
        "v_add_u32 v7, s29, v7\n"
        "s_waitcnt vmcnt(23)\n"
        "global_load_dwordx4 v[200:203], v7, s[6:7] nt\n"
        "v_add_u32 v7, s29, v7\n"
        "global_load_dwordx4 v[204:207], v7, s[6:7] nt\n"
        "v_add_u32 v7, s29, v7\n"
        "v_perm_b32 v104, v108, v104, s30\n"
        "v_perm_b32 v112, v116, v112, s30\n"
        "v_perm_b32 v105, v109, v105, s30\n"
        "v_perm_b32 v113, v117, v113, s30\n"
        "v_perm_b32 v106, v110, v106, s30\n"
        "v_perm_b32 v114, v118, v114, s30\n"
        "v_perm_b32 v107, v111, v107, s30\n"
        "v_perm_b32 v115, v119, v115, s30\n"
        "v_lshl_or_b32 v36, v112, 16, v104\n"
        "v_lshl_or_b32 v40, v113, 16, v105\n"
        "v_lshl_or_b32 v38, v114, 16, v106\n"
        "v_lshl_or_b32 v42, v115, 16, v107\n"
        "s_waitcnt vmcnt(23)\n"
        "global_load_dwordx4 v[208:211], v7, s[6:7] nt\n"
        "v_add_u32 v7, s29, v7\n"
        "global_load_dwordx4 v[212:215], v7, s[6:7] nt\n"
        "v_add_u32 v7, s29, v7\n"
        "s_waitcnt vmcnt(23)\n"
        "global_load_dwordx4 v[216:219], v7, s[6:7] nt\n"
        "v_add_u32 v7, s29, v7\n"
        "global_load_dwordx4 v[220:223], v7, s[6:7] nt\n"
        "v_add_u32 v7, s29, v7\n"
        "v_perm_b32 v120, v124, v120, s30\n"
        "v_perm_b32 v128, v132, v128, s30\n"
        "v_perm_b32 v121, v125, v121, s30\n"
        "v_perm_b32 v129, v133, v129, s30\n"
        "v_perm_b32 v122, v126, v122, s30\n"
        "v_perm_b32 v130, v134, v130, s30\n"
        "v_perm_b32 v123, v127, v123, s30\n"
        "v_perm_b32 v131, v135, v131, s30\n"
        "v_lshl_or_b32 v37, v128, 16, v120\n"
        "v_lshl_or_b32 v41, v129, 16, v121\n"
        "v_lshl_or_b32 v39, v130, 16, v122\n"
        "v_lshl_or_b32 v43, v131, 16, v123\n"
        "s_nop 0\n"
        "v_permlane32_swap_b32 v36, v38\n"
        "v_permlane32_swap_b32 v37, v39\n"
        "v_permlane32_swap_b32 v40, v42\n"
        "v_permlane32_swap_b32 v41, v43\n"
        "s_nop 0\n"
        "ds_write_b128 v17, v[36:39] offset:32768\n"
        "ds_write_b128 v17, v[40:43] offset:33792\n"
        "v_dot4_i32_i8 v28, v36, s31, v28\n"
        "v_dot4_i32_i8 v29, v40, s31, v29\n"
        "v_dot4_i32_i8 v28, v37, s31, v28\n"
        "v_dot4_i32_i8 v29, v41, s31, v29\n"
        "v_dot4_i32_i8 v28, v38, s31, v28\n"
        "v_dot4_i32_i8 v29, v42, s31, v29\n"
        "v_dot4_i32_i8 v28, v39, s31, v28\n"
        "v_dot4_i32_i8 v29, v43, s31, v29\n"
        "s_nop 2\n"
        "v_and_b32 v28, 0xffff, v28\n"
        "v_lshl_or_b32 v28, v29, 16, v28\n"
        "ds_write_b64 v21, v[28:29] offset:19712\n"
        "ds_write_b32 v25, v20 offset:1544\n"
        "v_add_u32 v20, 1, v20\n"
        "s_waitcnt vmcnt(23)\n"
        "global_load_dwordx4 v[224:227], v7, s[6:7] nt\n"
        "v_add_u32 v7, s29, v7\n"
        "global_load_dwordx4 v[228:231], v7, s[6:7] nt\n"
        "v_mov_b32 v12, 1\n"
        "ds_write_b32 v25, v12 offset:512\n"
        "s_waitcnt vmcnt(23)\n"
        "v_perm_b32 v136, v140, v136, s30\n"
        "v_perm_b32 v144, v148, v144, s30\n"
        "v_perm_b32 v137, v141, v137, s30\n"
        "v_perm_b32 v145, v149, v145, s30\n"
        "v_perm_b32 v138, v142, v138, s30\n"
        "v_perm_b32 v146, v150, v146, s30\n"
        "v_perm_b32 v139, v143, v139, s30\n"
        "v_perm_b32 v147, v151, v147, s30\n"
        "v_lshl_or_b32 v36, v144, 16, v136\n"
        "v_lshl_or_b32 v40, v145, 16, v137\n"
        "v_lshl_or_b32 v38, v146, 16, v138\n"
        "v_lshl_or_b32 v42, v147, 16, v139\n"
        "s_waitcnt vmcnt(21)\n"
        "s_waitcnt vmcnt(16)\n"
        "v_perm_b32 v152, v156, v152, s30\n"
        "v_perm_b32 v160, v164, v160, s30\n"
        "v_perm_b32 v153, v157, v153, s30\n"
        "v_perm_b32 v161, v165, v161, s30\n"
        "v_perm_b32 v154, v158, v154, s30\n"
        "v_perm_b32 v162, v166, v162, s30\n"
        "v_perm_b32 v155, v159, v155, s30\n"
        "v_perm_b32 v163, v167, v163, s30\n"
        "v_lshl_or_b32 v37, v160, 16, v152\n"
        "v_lshl_or_b32 v41, v161, 16, v153\n"
        "v_lshl_or_b32 v39, v162, 16, v154\n"
        "v_lshl_or_b32 v43, v163, 16, v155\n"
        "s_nop 0\n"
        "v_permlane32_swap_b32 v36, v38\n"
        "v_permlane32_swap_b32 v37, v39\n"
        "v_permlane32_swap_b32 v40, v42\n"
        "v_permlane32_swap_b32 v41, v43\n"
        "s_nop 0\n"
        "global_store_dwordx4 v35, v[36:39], s[44:45] sc1\n"
        "global_store_dwordx4 v35, v[40:43], s[44:45] offset:1024 sc1\n"
        "v_dot4_i32_i8 v30, v36, s31, v30\n"
        "v_dot4_i32_i8 v31, v40, s31, v31\n"
        "v_dot4_i32_i8 v30, v37, s31, v30\n"
        "v_dot4_i32_i8 v31, v41, s31, v31\n"
        "v_dot4_i32_i8 v30, v38, s31, v30\n"
        "v_dot4_i32_i8 v31, v42, s31, v31\n"
        "v_dot4_i32_i8 v30, v39, s31, v30\n"
        "v_dot4_i32_i8 v31, v43, s31, v31\n"
        "s_nop 2\n"
        "v_permlane16_swap_b32 v30, v31\n"
        "s_nop 0\n"
        "v_add_u32 v30, v30, v31\n"
        "v_mov_b32 v31, 0\n"
        "s_nop 0\n"
        "v_add_u32_dpp v34, v30, v30 row_ror:8 row_mask:0xf bank_mask:0xf\n"
        "v_mov_b32 v30, 0\n"
        "s_mov_b32 s38, 8388608\n"
        "W8_tg0:\n"
        "ds_read_b32 v12, v25 offset:1024\n"
        "s_sub_u32 s38, s38, 1\n"
        "s_waitcnt lgkmcnt(0)\n"
        "s_cbranch_scc1 W8_tg0_x\n"
        "v_readfirstlane_b32 s39, v12\n"
        "s_cmp_ge_u32 s39, 1\n"
        "s_cbranch_scc1 W8_tg0_x\n"
        "s_sleep 1\n"
        "s_branch W8_tg0\n"
        "W8_tg0_x:\n"
        "v_lshrrev_b32 v12, 0, v0\n"
        "v_lshlrev_b32 v12, 7, v12\n"
        "v_add_u32 v12, 134408, v12\n"
        "s_mov_b64 exec, 3\n"
        "ds_read_b64 v[26:27], v12\n"
        "s_mov_b64 exec, -1\n"
        "s_waitcnt vmcnt(16)\n"
        "s_waitcnt vmcnt(14)\n"
        "v_perm_b32 v168, v172, v168, s30\n"
        "v_perm_b32 v176, v180, v176, s30\n"
        "v_perm_b32 v169, v173, v169, s30\n"
        "v_perm_b32 v177, v181, v177, s30\n"
        "v_perm_b32 v170, v174, v170, s30\n"
        "v_perm_b32 v178, v182, v178, s30\n"
        "v_perm_b32 v171, v175, v171, s30\n"
        "v_perm_b32 v179, v183, v179, s30\n"
        "v_lshl_or_b32 v36, v176, 16, v168\n"
        "v_lshl_or_b32 v40, v177, 16, v169\n"
        "v_lshl_or_b32 v44, v178, 16, v170\n"
        "v_lshl_or_b32 v48, v179, 16, v171\n"
        "v_dot4_i32_i8 v30, v36, s31, v30\n"
        "v_dot4_i32_i8 v31, v40, s31, v31\n"
        "v_dot4_i32_i8 v32, v44, s31, v32\n"
        "v_dot4_i32_i8 v33, v48, s31, v33\n"
        "s_waitcnt vmcnt(12)\n"
        "s_waitcnt vmcnt(10)\n"
        "v_perm_b32 v184, v188, v184, s30\n"
        "v_perm_b32 v192, v196, v192, s30\n"
        "v_perm_b32 v185, v189, v185, s30\n"
        "v_perm_b32 v193, v197, v193, s30\n"
        "v_perm_b32 v186, v190, v186, s30\n"
        "v_perm_b32 v194, v198, v194, s30\n"
        "v_perm_b32 v187, v191, v187, s30\n"
        "v_perm_b32 v195, v199, v195, s30\n"
        "v_lshl_or_b32 v37, v192, 16, v184\n"
        "v_lshl_or_b32 v41, v193, 16, v185\n"
        "v_lshl_or_b32 v45, v194, 16, v186\n"
        "v_lshl_or_b32 v49, v195, 16, v187\n"
        "v_dot4_i32_i8 v30, v37, s31, v30\n"
        "v_dot4_i32_i8 v31, v41, s31, v31\n"
        "v_dot4_i32_i8 v32, v45, s31, v32\n"
        "v_dot4_i32_i8 v33, v49, s31, v33\n"
        "s_waitcnt vmcnt(8)\n"
        "s_waitcnt vmcnt(6)\n"
        "v_perm_b32 v200, v204, v200, s30\n"
        "v_perm_b32 v208, v212, v208, s30\n"
        "v_perm_b32 v201, v205, v201, s30\n"
        "v_perm_b32 v209, v213, v209, s30\n"
        "v_perm_b32 v202, v206, v202, s30\n"
        "v_perm_b32 v210, v214, v210, s30\n"
        "v_perm_b32 v203, v207, v203, s30\n"
        "v_perm_b32 v211, v215, v211, s30\n"
        "v_lshl_or_b32 v38, v208, 16, v200\n"
        "v_lshl_or_b32 v42, v209, 16, v201\n"
        "v_lshl_or_b32 v46, v210, 16, v202\n"
        "v_lshl_or_b32 v50, v211, 16, v203\n"
        "v_dot4_i32_i8 v30, v38, s31, v30\n"
        "v_dot4_i32_i8 v31, v42, s31, v31\n"
        "v_dot4_i32_i8 v32, v46, s31, v32\n"
        "v_dot4_i32_i8 v33, v50, s31, v33\n"
        "s_waitcnt vmcnt(4)\n"
        "s_waitcnt vmcnt(2)\n"
        "v_perm_b32 v216, v220, v216, s30\n"
        "v_perm_b32 v224, v228, v224, s30\n"
        "v_perm_b32 v217, v221, v217, s30\n"
        "v_perm_b32 v225, v229, v225, s30\n"
        "v_perm_b32 v218, v222, v218, s30\n"
        "v_perm_b32 v226, v230, v226, s30\n"
        "v_perm_b32 v219, v223, v219, s30\n"
        "v_perm_b32 v227, v231, v227, s30\n"
        "v_lshl_or_b32 v39, v224, 16, v216\n"
        "v_lshl_or_b32 v43, v225, 16, v217\n"
        "v_lshl_or_b32 v47, v226, 16, v218\n"
        "v_lshl_or_b32 v51, v227, 16, v219\n"
        "ds_write_b32 v25, v20 offset:1544\n"
        "global_store_dwordx4 v19, v[36:39], s[44:45] offset:0 sc1\n"
        "global_store_dwordx4 v19, v[40:43], s[44:45] offset:1024 sc1\n"
        "global_store_dwordx4 v19, v[44:47], s[44:45] offset:2048 sc1\n"
        "global_store_dwordx4 v19, v[48:51], s[44:45] offset:3072 sc1\n"
        "v_dot4_i32_i8 v30, v39, s31, v30\n"
        "v_dot4_i32_i8 v31, v43, s31, v31\n"
        "v_dot4_i32_i8 v32, v47, s31, v32\n"
        "v_dot4_i32_i8 v33, v51, s31, v33\n"
        "s_nop 2\n"
        "v_permlane32_swap_b32 v30, v32\n"
        "v_permlane32_swap_b32 v31, v33\n"
        "s_nop 0\n"
        "v_add_u32 v30, v30, v32\n"
        "v_add_u32 v31, v31, v33\n"
        "s_nop 1\n"
        "v_permlane16_swap_b32 v30, v31\n"
        "s_nop 0\n"
        "v_add_u32 v30, v30, v31\n"
        "global_store_short v22, v30, s[46:47] offset:256 sc1\n"
        "s_mov_b32 exec_lo, 0x00ff00ff\n"
        "s_mov_b32 exec_hi, 0x00ff00ff\n"
        "global_store_short v23, v34, s[46:47] offset:384 sc1\n"
        "s_mov_b64 exec, 2\n"
        "s_waitcnt vmcnt(0) lgkmcnt(0)\n"
        "global_atomic_swap_x2 v24, v[26:27], s[8:9] sc1\n"
        "s_mov_b64 exec, -1\n"
        "s_load_dword s48, s[18:19], 0x0\n"
        "v_mov_b32 v32, v252\n"
        "v_mov_b32 v34, v248\n"
        "v_mov_b32 v35, v249\n"
        "v_mov_b32 v38, v250\n"
        "v_mov_b32 v39, v251\n"
        "s_lshl_b32 s24, s22, 13\n"
        "s_add_u32 s24, s24, 5771264\n"
        "v_add_u32 v21, s24, v19\n"
        "v_add_u32 v22, 8192, v21\n"
        "v_lshlrev_b32 v23, 5, v61\n"
        "v_add_u32 v23, 131072, v23\n"
        "s_waitcnt vmcnt(0)\n"
        "v_lshlrev_b32 v32, 16, v32\n"
        "v_and_b32 v37, 0xffff0000, v35\n"
        "v_lshlrev_b32 v36, 16, v35\n"
        "v_and_b32 v35, 0xffff0000, v34\n"
        "v_lshlrev_b32 v34, 16, v34\n"
        "v_and_b32 v41, 0xffff0000, v39\n"
        "v_lshlrev_b32 v40, 16, v39\n"
        "v_and_b32 v39, 0xffff0000, v38\n"
        "v_lshlrev_b32 v38, 16, v38\n"
        "v_mul_f32 v44, v32, v34\n"
        "v_mul_f32 v46, v32, v35\n"
        "v_mul_f32 v48, v32, v36\n"
        "v_mul_f32 v50, v32, v37\n"
        "s_mov_b32 s49, 6\n"
        "s_mov_b32 s50, 268435455\n"
        "s_mov_b32 s51, 268435455\n"
        "v_mov_b32 v33, 3\n"
        "W8_tab:\n"
        "s_mov_b64 exec, -1\n"
        "s_mov_b32 s38, 8388608\n"
        "W8_tr:\n"
        "ds_read_b32 v12, v25 offset:1024\n"
        "s_sub_u32 s38, s38, 1\n"
        "s_waitcnt lgkmcnt(0)\n"
        "s_cbranch_scc1 W8_tr_x\n"
        "v_readfirstlane_b32 s39, v12\n"
        "s_cmp_ge_u32 s39, 21\n"
        "s_cbranch_scc1 W8_tr_x\n"
        "s_sleep 2\n"
        "s_branch W8_tr\n"
        "W8_tr_x:\n"
        "s_mov_b32 s38, 8388608\n"
        "W8_tf:\n"
        "ds_read_b32 v12, v25 offset:0\n"
        "s_sub_u32 s38, s38, 1\n"
        "s_waitcnt lgkmcnt(0)\n"
        "s_cbranch_scc1 W8_tf_x\n"
        "v_readfirstlane_b32 s39, v12\n"
        "s_cmp_ge_u32 s39, 12\n"
        "s_cbranch_scc1 W8_tf_x\n"
        "s_sleep 2\n"
        "s_branch W8_tf\n"
        "W8_tf_x:\n"
        "s_mov_b64 exec, s[50:51]\n"
        "global_load_dwordx4 v[64:67], v21, s[8:9] offset:-4096 sc1\n"
        "global_load_dwordx4 v[68:71], v21, s[8:9] offset:-3072 sc1\n"
        "global_load_dwordx4 v[72:75], v21, s[8:9] offset:-2048 sc1\n"
        "global_load_dwordx4 v[76:79], v21, s[8:9] offset:-1024 sc1\n"
        "global_load_dwordx4 v[80:83], v21, s[8:9] offset:0 sc1\n"
        "global_load_dwordx4 v[84:87], v21, s[8:9] offset:1024 sc1\n"
        "global_load_dwordx4 v[88:91], v21, s[8:9] offset:2048 sc1\n"
        "global_load_dwordx4 v[92:95], v21, s[8:9] offset:3072 sc1\n"
        "s_waitcnt vmcnt(0)\n"
        "v_pk_add_i16 v64, v64, v68\n"
        "v_pk_add_i16 v65, v65, v69\n"
        "v_pk_add_i16 v66, v66, v70\n"
        "v_pk_add_i16 v67, v67, v71\n"
        "v_pk_add_i16 v72, v72, v76\n"
        "v_pk_add_i16 v73, v73, v77\n"
        "v_pk_add_i16 v74, v74, v78\n"
        "v_pk_add_i16 v75, v75, v79\n"
        "v_pk_add_i16 v80, v80, v84\n"
        "v_pk_add_i16 v81, v81, v85\n"
        "v_pk_add_i16 v82, v82, v86\n"
        "v_pk_add_i16 v83, v83, v87\n"
        "v_pk_add_i16 v88, v88, v92\n"
        "v_pk_add_i16 v89, v89, v93\n"
        "v_pk_add_i16 v90, v90, v94\n"
        "v_pk_add_i16 v91, v91, v95\n"
        "v_bfe_i32 v68, v64, 0, 16\n"
        "v_ashrrev_i32 v64, 16, v64\n"
        "v_bfe_i32 v69, v65, 0, 16\n"
        "v_ashrrev_i32 v65, 16, v65\n"
        "v_bfe_i32 v70, v66, 0, 16\n"
        "v_ashrrev_i32 v66, 16, v66\n"
        "v_bfe_i32 v71, v67, 0, 16\n"
        "v_ashrrev_i32 v67, 16, v67\n"
        "v_bfe_i32 v76, v72, 0, 16\n"
        "v_ashrrev_i32 v72, 16, v72\n"
        "v_bfe_i32 v77, v73, 0, 16\n"
        "v_ashrrev_i32 v73, 16, v73\n"
        "v_bfe_i32 v78, v74, 0, 16\n"
        "v_ashrrev_i32 v74, 16, v74\n"
        "v_bfe_i32 v79, v75, 0, 16\n"
        "v_ashrrev_i32 v75, 16, v75\n"
        "v_bfe_i32 v84, v80, 0, 16\n"
        "v_ashrrev_i32 v80, 16, v80\n"
        "v_bfe_i32 v85, v81, 0, 16\n"
        "v_ashrrev_i32 v81, 16, v81\n"
        "v_bfe_i32 v86, v82, 0, 16\n"
        "v_ashrrev_i32 v82, 16, v82\n"
        "v_bfe_i32 v87, v83, 0, 16\n"
        "v_ashrrev_i32 v83, 16, v83\n"
        "v_bfe_i32 v92, v88, 0, 16\n"
        "v_ashrrev_i32 v88, 16, v88\n"
        "v_bfe_i32 v93, v89, 0, 16\n"
        "v_ashrrev_i32 v89, 16, v89\n"
        "v_bfe_i32 v94, v90, 0, 16\n"
        "v_ashrrev_i32 v90, 16, v90\n"
        "v_bfe_i32 v95, v91, 0, 16\n"
        "v_ashrrev_i32 v91, 16, v91\n"
        "v_add3_u32 v100, v68, v76, v84\n"
        "v_add3_u32 v101, v64, v72, v80\n"
        "v_add3_u32 v102, v69, v77, v85\n"
        "v_add3_u32 v103, v65, v73, v81\n"
        "v_add3_u32 v104, v70, v78, v86\n"
        "v_add3_u32 v105, v66, v74, v82\n"
        "v_add3_u32 v106, v71, v79, v87\n"
        "v_add3_u32 v107, v67, v75, v83\n"
        "v_add_u32 v100, v100, v92\n"
        "v_add_u32 v101, v101, v88\n"
        "v_add_u32 v102, v102, v93\n"
        "v_add_u32 v103, v103, v89\n"
        "v_add_u32 v104, v104, v94\n"
        "v_add_u32 v105, v105, v90\n"
        "v_add_u32 v106, v106, v95\n"
        "v_add_u32 v107, v107, v91\n"
        "s_nop 1\n"
        "v_permlane32_swap_b32 v100, v104\n"
        "v_permlane32_swap_b32 v101, v105\n"
        "v_permlane32_swap_b32 v102, v106\n"
        "v_permlane32_swap_b32 v103, v107\n"
        "s_nop 0\n"
        "v_add_u32 v52, v100, v104\n"
        "v_add_u32 v53, v101, v105\n"
        "v_add_u32 v54, v102, v106\n"
        "v_add_u32 v55, v103, v107\n"
        "v_mul_lo_u32 v52, v52, s48\n"
        "v_mul_lo_u32 v53, v53, s48\n"
        "v_mul_lo_u32 v54, v54, s48\n"
        "v_mul_lo_u32 v55, v55, s48\n"
        "v_cvt_f32_i32 v52, v52\n"
        "v_cvt_f32_i32 v53, v53\n"
        "v_cvt_f32_i32 v54, v54\n"
        "v_cvt_f32_i32 v55, v55\n"
        "v_fma_f32 v45, -v52, v44, v38\n"
        "v_fma_f32 v47, -v53, v46, v39\n"
        "v_fma_f32 v49, -v54, v48, v40\n"
        "v_fma_f32 v51, -v55, v50, v41\n"
        "ds_write_b128 v23, v[44:47]\n"
        "ds_write_b128 v23, v[48:51] offset:16\n"
        "s_mov_b64 exec, -1\n"
        "ds_write_b32 v25, v33 offset:512\n"
        "v_add_u32 v33, 1, v33\n"
        "s_mov_b32 s50, 0\n"
        "s_mov_b32 s51, 0x00ffffff\n"
        "s_add_u32 s49, s49, 2\n"
        "s_cmp_eq_u32 s49, 6\n"
        "s_cbranch_scc1 W8_tab\n"
        "s_branch W8_fin\n"
        "W8_other:\n"
        "v_and_b32 v1, 63, v0\n"
        "s_add_u32 s33, s21, s22\n"
        "s_cmp_ge_u32 s20, 4\n"
        "s_cbranch_scc1 W8_comp\n"
        "v_lshlrev_b32 v25, 2, v1\n"
        "v_add_u32 v25, 133124, v25\n"
        "s_cmp_eq_u32 s20, 1\n"
        "s_cbranch_scc1 W8_load\n"
        "s_and_b32 s30, s20, 1\n"
        "s_lshl_b32 s24, s21, 9\n"
        "s_add_u32 s24, s24, 6029312\n"
        "s_lshl_b32 s25, s22, 10\n"
        "s_add_u32 s25, s25, 6291456\n"
        "s_add_u32 s26, s22, 16\n"
        "s_cmp_eq_u32 s30, 0\n"
        "s_cselect_b32 s24, s24, s25\n"
        "s_cselect_b32 s26, s21, s26\n"
        "s_cselect_b32 s31, s22, s21\n"
        "s_mul_i32 s27, s30, 768\n"
        "s_add_u32 s27, s27, 256\n"
        "s_lshl_b32 s26, s26, 7\n"
        "s_add_u32 s26, s26, 6815744\n"
        "v_mov_b32 v5, s26\n"
        "v_add_u32 v8, s27, v25\n"
        "s_lshl_b32 s26, s30, 7\n"
        "s_add_u32 s26, s26, 134408\n"
        "v_mov_b32 v12, s26\n"
        "s_add_u32 s24, s8, s24\n"
        "s_addc_u32 s25, s9, 0\n"
        "s_add_u32 s28, s30, 1\n"
        "v_mov_b32 v13, s28\n"
        "s_lshl_b32 s28, s30, 0\n"
        "s_add_u32 s28, s28, 4\n"
        "s_mov_b32 s38, 8388608\n"
        "W8_tg2:\n"
        "ds_read_b32 v14, v25 offset:1024\n"
        "s_sub_u32 s38, s38, 1\n"
        "s_waitcnt lgkmcnt(0)\n"
        "s_cbranch_scc1 W8_tg2_x\n"
        "v_readfirstlane_b32 s39, v14\n"
        "s_cmp_ge_u32 s39, 1\n"
        "s_cbranch_scc1 W8_tg2_x\n"
        "s_sleep 2\n"
        "s_branch W8_tg2\n"
        "W8_tg2_x:\n"
        "ds_read_b64 v[6:7], v12\n"
        "s_mov_b32 s38, 8388608\n"
        "W8_pst:\n"
        "ds_read_b32 v14, v25 offset:1544\n"
        "s_sub_u32 s38, s38, 1\n"
        "s_waitcnt lgkmcnt(0)\n"
        "s_cbranch_scc1 W8_pst_x\n"
        "v_readfirstlane_b32 s39, v14\n"
        "s_cmp_ge_u32 s39, 3\n"
        "s_cbranch_scc1 W8_pst_x\n"
        "s_sleep 4\n"
        "s_branch W8_pst\n"
        "W8_pst_x:\n"
        "s_add_u32 s14, s21, s22\n"
        "s_and_b32 s12, s14, 2\n"
        "s_lshl_b32 s12, s12, 5\n"
        "s_xor_b32 s13, s12, 64\n"
        "s_and_b32 s14, s14, 1\n"
        "v_add_u32 v16, 1, v13\n"
        "v_add_u32 v17, 2, v13\n"
        "v_add_u32 v18, 3, v13\n"
        "v_mov_b32 v15, v13\n"
        "s_mov_b32 s29, 2097152\n"
        "W8_pl:\n"
        "s_load_dwordx16 s[32:47], s[24:25], s12 glc\n"
        "s_load_dwordx16 s[48:63], s[24:25], s13 glc\n"
        "s_sub_u32 s29, s29, 1\n"
        "s_cselect_b32 s27, 1, 0\n"
        "s_cmp_eq_u32 s14, 1\n"
        "s_waitcnt lgkmcnt(0)\n"
        "s_cbranch_scc1 W8_plb\n"
        "v_cmpx_eq_u64 vcc, s[32:33], v[6:7]\n"
        "v_cmpx_eq_u64 vcc, s[34:35], v[6:7]\n"
        "v_cmpx_eq_u64 vcc, s[36:37], v[6:7]\n"
        "v_cmpx_eq_u64 vcc, s[38:39], v[6:7]\n"
        "s_nop 3\n"
        "ds_write_b32 v8, v15\n"
        "v_cmpx_eq_u64 vcc, s[40:41], v[6:7]\n"
        "v_cmpx_eq_u64 vcc, s[42:43], v[6:7]\n"
        "v_cmpx_eq_u64 vcc, s[44:45], v[6:7]\n"
        "v_cmpx_eq_u64 vcc, s[46:47], v[6:7]\n"
        "s_nop 3\n"
        "ds_write_b32 v8, v16\n"
        "v_cmpx_eq_u64 vcc, s[48:49], v[6:7]\n"
        "v_cmpx_eq_u64 vcc, s[50:51], v[6:7]\n"
        "v_cmpx_eq_u64 vcc, s[52:53], v[6:7]\n"
        "v_cmpx_eq_u64 vcc, s[54:55], v[6:7]\n"
        "s_nop 3\n"
        "ds_write_b32 v8, v17\n"
        "v_cmpx_eq_u64 vcc, s[56:57], v[6:7]\n"
        "v_cmpx_eq_u64 vcc, s[58:59], v[6:7]\n"
        "v_cmpx_eq_u64 vcc, s[60:61], v[6:7]\n"
        "v_cmpx_eq_u64 vcc, s[62:63], v[6:7]\n"
        "s_branch W8_plc\n"
        "W8_plb:\n"
        "v_cmpx_eq_u64 vcc, s[40:41], v[6:7]\n"
        "v_cmpx_eq_u64 vcc, s[42:43], v[6:7]\n"
        "v_cmpx_eq_u64 vcc, s[44:45], v[6:7]\n"
        "v_cmpx_eq_u64 vcc, s[46:47], v[6:7]\n"
        "s_nop 3\n"
        "ds_write_b32 v8, v15\n"
        "v_cmpx_eq_u64 vcc, s[32:33], v[6:7]\n"
        "v_cmpx_eq_u64 vcc, s[34:35], v[6:7]\n"
        "v_cmpx_eq_u64 vcc, s[36:37], v[6:7]\n"
        "v_cmpx_eq_u64 vcc, s[38:39], v[6:7]\n"
        "s_nop 3\n"
        "ds_write_b32 v8, v16\n"
        "v_cmpx_eq_u64 vcc, s[56:57], v[6:7]\n"
        "v_cmpx_eq_u64 vcc, s[58:59], v[6:7]\n"
        "v_cmpx_eq_u64 vcc, s[60:61], v[6:7]\n"
        "v_cmpx_eq_u64 vcc, s[62:63], v[6:7]\n"
        "s_nop 3\n"
        "ds_write_b32 v8, v17\n"
        "v_cmpx_eq_u64 vcc, s[48:49], v[6:7]\n"
        "v_cmpx_eq_u64 vcc, s[50:51], v[6:7]\n"
        "v_cmpx_eq_u64 vcc, s[52:53], v[6:7]\n"
        "v_cmpx_eq_u64 vcc, s[54:55], v[6:7]\n"
        "W8_plc:\n"
        "s_nop 4\n"
        "s_cbranch_execnz W8_pg\n"
        "s_mov_b64 exec, -1\n"
        "s_cmp_eq_u32 s27, 1\n"
        "s_cbranch_scc1 W8_pg\n"
        "s_sleep 1\n"
        "s_branch W8_pl\n"
        "W8_pg:\n"
        "ds_write_b32 v8, v18\n"
        "s_max_i32 s29, s29, 0\n"
        "v_add_u32 v15, 4, v15\n"
        "v_add_u32 v16, 4, v16\n"
        "v_add_u32 v17, 4, v17\n"
        "v_add_u32 v18, 4, v18\n"
        "s_add_u32 s24, s24, 128\n"
        "s_addc_u32 s25, s25, 0\n"
        "s_sub_u32 s28, s28, 1\n"
        "s_cmp_lg_u32 s28, 0\n"
        "s_cbranch_scc1 W8_pl\n"
        "s_cmp_lg_u32 s31, 0\n"
        "s_cbranch_scc1 W8_fin\n"
        "s_mov_b64 exec, 1\n"
        "global_store_dwordx2 v5, v[6:7], s[8:9] sc1\n"
        "s_branch W8_fin\n"
        "W8_load:\n"
        "v_lshlrev_b32 v2, 4, v1\n"
        "s_xor_b32 s24, s33, 0\n"
        "s_and_b32 s24, s24, 3\n"
        "s_lshl_b32 s25, s24, 13\n"
        "v_add_u32 v4, s25, v2\n"
        "v_add_u32 v8, 4096, v4\n"
        "s_mul_i32 s25, s24, 6144\n"
        "v_add_u32 v12, s25, v2\n"
        "s_xor_b32 s24, s33, 1\n"
        "s_and_b32 s24, s24, 3\n"
        "s_lshl_b32 s25, s24, 13\n"
        "v_add_u32 v5, s25, v2\n"
        "v_add_u32 v9, 4096, v5\n"
        "s_mul_i32 s25, s24, 6144\n"
        "v_add_u32 v13, s25, v2\n"
        "s_xor_b32 s24, s33, 2\n"
        "s_and_b32 s24, s24, 3\n"
        "s_lshl_b32 s25, s24, 13\n"
        "v_add_u32 v6, s25, v2\n"
        "v_add_u32 v10, 4096, v6\n"
        "s_mul_i32 s25, s24, 6144\n"
        "v_add_u32 v14, s25, v2\n"
        "s_xor_b32 s24, s33, 3\n"
        "s_and_b32 s24, s24, 3\n"
        "s_lshl_b32 s25, s24, 13\n"
        "v_add_u32 v7, s25, v2\n"
        "v_add_u32 v11, 4096, v7\n"
        "s_mul_i32 s25, s24, 6144\n"
        "v_add_u32 v15, s25, v2\n"
        "s_waitcnt lgkmcnt(0)\n"
        "s_lshl_b32 s24, s22, 17\n"
        "s_add_u32 s24, s24, 2097152\n"
        "s_add_u32 s40, s8, s24\n"
        "s_addc_u32 s41, s9, 0\n"
        "s_mul_i32 s24, s22, 98304\n"
        "s_add_u32 s24, s24, 4194304\n"
        "s_add_u32 s42, s8, s24\n"
        "s_addc_u32 s43, s9, 0\n"
        "s_lshr_b32 s37, s21, 2\n"
        "s_add_u32 s24, s21, s22\n"
        "s_xor_b32 s37, s37, s24\n"
        "s_and_b32 s37, s37, 3\n"
        "s_and_b32 s24, s21, 3\n"
        "s_lshl2_add_u32 s37, s37, s24\n"
        "s_lshl_b32 s37, 1, s37\n"
        "s_mov_b32 s34, 0\n"
        "s_mov_b32 s35, 2\n"
        "v_mov_b32 v3, -2\n"
        "s_mov_b32 s38, 8388608\n"
        "W8_lgo:\n"
        "ds_read_b32 v20, v25 offset:512\n"
        "s_sub_u32 s38, s38, 1\n"
        "s_waitcnt lgkmcnt(0)\n"
        "s_cbranch_scc1 W8_lgo_x\n"
        "v_readfirstlane_b32 s39, v20\n"
        "s_cmp_ge_u32 s39, 1\n"
        "s_cbranch_scc1 W8_lgo_x\n"
        "s_sleep 2\n"
        "s_branch W8_lgo\n"
        "W8_lgo_x:\n"
        "s_mov_b32 s38, 8388608\n"
        "W8_lr00:\n"
        "ds_read_b32 v20, v25 offset:1024\n"
        "s_sub_u32 s38, s38, 1\n"
        "s_waitcnt lgkmcnt(0)\n"
        "s_cbranch_scc1 W8_lr00_x\n"
        "v_readfirstlane_b32 s39, v20\n"
        "s_cmp_ge_u32 s39, 2\n"
        "s_cbranch_scc1 W8_lr00_x\n"
        "s_sleep 1\n"
        "s_branch W8_lr00\n"
        "W8_lr00_x:\n"
        "s_mov_b32 m0, s34\n"
        "s_add_u32 s34, s34, 4096\n"
        "s_bitcmp1_b32 s37, 0\n"
        "s_cselect_b64 exec, 0, -1\n"
        "global_load_lds_dwordx4 v4, s[40:41] offset:0 sc1\n"
        "global_load_lds_dwordx4 v4, s[40:41] offset:1024 sc1\n"
        "s_bitcmp1_b32 s37, 1\n"
        "s_cselect_b64 exec, 0, -1\n"
        "global_load_lds_dwordx4 v4, s[40:41] offset:2048 sc1\n"
        "global_load_lds_dwordx4 v4, s[40:41] offset:3072 sc1\n"
        "s_mov_b32 m0, s34\n"
        "s_add_u32 s34, s34, 4096\n"
        "s_bitcmp1_b32 s37, 2\n"
        "s_cselect_b64 exec, 0, -1\n"
        "global_load_lds_dwordx4 v8, s[40:41] offset:0 sc1\n"
        "global_load_lds_dwordx4 v8, s[40:41] offset:1024 sc1\n"
        "s_bitcmp1_b32 s37, 3\n"
        "s_cselect_b64 exec, 0, -1\n"
        "global_load_lds_dwordx4 v8, s[40:41] offset:2048 sc1\n"
        "global_load_lds_dwordx4 v8, s[40:41] offset:3072 sc1\n"
        "s_mov_b64 exec, -1\n"
        "s_cmp_ge_u32 s39, 3\n"
        "s_cbranch_scc1 W8_lg1_x\n"
        "ds_read_b32 v20, v25 offset:1024\n"
        "s_waitcnt lgkmcnt(0)\n"
        "v_readfirstlane_b32 s39, v20\n"
        "s_cmp_ge_u32 s39, 3\n"
        "s_cbranch_scc1 W8_lg1_x\n"
        "v_mov_b32 v21, 1\n"
        "s_waitcnt vmcnt(0)\n"
        "ds_max_i32 v25, v21 offset:0\n"
        "s_mov_b32 s38, 8388608\n"
        "W8_lg1:\n"
        "ds_read_b32 v20, v25 offset:1024\n"
        "s_sub_u32 s38, s38, 1\n"
        "s_waitcnt lgkmcnt(0)\n"
        "s_cbranch_scc1 W8_lg1_x\n"
        "v_readfirstlane_b32 s39, v20\n"
        "s_cmp_ge_u32 s39, 3\n"
        "s_cbranch_scc1 W8_lg1_x\n"
        "s_sleep 1\n"
        "s_branch W8_lg1\n"
        "W8_lg1_x:\n"
        "s_mov_b32 m0, s34\n"
        "s_add_u32 s34, s34, 4096\n"
        "s_bitcmp1_b32 s37, 4\n"
        "s_cselect_b64 exec, 0, -1\n"
        "global_load_lds_dwordx4 v5, s[40:41] offset:0 sc1\n"
        "global_load_lds_dwordx4 v5, s[40:41] offset:1024 sc1\n"
        "s_bitcmp1_b32 s37, 5\n"
        "s_cselect_b64 exec, 0, -1\n"
        "global_load_lds_dwordx4 v5, s[40:41] offset:2048 sc1\n"
        "global_load_lds_dwordx4 v5, s[40:41] offset:3072 sc1\n"
        "s_mov_b32 m0, s34\n"
        "s_add_u32 s34, s34, 4096\n"
        "s_bitcmp1_b32 s37, 6\n"
        "s_cselect_b64 exec, 0, -1\n"
        "global_load_lds_dwordx4 v9, s[40:41] offset:0 sc1\n"
        "global_load_lds_dwordx4 v9, s[40:41] offset:1024 sc1\n"
        "s_bitcmp1_b32 s37, 7\n"
        "s_cselect_b64 exec, 0, -1\n"
        "global_load_lds_dwordx4 v9, s[40:41] offset:2048 sc1\n"
        "global_load_lds_dwordx4 v9, s[40:41] offset:3072 sc1\n"
        "s_mov_b64 exec, -1\n"
        "v_mov_b32 v21, 1\n"
        "s_waitcnt vmcnt(8)\n"
        "ds_max_i32 v25, v21 offset:0\n"
        "s_cmp_ge_u32 s39, 4\n"
        "s_cbranch_scc1 W8_lg2_x\n"
        "ds_read_b32 v20, v25 offset:1024\n"
        "s_waitcnt lgkmcnt(0)\n"
        "v_readfirstlane_b32 s39, v20\n"
        "s_cmp_ge_u32 s39, 4\n"
        "s_cbranch_scc1 W8_lg2_x\n"
        "v_mov_b32 v21, 2\n"
        "s_waitcnt vmcnt(0)\n"
        "ds_max_i32 v25, v21 offset:0\n"
        "s_mov_b32 s38, 8388608\n"
        "W8_lg2:\n"
        "ds_read_b32 v20, v25 offset:1024\n"
        "s_sub_u32 s38, s38, 1\n"
        "s_waitcnt lgkmcnt(0)\n"
        "s_cbranch_scc1 W8_lg2_x\n"
        "v_readfirstlane_b32 s39, v20\n"
        "s_cmp_ge_u32 s39, 4\n"
        "s_cbranch_scc1 W8_lg2_x\n"
        "s_sleep 1\n"
        "s_branch W8_lg2\n"
        "W8_lg2_x:\n"
        "s_mov_b32 m0, s34\n"
        "s_add_u32 s34, s34, 4096\n"
        "s_bitcmp1_b32 s37, 8\n"
        "s_cselect_b64 exec, 0, -1\n"
        "global_load_lds_dwordx4 v6, s[40:41] offset:0 sc1\n"
        "global_load_lds_dwordx4 v6, s[40:41] offset:1024 sc1\n"
        "s_bitcmp1_b32 s37, 9\n"
        "s_cselect_b64 exec, 0, -1\n"
        "global_load_lds_dwordx4 v6, s[40:41] offset:2048 sc1\n"
        "global_load_lds_dwordx4 v6, s[40:41] offset:3072 sc1\n"
        "s_mov_b32 m0, s34\n"
        "s_add_u32 s34, s34, 4096\n"
        "s_bitcmp1_b32 s37, 10\n"
        "s_cselect_b64 exec, 0, -1\n"
        "global_load_lds_dwordx4 v10, s[40:41] offset:0 sc1\n"
        "global_load_lds_dwordx4 v10, s[40:41] offset:1024 sc1\n"
        "s_bitcmp1_b32 s37, 11\n"
        "s_cselect_b64 exec, 0, -1\n"
        "global_load_lds_dwordx4 v10, s[40:41] offset:2048 sc1\n"
        "global_load_lds_dwordx4 v10, s[40:41] offset:3072 sc1\n"
        "s_mov_b64 exec, -1\n"
        "s_cmp_ge_u32 s39, 5\n"
        "s_cbranch_scc1 W8_lg3_x\n"
        "ds_read_b32 v20, v25 offset:1024\n"
        "s_waitcnt lgkmcnt(0)\n"
        "v_readfirstlane_b32 s39, v20\n"
        "s_cmp_ge_u32 s39, 5\n"
        "s_cbranch_scc1 W8_lg3_x\n"
        "v_mov_b32 v21, 2\n"
        "s_waitcnt vmcnt(8)\n"
        "ds_max_i32 v25, v21 offset:0\n"
        "v_mov_b32 v21, 3\n"
        "s_waitcnt vmcnt(0)\n"
        "ds_max_i32 v25, v21 offset:0\n"
        "s_mov_b32 s38, 8388608\n"
        "W8_lg3:\n"
        "ds_read_b32 v20, v25 offset:1024\n"
        "s_sub_u32 s38, s38, 1\n"
        "s_waitcnt lgkmcnt(0)\n"
        "s_cbranch_scc1 W8_lg3_x\n"
        "v_readfirstlane_b32 s39, v20\n"
        "s_cmp_ge_u32 s39, 5\n"
        "s_cbranch_scc1 W8_lg3_x\n"
        "s_sleep 1\n"
        "s_branch W8_lg3\n"
        "W8_lg3_x:\n"
        "s_mov_b32 m0, s34\n"
        "s_add_u32 s34, s34, 4096\n"
        "s_bitcmp1_b32 s37, 12\n"
        "s_cselect_b64 exec, 0, -1\n"
        "global_load_lds_dwordx4 v7, s[40:41] offset:0 sc1\n"
        "global_load_lds_dwordx4 v7, s[40:41] offset:1024 sc1\n"
        "s_bitcmp1_b32 s37, 13\n"
        "s_cselect_b64 exec, 0, -1\n"
        "global_load_lds_dwordx4 v7, s[40:41] offset:2048 sc1\n"
        "global_load_lds_dwordx4 v7, s[40:41] offset:3072 sc1\n"
        "s_mov_b32 m0, s34\n"
        "s_add_u32 s34, s34, 4096\n"
        "s_bitcmp1_b32 s37, 14\n"
        "s_cselect_b64 exec, 0, -1\n"
        "global_load_lds_dwordx4 v11, s[40:41] offset:0 sc1\n"
        "global_load_lds_dwordx4 v11, s[40:41] offset:1024 sc1\n"
        "s_bitcmp1_b32 s37, 15\n"
        "s_cselect_b64 exec, 0, -1\n"
        "global_load_lds_dwordx4 v11, s[40:41] offset:2048 sc1\n"
        "global_load_lds_dwordx4 v11, s[40:41] offset:3072 sc1\n"
        "s_mov_b64 exec, -1\n"
        "v_mov_b32 v21, 2\n"
        "s_waitcnt vmcnt(16)\n"
        "ds_max_i32 v25, v21 offset:0\n"
        "v_mov_b32 v3, 2\n"
        "s_add_u32 s40, s40, 32768\n"
        "s_addc_u32 s41, s41, 0\n"
        "s_mov_b32 s35, 3\n"
        "s_mov_b32 s36, 6\n"
        "W8_lq:\n"
        "ds_read_b32 v20, v25 offset:1024\n"
        "s_waitcnt lgkmcnt(0)\n"
        "v_readfirstlane_b32 s39, v20\n"
        "s_cmp_ge_u32 s39, s36\n"
        "s_cbranch_scc1 W8_lr_x\n"
        "v_add_u32 v21, 1, v3\n"
        "s_waitcnt vmcnt(8)\n"
        "ds_max_i32 v25, v21 offset:0\n"
        "v_add_u32 v21, 2, v3\n"
        "s_waitcnt vmcnt(0)\n"
        "ds_max_i32 v25, v21 offset:0\n"
        "s_mov_b32 s38, 8388608\n"
        "W8_lr:\n"
        "ds_read_b32 v20, v25 offset:1024\n"
        "s_sub_u32 s38, s38, 1\n"
        "s_waitcnt lgkmcnt(0)\n"
        "s_cbranch_scc1 W8_lr_x\n"
        "v_readfirstlane_b32 s39, v20\n"
        "s_cmp_ge_u32 s39, s36\n"
        "s_cbranch_scc1 W8_lr_x\n"
        "s_sleep 1\n"
        "s_branch W8_lr\n"
        "W8_lr_x:\n"
        "s_mov_b32 m0, s34\n"
        "s_add_u32 s34, s34, 4096\n"
        "s_bitcmp1_b32 s37, 0\n"
        "s_cselect_b64 exec, 0, -1\n"
        "global_load_lds_dwordx4 v4, s[40:41] offset:0 sc1\n"
        "global_load_lds_dwordx4 v4, s[40:41] offset:1024 sc1\n"
        "s_bitcmp1_b32 s37, 1\n"
        "s_cselect_b64 exec, 0, -1\n"
        "global_load_lds_dwordx4 v4, s[40:41] offset:2048 sc1\n"
        "global_load_lds_dwordx4 v4, s[40:41] offset:3072 sc1\n"
        "s_mov_b32 m0, s34\n"
        "s_add_u32 s34, s34, 4096\n"
        "s_bitcmp1_b32 s37, 2\n"
        "s_cselect_b64 exec, 0, -1\n"
        "global_load_lds_dwordx4 v8, s[40:41] offset:0 sc1\n"
        "global_load_lds_dwordx4 v8, s[40:41] offset:1024 sc1\n"
        "s_bitcmp1_b32 s37, 3\n"
        "s_cselect_b64 exec, 0, -1\n"
        "global_load_lds_dwordx4 v8, s[40:41] offset:2048 sc1\n"
        "global_load_lds_dwordx4 v8, s[40:41] offset:3072 sc1\n"
        "s_mov_b64 exec, -1\n"
        "v_add_u32 v3, 1, v3\n"
        "s_waitcnt vmcnt(16)\n"
        "ds_max_i32 v25, v3 offset:0\n"
        "s_add_u32 s36, s36, 1\n"
        "s_cmp_ge_u32 s39, s36\n"
        "s_cbranch_scc1 W8_lu1_x\n"
        "ds_read_b32 v20, v25 offset:1024\n"
        "s_waitcnt lgkmcnt(0)\n"
        "v_readfirstlane_b32 s39, v20\n"
        "s_cmp_ge_u32 s39, s36\n"
        "s_cbranch_scc1 W8_lu1_x\n"
        "v_add_u32 v21, 1, v3\n"
        "s_waitcnt vmcnt(8)\n"
        "ds_max_i32 v25, v21 offset:0\n"
        "v_add_u32 v21, 2, v3\n"
        "s_waitcnt vmcnt(0)\n"
        "ds_max_i32 v25, v21 offset:0\n"
        "s_mov_b32 s38, 8388608\n"
        "W8_lu1:\n"
        "ds_read_b32 v20, v25 offset:1024\n"
        "s_sub_u32 s38, s38, 1\n"
        "s_waitcnt lgkmcnt(0)\n"
        "s_cbranch_scc1 W8_lu1_x\n"
        "v_readfirstlane_b32 s39, v20\n"
        "s_cmp_ge_u32 s39, s36\n"
        "s_cbranch_scc1 W8_lu1_x\n"
        "s_sleep 1\n"
        "s_branch W8_lu1\n"
        "W8_lu1_x:\n"
        "s_mov_b32 m0, s34\n"
        "s_add_u32 s34, s34, 4096\n"
        "s_bitcmp1_b32 s37, 4\n"
        "s_cselect_b64 exec, 0, -1\n"
        "global_load_lds_dwordx4 v5, s[40:41] offset:0 sc1\n"
        "global_load_lds_dwordx4 v5, s[40:41] offset:1024 sc1\n"
        "s_bitcmp1_b32 s37, 5\n"
        "s_cselect_b64 exec, 0, -1\n"
        "global_load_lds_dwordx4 v5, s[40:41] offset:2048 sc1\n"
        "global_load_lds_dwordx4 v5, s[40:41] offset:3072 sc1\n"
        "s_mov_b32 m0, s34\n"
        "s_add_u32 s34, s34, 4096\n"
        "s_bitcmp1_b32 s37, 6\n"
        "s_cselect_b64 exec, 0, -1\n"
        "global_load_lds_dwordx4 v9, s[40:41] offset:0 sc1\n"
        "global_load_lds_dwordx4 v9, s[40:41] offset:1024 sc1\n"
        "s_bitcmp1_b32 s37, 7\n"
        "s_cselect_b64 exec, 0, -1\n"
        "global_load_lds_dwordx4 v9, s[40:41] offset:2048 sc1\n"
        "global_load_lds_dwordx4 v9, s[40:41] offset:3072 sc1\n"
        "s_mov_b64 exec, -1\n"
        "v_add_u32 v3, 1, v3\n"
        "s_waitcnt vmcnt(16)\n"
        "ds_max_i32 v25, v3 offset:0\n"
        "s_add_u32 s36, s36, 1\n"
        "s_cmp_ge_u32 s39, s36\n"
        "s_cbranch_scc1 W8_lu2_x\n"
        "ds_read_b32 v20, v25 offset:1024\n"
        "s_waitcnt lgkmcnt(0)\n"
        "v_readfirstlane_b32 s39, v20\n"
        "s_cmp_ge_u32 s39, s36\n"
        "s_cbranch_scc1 W8_lu2_x\n"
        "v_add_u32 v21, 1, v3\n"
        "s_waitcnt vmcnt(8)\n"
        "ds_max_i32 v25, v21 offset:0\n"
        "v_add_u32 v21, 2, v3\n"
        "s_waitcnt vmcnt(0)\n"
        "ds_max_i32 v25, v21 offset:0\n"
        "s_mov_b32 s38, 8388608\n"
        "W8_lu2:\n"
        "ds_read_b32 v20, v25 offset:1024\n"
        "s_sub_u32 s38, s38, 1\n"
        "s_waitcnt lgkmcnt(0)\n"
        "s_cbranch_scc1 W8_lu2_x\n"
        "v_readfirstlane_b32 s39, v20\n"
        "s_cmp_ge_u32 s39, s36\n"
        "s_cbranch_scc1 W8_lu2_x\n"
        "s_sleep 1\n"
        "s_branch W8_lu2\n"
        "W8_lu2_x:\n"
        "s_mov_b32 m0, s34\n"
        "s_add_u32 s34, s34, 4096\n"
        "s_bitcmp1_b32 s37, 8\n"
        "s_cselect_b64 exec, 0, -1\n"
        "global_load_lds_dwordx4 v6, s[40:41] offset:0 sc1\n"
        "global_load_lds_dwordx4 v6, s[40:41] offset:1024 sc1\n"
        "s_bitcmp1_b32 s37, 9\n"
        "s_cselect_b64 exec, 0, -1\n"
        "global_load_lds_dwordx4 v6, s[40:41] offset:2048 sc1\n"
        "global_load_lds_dwordx4 v6, s[40:41] offset:3072 sc1\n"
        "s_mov_b32 m0, s34\n"
        "s_add_u32 s34, s34, 4096\n"
        "s_bitcmp1_b32 s37, 10\n"
        "s_cselect_b64 exec, 0, -1\n"
        "global_load_lds_dwordx4 v10, s[40:41] offset:0 sc1\n"
        "global_load_lds_dwordx4 v10, s[40:41] offset:1024 sc1\n"
        "s_bitcmp1_b32 s37, 11\n"
        "s_cselect_b64 exec, 0, -1\n"
        "global_load_lds_dwordx4 v10, s[40:41] offset:2048 sc1\n"
        "global_load_lds_dwordx4 v10, s[40:41] offset:3072 sc1\n"
        "s_mov_b64 exec, -1\n"
        "v_add_u32 v3, 1, v3\n"
        "s_waitcnt vmcnt(16)\n"
        "ds_max_i32 v25, v3 offset:0\n"
        "s_add_u32 s36, s36, 1\n"
        "s_cmp_ge_u32 s39, s36\n"
        "s_cbranch_scc1 W8_lu3_x\n"
        "ds_read_b32 v20, v25 offset:1024\n"
        "s_waitcnt lgkmcnt(0)\n"
        "v_readfirstlane_b32 s39, v20\n"
        "s_cmp_ge_u32 s39, s36\n"
        "s_cbranch_scc1 W8_lu3_x\n"
        "v_add_u32 v21, 1, v3\n"
        "s_waitcnt vmcnt(8)\n"
        "ds_max_i32 v25, v21 offset:0\n"
        "v_add_u32 v21, 2, v3\n"
        "s_waitcnt vmcnt(0)\n"
        "ds_max_i32 v25, v21 offset:0\n"
        "s_mov_b32 s38, 8388608\n"
        "W8_lu3:\n"
        "ds_read_b32 v20, v25 offset:1024\n"
        "s_sub_u32 s38, s38, 1\n"
        "s_waitcnt lgkmcnt(0)\n"
        "s_cbranch_scc1 W8_lu3_x\n"
        "v_readfirstlane_b32 s39, v20\n"
        "s_cmp_ge_u32 s39, s36\n"
        "s_cbranch_scc1 W8_lu3_x\n"
        "s_sleep 1\n"
        "s_branch W8_lu3\n"
        "W8_lu3_x:\n"
        "s_mov_b32 m0, s34\n"
        "s_add_u32 s34, s34, 4096\n"
        "s_bitcmp1_b32 s37, 12\n"
        "s_cselect_b64 exec, 0, -1\n"
        "global_load_lds_dwordx4 v7, s[40:41] offset:0 sc1\n"
        "global_load_lds_dwordx4 v7, s[40:41] offset:1024 sc1\n"
        "s_bitcmp1_b32 s37, 13\n"
        "s_cselect_b64 exec, 0, -1\n"
        "global_load_lds_dwordx4 v7, s[40:41] offset:2048 sc1\n"
        "global_load_lds_dwordx4 v7, s[40:41] offset:3072 sc1\n"
        "s_mov_b32 m0, s34\n"
        "s_add_u32 s34, s34, 4096\n"
        "s_bitcmp1_b32 s37, 14\n"
        "s_cselect_b64 exec, 0, -1\n"
        "global_load_lds_dwordx4 v11, s[40:41] offset:0 sc1\n"
        "global_load_lds_dwordx4 v11, s[40:41] offset:1024 sc1\n"
        "s_bitcmp1_b32 s37, 15\n"
        "s_cselect_b64 exec, 0, -1\n"
        "global_load_lds_dwordx4 v11, s[40:41] offset:2048 sc1\n"
        "global_load_lds_dwordx4 v11, s[40:41] offset:3072 sc1\n"
        "s_mov_b64 exec, -1\n"
        "v_add_u32 v3, 1, v3\n"
        "s_waitcnt vmcnt(16)\n"
        "ds_max_i32 v25, v3 offset:0\n"
        "s_add_u32 s40, s40, 32768\n"
        "s_addc_u32 s41, s41, 0\n"
        "s_add_u32 s36, s36, 1\n"
        "s_add_u32 s35, s35, 1\n"
        "s_cmp_lt_u32 s35, 6\n"
        "s_cbranch_scc1 W8_lq\n"
        "s_mov_b32 s34, 0\n"
        "ds_read_b32 v20, v25 offset:1024\n"
        "s_waitcnt lgkmcnt(0)\n"
        "v_readfirstlane_b32 s39, v20\n"
        "s_cmp_ge_u32 s39, 21\n"
        "s_cbranch_scc1 W8_lr0_x\n"
        "v_add_u32 v21, 1, v3\n"
        "s_waitcnt vmcnt(8)\n"
        "ds_max_i32 v25, v21 offset:0\n"
        "v_add_u32 v21, 2, v3\n"
        "s_waitcnt vmcnt(0)\n"
        "ds_max_i32 v25, v21 offset:0\n"
        "s_mov_b32 s38, 8388608\n"
        "W8_lr0:\n"
        "ds_read_b32 v20, v25 offset:1024\n"
        "s_sub_u32 s38, s38, 1\n"
        "s_waitcnt lgkmcnt(0)\n"
        "s_cbranch_scc1 W8_lr0_x\n"
        "v_readfirstlane_b32 s39, v20\n"
        "s_cmp_ge_u32 s39, 21\n"
        "s_cbranch_scc1 W8_lr0_x\n"
        "s_sleep 1\n"
        "s_branch W8_lr0\n"
        "W8_lr0_x:\n"
        "ds_read_b32 v20, v25 offset:768\n"
        "s_waitcnt lgkmcnt(0)\n"
        "v_readfirstlane_b32 s39, v20\n"
        "s_cmp_ge_u32 s39, 533504\n"
        "s_cbranch_scc1 W8_lf0_x\n"
        "v_add_u32 v21, 1, v3\n"
        "s_waitcnt vmcnt(8)\n"
        "ds_max_i32 v25, v21 offset:0\n"
        "v_add_u32 v21, 2, v3\n"
        "s_waitcnt vmcnt(0)\n"
        "ds_max_i32 v25, v21 offset:0\n"
        "s_mov_b32 s38, 8388608\n"
        "W8_lf0:\n"
        "ds_read_b32 v20, v25 offset:768\n"
        "s_sub_u32 s38, s38, 1\n"
        "s_waitcnt lgkmcnt(0)\n"
        "s_cbranch_scc1 W8_lf0_x\n"
        "v_readfirstlane_b32 s39, v20\n"
        "s_cmp_ge_u32 s39, 533504\n"
        "s_cbranch_scc1 W8_lf0_x\n"
        "s_sleep 1\n"
        "s_branch W8_lf0\n"
        "W8_lf0_x:\n"
        "s_add_u32 s40, s42, 4096\n"
        "s_addc_u32 s41, s43, 0\n"
        "W8_ll0:\n"
        "s_mov_b32 m0, s34\n"
        "s_add_u32 s34, s34, 2048\n"
        "global_load_lds_dwordx4 v12, s[40:41] sc1\n"
        "global_load_lds_dwordx4 v12, s[40:41] offset:1024 sc1\n"
        "s_mov_b32 m0, s34\n"
        "s_add_u32 s34, s34, 2048\n"
        "global_load_lds_dwordx4 v13, s[40:41] sc1\n"
        "global_load_lds_dwordx4 v13, s[40:41] offset:1024 sc1\n"
        "s_mov_b32 m0, s34\n"
        "s_add_u32 s34, s34, 2048\n"
        "global_load_lds_dwordx4 v14, s[40:41] sc1\n"
        "global_load_lds_dwordx4 v14, s[40:41] offset:1024 sc1\n"
        "s_mov_b32 m0, s34\n"
        "s_add_u32 s34, s34, 2048\n"
        "global_load_lds_dwordx4 v15, s[40:41] sc1\n"
        "global_load_lds_dwordx4 v15, s[40:41] offset:1024 sc1\n"
        "s_add_u32 s40, s40, 24576\n"
        "s_addc_u32 s41, s41, 0\n"
        "v_add_u32 v3, 1, v3\n"
        "s_waitcnt vmcnt(16)\n"
        "ds_max_i32 v25, v3 offset:0\n"
        "s_cmp_lt_u32 s34, 32768\n"
        "s_cbranch_scc1 W8_ll0\n"
        "v_add_u32 v3, -1, v3\n"
        "ds_read_b32 v20, v25 offset:772\n"
        "s_waitcnt lgkmcnt(0)\n"
        "v_readfirstlane_b32 s39, v20\n"
        "s_cmp_ge_u32 s39, 533504\n"
        "s_cbranch_scc1 W8_lf1_x\n"
        "v_add_u32 v21, 1, v3\n"
        "s_waitcnt vmcnt(16)\n"
        "ds_max_i32 v25, v21 offset:0\n"
        "v_add_u32 v21, 2, v3\n"
        "s_waitcnt vmcnt(8)\n"
        "ds_max_i32 v25, v21 offset:0\n"
        "v_add_u32 v21, 3, v3\n"
        "s_waitcnt vmcnt(0)\n"
        "ds_max_i32 v25, v21 offset:0\n"
        "s_mov_b32 s38, 8388608\n"
        "W8_lf1:\n"
        "ds_read_b32 v20, v25 offset:772\n"
        "s_sub_u32 s38, s38, 1\n"
        "s_waitcnt lgkmcnt(0)\n"
        "s_cbranch_scc1 W8_lf1_x\n"
        "v_readfirstlane_b32 s39, v20\n"
        "s_cmp_ge_u32 s39, 533504\n"
        "s_cbranch_scc1 W8_lf1_x\n"
        "s_sleep 1\n"
        "s_branch W8_lf1\n"
        "W8_lf1_x:\n"
        "s_add_u32 s40, s42, 0\n"
        "s_addc_u32 s41, s43, 0\n"
        "W8_ll1:\n"
        "s_mov_b32 m0, s34\n"
        "s_add_u32 s34, s34, 2048\n"
        "global_load_lds_dwordx4 v12, s[40:41] sc1\n"
        "global_load_lds_dwordx4 v12, s[40:41] offset:1024 sc1\n"
        "s_mov_b32 m0, s34\n"
        "s_add_u32 s34, s34, 2048\n"
        "global_load_lds_dwordx4 v13, s[40:41] sc1\n"
        "global_load_lds_dwordx4 v13, s[40:41] offset:1024 sc1\n"
        "s_mov_b32 m0, s34\n"
        "s_add_u32 s34, s34, 2048\n"
        "global_load_lds_dwordx4 v14, s[40:41] sc1\n"
        "global_load_lds_dwordx4 v14, s[40:41] offset:1024 sc1\n"
        "s_mov_b32 m0, s34\n"
        "s_add_u32 s34, s34, 2048\n"
        "global_load_lds_dwordx4 v15, s[40:41] sc1\n"
        "global_load_lds_dwordx4 v15, s[40:41] offset:1024 sc1\n"
        "s_add_u32 s40, s40, 24576\n"
        "s_addc_u32 s41, s41, 0\n"
        "v_add_u32 v3, 1, v3\n"
        "s_waitcnt vmcnt(24)\n"
        "ds_max_i32 v25, v3 offset:0\n"
        "s_cmp_lt_u32 s34, 65536\n"
        "s_cbranch_scc1 W8_ll1\n"
        "v_add_u32 v3, -1, v3\n"
        "ds_read_b32 v20, v25 offset:776\n"
        "s_waitcnt lgkmcnt(0)\n"
        "v_readfirstlane_b32 s39, v20\n"
        "s_cmp_ge_u32 s39, 533504\n"
        "s_cbranch_scc1 W8_lf2_x\n"
        "v_add_u32 v21, 1, v3\n"
        "s_waitcnt vmcnt(24)\n"
        "ds_max_i32 v25, v21 offset:0\n"
        "v_add_u32 v21, 2, v3\n"
        "s_waitcnt vmcnt(16)\n"
        "ds_max_i32 v25, v21 offset:0\n"
        "v_add_u32 v21, 3, v3\n"
        "s_waitcnt vmcnt(8)\n"
        "ds_max_i32 v25, v21 offset:0\n"
        "v_add_u32 v21, 4, v3\n"
        "s_waitcnt vmcnt(0)\n"
        "ds_max_i32 v25, v21 offset:0\n"
        "s_mov_b32 s38, 8388608\n"
        "W8_lf2:\n"
        "ds_read_b32 v20, v25 offset:776\n"
        "s_sub_u32 s38, s38, 1\n"
        "s_waitcnt lgkmcnt(0)\n"
        "s_cbranch_scc1 W8_lf2_x\n"
        "v_readfirstlane_b32 s39, v20\n"
        "s_cmp_ge_u32 s39, 533504\n"
        "s_cbranch_scc1 W8_lf2_x\n"
        "s_sleep 1\n"
        "s_branch W8_lf2\n"
        "W8_lf2_x:\n"
        "s_add_u32 s40, s42, 2048\n"
        "s_addc_u32 s41, s43, 0\n"
        "W8_ll2:\n"
        "s_mov_b32 m0, s34\n"
        "s_add_u32 s34, s34, 2048\n"
        "global_load_lds_dwordx4 v12, s[40:41] sc1\n"
        "global_load_lds_dwordx4 v12, s[40:41] offset:1024 sc1\n"
        "s_mov_b32 m0, s34\n"
        "s_add_u32 s34, s34, 2048\n"
        "global_load_lds_dwordx4 v13, s[40:41] sc1\n"
        "global_load_lds_dwordx4 v13, s[40:41] offset:1024 sc1\n"
        "s_mov_b32 m0, s34\n"
        "s_add_u32 s34, s34, 2048\n"
        "global_load_lds_dwordx4 v14, s[40:41] sc1\n"
        "global_load_lds_dwordx4 v14, s[40:41] offset:1024 sc1\n"
        "s_mov_b32 m0, s34\n"
        "s_add_u32 s34, s34, 2048\n"
        "global_load_lds_dwordx4 v15, s[40:41] sc1\n"
        "global_load_lds_dwordx4 v15, s[40:41] offset:1024 sc1\n"
        "s_add_u32 s40, s40, 24576\n"
        "s_addc_u32 s41, s41, 0\n"
        "v_add_u32 v3, 1, v3\n"
        "s_waitcnt vmcnt(32)\n"
        "ds_max_i32 v25, v3 offset:0\n"
        "s_cmp_lt_u32 s34, 98304\n"
        "s_cbranch_scc1 W8_ll2\n"
        "v_add_u32 v3, 1, v3\n"
        "s_waitcnt vmcnt(24)\n"
        "ds_max_i32 v25, v3 offset:0\n"
        "v_add_u32 v3, 1, v3\n"
        "s_waitcnt vmcnt(16)\n"
        "ds_max_i32 v25, v3 offset:0\n"
        "v_add_u32 v3, 1, v3\n"
        "s_waitcnt vmcnt(8)\n"
        "ds_max_i32 v25, v3 offset:0\n"
        "v_add_u32 v3, 1, v3\n"
        "s_waitcnt vmcnt(0)\n"
        "ds_max_i32 v25, v3 offset:0\n"
        "s_branch W8_fin\n"
        "W8_comp:\n"
        "s_setprio 3\n"
        "s_lshl_b32 s26, s21, 2\n"
        "s_add_u32 s26, s26, s20\n"
        "s_sub_u32 s26, s26, 4\n"
        "v_lshrrev_b32 v2, 4, v1\n"
        "v_and_b32 v3, 15, v1\n"
        "v_lshlrev_b32 v248, 11, v2\n"
        "v_lshl_add_u32 v248, v3, 4, v248\n"
        "v_add_u32 v249, 65536, v248\n"
        "v_mov_b32 v250, 133376\n"
        "v_lshlrev_b32 v240, 4, v1\n"
        "v_mul_u32_u24 v2, 28672, v2\n"
        "v_lshl_add_u32 v251, v3, 3, v2\n"
        "v_and_b32 v4, 7, v1\n"
        "v_lshl_add_u32 v252, v4, 3, v2\n"
        "v_bfe_u32 v4, v1, 3, 1\n"
        "v_lshl_add_u32 v252, v4, 2, v252\n"
        "s_mul_i32 s24, s22, 192\n"
        "s_sub_u32 s24, 6144, s24\n"
        "s_lshl_b32 s25, s22, 7\n"
        "s_sub_u32 s37, 3840, s25\n"
        "v_add_u32 v252, s24, v252\n"
        "v_lshlrev_b32 v253, 5, v3\n"
        "v_add_u32 v253, 131072, v253\n"
        "s_waitcnt lgkmcnt(0)\n"
        "s_lshl_b32 s27, s26, 15\n"
        "s_xor_b32 s24, s33, 0\n"
        "s_and_b32 s24, s24, 3\n"
        "s_lshl_b32 s24, s24, 11\n"
        "s_add_u32 s24, s24, s27\n"
        "s_add_u32 s56, s8, s24\n"
        "s_addc_u32 s57, s9, 0\n"
        "s_xor_b32 s24, s33, 1\n"
        "s_and_b32 s24, s24, 3\n"
        "s_lshl_b32 s24, s24, 11\n"
        "s_add_u32 s24, s24, s27\n"
        "s_add_u32 s58, s8, s24\n"
        "s_addc_u32 s59, s9, 0\n"
        "s_xor_b32 s24, s33, 2\n"
        "s_and_b32 s24, s24, 3\n"
        "s_lshl_b32 s24, s24, 11\n"
        "s_add_u32 s24, s24, s27\n"
        "s_add_u32 s60, s8, s24\n"
        "s_addc_u32 s61, s9, 0\n"
        "s_xor_b32 s24, s33, 3\n"
        "s_and_b32 s24, s24, 3\n"
        "s_lshl_b32 s24, s24, 11\n"
        "s_add_u32 s24, s24, s27\n"
        "s_add_u32 s62, s8, s24\n"
        "s_addc_u32 s63, s9, 0\n"
        "s_mul_i32 s24, s26, 229376\n"
        "s_mul_i32 s25, s22, 256\n"
        "s_add_u32 s24, s24, s25\n"
        "s_add_u32 s40, s10, s24\n"
        "s_addc_u32 s41, s11, 0\n"
        "s_add_u32 s42, s40, 7168\n"
        "s_addc_u32 s43, s41, 0\n"
        "s_add_u32 s44, s40, 14336\n"
        "s_addc_u32 s45, s41, 0\n"
        "s_add_u32 s46, s40, 21504\n"
        "s_addc_u32 s47, s41, 0\n"
        "s_add_u32 s48, s40, 114688\n"
        "s_addc_u32 s49, s41, 0\n"
        "s_add_u32 s50, s40, 121856\n"
        "s_addc_u32 s51, s41, 0\n"
        "s_add_u32 s52, s40, 129024\n"
        "s_addc_u32 s53, s41, 0\n"
        "s_add_u32 s54, s40, 136192\n"
        "s_addc_u32 s55, s41, 0\n"
        "s_lshr_b32 s16, s22, 2\n"
        "s_xor_b32 s16, s16, s33\n"
        "s_and_b32 s16, s16, 3\n"
        "s_and_b32 s24, s22, 3\n"
        "s_add_u32 s24, s24, 4\n"
        "s_cmp_eq_u32 s24, s20\n"
        "s_cselect_b32 s16, s16, 4\n"
        "s_cmp_eq_u32 s16, 0\n"
        "s_cselect_b64 s[4:5], 0, -1\n"
        "s_cmp_eq_u32 s16, 1\n"
        "s_cselect_b64 s[6:7], 0, -1\n"
        "s_cmp_eq_u32 s16, 2\n"
        "s_cselect_b64 s[12:13], 0, -1\n"
        "s_cmp_eq_u32 s16, 3\n"
        "s_cselect_b64 s[14:15], 0, -1\n"
        "v_lshrrev_b32 v242, 4, v1\n"
        "v_lshlrev_b32 v242, 4, v242\n"
        "v_lshl_add_u32 v242, v3, 6, v242\n"
        "v_add_u32 v242, 135232, v242\n"
        "s_sub_u32 s28, s20, 4\n"
        "s_mul_i32 s24, s21, 512\n"
        "s_mul_i32 s25, s22, 8\n"
        "s_add_u32 s24, s24, s25\n"
        "s_add_u32 s24, s24, 6029312\n"
        "s_mul_i32 s25, s22, 1024\n"
        "s_mul_i32 s27, s21, 8\n"
        "s_add_u32 s25, s25, s27\n"
        "s_add_u32 s25, s25, 6291456\n"
        "s_sub_u32 s25, s25, s24\n"
        "v_mul_lo_u32 v24, v1, s25\n"
        "s_mul_i32 s25, s28, 128\n"
        "s_add_u32 s24, s24, s25\n"
        "v_add_u32 v24, s24, v24\n"
        "s_mul_i32 s29, s28, 5120\n"
        "s_add_u32 s29, s29, 135232\n"
        "v_and_b32 v6, 7, v1\n"
        "v_lshrrev_b32 v7, 3, v1\n"
        "v_lshrrev_b32 v8, 4, v1\n"
        "v_lshl_add_u32 v8, v8, 4, s29\n"
        "v_lshl_add_u32 v8, v3, 6, v8\n"
        "s_lshr_b32 s24, s21, 2\n"
        "s_add_u32 s25, s21, s22\n"
        "s_xor_b32 s24, s24, s25\n"
        "s_and_b32 s24, s24, 3\n"
        "s_and_b32 s25, s21, 3\n"
        "s_lshl2_add_u32 s24, s24, s25\n"
        "s_lshl4_add_u32 s24, s28, s24\n"
        "s_lshl_b32 s24, s24, 11\n"
        "v_add_u32 v9, s24, v240\n"
        "s_and_b32 s24, s22, 3\n"
        "s_lshl2_add_u32 s24, s21, s24\n"
        "s_lshl2_add_u32 s24, s24, s28\n"
        "s_lshr_b32 s25, s22, 2\n"
        "s_lshl2_add_u32 s24, s24, s25\n"
        "s_lshl_b32 s24, s24, 11\n"
        "v_add_u32 v11, s24, v240\n"
        "v_lshrrev_b32 v10, 5, v1\n"
        "s_lshl_b32 s24, s22, 2\n"
        "s_add_u32 s24, s24, s28\n"
        "s_lshl_b32 s24, s24, 4\n"
        "s_add_u32 s24, s24, s21\n"
        "s_lshl_b32 s24, s24, 11\n"
        "s_add_u32 s24, s24, 2097152\n"
        "v_add_u32 v13, s24, v240\n"
        "s_lshl_b32 s24, s22, 4\n"
        "s_add_u32 s24, s24, s21\n"
        "s_lshl_b32 s24, s24, 9\n"
        "s_add_u32 s24, s24, 5767168\n"
        "v_and_b32 v14, 31, v1\n"
        "v_lshl_add_u32 v14, v14, 3, s24\n"
        "v_lshl_add_u32 v14, v10, 2, v14\n"
        "v_lshlrev_b32 v15, 3, v1\n"
        "v_add_u32 v15, 154944, v15\n"
        "s_add_u32 s28, s28, 1\n"
        "s_mov_b32 s38, 8388608\n"
        "W8_tgc:\n"
        "ds_read_b32 v12, v250 offset:772\n"
        "s_sub_u32 s38, s38, 1\n"
        "s_waitcnt lgkmcnt(0)\n"
        "s_cbranch_scc1 W8_tgc_x\n"
        "v_readfirstlane_b32 s39, v12\n"
        "s_cmp_ge_u32 s39, 1\n"
        "s_cbranch_scc1 W8_tgc_x\n"
        "s_sleep 1\n"
        "s_branch W8_tgc\n"
        "W8_tgc_x:\n"
        "v_lshrrev_b32 v12, 0, v1\n"
        "v_lshlrev_b32 v12, 7, v12\n"
        "v_add_u32 v12, 134408, v12\n"
        "s_mov_b64 exec, 3\n"
        "ds_read_b64 v[26:27], v12\n"
        "s_mov_b64 exec, -1\n"
        "s_mov_b32 s38, 8388608\n"
        "W8_pkc:\n"
        "ds_read_b32 v12, v250 offset:1292\n"
        "s_sub_u32 s38, s38, 1\n"
        "s_waitcnt lgkmcnt(0)\n"
        "s_cbranch_scc1 W8_pkc_x\n"
        "v_readfirstlane_b32 s39, v12\n"
        "s_cmp_ge_u32 s39, s28\n"
        "s_cbranch_scc1 W8_pkc_x\n"
        "s_sleep 1\n"
        "s_branch W8_pkc\n"
        "W8_pkc_x:\n"
        "ds_read_b128 v[28:31], v8\n"
        "ds_read_b128 v[32:35], v8 offset:1024\n"
        "ds_read_b128 v[36:39], v9 offset:0\n"
        "ds_read_b128 v[40:43], v9 offset:1024\n"
        "ds_read_b64 v[44:45], v15\n"
        "s_waitcnt lgkmcnt(3)\n"
        "global_store_dwordx4 v11, v[28:31], s[8:9] offset:0 sc1\n"
        "global_store_dwordx4 v11, v[32:35], s[8:9] offset:1024 sc1\n"
        "s_waitcnt lgkmcnt(0)\n"
        "global_store_dwordx4 v13, v[36:39], s[8:9] sc1\n"
        "global_store_dwordx4 v13, v[40:43], s[8:9] offset:1024 sc1\n"
        "s_cmp_lg_u32 s28, 4\n"
        "s_cbranch_scc1 W8_ncs\n"
        "global_store_dword v14, v44, s[8:9] sc1\n"
        "W8_ncs:\n"
        "s_mov_b64 exec, 3\n"
        "s_waitcnt vmcnt(0)\n"
        "global_atomic_swap_x2 v24, v[26:27], s[8:9] sc1\n"
        "s_mov_b64 exec, -1\n"
        "s_mov_b32 s38, 8388608\n"
        "W8_cgo:\n"
        "ds_read_b32 v254, v250 offset:260\n"
        "s_sub_u32 s38, s38, 1\n"
        "s_waitcnt lgkmcnt(0)\n"
        "s_cbranch_scc1 W8_cgo_x\n"
        "v_readfirstlane_b32 s39, v254\n"
        "s_cmp_ge_u32 s39, 1\n"
        "s_cbranch_scc1 W8_cgo_x\n"
        "s_sleep 2\n"
        "s_branch W8_cgo\n"
        "W8_cgo_x:\n"
        "s_cmp_lg_u32 s16, 0\n"
        "s_cbranch_scc1 W8_oa0\n"
        "ds_read_b128 v[0:3], v242 offset:0\n"
        "ds_read_b128 v[4:7], v242 offset:1024\n"
        "ds_read_b128 v[32:35], v242 offset:5120\n"
        "ds_read_b128 v[36:39], v242 offset:6144\n"
        "ds_read_b128 v[64:67], v242 offset:10240\n"
        "ds_read_b128 v[68:71], v242 offset:11264\n"
        "ds_read_b128 v[96:99], v242 offset:15360\n"
        "ds_read_b128 v[100:103], v242 offset:16384\n"
        "W8_oa0:\n"
        "s_cmp_lg_u32 s16, 1\n"
        "s_cbranch_scc1 W8_oa1\n"
        "ds_read_b128 v[8:11], v242 offset:0\n"
        "ds_read_b128 v[12:15], v242 offset:1024\n"
        "ds_read_b128 v[40:43], v242 offset:5120\n"
        "ds_read_b128 v[44:47], v242 offset:6144\n"
        "ds_read_b128 v[72:75], v242 offset:10240\n"
        "ds_read_b128 v[76:79], v242 offset:11264\n"
        "ds_read_b128 v[104:107], v242 offset:15360\n"
        "ds_read_b128 v[108:111], v242 offset:16384\n"
        "W8_oa1:\n"
        "s_cmp_lg_u32 s16, 2\n"
        "s_cbranch_scc1 W8_oa2\n"
        "ds_read_b128 v[16:19], v242 offset:0\n"
        "ds_read_b128 v[20:23], v242 offset:1024\n"
        "ds_read_b128 v[48:51], v242 offset:5120\n"
        "ds_read_b128 v[52:55], v242 offset:6144\n"
        "ds_read_b128 v[80:83], v242 offset:10240\n"
        "ds_read_b128 v[84:87], v242 offset:11264\n"
        "ds_read_b128 v[112:115], v242 offset:15360\n"
        "ds_read_b128 v[116:119], v242 offset:16384\n"
        "W8_oa2:\n"
        "s_cmp_lg_u32 s16, 3\n"
        "s_cbranch_scc1 W8_oa3\n"
        "ds_read_b128 v[24:27], v242 offset:0\n"
        "ds_read_b128 v[28:31], v242 offset:1024\n"
        "ds_read_b128 v[56:59], v242 offset:5120\n"
        "ds_read_b128 v[60:63], v242 offset:6144\n"
        "ds_read_b128 v[88:91], v242 offset:10240\n"
        "ds_read_b128 v[92:95], v242 offset:11264\n"
        "ds_read_b128 v[120:123], v242 offset:15360\n"
        "ds_read_b128 v[124:127], v242 offset:16384\n"
        "W8_oa3:\n"
        "s_mov_b32 s38, 8388608\n"
        "W8_rd0:\n"
        "ds_read_b32 v254, v250 offset:4\n"
        "s_sub_u32 s38, s38, 1\n"
        "s_waitcnt lgkmcnt(0)\n"
        "s_cbranch_scc1 W8_rd0_x\n"
        "v_readfirstlane_b32 s39, v254\n"
        "s_cmp_ge_u32 s39, 1\n"
        "s_cbranch_scc1 W8_rd0_x\n"
        "s_sleep 1\n"
        "s_branch W8_rd0\n"
        "W8_rd0_x:\n"
        "s_mov_b64 exec, s[4:5]\n"
        "global_load_dwordx4 v[0:3], v240, s[56:57] offset:0 sc1\n"
        "s_mov_b64 exec, -1\n"
        "s_mov_b64 exec, s[4:5]\n"
        "global_load_dwordx4 v[4:7], v240, s[56:57] offset:1024 sc1\n"
        "s_mov_b64 exec, -1\n"
        "s_sleep 2\n"
        "s_cmp_ge_u32 s39, 2\n"
        "s_cbranch_scc1 W8_rd1_x\n"
        "s_mov_b32 s38, 8388608\n"
        "W8_rd1:\n"
        "ds_read_b32 v254, v250 offset:4\n"
        "s_sub_u32 s38, s38, 1\n"
        "s_waitcnt lgkmcnt(0)\n"
        "s_cbranch_scc1 W8_rd1_x\n"
        "v_readfirstlane_b32 s39, v254\n"
        "s_cmp_ge_u32 s39, 2\n"
        "s_cbranch_scc1 W8_rd1_x\n"
        "s_sleep 1\n"
        "s_branch W8_rd1\n"
        "W8_rd1_x:\n"
        "s_mov_b64 exec, s[6:7]\n"
        "global_load_dwordx4 v[8:11], v240, s[58:59] offset:0 sc1\n"
        "s_mov_b64 exec, -1\n"
        "s_mov_b64 exec, s[6:7]\n"
        "global_load_dwordx4 v[12:15], v240, s[58:59] offset:1024 sc1\n"
        "s_mov_b64 exec, -1\n"
        "W8_dwx:\n"
        "ds_read_b32 v254, v250 offset:0\n"
        "s_waitcnt lgkmcnt(0)\n"
        "v_cmp_gt_u32 vcc, 1, v254\n"
        "s_cbranch_vccnz W8_s0\n"
        "W8_c0:\n"
        "ds_read_b32 v241, v250 offset:4\n"
        "ds_read_b128 v[208:211], v248 offset:0\n"
        "ds_read_b128 v[212:215], v248 offset:1024\n"
        "ds_read_b128 v[216:219], v248 offset:512\n"
        "ds_read_b128 v[220:223], v248 offset:1536\n"
        "ds_read_b128 v[224:227], v248 offset:256\n"
        "ds_read_b128 v[228:231], v248 offset:1280\n"
        "s_waitcnt vmcnt(2)\n"
        "s_waitcnt lgkmcnt(5)\n"
        "v_mfma_i32_16x16x64_i8 v[128:131], v[0:3], v[208:211], 0\n"
        "v_cmp_gt_u32 vcc, 4, v241\n"
        "s_cbranch_vccnz W8_sZ\n"
        "W8_cZ:\n"
        "ds_read_b32 v254, v250 offset:0\n"
        "v_mfma_i32_16x16x64_i8 v[132:135], v[4:7], v[208:211], 0\n"
        "ds_read_b128 v[232:235], v248 offset:768\n"
        "s_waitcnt lgkmcnt(6)\n"
        "v_mfma_i32_16x16x64_i8 v[136:139], v[0:3], v[212:215], 0\n"
        "s_mov_b64 exec, s[12:13]\n"
        "global_load_dwordx4 v[16:19], v240, s[60:61] offset:0 sc1\n"
        "s_mov_b64 exec, -1\n"
        "v_mfma_i32_16x16x64_i8 v[140:143], v[4:7], v[212:215], 0\n"
        "ds_read_b128 v[236:239], v248 offset:1792\n"
        "s_waitcnt lgkmcnt(2)\n"
        "v_cmp_gt_u32 vcc, 2, v254\n"
        "v_mfma_i32_16x16x64_i8 v[144:147], v[0:3], v[216:219], 0\n"
        "s_cbranch_vccnz W8_s1\n"
        "W8_c1:\n"
        "v_mfma_i32_16x16x64_i8 v[148:151], v[4:7], v[216:219], 0\n"
        "v_cmp_gt_u32 vcc, 5, v241\n"
        "ds_read_b128 v[208:211], v248 offset:8192\n"
        "v_mfma_i32_16x16x64_i8 v[152:155], v[0:3], v[220:223], 0\n"
        "s_cbranch_vccnz W8_sR0\n"
        "W8_cR0:\n"
        "ds_read_b32 v254, v250 offset:0\n"
        "v_mfma_i32_16x16x64_i8 v[156:159], v[4:7], v[220:223], 0\n"
        "ds_read_b128 v[212:215], v248 offset:9216\n"
        "v_mfma_i32_16x16x64_i8 v[160:163], v[0:3], v[224:227], 0\n"
        "ds_read_b32 v241, v250 offset:4\n"
        "s_mov_b64 exec, s[12:13]\n"
        "global_load_dwordx4 v[20:23], v240, s[60:61] offset:1024 sc1\n"
        "s_mov_b64 exec, -1\n"
        "v_mfma_i32_16x16x64_i8 v[164:167], v[4:7], v[224:227], 0\n"
        "ds_read_b128 v[216:219], v248 offset:8704\n"
        "v_mfma_i32_16x16x64_i8 v[168:171], v[0:3], v[228:231], 0\n"
        "s_mov_b64 exec, s[14:15]\n"
        "global_load_dwordx4 v[24:27], v240, s[62:63] offset:0 sc1\n"
        "s_mov_b64 exec, -1\n"
        "v_mfma_i32_16x16x64_i8 v[172:175], v[4:7], v[228:231], 0\n"
        "ds_read_b128 v[220:223], v248 offset:9728\n"
        "s_waitcnt lgkmcnt(7)\n"
        "v_mfma_i32_16x16x64_i8 v[176:179], v[0:3], v[232:235], 0\n"
        "s_mov_b64 exec, s[14:15]\n"
        "global_load_dwordx4 v[28:31], v240, s[62:63] offset:1024 sc1\n"
        "s_mov_b64 exec, -1\n"
        "v_add_u32 v240, 8192, v240\n"
        "v_mfma_i32_16x16x64_i8 v[180:183], v[4:7], v[232:235], 0\n"
        "ds_read_b128 v[224:227], v248 offset:8448\n"
        "s_waitcnt lgkmcnt(7)\n"
        "v_mfma_i32_16x16x64_i8 v[184:187], v[0:3], v[236:239], 0\n"
        "v_mfma_i32_16x16x64_i8 v[188:191], v[4:7], v[236:239], 0\n"
        "ds_read_b128 v[228:231], v248 offset:9472\n"
        "s_waitcnt vmcnt(4)\n"
        "s_waitcnt lgkmcnt(7)\n"
        "v_mfma_i32_16x16x64_i8 v[128:131], v[8:11], v[208:211], v[128:131]\n"
        "v_mfma_i32_16x16x64_i8 v[132:135], v[12:15], v[208:211], v[132:135]\n"
        "ds_read_b128 v[232:235], v248 offset:8960\n"
        "s_waitcnt lgkmcnt(6)\n"
        "v_mfma_i32_16x16x64_i8 v[136:139], v[8:11], v[212:215], v[136:139]\n"
        "s_mov_b64 exec, s[4:5]\n"
        "global_load_dwordx4 v[32:35], v240, s[56:57] offset:0 sc1\n"
        "s_mov_b64 exec, -1\n"
        "v_mfma_i32_16x16x64_i8 v[140:143], v[12:15], v[212:215], v[140:143]\n"
        "ds_read_b128 v[236:239], v248 offset:9984\n"
        "v_cmp_gt_u32 vcc, 3, v254\n"
        "s_waitcnt lgkmcnt(5)\n"
        "v_mfma_i32_16x16x64_i8 v[144:147], v[8:11], v[216:219], v[144:147]\n"
        "s_cbranch_vccnz W8_s2\n"
        "W8_c2:\n"
        "v_mfma_i32_16x16x64_i8 v[148:151], v[12:15], v[216:219], v[148:151]\n"
        "v_cmp_gt_u32 vcc, 6, v241\n"
        "ds_read_b128 v[208:211], v248 offset:16384\n"
        "s_waitcnt lgkmcnt(5)\n"
        "v_mfma_i32_16x16x64_i8 v[152:155], v[8:11], v[220:223], v[152:155]\n"
        "s_cbranch_vccnz W8_sR1\n"
        "W8_cR1:\n"
        "ds_read_b32 v254, v250 offset:0\n"
        "v_mfma_i32_16x16x64_i8 v[156:159], v[12:15], v[220:223], v[156:159]\n"
        "ds_read_b128 v[212:215], v248 offset:17408\n"
        "s_waitcnt lgkmcnt(6)\n"
        "v_mfma_i32_16x16x64_i8 v[160:163], v[8:11], v[224:227], v[160:163]\n"
        "ds_read_b32 v241, v250 offset:4\n"
        "s_mov_b64 exec, s[4:5]\n"
        "global_load_dwordx4 v[36:39], v240, s[56:57] offset:1024 sc1\n"
        "s_mov_b64 exec, -1\n"
        "v_mfma_i32_16x16x64_i8 v[164:167], v[12:15], v[224:227], v[164:167]\n"
        "ds_read_b128 v[216:219], v248 offset:16896\n"
        "s_waitcnt lgkmcnt(7)\n"
        "v_mfma_i32_16x16x64_i8 v[168:171], v[8:11], v[228:231], v[168:171]\n"
        "v_mfma_i32_16x16x64_i8 v[172:175], v[12:15], v[228:231], v[172:175]\n"
        "ds_read_b128 v[220:223], v248 offset:17920\n"
        "s_waitcnt lgkmcnt(7)\n"
        "v_mfma_i32_16x16x64_i8 v[176:179], v[8:11], v[232:235], v[176:179]\n"
        "v_mfma_i32_16x16x64_i8 v[180:183], v[12:15], v[232:235], v[180:183]\n"
        "ds_read_b128 v[224:227], v248 offset:16640\n"
        "s_waitcnt lgkmcnt(7)\n"
        "v_mfma_i32_16x16x64_i8 v[184:187], v[8:11], v[236:239], v[184:187]\n"
        "s_mov_b64 exec, s[6:7]\n"
        "global_load_dwordx4 v[40:43], v240, s[58:59] offset:0 sc1\n"
        "s_mov_b64 exec, -1\n"
        "v_mfma_i32_16x16x64_i8 v[188:191], v[12:15], v[236:239], v[188:191]\n"
        "ds_read_b128 v[228:231], v248 offset:17664\n"
        "s_waitcnt vmcnt(5)\n"
        "s_waitcnt lgkmcnt(7)\n"
        "v_mfma_i32_16x16x64_i8 v[128:131], v[16:19], v[208:211], v[128:131]\n"
        "v_mfma_i32_16x16x64_i8 v[132:135], v[20:23], v[208:211], v[132:135]\n"
        "ds_read_b128 v[232:235], v248 offset:17152\n"
        "s_waitcnt lgkmcnt(6)\n"
        "v_mfma_i32_16x16x64_i8 v[136:139], v[16:19], v[212:215], v[136:139]\n"
        "v_mfma_i32_16x16x64_i8 v[140:143], v[20:23], v[212:215], v[140:143]\n"
        "ds_read_b128 v[236:239], v248 offset:18176\n"
        "v_cmp_gt_u32 vcc, 4, v254\n"
        "s_waitcnt lgkmcnt(5)\n"
        "v_mfma_i32_16x16x64_i8 v[144:147], v[16:19], v[216:219], v[144:147]\n"
        "s_cbranch_vccnz W8_s3\n"
        "W8_c3:\n"
        "v_mfma_i32_16x16x64_i8 v[148:151], v[20:23], v[216:219], v[148:151]\n"
        "v_cmp_gt_u32 vcc, 7, v241\n"
        "ds_read_b128 v[208:211], v248 offset:24576\n"
        "s_waitcnt lgkmcnt(5)\n"
        "v_mfma_i32_16x16x64_i8 v[152:155], v[16:19], v[220:223], v[152:155]\n"
        "s_cbranch_vccnz W8_sR2\n"
        "W8_cR2:\n"
        "ds_read_b32 v254, v250 offset:0\n"
        "v_mfma_i32_16x16x64_i8 v[156:159], v[20:23], v[220:223], v[156:159]\n"
        "ds_read_b128 v[212:215], v248 offset:25600\n"
        "s_waitcnt lgkmcnt(6)\n"
        "v_mfma_i32_16x16x64_i8 v[160:163], v[16:19], v[224:227], v[160:163]\n"
        "ds_read_b32 v241, v250 offset:4\n"
        "s_mov_b64 exec, s[6:7]\n"
        "global_load_dwordx4 v[44:47], v240, s[58:59] offset:1024 sc1\n"
        "s_mov_b64 exec, -1\n"
        "v_mfma_i32_16x16x64_i8 v[164:167], v[20:23], v[224:227], v[164:167]\n"
        "ds_read_b128 v[216:219], v248 offset:25088\n"
        "s_waitcnt lgkmcnt(7)\n"
        "v_mfma_i32_16x16x64_i8 v[168:171], v[16:19], v[228:231], v[168:171]\n"
        "v_mfma_i32_16x16x64_i8 v[172:175], v[20:23], v[228:231], v[172:175]\n"
        "ds_read_b128 v[220:223], v248 offset:26112\n"
        "s_waitcnt lgkmcnt(7)\n"
        "v_mfma_i32_16x16x64_i8 v[176:179], v[16:19], v[232:235], v[176:179]\n"
        "v_mfma_i32_16x16x64_i8 v[180:183], v[20:23], v[232:235], v[180:183]\n"
        "ds_read_b128 v[224:227], v248 offset:24832\n"
        "s_waitcnt lgkmcnt(7)\n"
        "v_mfma_i32_16x16x64_i8 v[184:187], v[16:19], v[236:239], v[184:187]\n"
        "s_mov_b64 exec, s[12:13]\n"
        "global_load_dwordx4 v[48:51], v240, s[60:61] offset:0 sc1\n"
        "s_mov_b64 exec, -1\n"
        "v_mfma_i32_16x16x64_i8 v[188:191], v[20:23], v[236:239], v[188:191]\n"
        "ds_read_b128 v[228:231], v248 offset:25856\n"
        "s_waitcnt vmcnt(5)\n"
        "s_waitcnt lgkmcnt(7)\n"
        "v_mfma_i32_16x16x64_i8 v[128:131], v[24:27], v[208:211], v[128:131]\n"
        "v_mfma_i32_16x16x64_i8 v[132:135], v[28:31], v[208:211], v[132:135]\n"
        "ds_read_b128 v[232:235], v248 offset:25344\n"
        "s_waitcnt lgkmcnt(6)\n"
        "v_mfma_i32_16x16x64_i8 v[136:139], v[24:27], v[212:215], v[136:139]\n"
        "v_mfma_i32_16x16x64_i8 v[140:143], v[28:31], v[212:215], v[140:143]\n"
        "ds_read_b128 v[236:239], v248 offset:26368\n"
        "v_cmp_gt_u32 vcc, 5, v254\n"
        "s_waitcnt lgkmcnt(5)\n"
        "v_mfma_i32_16x16x64_i8 v[144:147], v[24:27], v[216:219], v[144:147]\n"
        "s_cbranch_vccnz W8_s4\n"
        "W8_c4:\n"
        "v_mfma_i32_16x16x64_i8 v[148:151], v[28:31], v[216:219], v[148:151]\n"
        "v_cmp_gt_u32 vcc, 8, v241\n"
        "ds_read_b128 v[208:211], v248 offset:32768\n"
        "s_waitcnt lgkmcnt(5)\n"
        "v_mfma_i32_16x16x64_i8 v[152:155], v[24:27], v[220:223], v[152:155]\n"
        "s_cbranch_vccnz W8_sR3\n"
        "W8_cR3:\n"
        "ds_read_b32 v254, v250 offset:0\n"
        "v_mfma_i32_16x16x64_i8 v[156:159], v[28:31], v[220:223], v[156:159]\n"
        "ds_read_b128 v[212:215], v248 offset:33792\n"
        "s_waitcnt lgkmcnt(6)\n"
        "v_mfma_i32_16x16x64_i8 v[160:163], v[24:27], v[224:227], v[160:163]\n"
        "ds_read_b32 v241, v250 offset:4\n"
        "s_mov_b64 exec, 1\n"
        "ds_add_u32 v250, v250 offset:516\n"
        "s_mov_b64 exec, -1\n"
        "v_mfma_i32_16x16x64_i8 v[164:167], v[28:31], v[224:227], v[164:167]\n"
        "ds_read_b128 v[216:219], v248 offset:33280\n"
        "s_waitcnt lgkmcnt(8)\n"
        "v_mfma_i32_16x16x64_i8 v[168:171], v[24:27], v[228:231], v[168:171]\n"
        "s_mov_b64 exec, s[12:13]\n"
        "global_load_dwordx4 v[52:55], v240, s[60:61] offset:1024 sc1\n"
        "s_mov_b64 exec, -1\n"
        "v_mfma_i32_16x16x64_i8 v[172:175], v[28:31], v[228:231], v[172:175]\n"
        "ds_read_b128 v[220:223], v248 offset:34304\n"
        "s_waitcnt lgkmcnt(8)\n"
        "v_mfma_i32_16x16x64_i8 v[176:179], v[24:27], v[232:235], v[176:179]\n"
        "v_mfma_i32_16x16x64_i8 v[180:183], v[28:31], v[232:235], v[180:183]\n"
        "ds_read_b128 v[224:227], v248 offset:33024\n"
        "s_waitcnt lgkmcnt(8)\n"
        "v_mfma_i32_16x16x64_i8 v[184:187], v[24:27], v[236:239], v[184:187]\n"
        "v_mfma_i32_16x16x64_i8 v[188:191], v[28:31], v[236:239], v[188:191]\n"
        "ds_read_b128 v[228:231], v248 offset:34048\n"
        "s_waitcnt vmcnt(4)\n"
        "s_waitcnt lgkmcnt(8)\n"
        "v_mfma_i32_16x16x64_i8 v[128:131], v[32:35], v[208:211], v[128:131]\n"
        "s_mov_b64 exec, s[14:15]\n"
        "global_load_dwordx4 v[56:59], v240, s[62:63] offset:0 sc1\n"
        "s_mov_b64 exec, -1\n"
        "v_mfma_i32_16x16x64_i8 v[132:135], v[36:39], v[208:211], v[132:135]\n"
        "ds_read_b128 v[232:235], v248 offset:33536\n"
        "s_waitcnt lgkmcnt(7)\n"
        "v_mfma_i32_16x16x64_i8 v[136:139], v[32:35], v[212:215], v[136:139]\n"
        "v_mfma_i32_16x16x64_i8 v[140:143], v[36:39], v[212:215], v[140:143]\n"
        "ds_read_b128 v[236:239], v248 offset:34560\n"
        "v_cmp_gt_u32 vcc, 6, v254\n"
        "s_waitcnt lgkmcnt(5)\n"
        "v_mfma_i32_16x16x64_i8 v[144:147], v[32:35], v[216:219], v[144:147]\n"
        "s_cbranch_vccnz W8_s5\n"
        "W8_c5:\n"
        "v_mfma_i32_16x16x64_i8 v[148:151], v[36:39], v[216:219], v[148:151]\n"
        "v_cmp_gt_u32 vcc, 9, v241\n"
        "ds_read_b128 v[208:211], v248 offset:40960\n"
        "s_waitcnt lgkmcnt(5)\n"
        "v_mfma_i32_16x16x64_i8 v[152:155], v[32:35], v[220:223], v[152:155]\n"
        "s_cbranch_vccnz W8_sR4\n"
        "W8_cR4:\n"
        "ds_read_b32 v254, v250 offset:0\n"
        "v_mfma_i32_16x16x64_i8 v[156:159], v[36:39], v[220:223], v[156:159]\n"
        "ds_read_b128 v[212:215], v248 offset:41984\n"
        "s_waitcnt lgkmcnt(6)\n"
        "v_mfma_i32_16x16x64_i8 v[160:163], v[32:35], v[224:227], v[160:163]\n"
        "ds_read_b32 v241, v250 offset:4\n"
        "s_mov_b64 exec, s[14:15]\n"
        "global_load_dwordx4 v[60:63], v240, s[62:63] offset:1024 sc1\n"
        "s_mov_b64 exec, -1\n"
        "v_add_u32 v240, 8192, v240\n"
        "v_mfma_i32_16x16x64_i8 v[164:167], v[36:39], v[224:227], v[164:167]\n"
        "ds_read_b128 v[216:219], v248 offset:41472\n"
        "s_waitcnt lgkmcnt(7)\n"
        "v_mfma_i32_16x16x64_i8 v[168:171], v[32:35], v[228:231], v[168:171]\n"
        "v_mfma_i32_16x16x64_i8 v[172:175], v[36:39], v[228:231], v[172:175]\n"
        "ds_read_b128 v[220:223], v248 offset:42496\n"
        "s_waitcnt lgkmcnt(7)\n"
        "v_mfma_i32_16x16x64_i8 v[176:179], v[32:35], v[232:235], v[176:179]\n"
        "v_mfma_i32_16x16x64_i8 v[180:183], v[36:39], v[232:235], v[180:183]\n"
        "ds_read_b128 v[224:227], v248 offset:41216\n"
        "s_waitcnt lgkmcnt(7)\n"
        "v_mfma_i32_16x16x64_i8 v[184:187], v[32:35], v[236:239], v[184:187]\n"
        "s_mov_b64 exec, s[4:5]\n"
        "global_load_dwordx4 v[64:67], v240, s[56:57] offset:0 sc1\n"
        "s_mov_b64 exec, -1\n"
        "v_mfma_i32_16x16x64_i8 v[188:191], v[36:39], v[236:239], v[188:191]\n"
        "ds_read_b128 v[228:231], v248 offset:42240\n"
        "s_waitcnt vmcnt(5)\n"
        "s_waitcnt lgkmcnt(7)\n"
        "v_mfma_i32_16x16x64_i8 v[128:131], v[40:43], v[208:211], v[128:131]\n"
        "v_mfma_i32_16x16x64_i8 v[132:135], v[44:47], v[208:211], v[132:135]\n"
        "ds_read_b128 v[232:235], v248 offset:41728\n"
        "s_waitcnt lgkmcnt(6)\n"
        "v_mfma_i32_16x16x64_i8 v[136:139], v[40:43], v[212:215], v[136:139]\n"
        "v_mfma_i32_16x16x64_i8 v[140:143], v[44:47], v[212:215], v[140:143]\n"
        "ds_read_b128 v[236:239], v248 offset:42752\n"
        "v_cmp_gt_u32 vcc, 7, v254\n"
        "s_waitcnt lgkmcnt(5)\n"
        "v_mfma_i32_16x16x64_i8 v[144:147], v[40:43], v[216:219], v[144:147]\n"
        "s_cbranch_vccnz W8_s6\n"
        "W8_c6:\n"
        "v_mfma_i32_16x16x64_i8 v[148:151], v[44:47], v[216:219], v[148:151]\n"
        "v_cmp_gt_u32 vcc, 10, v241\n"
        "ds_read_b128 v[208:211], v248 offset:49152\n"
        "s_waitcnt lgkmcnt(5)\n"
        "v_mfma_i32_16x16x64_i8 v[152:155], v[40:43], v[220:223], v[152:155]\n"
        "s_cbranch_vccnz W8_sR5\n"
        "W8_cR5:\n"
        "ds_read_b32 v254, v250 offset:0\n"
        "v_mfma_i32_16x16x64_i8 v[156:159], v[44:47], v[220:223], v[156:159]\n"
        "ds_read_b128 v[212:215], v248 offset:50176\n"
        "s_waitcnt lgkmcnt(6)\n"
        "v_mfma_i32_16x16x64_i8 v[160:163], v[40:43], v[224:227], v[160:163]\n"
        "ds_read_b32 v241, v250 offset:4\n"
        "s_mov_b64 exec, s[4:5]\n"
        "global_load_dwordx4 v[68:71], v240, s[56:57] offset:1024 sc1\n"
        "s_mov_b64 exec, -1\n"
        "v_mfma_i32_16x16x64_i8 v[164:167], v[44:47], v[224:227], v[164:167]\n"
        "ds_read_b128 v[216:219], v248 offset:49664\n"
        "s_waitcnt lgkmcnt(7)\n"
        "v_mfma_i32_16x16x64_i8 v[168:171], v[40:43], v[228:231], v[168:171]\n"
        "v_mfma_i32_16x16x64_i8 v[172:175], v[44:47], v[228:231], v[172:175]\n"
        "ds_read_b128 v[220:223], v248 offset:50688\n"
        "s_waitcnt lgkmcnt(7)\n"
        "v_mfma_i32_16x16x64_i8 v[176:179], v[40:43], v[232:235], v[176:179]\n"
        "v_mfma_i32_16x16x64_i8 v[180:183], v[44:47], v[232:235], v[180:183]\n"
        "ds_read_b128 v[224:227], v248 offset:49408\n"
        "s_waitcnt lgkmcnt(7)\n"
        "v_mfma_i32_16x16x64_i8 v[184:187], v[40:43], v[236:239], v[184:187]\n"
        "s_mov_b64 exec, s[6:7]\n"
        "global_load_dwordx4 v[72:75], v240, s[58:59] offset:0 sc1\n"
        "s_mov_b64 exec, -1\n"
        "v_mfma_i32_16x16x64_i8 v[188:191], v[44:47], v[236:239], v[188:191]\n"
        "ds_read_b128 v[228:231], v248 offset:50432\n"
        "s_waitcnt vmcnt(5)\n"
        "s_waitcnt lgkmcnt(7)\n"
        "v_mfma_i32_16x16x64_i8 v[128:131], v[48:51], v[208:211], v[128:131]\n"
        "v_mfma_i32_16x16x64_i8 v[132:135], v[52:55], v[208:211], v[132:135]\n"
        "ds_read_b128 v[232:235], v248 offset:49920\n"
        "s_waitcnt lgkmcnt(6)\n"
        "v_mfma_i32_16x16x64_i8 v[136:139], v[48:51], v[212:215], v[136:139]\n"
        "v_mfma_i32_16x16x64_i8 v[140:143], v[52:55], v[212:215], v[140:143]\n"
        "ds_read_b128 v[236:239], v248 offset:50944\n"
        "v_cmp_gt_u32 vcc, 8, v254\n"
        "s_waitcnt lgkmcnt(5)\n"
        "v_mfma_i32_16x16x64_i8 v[144:147], v[48:51], v[216:219], v[144:147]\n"
        "s_cbranch_vccnz W8_s7\n"
        "W8_c7:\n"
        "v_mfma_i32_16x16x64_i8 v[148:151], v[52:55], v[216:219], v[148:151]\n"
        "v_cmp_gt_u32 vcc, 11, v241\n"
        "ds_read_b128 v[208:211], v248 offset:57344\n"
        "s_waitcnt lgkmcnt(5)\n"
        "v_mfma_i32_16x16x64_i8 v[152:155], v[48:51], v[220:223], v[152:155]\n"
        "s_cbranch_vccnz W8_sR6\n"
        "W8_cR6:\n"
        "ds_read_b32 v254, v250 offset:0\n"
        "v_mfma_i32_16x16x64_i8 v[156:159], v[52:55], v[220:223], v[156:159]\n"
        "ds_read_b128 v[212:215], v248 offset:58368\n"
        "s_waitcnt lgkmcnt(6)\n"
        "v_mfma_i32_16x16x64_i8 v[160:163], v[48:51], v[224:227], v[160:163]\n"
        "ds_read_b32 v241, v250 offset:4\n"
        "s_mov_b64 exec, s[6:7]\n"
        "global_load_dwordx4 v[76:79], v240, s[58:59] offset:1024 sc1\n"
        "s_mov_b64 exec, -1\n"
        "v_mfma_i32_16x16x64_i8 v[164:167], v[52:55], v[224:227], v[164:167]\n"
        "ds_read_b128 v[216:219], v248 offset:57856\n"
        "s_waitcnt lgkmcnt(7)\n"
        "v_mfma_i32_16x16x64_i8 v[168:171], v[48:51], v[228:231], v[168:171]\n"
        "v_mfma_i32_16x16x64_i8 v[172:175], v[52:55], v[228:231], v[172:175]\n"
        "ds_read_b128 v[220:223], v248 offset:58880\n"
        "s_waitcnt lgkmcnt(7)\n"
        "v_mfma_i32_16x16x64_i8 v[176:179], v[48:51], v[232:235], v[176:179]\n"
        "v_mfma_i32_16x16x64_i8 v[180:183], v[52:55], v[232:235], v[180:183]\n"
        "ds_read_b128 v[224:227], v248 offset:57600\n"
        "s_waitcnt lgkmcnt(7)\n"
        "v_mfma_i32_16x16x64_i8 v[184:187], v[48:51], v[236:239], v[184:187]\n"
        "s_mov_b64 exec, s[12:13]\n"
        "global_load_dwordx4 v[80:83], v240, s[60:61] offset:0 sc1\n"
        "s_mov_b64 exec, -1\n"
        "v_mfma_i32_16x16x64_i8 v[188:191], v[52:55], v[236:239], v[188:191]\n"
        "ds_read_b128 v[228:231], v248 offset:58624\n"
        "s_waitcnt vmcnt(5)\n"
        "s_waitcnt lgkmcnt(7)\n"
        "v_mfma_i32_16x16x64_i8 v[128:131], v[56:59], v[208:211], v[128:131]\n"
        "v_mfma_i32_16x16x64_i8 v[132:135], v[60:63], v[208:211], v[132:135]\n"
        "ds_read_b128 v[232:235], v248 offset:58112\n"
        "s_waitcnt lgkmcnt(6)\n"
        "v_mfma_i32_16x16x64_i8 v[136:139], v[56:59], v[212:215], v[136:139]\n"
        "v_mfma_i32_16x16x64_i8 v[140:143], v[60:63], v[212:215], v[140:143]\n"
        "ds_read_b128 v[236:239], v248 offset:59136\n"
        "v_cmp_gt_u32 vcc, 9, v254\n"
        "s_waitcnt lgkmcnt(5)\n"
        "v_mfma_i32_16x16x64_i8 v[144:147], v[56:59], v[216:219], v[144:147]\n"
        "s_cbranch_vccnz W8_s8\n"
        "W8_c8:\n"
        "v_mfma_i32_16x16x64_i8 v[148:151], v[60:63], v[216:219], v[148:151]\n"
        "v_cmp_gt_u32 vcc, 12, v241\n"
        "ds_read_b128 v[208:211], v249 offset:0\n"
        "s_waitcnt lgkmcnt(5)\n"
        "v_mfma_i32_16x16x64_i8 v[152:155], v[56:59], v[220:223], v[152:155]\n"
        "s_cbranch_vccnz W8_sR7\n"
        "W8_cR7:\n"
        "ds_read_b32 v254, v250 offset:0\n"
        "v_mfma_i32_16x16x64_i8 v[156:159], v[60:63], v[220:223], v[156:159]\n"
        "ds_read_b128 v[212:215], v249 offset:1024\n"
        "s_waitcnt lgkmcnt(6)\n"
        "v_mfma_i32_16x16x64_i8 v[160:163], v[56:59], v[224:227], v[160:163]\n"
        "ds_read_b32 v241, v250 offset:4\n"
        "s_mov_b64 exec, 1\n"
        "ds_add_u32 v250, v250 offset:520\n"
        "s_mov_b64 exec, -1\n"
        "v_mfma_i32_16x16x64_i8 v[164:167], v[60:63], v[224:227], v[164:167]\n"
        "ds_read_b128 v[216:219], v249 offset:512\n"
        "s_waitcnt lgkmcnt(8)\n"
        "v_mfma_i32_16x16x64_i8 v[168:171], v[56:59], v[228:231], v[168:171]\n"
        "s_mov_b64 exec, s[12:13]\n"
        "global_load_dwordx4 v[84:87], v240, s[60:61] offset:1024 sc1\n"
        "s_mov_b64 exec, -1\n"
        "v_mfma_i32_16x16x64_i8 v[172:175], v[60:63], v[228:231], v[172:175]\n"
        "ds_read_b128 v[220:223], v249 offset:1536\n"
        "s_waitcnt lgkmcnt(8)\n"
        "v_mfma_i32_16x16x64_i8 v[176:179], v[56:59], v[232:235], v[176:179]\n"
        "v_mfma_i32_16x16x64_i8 v[180:183], v[60:63], v[232:235], v[180:183]\n"
        "ds_read_b128 v[224:227], v249 offset:256\n"
        "s_waitcnt lgkmcnt(8)\n"
        "v_mfma_i32_16x16x64_i8 v[184:187], v[56:59], v[236:239], v[184:187]\n"
        "v_mfma_i32_16x16x64_i8 v[188:191], v[60:63], v[236:239], v[188:191]\n"
        "ds_read_b128 v[228:231], v249 offset:1280\n"
        "s_waitcnt vmcnt(4)\n"
        "s_waitcnt lgkmcnt(8)\n"
        "v_mfma_i32_16x16x64_i8 v[128:131], v[64:67], v[208:211], v[128:131]\n"
        "s_mov_b64 exec, s[14:15]\n"
        "global_load_dwordx4 v[88:91], v240, s[62:63] offset:0 sc1\n"
        "s_mov_b64 exec, -1\n"
        "v_mfma_i32_16x16x64_i8 v[132:135], v[68:71], v[208:211], v[132:135]\n"
        "ds_read_b128 v[232:235], v249 offset:768\n"
        "s_waitcnt lgkmcnt(7)\n"
        "v_mfma_i32_16x16x64_i8 v[136:139], v[64:67], v[212:215], v[136:139]\n"
        "v_mfma_i32_16x16x64_i8 v[140:143], v[68:71], v[212:215], v[140:143]\n"
        "ds_read_b128 v[236:239], v249 offset:1792\n"
        "v_cmp_gt_u32 vcc, 10, v254\n"
        "s_waitcnt lgkmcnt(5)\n"
        "v_mfma_i32_16x16x64_i8 v[144:147], v[64:67], v[216:219], v[144:147]\n"
        "s_cbranch_vccnz W8_s9\n"
        "W8_c9:\n"
        "v_mfma_i32_16x16x64_i8 v[148:151], v[68:71], v[216:219], v[148:151]\n"
        "v_cmp_gt_u32 vcc, 13, v241\n"
        "ds_read_b128 v[208:211], v249 offset:8192\n"
        "s_waitcnt lgkmcnt(5)\n"
        "v_mfma_i32_16x16x64_i8 v[152:155], v[64:67], v[220:223], v[152:155]\n"
        "s_cbranch_vccnz W8_sR8\n"
        "W8_cR8:\n"
        "ds_read_b32 v254, v250 offset:0\n"
        "v_mfma_i32_16x16x64_i8 v[156:159], v[68:71], v[220:223], v[156:159]\n"
        "ds_read_b128 v[212:215], v249 offset:9216\n"
        "s_waitcnt lgkmcnt(6)\n"
        "v_mfma_i32_16x16x64_i8 v[160:163], v[64:67], v[224:227], v[160:163]\n"
        "ds_read_b32 v241, v250 offset:4\n"
        "s_mov_b64 exec, s[14:15]\n"
        "global_load_dwordx4 v[92:95], v240, s[62:63] offset:1024 sc1\n"
        "s_mov_b64 exec, -1\n"
        "v_add_u32 v240, 8192, v240\n"
        "v_mfma_i32_16x16x64_i8 v[164:167], v[68:71], v[224:227], v[164:167]\n"
        "ds_read_b128 v[216:219], v249 offset:8704\n"
        "s_waitcnt lgkmcnt(7)\n"
        "v_mfma_i32_16x16x64_i8 v[168:171], v[64:67], v[228:231], v[168:171]\n"
        "v_mfma_i32_16x16x64_i8 v[172:175], v[68:71], v[228:231], v[172:175]\n"
        "ds_read_b128 v[220:223], v249 offset:9728\n"
        "s_waitcnt lgkmcnt(7)\n"
        "v_mfma_i32_16x16x64_i8 v[176:179], v[64:67], v[232:235], v[176:179]\n"
        "v_mfma_i32_16x16x64_i8 v[180:183], v[68:71], v[232:235], v[180:183]\n"
        "ds_read_b128 v[224:227], v249 offset:8448\n"
        "s_waitcnt lgkmcnt(7)\n"
        "v_mfma_i32_16x16x64_i8 v[184:187], v[64:67], v[236:239], v[184:187]\n"
        "s_mov_b64 exec, s[4:5]\n"
        "global_load_dwordx4 v[96:99], v240, s[56:57] offset:0 sc1\n"
        "s_mov_b64 exec, -1\n"
        "v_mfma_i32_16x16x64_i8 v[188:191], v[68:71], v[236:239], v[188:191]\n"
        "ds_read_b128 v[228:231], v249 offset:9472\n"
        "s_waitcnt vmcnt(5)\n"
        "s_waitcnt lgkmcnt(7)\n"
        "v_mfma_i32_16x16x64_i8 v[128:131], v[72:75], v[208:211], v[128:131]\n"
        "v_mfma_i32_16x16x64_i8 v[132:135], v[76:79], v[208:211], v[132:135]\n"
        "ds_read_b128 v[232:235], v249 offset:8960\n"
        "s_waitcnt lgkmcnt(6)\n"
        "v_mfma_i32_16x16x64_i8 v[136:139], v[72:75], v[212:215], v[136:139]\n"
        "v_mfma_i32_16x16x64_i8 v[140:143], v[76:79], v[212:215], v[140:143]\n"
        "ds_read_b128 v[236:239], v249 offset:9984\n"
        "v_cmp_gt_u32 vcc, 11, v254\n"
        "s_waitcnt lgkmcnt(5)\n"
        "v_mfma_i32_16x16x64_i8 v[144:147], v[72:75], v[216:219], v[144:147]\n"
        "s_cbranch_vccnz W8_s10\n"
        "W8_c10:\n"
        "v_mfma_i32_16x16x64_i8 v[148:151], v[76:79], v[216:219], v[148:151]\n"
        "v_cmp_gt_u32 vcc, 14, v241\n"
        "ds_read_b128 v[208:211], v249 offset:16384\n"
        "s_waitcnt lgkmcnt(5)\n"
        "v_mfma_i32_16x16x64_i8 v[152:155], v[72:75], v[220:223], v[152:155]\n"
        "s_cbranch_vccnz W8_sR9\n"
        "W8_cR9:\n"
        "ds_read_b32 v254, v250 offset:0\n"
        "v_mfma_i32_16x16x64_i8 v[156:159], v[76:79], v[220:223], v[156:159]\n"
        "ds_read_b128 v[212:215], v249 offset:17408\n"
        "s_waitcnt lgkmcnt(6)\n"
        "v_mfma_i32_16x16x64_i8 v[160:163], v[72:75], v[224:227], v[160:163]\n"
        "ds_read_b32 v241, v250 offset:4\n"
        "s_mov_b64 exec, s[4:5]\n"
        "global_load_dwordx4 v[100:103], v240, s[56:57] offset:1024 sc1\n"
        "s_mov_b64 exec, -1\n"
        "v_mfma_i32_16x16x64_i8 v[164:167], v[76:79], v[224:227], v[164:167]\n"
        "ds_read_b128 v[216:219], v249 offset:16896\n"
        "s_waitcnt lgkmcnt(7)\n"
        "v_mfma_i32_16x16x64_i8 v[168:171], v[72:75], v[228:231], v[168:171]\n"
        "v_mfma_i32_16x16x64_i8 v[172:175], v[76:79], v[228:231], v[172:175]\n"
        "ds_read_b128 v[220:223], v249 offset:17920\n"
        "s_waitcnt lgkmcnt(7)\n"
        "v_mfma_i32_16x16x64_i8 v[176:179], v[72:75], v[232:235], v[176:179]\n"
        "v_mfma_i32_16x16x64_i8 v[180:183], v[76:79], v[232:235], v[180:183]\n"
        "ds_read_b128 v[224:227], v249 offset:16640\n"
        "s_waitcnt lgkmcnt(7)\n"
        "v_mfma_i32_16x16x64_i8 v[184:187], v[72:75], v[236:239], v[184:187]\n"
        "s_mov_b64 exec, s[6:7]\n"
        "global_load_dwordx4 v[104:107], v240, s[58:59] offset:0 sc1\n"
        "s_mov_b64 exec, -1\n"
        "v_mfma_i32_16x16x64_i8 v[188:191], v[76:79], v[236:239], v[188:191]\n"
        "ds_read_b128 v[228:231], v249 offset:17664\n"
        "s_waitcnt vmcnt(5)\n"
        "s_waitcnt lgkmcnt(7)\n"
        "v_mfma_i32_16x16x64_i8 v[128:131], v[80:83], v[208:211], v[128:131]\n"
        "v_mfma_i32_16x16x64_i8 v[132:135], v[84:87], v[208:211], v[132:135]\n"
        "ds_read_b128 v[232:235], v249 offset:17152\n"
        "s_waitcnt lgkmcnt(6)\n"
        "v_mfma_i32_16x16x64_i8 v[136:139], v[80:83], v[212:215], v[136:139]\n"
        "v_mfma_i32_16x16x64_i8 v[140:143], v[84:87], v[212:215], v[140:143]\n"
        "ds_read_b128 v[236:239], v249 offset:18176\n"
        "v_cmp_gt_u32 vcc, 12, v254\n"
        "s_waitcnt lgkmcnt(5)\n"
        "v_mfma_i32_16x16x64_i8 v[144:147], v[80:83], v[216:219], v[144:147]\n"
        "s_cbranch_vccnz W8_s11\n"
        "W8_c11:\n"
        "v_mfma_i32_16x16x64_i8 v[148:151], v[84:87], v[216:219], v[148:151]\n"
        "v_cmp_gt_u32 vcc, 15, v241\n"
        "ds_read_b128 v[208:211], v249 offset:24576\n"
        "s_waitcnt lgkmcnt(5)\n"
        "v_mfma_i32_16x16x64_i8 v[152:155], v[80:83], v[220:223], v[152:155]\n"
        "s_cbranch_vccnz W8_sR10\n"
        "W8_cR10:\n"
        "ds_read_b32 v254, v250 offset:0\n"
        "v_mfma_i32_16x16x64_i8 v[156:159], v[84:87], v[220:223], v[156:159]\n"
        "ds_read_b128 v[212:215], v249 offset:25600\n"
        "s_waitcnt lgkmcnt(6)\n"
        "v_mfma_i32_16x16x64_i8 v[160:163], v[80:83], v[224:227], v[160:163]\n"
        "ds_read_b32 v241, v250 offset:4\n"
        "s_mov_b64 exec, s[6:7]\n"
        "global_load_dwordx4 v[108:111], v240, s[58:59] offset:1024 sc1\n"
        "s_mov_b64 exec, -1\n"
        "v_mfma_i32_16x16x64_i8 v[164:167], v[84:87], v[224:227], v[164:167]\n"
        "ds_read_b128 v[216:219], v249 offset:25088\n"
        "s_waitcnt lgkmcnt(7)\n"
        "v_mfma_i32_16x16x64_i8 v[168:171], v[80:83], v[228:231], v[168:171]\n"
        "v_mfma_i32_16x16x64_i8 v[172:175], v[84:87], v[228:231], v[172:175]\n"
        "ds_read_b128 v[220:223], v249 offset:26112\n"
        "s_waitcnt lgkmcnt(7)\n"
        "v_mfma_i32_16x16x64_i8 v[176:179], v[80:83], v[232:235], v[176:179]\n"
        "v_mfma_i32_16x16x64_i8 v[180:183], v[84:87], v[232:235], v[180:183]\n"
        "ds_read_b128 v[224:227], v249 offset:24832\n"
        "s_waitcnt lgkmcnt(7)\n"
        "v_mfma_i32_16x16x64_i8 v[184:187], v[80:83], v[236:239], v[184:187]\n"
        "s_mov_b64 exec, s[12:13]\n"
        "global_load_dwordx4 v[112:115], v240, s[60:61] offset:0 sc1\n"
        "s_mov_b64 exec, -1\n"
        "v_mfma_i32_16x16x64_i8 v[188:191], v[84:87], v[236:239], v[188:191]\n"
        "ds_read_b128 v[228:231], v249 offset:25856\n"
        "s_waitcnt vmcnt(5)\n"
        "s_waitcnt lgkmcnt(7)\n"
        "v_mfma_i32_16x16x64_i8 v[128:131], v[88:91], v[208:211], v[128:131]\n"
        "v_mfma_i32_16x16x64_i8 v[132:135], v[92:95], v[208:211], v[132:135]\n"
        "ds_read_b128 v[232:235], v249 offset:25344\n"
        "s_waitcnt lgkmcnt(6)\n"
        "v_mfma_i32_16x16x64_i8 v[136:139], v[88:91], v[212:215], v[136:139]\n"
        "v_mfma_i32_16x16x64_i8 v[140:143], v[92:95], v[212:215], v[140:143]\n"
        "ds_read_b128 v[236:239], v249 offset:26368\n"
        "v_cmp_gt_u32 vcc, 13, v254\n"
        "s_waitcnt lgkmcnt(5)\n"
        "v_mfma_i32_16x16x64_i8 v[144:147], v[88:91], v[216:219], v[144:147]\n"
        "s_cbranch_vccnz W8_s12\n"
        "W8_c12:\n"
        "v_mfma_i32_16x16x64_i8 v[148:151], v[92:95], v[216:219], v[148:151]\n"
        "v_cmp_gt_u32 vcc, 16, v241\n"
        "ds_read_b128 v[208:211], v249 offset:32768\n"
        "s_waitcnt lgkmcnt(5)\n"
        "v_mfma_i32_16x16x64_i8 v[152:155], v[88:91], v[220:223], v[152:155]\n"
        "s_cbranch_vccnz W8_sR11\n"
        "W8_cR11:\n"
        "ds_read_b32 v254, v250 offset:0\n"
        "v_mfma_i32_16x16x64_i8 v[156:159], v[92:95], v[220:223], v[156:159]\n"
        "ds_read_b128 v[212:215], v249 offset:33792\n"
        "s_waitcnt lgkmcnt(6)\n"
        "v_mfma_i32_16x16x64_i8 v[160:163], v[88:91], v[224:227], v[160:163]\n"
        "s_mov_b64 exec, 1\n"
        "ds_add_u32 v250, v250 offset:524\n"
        "s_mov_b64 exec, -1\n"
        "v_mfma_i32_16x16x64_i8 v[164:167], v[92:95], v[224:227], v[164:167]\n"
        "ds_read_b128 v[216:219], v249 offset:33280\n"
        "s_waitcnt lgkmcnt(7)\n"
        "v_mfma_i32_16x16x64_i8 v[168:171], v[88:91], v[228:231], v[168:171]\n"
        "s_mov_b64 exec, s[12:13]\n"
        "global_load_dwordx4 v[116:119], v240, s[60:61] offset:1024 sc1\n"
        "s_mov_b64 exec, -1\n"
        "v_mfma_i32_16x16x64_i8 v[172:175], v[92:95], v[228:231], v[172:175]\n"
        "ds_read_b128 v[220:223], v249 offset:34304\n"
        "s_waitcnt lgkmcnt(7)\n"
        "v_mfma_i32_16x16x64_i8 v[176:179], v[88:91], v[232:235], v[176:179]\n"
        "v_mfma_i32_16x16x64_i8 v[180:183], v[92:95], v[232:235], v[180:183]\n"
        "ds_read_b128 v[224:227], v249 offset:33024\n"
        "s_waitcnt lgkmcnt(7)\n"
        "v_mfma_i32_16x16x64_i8 v[184:187], v[88:91], v[236:239], v[184:187]\n"
        "v_mfma_i32_16x16x64_i8 v[188:191], v[92:95], v[236:239], v[188:191]\n"
        "ds_read_b128 v[228:231], v249 offset:34048\n"
        "s_waitcnt vmcnt(4)\n"
        "s_waitcnt lgkmcnt(7)\n"
        "v_mfma_i32_16x16x64_i8 v[128:131], v[96:99], v[208:211], v[128:131]\n"
        "s_mov_b64 exec, s[14:15]\n"
        "global_load_dwordx4 v[120:123], v240, s[62:63] offset:0 sc1\n"
        "s_mov_b64 exec, -1\n"
        "v_mfma_i32_16x16x64_i8 v[132:135], v[100:103], v[208:211], v[132:135]\n"
        "ds_read_b128 v[232:235], v249 offset:33536\n"
        "s_waitcnt lgkmcnt(6)\n"
        "v_mfma_i32_16x16x64_i8 v[136:139], v[96:99], v[212:215], v[136:139]\n"
        "v_and_b32 v248, 0x3ff, v240\n"
        "v_mfma_i32_16x16x64_i8 v[140:143], v[100:103], v[212:215], v[140:143]\n"
        "ds_read_b128 v[236:239], v249 offset:34560\n"
        "v_cmp_gt_u32 vcc, 14, v254\n"
        "s_waitcnt lgkmcnt(5)\n"
        "v_mfma_i32_16x16x64_i8 v[144:147], v[96:99], v[216:219], v[144:147]\n"
        "s_cbranch_vccnz W8_s13\n"
        "W8_c13:\n"
        "v_mfma_i32_16x16x64_i8 v[148:151], v[100:103], v[216:219], v[148:151]\n"
        "ds_read_b128 v[208:211], v249 offset:40960\n"
        "s_waitcnt lgkmcnt(5)\n"
        "v_mfma_i32_16x16x64_i8 v[152:155], v[96:99], v[220:223], v[152:155]\n"
        "ds_read_b32 v254, v250 offset:0\n"
        "v_mfma_i32_16x16x64_i8 v[156:159], v[100:103], v[220:223], v[156:159]\n"
        "ds_read_b128 v[212:215], v249 offset:41984\n"
        "s_waitcnt lgkmcnt(6)\n"
        "v_mfma_i32_16x16x64_i8 v[160:163], v[96:99], v[224:227], v[160:163]\n"
        "s_mov_b64 exec, s[14:15]\n"
        "global_load_dwordx4 v[124:127], v240, s[62:63] offset:1024 sc1\n"
        "s_mov_b64 exec, -1\n"
        "v_add_u32 v240, 8192, v240\n"
        "v_mfma_i32_16x16x64_i8 v[164:167], v[100:103], v[224:227], v[164:167]\n"
        "ds_read_b128 v[216:219], v249 offset:41472\n"
        "s_waitcnt lgkmcnt(6)\n"
        "v_mfma_i32_16x16x64_i8 v[168:171], v[96:99], v[228:231], v[168:171]\n"
        "v_mfma_i32_16x16x64_i8 v[172:175], v[100:103], v[228:231], v[172:175]\n"
        "ds_read_b128 v[220:223], v249 offset:42496\n"
        "s_waitcnt lgkmcnt(6)\n"
        "v_mfma_i32_16x16x64_i8 v[176:179], v[96:99], v[232:235], v[176:179]\n"
        "v_mfma_i32_16x16x64_i8 v[180:183], v[100:103], v[232:235], v[180:183]\n"
        "ds_read_b128 v[224:227], v249 offset:41216\n"
        "s_waitcnt lgkmcnt(6)\n"
        "v_mfma_i32_16x16x64_i8 v[184:187], v[96:99], v[236:239], v[184:187]\n"
        "v_mfma_i32_16x16x64_i8 v[188:191], v[100:103], v[236:239], v[188:191]\n"
        "ds_read_b128 v[228:231], v249 offset:42240\n"
        "s_waitcnt vmcnt(4)\n"
        "s_waitcnt lgkmcnt(6)\n"
        "v_mfma_i32_16x16x64_i8 v[128:131], v[104:107], v[208:211], v[128:131]\n"
        "v_mfma_i32_16x16x64_i8 v[132:135], v[108:111], v[208:211], v[132:135]\n"
        "ds_read_b128 v[232:235], v249 offset:41728\n"
        "s_waitcnt lgkmcnt(5)\n"
        "v_mfma_i32_16x16x64_i8 v[136:139], v[104:107], v[212:215], v[136:139]\n"
        "v_mfma_i32_16x16x64_i8 v[140:143], v[108:111], v[212:215], v[140:143]\n"
        "ds_read_b128 v[236:239], v249 offset:42752\n"
        "v_cmp_gt_u32 vcc, 15, v254\n"
        "s_waitcnt lgkmcnt(5)\n"
        "v_mfma_i32_16x16x64_i8 v[144:147], v[104:107], v[216:219], v[144:147]\n"
        "s_cbranch_vccnz W8_s14\n"
        "W8_c14:\n"
        "v_mfma_i32_16x16x64_i8 v[148:151], v[108:111], v[216:219], v[148:151]\n"
        "ds_read_b128 v[208:211], v249 offset:49152\n"
        "s_waitcnt lgkmcnt(5)\n"
        "v_mfma_i32_16x16x64_i8 v[152:155], v[104:107], v[220:223], v[152:155]\n"
        "ds_read_b32 v254, v250 offset:0\n"
        "v_mfma_i32_16x16x64_i8 v[156:159], v[108:111], v[220:223], v[156:159]\n"
        "ds_read_b128 v[212:215], v249 offset:50176\n"
        "s_waitcnt lgkmcnt(6)\n"
        "v_mfma_i32_16x16x64_i8 v[160:163], v[104:107], v[224:227], v[160:163]\n"
        "v_mfma_i32_16x16x64_i8 v[164:167], v[108:111], v[224:227], v[164:167]\n"
        "ds_read_b128 v[216:219], v249 offset:49664\n"
        "s_waitcnt lgkmcnt(6)\n"
        "v_mfma_i32_16x16x64_i8 v[168:171], v[104:107], v[228:231], v[168:171]\n"
        "v_mfma_i32_16x16x64_i8 v[172:175], v[108:111], v[228:231], v[172:175]\n"
        "ds_read_b128 v[220:223], v249 offset:50688\n"
        "s_waitcnt lgkmcnt(6)\n"
        "v_mfma_i32_16x16x64_i8 v[176:179], v[104:107], v[232:235], v[176:179]\n"
        "v_mfma_i32_16x16x64_i8 v[180:183], v[108:111], v[232:235], v[180:183]\n"
        "ds_read_b128 v[224:227], v249 offset:49408\n"
        "s_waitcnt lgkmcnt(6)\n"
        "v_mfma_i32_16x16x64_i8 v[184:187], v[104:107], v[236:239], v[184:187]\n"
        "v_mfma_i32_16x16x64_i8 v[188:191], v[108:111], v[236:239], v[188:191]\n"
        "ds_read_b128 v[228:231], v249 offset:50432\n"
        "s_waitcnt vmcnt(2)\n"
        "s_waitcnt lgkmcnt(6)\n"
        "v_mfma_i32_16x16x64_i8 v[128:131], v[112:115], v[208:211], v[128:131]\n"
        "v_mfma_i32_16x16x64_i8 v[132:135], v[116:119], v[208:211], v[132:135]\n"
        "ds_read_b128 v[232:235], v249 offset:49920\n"
        "s_waitcnt lgkmcnt(5)\n"
        "v_mfma_i32_16x16x64_i8 v[136:139], v[112:115], v[212:215], v[136:139]\n"
        "v_mfma_i32_16x16x64_i8 v[140:143], v[116:119], v[212:215], v[140:143]\n"
        "ds_read_b128 v[236:239], v249 offset:50944\n"
        "v_cmp_gt_u32 vcc, 16, v254\n"
        "s_waitcnt lgkmcnt(5)\n"
        "v_mfma_i32_16x16x64_i8 v[144:147], v[112:115], v[216:219], v[144:147]\n"
        "s_cbranch_vccnz W8_s15\n"
        "W8_c15:\n"
        "v_mfma_i32_16x16x64_i8 v[148:151], v[116:119], v[216:219], v[148:151]\n"
        "ds_read_b128 v[208:211], v249 offset:57344\n"
        "s_waitcnt lgkmcnt(5)\n"
        "v_mfma_i32_16x16x64_i8 v[152:155], v[112:115], v[220:223], v[152:155]\n"
        "ds_read_b32 v254, v250 offset:0\n"
        "v_mfma_i32_16x16x64_i8 v[156:159], v[116:119], v[220:223], v[156:159]\n"
        "ds_read_b128 v[212:215], v249 offset:58368\n"
        "s_waitcnt lgkmcnt(6)\n"
        "v_mfma_i32_16x16x64_i8 v[160:163], v[112:115], v[224:227], v[160:163]\n"
        "v_mfma_i32_16x16x64_i8 v[164:167], v[116:119], v[224:227], v[164:167]\n"
        "ds_read_b128 v[216:219], v249 offset:57856\n"
        "s_waitcnt lgkmcnt(6)\n"
        "v_mfma_i32_16x16x64_i8 v[168:171], v[112:115], v[228:231], v[168:171]\n"
        "ds_read_b32 v255, v250 offset:260\n"
        "v_mfma_i32_16x16x64_i8 v[172:175], v[116:119], v[228:231], v[172:175]\n"
        "ds_read_b128 v[220:223], v249 offset:58880\n"
        "s_waitcnt lgkmcnt(7)\n"
        "v_mfma_i32_16x16x64_i8 v[176:179], v[112:115], v[232:235], v[176:179]\n"
        "v_mfma_i32_16x16x64_i8 v[180:183], v[116:119], v[232:235], v[180:183]\n"
        "ds_read_b128 v[224:227], v249 offset:57600\n"
        "s_waitcnt lgkmcnt(7)\n"
        "v_mfma_i32_16x16x64_i8 v[184:187], v[112:115], v[236:239], v[184:187]\n"
        "v_mfma_i32_16x16x64_i8 v[188:191], v[116:119], v[236:239], v[188:191]\n"
        "ds_read_b128 v[228:231], v249 offset:58624\n"
        "s_waitcnt vmcnt(0)\n"
        "s_waitcnt lgkmcnt(5)\n"
        "v_mfma_i32_16x16x64_i8 v[128:131], v[120:123], v[208:211], v[128:131]\n"
        "v_mfma_i32_16x16x64_i8 v[132:135], v[124:127], v[208:211], v[132:135]\n"
        "ds_read_b128 v[232:235], v249 offset:58112\n"
        "v_mfma_i32_16x16x64_i8 v[136:139], v[120:123], v[212:215], v[136:139]\n"
        "v_mfma_i32_16x16x64_i8 v[140:143], v[124:127], v[212:215], v[140:143]\n"
        "ds_read_b128 v[236:239], v249 offset:59136\n"
        "v_cmp_gt_u32 vcc, 18, v254\n"
        "s_waitcnt lgkmcnt(4)\n"
        "v_mfma_i32_16x16x64_i8 v[144:147], v[120:123], v[216:219], v[144:147]\n"
        "s_cbranch_vccnz W8_s16\n"
        "W8_c16:\n"
        "v_mfma_i32_16x16x64_i8 v[148:151], v[124:127], v[216:219], v[148:151]\n"
        "ds_read_b128 v[208:211], v248 offset:0\n"
        "v_mfma_i32_16x16x64_i8 v[152:155], v[120:123], v[220:223], v[152:155]\n"
        "v_mfma_i32_16x16x64_i8 v[156:159], v[124:127], v[220:223], v[156:159]\n"
        "ds_read_b128 v[212:215], v248 offset:1024\n"
        "s_waitcnt lgkmcnt(4)\n"
        "v_mfma_i32_16x16x64_i8 v[160:163], v[120:123], v[224:227], v[160:163]\n"
        "v_mfma_i32_16x16x64_i8 v[164:167], v[124:127], v[224:227], v[164:167]\n"
        "ds_read_b128 v[216:219], v248 offset:2048\n"
        "v_cmp_gt_u32 vcc, 2, v255\n"
        "v_mfma_i32_16x16x64_i8 v[168:171], v[120:123], v[228:231], v[168:171]\n"
        "v_add_u32 v249, 65536, v248\n"
        "s_cbranch_vccnz W8_sT\n"
        "W8_cT:\n"
        "v_mfma_i32_16x16x64_i8 v[172:175], v[124:127], v[228:231], v[172:175]\n"
        "ds_read_b128 v[220:223], v248 offset:3072\n"
        "s_waitcnt lgkmcnt(4)\n"
        "v_mfma_i32_16x16x64_i8 v[176:179], v[120:123], v[232:235], v[176:179]\n"
        "ds_read_b128 v[240:243], v253\n"
        "ds_read_b128 v[244:247], v253 offset:16\n"
        "v_mfma_i32_16x16x64_i8 v[180:183], v[124:127], v[232:235], v[180:183]\n"
        "ds_read_b128 v[224:227], v248 offset:4096\n"
        "v_mfma_i32_16x16x64_i8 v[184:187], v[120:123], v[236:239], v[184:187]\n"
        "v_cvt_f32_i32 v128, v128\n"
        "v_cvt_f32_i32 v129, v129\n"
        "v_cvt_f32_i32 v130, v130\n"
        "v_mfma_i32_16x16x64_i8 v[188:191], v[124:127], v[236:239], v[188:191]\n"
        "ds_read_b128 v[228:231], v248 offset:5120\n"
        "s_waitcnt lgkmcnt(6)\n"
        "v_mfma_i32_16x16x64_i8 v[192:195], v[0:3], v[208:211], 0\n"
        "v_cvt_f32_i32 v131, v131\n"
        "v_cvt_f32_i32 v136, v136\n"
        "v_mfma_i32_16x16x64_i8 v[196:199], v[4:7], v[208:211], 0\n"
        "ds_read_b128 v[232:235], v248 offset:6144\n"
        "v_cvt_f32_i32 v137, v137\n"
        "v_mfma_i32_16x16x64_i8 v[200:203], v[0:3], v[212:215], 0\n"
        "v_cvt_f32_i32 v138, v138\n"
        "v_cvt_f32_i32 v139, v139\n"
        "v_mfma_i32_16x16x64_i8 v[204:207], v[4:7], v[212:215], 0\n"
        "ds_read_b128 v[236:239], v248 offset:7168\n"
        "v_cvt_f32_i32 v144, v144\n"
        "s_waitcnt lgkmcnt(6)\n"
        "v_mfma_i32_16x16x64_i8 v[192:195], v[8:11], v[216:219], v[192:195]\n"
        "v_cvt_f32_i32 v145, v145\n"
        "v_cvt_f32_i32 v146, v146\n"
        "v_mfma_i32_16x16x64_i8 v[196:199], v[12:15], v[216:219], v[196:199]\n"
        "ds_read_b128 v[208:211], v248 offset:8192\n"
        "v_cvt_f32_i32 v147, v147\n"
        "v_mfma_i32_16x16x64_i8 v[200:203], v[8:11], v[220:223], v[200:203]\n"
        "ds_read_b32 v254, v250 offset:0\n"
        "v_cvt_f32_i32 v152, v152\n"
        "v_mfma_i32_16x16x64_i8 v[204:207], v[12:15], v[220:223], v[204:207]\n"
        "ds_read_b128 v[212:215], v248 offset:9216\n"
        "s_waitcnt lgkmcnt(5)\n"
        "v_mfma_i32_16x16x64_i8 v[192:195], v[16:19], v[224:227], v[192:195]\n"
        "v_cvt_f32_i32 v153, v153\n"
        "v_cvt_f32_i32 v154, v154\n"
        "v_cvt_f32_i32 v155, v155\n"
        "v_mfma_i32_16x16x64_i8 v[196:199], v[20:23], v[224:227], v[196:199]\n"
        "ds_read_b128 v[216:219], v248 offset:10240\n"
        "v_mfma_i32_16x16x64_i8 v[200:203], v[16:19], v[228:231], v[200:203]\n"
        "v_fma_f32 v128, v128, v240, v241\n"
        "v_fma_f32 v129, v129, v240, v241\n"
        "v_mfma_i32_16x16x64_i8 v[204:207], v[20:23], v[228:231], v[204:207]\n"
        "ds_read_b128 v[220:223], v248 offset:11264\n"
        "s_waitcnt lgkmcnt(5)\n"
        "v_mfma_i32_16x16x64_i8 v[192:195], v[24:27], v[232:235], v[192:195]\n"
        "v_fma_f32 v130, v130, v240, v241\n"
        "v_fma_f32 v131, v131, v240, v241\n"
        "v_mfma_i32_16x16x64_i8 v[196:199], v[28:31], v[232:235], v[196:199]\n"
        "ds_read_b128 v[224:227], v248 offset:12288\n"
        "v_fma_f32 v136, v136, v242, v243\n"
        "v_fma_f32 v137, v137, v242, v243\n"
        "v_mfma_i32_16x16x64_i8 v[200:203], v[24:27], v[236:239], v[200:203]\n"
        "v_fma_f32 v138, v138, v242, v243\n"
        "v_fma_f32 v139, v139, v242, v243\n"
        "v_mfma_i32_16x16x64_i8 v[204:207], v[28:31], v[236:239], v[204:207]\n"
        "ds_read_b128 v[228:231], v248 offset:13312\n"
        "s_waitcnt lgkmcnt(4)\n"
        "v_mfma_i32_16x16x64_i8 v[192:195], v[32:35], v[208:211], v[192:195]\n"
        "v_fma_f32 v144, v144, v244, v245\n"
        "v_fma_f32 v145, v145, v244, v245\n"
        "v_mfma_i32_16x16x64_i8 v[196:199], v[36:39], v[208:211], v[196:199]\n"
        "ds_read_b128 v[232:235], v248 offset:14336\n"
        "v_fma_f32 v146, v146, v244, v245\n"
        "v_mfma_i32_16x16x64_i8 v[200:203], v[32:35], v[212:215], v[200:203]\n"
        "v_fma_f32 v147, v147, v244, v245\n"
        "v_fma_f32 v152, v152, v246, v247\n"
        "v_fma_f32 v153, v153, v246, v247\n"
        "v_mfma_i32_16x16x64_i8 v[204:207], v[36:39], v[212:215], v[204:207]\n"
        "ds_read_b128 v[236:239], v248 offset:15360\n"
        "v_cmp_gt_u32 vcc, 20, v254\n"
        "s_waitcnt lgkmcnt(4)\n"
        "v_mfma_i32_16x16x64_i8 v[192:195], v[40:43], v[216:219], v[192:195]\n"
        "s_cbranch_vccnz W8_s18\n"
        "W8_c18:\n"
        "v_fma_f32 v154, v154, v246, v247\n"
        "v_mfma_i32_16x16x64_i8 v[196:199], v[44:47], v[216:219], v[196:199]\n"
        "ds_read_b128 v[208:211], v248 offset:16384\n"
        "v_fma_f32 v155, v155, v246, v247\n"
        "v_mfma_i32_16x16x64_i8 v[200:203], v[40:43], v[220:223], v[200:203]\n"
        "v_cvt_pk_bf16_f32 v128, v128, v136\n"
        "v_cvt_pk_bf16_f32 v136, v129, v137\n"
        "v_mfma_i32_16x16x64_i8 v[204:207], v[44:47], v[220:223], v[204:207]\n"
        "ds_read_b128 v[212:215], v248 offset:17408\n"
        "v_cvt_pk_bf16_f32 v130, v130, v138\n"
        "s_waitcnt lgkmcnt(4)\n"
        "v_mfma_i32_16x16x64_i8 v[192:195], v[48:51], v[224:227], v[192:195]\n"
        "v_cvt_pk_bf16_f32 v138, v131, v139\n"
        "v_cvt_pk_bf16_f32 v129, v144, v152\n"
        "v_mfma_i32_16x16x64_i8 v[196:199], v[52:55], v[224:227], v[196:199]\n"
        "ds_read_b128 v[216:219], v248 offset:18432\n"
        "global_store_dwordx2 v251, v[128:129], s[40:41] offset:0 sc1\n"
        "v_mfma_i32_16x16x64_i8 v[200:203], v[48:51], v[228:231], v[200:203]\n"
        "v_cvt_pk_bf16_f32 v137, v145, v153\n"
        "global_store_dwordx2 v251, v[136:137], s[42:43] offset:0 sc1\n"
        "v_mfma_i32_16x16x64_i8 v[204:207], v[52:55], v[228:231], v[204:207]\n"
        "ds_read_b128 v[220:223], v248 offset:19456\n"
        "s_waitcnt lgkmcnt(4)\n"
        "v_mfma_i32_16x16x64_i8 v[192:195], v[56:59], v[232:235], v[192:195]\n"
        "v_cvt_pk_bf16_f32 v131, v146, v154\n"
        "global_store_dwordx2 v251, v[130:131], s[44:45] offset:0 sc1\n"
        "v_cvt_pk_bf16_f32 v139, v147, v155\n"
        "v_mfma_i32_16x16x64_i8 v[196:199], v[60:63], v[232:235], v[196:199]\n"
        "ds_read_b128 v[224:227], v248 offset:20480\n"
        "global_store_dwordx2 v251, v[138:139], s[46:47] offset:0 sc1\n"
        "v_mfma_i32_16x16x64_i8 v[200:203], v[56:59], v[236:239], v[200:203]\n"
        "v_cvt_f32_i32 v132, v132\n"
        "v_cvt_f32_i32 v133, v133\n"
        "v_mfma_i32_16x16x64_i8 v[204:207], v[60:63], v[236:239], v[204:207]\n"
        "ds_read_b128 v[228:231], v248 offset:21504\n"
        "s_waitcnt lgkmcnt(4)\n"
        "v_mfma_i32_16x16x64_i8 v[192:195], v[64:67], v[208:211], v[192:195]\n"
        "v_cvt_f32_i32 v134, v134\n"
        "v_cvt_f32_i32 v135, v135\n"
        "v_mfma_i32_16x16x64_i8 v[196:199], v[68:71], v[208:211], v[196:199]\n"
        "ds_read_b128 v[232:235], v248 offset:22528\n"
        "v_cvt_f32_i32 v140, v140\n"
        "v_cvt_f32_i32 v141, v141\n"
        "v_mfma_i32_16x16x64_i8 v[200:203], v[64:67], v[212:215], v[200:203]\n"
        "v_cvt_f32_i32 v142, v142\n"
        "v_cvt_f32_i32 v143, v143\n"
        "v_mfma_i32_16x16x64_i8 v[204:207], v[68:71], v[212:215], v[204:207]\n"
        "ds_read_b128 v[236:239], v248 offset:23552\n"
        "s_waitcnt lgkmcnt(4)\n"
        "v_mfma_i32_16x16x64_i8 v[192:195], v[72:75], v[216:219], v[192:195]\n"
        "v_cvt_f32_i32 v148, v148\n"
        "v_cvt_f32_i32 v149, v149\n"
        "v_mfma_i32_16x16x64_i8 v[196:199], v[76:79], v[216:219], v[196:199]\n"
        "ds_read_b128 v[208:211], v248 offset:24576\n"
        "v_cvt_f32_i32 v150, v150\n"
        "v_mfma_i32_16x16x64_i8 v[200:203], v[72:75], v[220:223], v[200:203]\n"
        "ds_read_b32 v254, v250 offset:0\n"
        "v_cvt_f32_i32 v151, v151\n"
        "v_cvt_f32_i32 v156, v156\n"
        "v_mfma_i32_16x16x64_i8 v[204:207], v[76:79], v[220:223], v[204:207]\n"
        "ds_read_b128 v[212:215], v248 offset:25600\n"
        "s_waitcnt lgkmcnt(5)\n"
        "v_mfma_i32_16x16x64_i8 v[192:195], v[80:83], v[224:227], v[192:195]\n"
        "v_cvt_f32_i32 v157, v157\n"
        "v_cvt_f32_i32 v158, v158\n"
        "v_mfma_i32_16x16x64_i8 v[196:199], v[84:87], v[224:227], v[196:199]\n"
        "ds_read_b128 v[216:219], v248 offset:26624\n"
        "v_cvt_f32_i32 v159, v159\n"
        "v_mfma_i32_16x16x64_i8 v[200:203], v[80:83], v[228:231], v[200:203]\n"
        "v_fma_f32 v132, v132, v240, v241\n"
        "v_fma_f32 v133, v133, v240, v241\n"
        "v_mfma_i32_16x16x64_i8 v[204:207], v[84:87], v[228:231], v[204:207]\n"
        "ds_read_b128 v[220:223], v248 offset:27648\n"
        "v_fma_f32 v134, v134, v240, v241\n"
        "s_waitcnt lgkmcnt(5)\n"
        "v_mfma_i32_16x16x64_i8 v[192:195], v[88:91], v[232:235], v[192:195]\n"
        "v_fma_f32 v135, v135, v240, v241\n"
        "v_fma_f32 v140, v140, v242, v243\n"
        "v_mfma_i32_16x16x64_i8 v[196:199], v[92:95], v[232:235], v[196:199]\n"
        "ds_read_b128 v[224:227], v248 offset:28672\n"
        "v_fma_f32 v141, v141, v242, v243\n"
        "v_mfma_i32_16x16x64_i8 v[200:203], v[88:91], v[236:239], v[200:203]\n"
        "v_fma_f32 v142, v142, v242, v243\n"
        "v_fma_f32 v143, v143, v242, v243\n"
        "v_mfma_i32_16x16x64_i8 v[204:207], v[92:95], v[236:239], v[204:207]\n"
        "ds_read_b128 v[228:231], v248 offset:29696\n"
        "s_waitcnt lgkmcnt(4)\n"
        "v_mfma_i32_16x16x64_i8 v[192:195], v[96:99], v[208:211], v[192:195]\n"
        "v_fma_f32 v148, v148, v244, v245\n"
        "v_fma_f32 v149, v149, v244, v245\n"
        "v_fma_f32 v150, v150, v244, v245\n"
        "v_mfma_i32_16x16x64_i8 v[196:199], v[100:103], v[208:211], v[196:199]\n"
        "ds_read_b128 v[232:235], v248 offset:30720\n"
        "v_fma_f32 v151, v151, v244, v245\n"
        "v_mfma_i32_16x16x64_i8 v[200:203], v[96:99], v[212:215], v[200:203]\n"
        "v_fma_f32 v156, v156, v246, v247\n"
        "v_fma_f32 v157, v157, v246, v247\n"
        "v_mfma_i32_16x16x64_i8 v[204:207], v[100:103], v[212:215], v[204:207]\n"
        "ds_read_b128 v[236:239], v248 offset:31744\n"
        "v_cmp_gt_u32 vcc, 22, v254\n"
        "s_waitcnt lgkmcnt(4)\n"
        "v_mfma_i32_16x16x64_i8 v[192:195], v[104:107], v[216:219], v[192:195]\n"
        "s_cbranch_vccnz W8_s20\n"
        "W8_c20:\n"
        "v_fma_f32 v158, v158, v246, v247\n"
        "v_mfma_i32_16x16x64_i8 v[196:199], v[108:111], v[216:219], v[196:199]\n"
        "ds_read_b128 v[208:211], v248 offset:32768\n"
        "v_fma_f32 v159, v159, v246, v247\n"
        "v_cvt_pk_bf16_f32 v132, v132, v140\n"
        "v_mfma_i32_16x16x64_i8 v[200:203], v[104:107], v[220:223], v[200:203]\n"
        "v_cvt_pk_bf16_f32 v140, v133, v141\n"
        "v_cvt_pk_bf16_f32 v134, v134, v142\n"
        "v_mfma_i32_16x16x64_i8 v[204:207], v[108:111], v[220:223], v[204:207]\n"
        "ds_read_b128 v[212:215], v248 offset:33792\n"
        "s_waitcnt lgkmcnt(4)\n"
        "v_mfma_i32_16x16x64_i8 v[192:195], v[112:115], v[224:227], v[192:195]\n"
        "v_cvt_pk_bf16_f32 v142, v135, v143\n"
        "v_cvt_pk_bf16_f32 v133, v148, v156\n"
        "v_mfma_i32_16x16x64_i8 v[196:199], v[116:119], v[224:227], v[196:199]\n"
        "ds_read_b128 v[216:219], v248 offset:34816\n"
        "global_store_dwordx2 v251, v[132:133], s[48:49] offset:0 sc1\n"
        "v_mfma_i32_16x16x64_i8 v[200:203], v[112:115], v[228:231], v[200:203]\n"
        "v_cvt_pk_bf16_f32 v141, v149, v157\n"
        "global_store_dwordx2 v251, v[140:141], s[50:51] offset:0 sc1\n"
        "v_cvt_pk_bf16_f32 v135, v150, v158\n"
        "v_mfma_i32_16x16x64_i8 v[204:207], v[116:119], v[228:231], v[204:207]\n"
        "ds_read_b128 v[220:223], v248 offset:35840\n"
        "s_waitcnt lgkmcnt(4)\n"
        "v_mfma_i32_16x16x64_i8 v[192:195], v[120:123], v[232:235], v[192:195]\n"
        "global_store_dwordx2 v251, v[134:135], s[52:53] offset:0 sc1\n"
        "v_cvt_pk_bf16_f32 v143, v151, v159\n"
        "v_mfma_i32_16x16x64_i8 v[196:199], v[124:127], v[232:235], v[196:199]\n"
        "ds_read_b128 v[224:227], v248 offset:36864\n"
        "global_store_dwordx2 v251, v[142:143], s[54:55] offset:0 sc1\n"
        "v_mfma_i32_16x16x64_i8 v[200:203], v[120:123], v[236:239], v[200:203]\n"
        "ds_read_b128 v[240:243], v253 offset:512\n"
        "ds_read_b128 v[244:247], v253 offset:528\n"
        "v_mfma_i32_16x16x64_i8 v[204:207], v[124:127], v[236:239], v[204:207]\n"
        "ds_read_b128 v[228:231], v248 offset:37888\n"
        "v_cvt_f32_i32 v160, v160\n"
        "s_waitcnt lgkmcnt(6)\n"
        "v_mfma_i32_16x16x64_i8 v[128:131], v[0:3], v[208:211], 0\n"
        "v_cvt_f32_i32 v161, v161\n"
        "v_cvt_f32_i32 v162, v162\n"
        "v_mfma_i32_16x16x64_i8 v[132:135], v[4:7], v[208:211], 0\n"
        "ds_read_b128 v[232:235], v248 offset:38912\n"
        "v_cvt_f32_i32 v163, v163\n"
        "v_mfma_i32_16x16x64_i8 v[136:139], v[0:3], v[212:215], 0\n"
        "v_cvt_f32_i32 v168, v168\n"
        "v_cvt_f32_i32 v169, v169\n"
        "v_cvt_f32_i32 v170, v170\n"
        "v_mfma_i32_16x16x64_i8 v[140:143], v[4:7], v[212:215], 0\n"
        "ds_read_b128 v[236:239], v248 offset:39936\n"
        "s_waitcnt lgkmcnt(6)\n"
        "v_mfma_i32_16x16x64_i8 v[128:131], v[8:11], v[216:219], v[128:131]\n"
        "v_cvt_f32_i32 v171, v171\n"
        "v_cvt_f32_i32 v176, v176\n"
        "v_mfma_i32_16x16x64_i8 v[132:135], v[12:15], v[216:219], v[132:135]\n"
        "ds_read_b128 v[208:211], v248 offset:40960\n"
        "v_cvt_f32_i32 v177, v177\n"
        "v_mfma_i32_16x16x64_i8 v[136:139], v[8:11], v[220:223], v[136:139]\n"
        "ds_read_b32 v254, v250 offset:0\n"
        "v_cvt_f32_i32 v178, v178\n"
        "v_cvt_f32_i32 v179, v179\n"
        "v_mfma_i32_16x16x64_i8 v[140:143], v[12:15], v[220:223], v[140:143]\n"
        "ds_read_b128 v[212:215], v248 offset:41984\n"
        "s_waitcnt lgkmcnt(5)\n"
        "v_mfma_i32_16x16x64_i8 v[128:131], v[16:19], v[224:227], v[128:131]\n"
        "v_cvt_f32_i32 v184, v184\n"
        "v_cvt_f32_i32 v185, v185\n"
        "v_mfma_i32_16x16x64_i8 v[132:135], v[20:23], v[224:227], v[132:135]\n"
        "ds_read_b128 v[216:219], v248 offset:43008\n"
        "v_cvt_f32_i32 v186, v186\n"
        "v_mfma_i32_16x16x64_i8 v[136:139], v[16:19], v[228:231], v[136:139]\n"
        "v_cvt_f32_i32 v187, v187\n"
        "v_fma_f32 v160, v160, v240, v241\n"
        "v_mfma_i32_16x16x64_i8 v[140:143], v[20:23], v[228:231], v[140:143]\n"
        "ds_read_b128 v[220:223], v248 offset:44032\n"
        "s_waitcnt lgkmcnt(5)\n"
        "v_mfma_i32_16x16x64_i8 v[128:131], v[24:27], v[232:235], v[128:131]\n"
        "v_fma_f32 v161, v161, v240, v241\n"
        "v_fma_f32 v162, v162, v240, v241\n"
        "v_mfma_i32_16x16x64_i8 v[132:135], v[28:31], v[232:235], v[132:135]\n"
        "ds_read_b128 v[224:227], v248 offset:45056\n"
        "v_fma_f32 v163, v163, v240, v241\n"
        "v_mfma_i32_16x16x64_i8 v[136:139], v[24:27], v[236:239], v[136:139]\n"
        "v_fma_f32 v168, v168, v242, v243\n"
        "v_fma_f32 v169, v169, v242, v243\n"
        "v_fma_f32 v170, v170, v242, v243\n"
        "v_mfma_i32_16x16x64_i8 v[140:143], v[28:31], v[236:239], v[140:143]\n"
        "ds_read_b128 v[228:231], v248 offset:46080\n"
        "s_waitcnt lgkmcnt(4)\n"
        "v_mfma_i32_16x16x64_i8 v[128:131], v[32:35], v[208:211], v[128:131]\n"
        "v_fma_f32 v171, v171, v242, v243\n"
        "v_fma_f32 v176, v176, v244, v245\n"
        "v_mfma_i32_16x16x64_i8 v[132:135], v[36:39], v[208:211], v[132:135]\n"
        "ds_read_b128 v[232:235], v248 offset:47104\n"
        "v_fma_f32 v177, v177, v244, v245\n"
        "v_mfma_i32_16x16x64_i8 v[136:139], v[32:35], v[212:215], v[136:139]\n"
        "v_fma_f32 v178, v178, v244, v245\n"
        "v_fma_f32 v179, v179, v244, v245\n"
        "v_fma_f32 v184, v184, v246, v247\n"
        "v_mfma_i32_16x16x64_i8 v[140:143], v[36:39], v[212:215], v[140:143]\n"
        "ds_read_b128 v[236:239], v248 offset:48128\n"
        "v_cmp_gt_u32 vcc, 24, v254\n"
        "s_waitcnt lgkmcnt(4)\n"
        "v_mfma_i32_16x16x64_i8 v[128:131], v[40:43], v[216:219], v[128:131]\n"
        "s_cbranch_vccnz W8_s22\n"
        "W8_c22:\n"
        "v_fma_f32 v185, v185, v246, v247\n"
        "v_mfma_i32_16x16x64_i8 v[132:135], v[44:47], v[216:219], v[132:135]\n"
        "ds_read_b128 v[208:211], v248 offset:49152\n"
        "v_fma_f32 v186, v186, v246, v247\n"
        "v_mfma_i32_16x16x64_i8 v[136:139], v[40:43], v[220:223], v[136:139]\n"
        "v_fma_f32 v187, v187, v246, v247\n"
        "v_cvt_pk_bf16_f32 v160, v160, v168\n"
        "v_cvt_pk_bf16_f32 v168, v161, v169\n"
        "v_mfma_i32_16x16x64_i8 v[140:143], v[44:47], v[220:223], v[140:143]\n"
        "ds_read_b128 v[212:215], v248 offset:50176\n"
        "s_waitcnt lgkmcnt(4)\n"
        "v_mfma_i32_16x16x64_i8 v[128:131], v[48:51], v[224:227], v[128:131]\n"
        "v_cvt_pk_bf16_f32 v162, v162, v170\n"
        "v_cvt_pk_bf16_f32 v170, v163, v171\n"
        "v_mfma_i32_16x16x64_i8 v[132:135], v[52:55], v[224:227], v[132:135]\n"
        "ds_read_b128 v[216:219], v248 offset:51200\n"
        "v_cvt_pk_bf16_f32 v161, v176, v184\n"
        "v_mfma_i32_16x16x64_i8 v[136:139], v[48:51], v[228:231], v[136:139]\n"
        "global_store_dwordx2 v251, v[160:161], s[40:41] offset:128 sc1\n"
        "v_cvt_pk_bf16_f32 v169, v177, v185\n"
        "global_store_dwordx2 v251, v[168:169], s[42:43] offset:128 sc1\n"
        "v_mfma_i32_16x16x64_i8 v[140:143], v[52:55], v[228:231], v[140:143]\n"
        "ds_read_b128 v[220:223], v248 offset:52224\n"
        "s_waitcnt lgkmcnt(4)\n"
        "v_mfma_i32_16x16x64_i8 v[128:131], v[56:59], v[232:235], v[128:131]\n"
        "v_cvt_pk_bf16_f32 v163, v178, v186\n"
        "global_store_dwordx2 v251, v[162:163], s[44:45] offset:128 sc1\n"
        "v_mfma_i32_16x16x64_i8 v[132:135], v[60:63], v[232:235], v[132:135]\n"
        "ds_read_b128 v[224:227], v248 offset:53248\n"
        "v_cvt_pk_bf16_f32 v171, v179, v187\n"
        "v_mfma_i32_16x16x64_i8 v[136:139], v[56:59], v[236:239], v[136:139]\n"
        "global_store_dwordx2 v251, v[170:171], s[46:47] offset:128 sc1\n"
        "v_cvt_f32_i32 v164, v164\n"
        "v_cvt_f32_i32 v165, v165\n"
        "v_mfma_i32_16x16x64_i8 v[140:143], v[60:63], v[236:239], v[140:143]\n"
        "ds_read_b128 v[228:231], v248 offset:54272\n"
        "s_waitcnt lgkmcnt(4)\n"
        "v_mfma_i32_16x16x64_i8 v[128:131], v[64:67], v[208:211], v[128:131]\n"
        "v_cvt_f32_i32 v166, v166\n"
        "v_cvt_f32_i32 v167, v167\n"
        "v_mfma_i32_16x16x64_i8 v[132:135], v[68:71], v[208:211], v[132:135]\n"
        "ds_read_b128 v[232:235], v248 offset:55296\n"
        "v_cvt_f32_i32 v172, v172\n"
        "v_mfma_i32_16x16x64_i8 v[136:139], v[64:67], v[212:215], v[136:139]\n"
        "v_cvt_f32_i32 v173, v173\n"
        "v_cvt_f32_i32 v174, v174\n"
        "v_cvt_f32_i32 v175, v175\n"
        "v_mfma_i32_16x16x64_i8 v[140:143], v[68:71], v[212:215], v[140:143]\n"
        "ds_read_b128 v[236:239], v248 offset:56320\n"
        "s_waitcnt lgkmcnt(4)\n"
        "v_mfma_i32_16x16x64_i8 v[128:131], v[72:75], v[216:219], v[128:131]\n"
        "v_cvt_f32_i32 v180, v180\n"
        "v_cvt_f32_i32 v181, v181\n"
        "v_mfma_i32_16x16x64_i8 v[132:135], v[76:79], v[216:219], v[132:135]\n"
        "ds_read_b128 v[208:211], v248 offset:57344\n"
        "v_cvt_f32_i32 v182, v182\n"
        "v_mfma_i32_16x16x64_i8 v[136:139], v[72:75], v[220:223], v[136:139]\n"
        "ds_read_b32 v254, v250 offset:0\n"
        "v_cvt_f32_i32 v183, v183\n"
        "v_cvt_f32_i32 v188, v188\n"
        "v_mfma_i32_16x16x64_i8 v[140:143], v[76:79], v[220:223], v[140:143]\n"
        "ds_read_b128 v[212:215], v248 offset:58368\n"
        "s_waitcnt lgkmcnt(5)\n"
        "v_mfma_i32_16x16x64_i8 v[128:131], v[80:83], v[224:227], v[128:131]\n"
        "v_cvt_f32_i32 v189, v189\n"
        "v_cvt_f32_i32 v190, v190\n"
        "v_mfma_i32_16x16x64_i8 v[132:135], v[84:87], v[224:227], v[132:135]\n"
        "ds_read_b128 v[216:219], v248 offset:59392\n"
        "v_cvt_f32_i32 v191, v191\n"
        "v_mfma_i32_16x16x64_i8 v[136:139], v[80:83], v[228:231], v[136:139]\n"
        "v_fma_f32 v164, v164, v240, v241\n"
        "v_fma_f32 v165, v165, v240, v241\n"
        "v_fma_f32 v166, v166, v240, v241\n"
        "v_mfma_i32_16x16x64_i8 v[140:143], v[84:87], v[228:231], v[140:143]\n"
        "ds_read_b128 v[220:223], v248 offset:60416\n"
        "s_waitcnt lgkmcnt(5)\n"
        "v_mfma_i32_16x16x64_i8 v[128:131], v[88:91], v[232:235], v[128:131]\n"
        "v_fma_f32 v167, v167, v240, v241\n"
        "v_fma_f32 v172, v172, v242, v243\n"
        "v_mfma_i32_16x16x64_i8 v[132:135], v[92:95], v[232:235], v[132:135]\n"
        "ds_read_b128 v[224:227], v248 offset:61440\n"
        "v_fma_f32 v173, v173, v242, v243\n"
        "v_mfma_i32_16x16x64_i8 v[136:139], v[88:91], v[236:239], v[136:139]\n"
        "v_fma_f32 v174, v174, v242, v243\n"
        "v_fma_f32 v175, v175, v242, v243\n"
        "v_fma_f32 v180, v180, v244, v245\n"
        "v_mfma_i32_16x16x64_i8 v[140:143], v[92:95], v[236:239], v[140:143]\n"
        "ds_read_b128 v[228:231], v248 offset:62464\n"
        "s_waitcnt lgkmcnt(4)\n"
        "v_mfma_i32_16x16x64_i8 v[128:131], v[96:99], v[208:211], v[128:131]\n"
        "v_fma_f32 v181, v181, v244, v245\n"
        "v_fma_f32 v182, v182, v244, v245\n"
        "v_mfma_i32_16x16x64_i8 v[132:135], v[100:103], v[208:211], v[132:135]\n"
        "ds_read_b128 v[232:235], v248 offset:63488\n"
        "v_fma_f32 v183, v183, v244, v245\n"
        "v_mfma_i32_16x16x64_i8 v[136:139], v[96:99], v[212:215], v[136:139]\n"
        "v_fma_f32 v188, v188, v246, v247\n"
        "v_fma_f32 v189, v189, v246, v247\n"
        "v_fma_f32 v190, v190, v246, v247\n"
        "v_mfma_i32_16x16x64_i8 v[140:143], v[100:103], v[212:215], v[140:143]\n"
        "ds_read_b128 v[236:239], v248 offset:64512\n"
        "v_cmp_gt_u32 vcc, 26, v254\n"
        "s_waitcnt lgkmcnt(4)\n"
        "v_mfma_i32_16x16x64_i8 v[128:131], v[104:107], v[216:219], v[128:131]\n"
        "s_cbranch_vccnz W8_s24\n"
        "W8_c24:\n"
        "v_fma_f32 v191, v191, v246, v247\n"
        "v_mfma_i32_16x16x64_i8 v[132:135], v[108:111], v[216:219], v[132:135]\n"
        "ds_read_b128 v[208:211], v249 offset:0\n"
        "v_cvt_pk_bf16_f32 v164, v164, v172\n"
        "v_mfma_i32_16x16x64_i8 v[136:139], v[104:107], v[220:223], v[136:139]\n"
        "v_cvt_pk_bf16_f32 v172, v165, v173\n"
        "v_cvt_pk_bf16_f32 v166, v166, v174\n"
        "v_cvt_pk_bf16_f32 v174, v167, v175\n"
        "v_mfma_i32_16x16x64_i8 v[140:143], v[108:111], v[220:223], v[140:143]\n"
        "ds_read_b128 v[212:215], v249 offset:1024\n"
        "s_waitcnt lgkmcnt(4)\n"
        "v_mfma_i32_16x16x64_i8 v[128:131], v[112:115], v[224:227], v[128:131]\n"
        "v_cvt_pk_bf16_f32 v165, v180, v188\n"
        "global_store_dwordx2 v251, v[164:165], s[48:49] offset:128 sc1\n"
        "v_mfma_i32_16x16x64_i8 v[132:135], v[116:119], v[224:227], v[132:135]\n"
        "ds_read_b128 v[216:219], v249 offset:2048\n"
        "v_cvt_pk_bf16_f32 v173, v181, v189\n"
        "v_mfma_i32_16x16x64_i8 v[136:139], v[112:115], v[228:231], v[136:139]\n"
        "global_store_dwordx2 v251, v[172:173], s[50:51] offset:128 sc1\n"
        "v_cvt_pk_bf16_f32 v167, v182, v190\n"
        "global_store_dwordx2 v251, v[166:167], s[52:53] offset:128 sc1\n"
        "v_mfma_i32_16x16x64_i8 v[140:143], v[116:119], v[228:231], v[140:143]\n"
        "ds_read_b128 v[220:223], v249 offset:3072\n"
        "s_waitcnt lgkmcnt(4)\n"
        "v_mfma_i32_16x16x64_i8 v[128:131], v[120:123], v[232:235], v[128:131]\n"
        "v_cvt_pk_bf16_f32 v175, v183, v191\n"
        "global_store_dwordx2 v251, v[174:175], s[54:55] offset:128 sc1\n"
        "v_mfma_i32_16x16x64_i8 v[132:135], v[124:127], v[232:235], v[132:135]\n"
        "ds_read_b128 v[224:227], v249 offset:4096\n"
        "v_add_u32 v251, s37, v251\n"
        "v_mfma_i32_16x16x64_i8 v[136:139], v[120:123], v[236:239], v[136:139]\n"
        "v_mfma_i32_16x16x64_i8 v[140:143], v[124:127], v[236:239], v[140:143]\n"
        "ds_read_b128 v[228:231], v249 offset:5120\n"
        "v_and_b32 v255, 0xfffffeff, v253\n"
        "s_waitcnt lgkmcnt(4)\n"
        "v_mfma_i32_16x16x64_i8 v[144:147], v[0:3], v[208:211], 0\n"
        "v_bfe_u32 v240, v253, 8, 1\n"
        "v_cvt_f32_i32 v192, v192\n"
        "v_mfma_i32_16x16x64_i8 v[148:151], v[4:7], v[208:211], 0\n"
        "ds_read_b128 v[232:235], v249 offset:6144\n"
        "v_lshl_add_u32 v255, v240, 4, v255\n"
        "v_mfma_i32_16x16x64_i8 v[152:155], v[0:3], v[212:215], 0\n"
        "ds_read_b128 v[244:247], v255 offset:1536\n"
        "ds_read_b128 v[240:243], v253 offset:1024\n"
        "v_mfma_i32_16x16x64_i8 v[156:159], v[4:7], v[212:215], 0\n"
        "ds_read_b128 v[236:239], v249 offset:7168\n"
        "s_waitcnt lgkmcnt(6)\n"
        "v_mfma_i32_16x16x64_i8 v[144:147], v[8:11], v[216:219], v[144:147]\n"
        "v_cvt_f32_i32 v193, v193\n"
        "v_cvt_f32_i32 v194, v194\n"
        "v_mfma_i32_16x16x64_i8 v[148:151], v[12:15], v[216:219], v[148:151]\n"
        "ds_read_b128 v[208:211], v249 offset:8192\n"
        "v_cvt_f32_i32 v195, v195\n"
        "v_cvt_f32_i32 v200, v200\n"
        "v_mfma_i32_16x16x64_i8 v[152:155], v[8:11], v[220:223], v[152:155]\n"
        "ds_read_b32 v254, v250 offset:0\n"
        "v_cvt_f32_i32 v201, v201\n"
        "v_mfma_i32_16x16x64_i8 v[156:159], v[12:15], v[220:223], v[156:159]\n"
        "ds_read_b128 v[212:215], v249 offset:9216\n"
        "s_waitcnt lgkmcnt(7)\n"
        "v_mfma_i32_16x16x64_i8 v[144:147], v[16:19], v[224:227], v[144:147]\n"
        "v_cvt_f32_i32 v202, v202\n"
        "v_cvt_f32_i32 v203, v203\n"
        "v_mfma_i32_16x16x64_i8 v[148:151], v[20:23], v[224:227], v[148:151]\n"
        "ds_read_b128 v[216:219], v249 offset:10240\n"
        "v_cvt_f32_i32 v196, v196\n"
        "v_mfma_i32_16x16x64_i8 v[152:155], v[16:19], v[228:231], v[152:155]\n"
        "v_cvt_f32_i32 v197, v197\n"
        "v_cvt_f32_i32 v198, v198\n"
        "v_mfma_i32_16x16x64_i8 v[156:159], v[20:23], v[228:231], v[156:159]\n"
        "ds_read_b128 v[220:223], v249 offset:11264\n"
        "s_waitcnt lgkmcnt(5)\n"
        "v_mfma_i32_16x16x64_i8 v[144:147], v[24:27], v[232:235], v[144:147]\n"
        "v_cvt_f32_i32 v199, v199\n"
        "v_cvt_f32_i32 v204, v204\n"
        "v_cvt_f32_i32 v205, v205\n"
        "v_mfma_i32_16x16x64_i8 v[148:151], v[28:31], v[232:235], v[148:151]\n"
        "ds_read_b128 v[224:227], v249 offset:12288\n"
        "v_cvt_f32_i32 v206, v206\n"
        "v_mfma_i32_16x16x64_i8 v[152:155], v[24:27], v[236:239], v[152:155]\n"
        "v_cvt_f32_i32 v207, v207\n"
        "v_mfma_i32_16x16x64_i8 v[156:159], v[28:31], v[236:239], v[156:159]\n"
        "ds_read_b128 v[228:231], v249 offset:13312\n"
        "s_waitcnt lgkmcnt(4)\n"
        "v_mfma_i32_16x16x64_i8 v[144:147], v[32:35], v[208:211], v[144:147]\n"
        "v_fma_f32 v192, v192, v244, v245\n"
        "v_fma_f32 v193, v193, v244, v245\n"
        "v_mfma_i32_16x16x64_i8 v[148:151], v[36:39], v[208:211], v[148:151]\n"
        "ds_read_b128 v[232:235], v249 offset:14336\n"
        "v_fma_f32 v194, v194, v244, v245\n"
        "v_mfma_i32_16x16x64_i8 v[152:155], v[32:35], v[212:215], v[152:155]\n"
        "v_fma_f32 v195, v195, v244, v245\n"
        "v_fma_f32 v200, v200, v246, v247\n"
        "v_fma_f32 v201, v201, v246, v247\n"
        "v_mfma_i32_16x16x64_i8 v[156:159], v[36:39], v[212:215], v[156:159]\n"
        "ds_read_b128 v[236:239], v249 offset:15360\n"
        "v_cmp_gt_u32 vcc, 28, v254\n"
        "s_waitcnt lgkmcnt(4)\n"
        "v_mfma_i32_16x16x64_i8 v[144:147], v[40:43], v[216:219], v[144:147]\n"
        "s_cbranch_vccnz W8_s26\n"
        "W8_c26:\n"
        "v_fma_f32 v202, v202, v246, v247\n"
        "v_mfma_i32_16x16x64_i8 v[148:151], v[44:47], v[216:219], v[148:151]\n"
        "ds_read_b128 v[208:211], v249 offset:16384\n"
        "v_fma_f32 v203, v203, v246, v247\n"
        "v_mfma_i32_16x16x64_i8 v[152:155], v[40:43], v[220:223], v[152:155]\n"
        "v_cvt_pk_bf16_f32 v192, v192, v200\n"
        "v_cvt_pk_bf16_f32 v200, v193, v201\n"
        "v_mfma_i32_16x16x64_i8 v[156:159], v[44:47], v[220:223], v[156:159]\n"
        "ds_read_b128 v[212:215], v249 offset:17408\n"
        "s_waitcnt lgkmcnt(4)\n"
        "v_mfma_i32_16x16x64_i8 v[144:147], v[48:51], v[224:227], v[144:147]\n"
        "v_cvt_pk_bf16_f32 v194, v194, v202\n"
        "v_cvt_pk_bf16_f32 v202, v195, v203\n"
        "v_mfma_i32_16x16x64_i8 v[148:151], v[52:55], v[224:227], v[148:151]\n"
        "ds_read_b128 v[216:219], v249 offset:18432\n"
        "global_store_dword v252, v192, s[40:41] sc1\n"
        "global_store_dword v252, v200, s[42:43] sc1\n"
        "v_mfma_i32_16x16x64_i8 v[152:155], v[48:51], v[228:231], v[152:155]\n"
        "global_store_dword v252, v194, s[44:45] sc1\n"
        "global_store_dword v252, v202, s[46:47] sc1\n"
        "v_mfma_i32_16x16x64_i8 v[156:159], v[52:55], v[228:231], v[156:159]\n"
        "ds_read_b128 v[220:223], v249 offset:19456\n"
        "s_waitcnt lgkmcnt(4)\n"
        "v_mfma_i32_16x16x64_i8 v[144:147], v[56:59], v[232:235], v[144:147]\n"
        "v_fma_f32 v196, v196, v244, v245\n"
        "v_fma_f32 v197, v197, v244, v245\n"
        "v_mfma_i32_16x16x64_i8 v[148:151], v[60:63], v[232:235], v[148:151]\n"
        "ds_read_b128 v[224:227], v249 offset:20480\n"
        "v_fma_f32 v198, v198, v244, v245\n"
        "v_mfma_i32_16x16x64_i8 v[152:155], v[56:59], v[236:239], v[152:155]\n"
        "v_fma_f32 v199, v199, v244, v245\n"
        "v_fma_f32 v204, v204, v246, v247\n"
        "v_mfma_i32_16x16x64_i8 v[156:159], v[60:63], v[236:239], v[156:159]\n"
        "ds_read_b128 v[228:231], v249 offset:21504\n"
        "v_fma_f32 v205, v205, v246, v247\n"
        "s_waitcnt lgkmcnt(4)\n"
        "v_mfma_i32_16x16x64_i8 v[144:147], v[64:67], v[208:211], v[144:147]\n"
        "v_fma_f32 v206, v206, v246, v247\n"
        "v_fma_f32 v207, v207, v246, v247\n"
        "v_mfma_i32_16x16x64_i8 v[148:151], v[68:71], v[208:211], v[148:151]\n"
        "ds_read_b128 v[232:235], v249 offset:22528\n"
        "v_cvt_pk_bf16_f32 v196, v196, v204\n"
        "v_mfma_i32_16x16x64_i8 v[152:155], v[64:67], v[212:215], v[152:155]\n"
        "v_cvt_pk_bf16_f32 v204, v197, v205\n"
        "v_cvt_pk_bf16_f32 v198, v198, v206\n"
        "v_mfma_i32_16x16x64_i8 v[156:159], v[68:71], v[212:215], v[156:159]\n"
        "ds_read_b128 v[236:239], v249 offset:23552\n"
        "s_waitcnt lgkmcnt(4)\n"
        "v_mfma_i32_16x16x64_i8 v[144:147], v[72:75], v[216:219], v[144:147]\n"
        "v_cvt_pk_bf16_f32 v206, v199, v207\n"
        "global_store_dword v252, v196, s[48:49] sc1\n"
        "v_mfma_i32_16x16x64_i8 v[148:151], v[76:79], v[216:219], v[148:151]\n"
        "ds_read_b128 v[208:211], v249 offset:24576\n"
        "global_store_dword v252, v204, s[50:51] sc1\n"
        "v_mfma_i32_16x16x64_i8 v[152:155], v[72:75], v[220:223], v[152:155]\n"
        "global_store_dword v252, v198, s[52:53] sc1\n"
        "global_store_dword v252, v206, s[54:55] sc1\n"
        "ds_read_b128 v[244:247], v253 offset:1040\n"
        "v_mfma_i32_16x16x64_i8 v[156:159], v[76:79], v[220:223], v[156:159]\n"
        "ds_read_b128 v[212:215], v249 offset:25600\n"
        "s_waitcnt lgkmcnt(5)\n"
        "v_mfma_i32_16x16x64_i8 v[144:147], v[80:83], v[224:227], v[144:147]\n"
        "v_cvt_f32_i32 v128, v128\n"
        "v_cvt_f32_i32 v129, v129\n"
        "v_mfma_i32_16x16x64_i8 v[148:151], v[84:87], v[224:227], v[148:151]\n"
        "ds_read_b128 v[216:219], v249 offset:26624\n"
        "v_cvt_f32_i32 v130, v130\n"
        "v_mfma_i32_16x16x64_i8 v[152:155], v[80:83], v[228:231], v[152:155]\n"
        "v_cvt_f32_i32 v131, v131\n"
        "v_cvt_f32_i32 v136, v136\n"
        "v_mfma_i32_16x16x64_i8 v[156:159], v[84:87], v[228:231], v[156:159]\n"
        "ds_read_b128 v[220:223], v249 offset:27648\n"
        "s_waitcnt lgkmcnt(5)\n"
        "v_mfma_i32_16x16x64_i8 v[144:147], v[88:91], v[232:235], v[144:147]\n"
        "v_cvt_f32_i32 v137, v137\n"
        "v_cvt_f32_i32 v138, v138\n"
        "v_cvt_f32_i32 v139, v139\n"
        "v_mfma_i32_16x16x64_i8 v[148:151], v[92:95], v[232:235], v[148:151]\n"
        "ds_read_b128 v[224:227], v249 offset:28672\n"
        "v_fma_f32 v128, v128, v240, v241\n"
        "v_mfma_i32_16x16x64_i8 v[152:155], v[88:91], v[236:239], v[152:155]\n"
        "v_fma_f32 v129, v129, v240, v241\n"
        "v_fma_f32 v130, v130, v240, v241\n"
        "v_mfma_i32_16x16x64_i8 v[156:159], v[92:95], v[236:239], v[156:159]\n"
        "ds_read_b128 v[228:231], v249 offset:29696\n"
        "s_waitcnt lgkmcnt(4)\n"
        "v_mfma_i32_16x16x64_i8 v[144:147], v[96:99], v[208:211], v[144:147]\n"
        "v_fma_f32 v131, v131, v240, v241\n"
        "v_fma_f32 v136, v136, v242, v243\n"
        "v_mfma_i32_16x16x64_i8 v[148:151], v[100:103], v[208:211], v[148:151]\n"
        "ds_read_b128 v[232:235], v249 offset:30720\n"
        "v_fma_f32 v137, v137, v242, v243\n"
        "v_mfma_i32_16x16x64_i8 v[152:155], v[96:99], v[212:215], v[152:155]\n"
        "v_fma_f32 v138, v138, v242, v243\n"
        "v_fma_f32 v139, v139, v242, v243\n"
        "v_mfma_i32_16x16x64_i8 v[156:159], v[100:103], v[212:215], v[156:159]\n"
        "ds_read_b128 v[236:239], v249 offset:31744\n"
        "v_cvt_pk_bf16_f32 v128, v128, v136\n"
        "s_waitcnt lgkmcnt(4)\n"
        "v_mfma_i32_16x16x64_i8 v[144:147], v[104:107], v[216:219], v[144:147]\n"
        "v_cvt_pk_bf16_f32 v136, v129, v137\n"
        "v_cvt_pk_bf16_f32 v130, v130, v138\n"
        "v_mfma_i32_16x16x64_i8 v[148:151], v[108:111], v[216:219], v[148:151]\n"
        "v_cvt_pk_bf16_f32 v138, v131, v139\n"
        "v_cvt_f32_i32 v132, v132\n"
        "v_mfma_i32_16x16x64_i8 v[152:155], v[104:107], v[220:223], v[152:155]\n"
        "v_cvt_f32_i32 v133, v133\n"
        "v_cvt_f32_i32 v134, v134\n"
        "v_mfma_i32_16x16x64_i8 v[156:159], v[108:111], v[220:223], v[156:159]\n"
        "v_cvt_f32_i32 v135, v135\n"
        "s_waitcnt lgkmcnt(2)\n"
        "v_mfma_i32_16x16x64_i8 v[144:147], v[112:115], v[224:227], v[144:147]\n"
        "v_cvt_f32_i32 v140, v140\n"
        "v_cvt_f32_i32 v141, v141\n"
        "v_mfma_i32_16x16x64_i8 v[148:151], v[116:119], v[224:227], v[148:151]\n"
        "v_cvt_f32_i32 v142, v142\n"
        "v_cvt_f32_i32 v143, v143\n"
        "v_fma_f32 v132, v132, v240, v241\n"
        "v_mfma_i32_16x16x64_i8 v[152:155], v[112:115], v[228:231], v[152:155]\n"
        "v_fma_f32 v133, v133, v240, v241\n"
        "v_fma_f32 v134, v134, v240, v241\n"
        "v_mfma_i32_16x16x64_i8 v[156:159], v[116:119], v[228:231], v[156:159]\n"
        "v_fma_f32 v135, v135, v240, v241\n"
        "s_waitcnt lgkmcnt(0)\n"
        "v_mfma_i32_16x16x64_i8 v[144:147], v[120:123], v[232:235], v[144:147]\n"
        "v_fma_f32 v140, v140, v242, v243\n"
        "v_fma_f32 v141, v141, v242, v243\n"
        "v_mfma_i32_16x16x64_i8 v[148:151], v[124:127], v[232:235], v[148:151]\n"
        "v_fma_f32 v142, v142, v242, v243\n"
        "v_fma_f32 v143, v143, v242, v243\n"
        "v_mfma_i32_16x16x64_i8 v[152:155], v[120:123], v[236:239], v[152:155]\n"
        "v_cvt_pk_bf16_f32 v132, v132, v140\n"
        "v_cvt_pk_bf16_f32 v140, v133, v141\n"
        "v_mfma_i32_16x16x64_i8 v[156:159], v[124:127], v[236:239], v[156:159]\n"
        "v_cvt_pk_bf16_f32 v134, v134, v142\n"
        "v_cvt_pk_bf16_f32 v142, v135, v143\n"
        "s_nop 5\n"
        "v_cvt_f32_i32 v144, v144\n"
        "v_cvt_f32_i32 v145, v145\n"
        "v_cvt_f32_i32 v146, v146\n"
        "v_cvt_f32_i32 v147, v147\n"
        "v_cvt_f32_i32 v152, v152\n"
        "v_cvt_f32_i32 v153, v153\n"
        "v_cvt_f32_i32 v154, v154\n"
        "v_cvt_f32_i32 v155, v155\n"
        "v_fma_f32 v144, v144, v244, v245\n"
        "v_fma_f32 v145, v145, v244, v245\n"
        "v_fma_f32 v146, v146, v244, v245\n"
        "v_fma_f32 v147, v147, v244, v245\n"
        "v_fma_f32 v152, v152, v246, v247\n"
        "v_fma_f32 v153, v153, v246, v247\n"
        "v_fma_f32 v154, v154, v246, v247\n"
        "v_fma_f32 v155, v155, v246, v247\n"
        "v_cvt_pk_bf16_f32 v129, v144, v152\n"
        "global_store_dwordx2 v251, v[128:129], s[40:41] offset:256 sc1\n"
        "v_cvt_pk_bf16_f32 v137, v145, v153\n"
        "global_store_dwordx2 v251, v[136:137], s[42:43] offset:256 sc1\n"
        "v_cvt_pk_bf16_f32 v131, v146, v154\n"
        "global_store_dwordx2 v251, v[130:131], s[44:45] offset:256 sc1\n"
        "v_cvt_pk_bf16_f32 v139, v147, v155\n"
        "global_store_dwordx2 v251, v[138:139], s[46:47] offset:256 sc1\n"
        "v_cvt_f32_i32 v148, v148\n"
        "v_cvt_f32_i32 v149, v149\n"
        "v_cvt_f32_i32 v150, v150\n"
        "v_cvt_f32_i32 v151, v151\n"
        "v_cvt_f32_i32 v156, v156\n"
        "v_cvt_f32_i32 v157, v157\n"
        "v_cvt_f32_i32 v158, v158\n"
        "v_cvt_f32_i32 v159, v159\n"
        "v_fma_f32 v148, v148, v244, v245\n"
        "v_fma_f32 v149, v149, v244, v245\n"
        "v_fma_f32 v150, v150, v244, v245\n"
        "v_fma_f32 v151, v151, v244, v245\n"
        "v_fma_f32 v156, v156, v246, v247\n"
        "v_fma_f32 v157, v157, v246, v247\n"
        "v_fma_f32 v158, v158, v246, v247\n"
        "v_fma_f32 v159, v159, v246, v247\n"
        "v_cvt_pk_bf16_f32 v133, v148, v156\n"
        "global_store_dwordx2 v251, v[132:133], s[48:49] offset:256 sc1\n"
        "v_cvt_pk_bf16_f32 v141, v149, v157\n"
        "global_store_dwordx2 v251, v[140:141], s[50:51] offset:256 sc1\n"
        "v_cvt_pk_bf16_f32 v135, v150, v158\n"
        "global_store_dwordx2 v251, v[134:135], s[52:53] offset:256 sc1\n"
        "v_cvt_pk_bf16_f32 v143, v151, v159\n"
        "global_store_dwordx2 v251, v[142:143], s[54:55] offset:256 sc1\n"
        "W8_fin:\n"
        "s_endpgm\n"
        "W8_g0_254:\n"
        "s_mov_b32 s38, 8388608\n"
        "W8_h0_254:\n"
        "ds_read_b32 v254, v250 offset:0\n"
        "s_sub_u32 s38, s38, 1\n"
        "s_waitcnt lgkmcnt(0)\n"
        "s_cbranch_scc1 W8_i0_254\n"
        "v_cmp_gt_u32 vcc, s34, v254\n"
        "s_cbranch_vccnz W8_h0_254\n"
        "W8_i0_254:\n"
        "s_setpc_b64 s[30:31]\n"
        "W8_g4_241:\n"
        "s_mov_b32 s38, 8388608\n"
        "W8_h4_241:\n"
        "ds_read_b32 v241, v250 offset:4\n"
        "s_sub_u32 s38, s38, 1\n"
        "s_waitcnt lgkmcnt(0)\n"
        "s_cbranch_scc1 W8_i4_241\n"
        "v_cmp_gt_u32 vcc, s34, v241\n"
        "s_cbranch_vccnz W8_h4_241\n"
        "W8_i4_241:\n"
        "s_setpc_b64 s[30:31]\n"
        "W8_g260_255:\n"
        "s_mov_b32 s38, 8388608\n"
        "W8_h260_255:\n"
        "ds_read_b32 v255, v250 offset:260\n"
        "s_sub_u32 s38, s38, 1\n"
        "s_waitcnt lgkmcnt(0)\n"
        "s_cbranch_scc1 W8_i260_255\n"
        "v_cmp_gt_u32 vcc, s34, v255\n"
        "s_cbranch_vccnz W8_h260_255\n"
        "W8_i260_255:\n"
        "s_setpc_b64 s[30:31]\n"
        "W8_s0:\n"
        "s_movk_i32 s34, 1\n"
        "s_call_b64 s[30:31], W8_g0_254\n"
        "s_branch W8_c0\n"
        "W8_sZ:\n"
        "s_movk_i32 s34, 4\n"
        "s_call_b64 s[30:31], W8_g4_241\n"
        "s_branch W8_cZ\n"
        "W8_s1:\n"
        "s_movk_i32 s34, 2\n"
        "s_call_b64 s[30:31], W8_g0_254\n"
        "s_branch W8_c1\n"
        "W8_sR0:\n"
        "s_movk_i32 s34, 5\n"
        "s_call_b64 s[30:31], W8_g4_241\n"
        "s_branch W8_cR0\n"
        "W8_s2:\n"
        "s_movk_i32 s34, 3\n"
        "s_call_b64 s[30:31], W8_g0_254\n"
        "s_branch W8_c2\n"
        "W8_sR1:\n"
        "s_movk_i32 s34, 6\n"
        "s_call_b64 s[30:31], W8_g4_241\n"
        "s_branch W8_cR1\n"
        "W8_s3:\n"
        "s_movk_i32 s34, 4\n"
        "s_call_b64 s[30:31], W8_g0_254\n"
        "s_branch W8_c3\n"
        "W8_sR2:\n"
        "s_movk_i32 s34, 7\n"
        "s_call_b64 s[30:31], W8_g4_241\n"
        "s_branch W8_cR2\n"
        "W8_s4:\n"
        "s_movk_i32 s34, 5\n"
        "s_call_b64 s[30:31], W8_g0_254\n"
        "s_branch W8_c4\n"
        "W8_sR3:\n"
        "s_movk_i32 s34, 8\n"
        "s_call_b64 s[30:31], W8_g4_241\n"
        "s_branch W8_cR3\n"
        "W8_s5:\n"
        "s_movk_i32 s34, 6\n"
        "s_call_b64 s[30:31], W8_g0_254\n"
        "s_branch W8_c5\n"
        "W8_sR4:\n"
        "s_movk_i32 s34, 9\n"
        "s_call_b64 s[30:31], W8_g4_241\n"
        "s_branch W8_cR4\n"
        "W8_s6:\n"
        "s_movk_i32 s34, 7\n"
        "s_call_b64 s[30:31], W8_g0_254\n"
        "s_branch W8_c6\n"
        "W8_sR5:\n"
        "s_movk_i32 s34, 10\n"
        "s_call_b64 s[30:31], W8_g4_241\n"
        "s_branch W8_cR5\n"
        "W8_s7:\n"
        "s_movk_i32 s34, 8\n"
        "s_call_b64 s[30:31], W8_g0_254\n"
        "s_branch W8_c7\n"
        "W8_sR6:\n"
        "s_movk_i32 s34, 11\n"
        "s_call_b64 s[30:31], W8_g4_241\n"
        "s_branch W8_cR6\n"
        "W8_s8:\n"
        "s_movk_i32 s34, 9\n"
        "s_call_b64 s[30:31], W8_g0_254\n"
        "s_branch W8_c8\n"
        "W8_sR7:\n"
        "s_movk_i32 s34, 12\n"
        "s_call_b64 s[30:31], W8_g4_241\n"
        "s_branch W8_cR7\n"
        "W8_s9:\n"
        "s_movk_i32 s34, 10\n"
        "s_call_b64 s[30:31], W8_g0_254\n"
        "s_branch W8_c9\n"
        "W8_sR8:\n"
        "s_movk_i32 s34, 13\n"
        "s_call_b64 s[30:31], W8_g4_241\n"
        "s_branch W8_cR8\n"
        "W8_s10:\n"
        "s_movk_i32 s34, 11\n"
        "s_call_b64 s[30:31], W8_g0_254\n"
        "s_branch W8_c10\n"
        "W8_sR9:\n"
        "s_movk_i32 s34, 14\n"
        "s_call_b64 s[30:31], W8_g4_241\n"
        "s_branch W8_cR9\n"
        "W8_s11:\n"
        "s_movk_i32 s34, 12\n"
        "s_call_b64 s[30:31], W8_g0_254\n"
        "s_branch W8_c11\n"
        "W8_sR10:\n"
        "s_movk_i32 s34, 15\n"
        "s_call_b64 s[30:31], W8_g4_241\n"
        "s_branch W8_cR10\n"
        "W8_s12:\n"
        "s_movk_i32 s34, 13\n"
        "s_call_b64 s[30:31], W8_g0_254\n"
        "s_branch W8_c12\n"
        "W8_sR11:\n"
        "s_movk_i32 s34, 16\n"
        "s_call_b64 s[30:31], W8_g4_241\n"
        "s_branch W8_cR11\n"
        "W8_s13:\n"
        "s_movk_i32 s34, 14\n"
        "s_call_b64 s[30:31], W8_g0_254\n"
        "s_branch W8_c13\n"
        "W8_s14:\n"
        "s_movk_i32 s34, 15\n"
        "s_call_b64 s[30:31], W8_g0_254\n"
        "s_branch W8_c14\n"
        "W8_s15:\n"
        "s_movk_i32 s34, 16\n"
        "s_call_b64 s[30:31], W8_g0_254\n"
        "s_branch W8_c15\n"
        "W8_s16:\n"
        "s_movk_i32 s34, 18\n"
        "s_call_b64 s[30:31], W8_g0_254\n"
        "s_branch W8_c16\n"
        "W8_sT:\n"
        "s_movk_i32 s34, 2\n"
        "s_call_b64 s[30:31], W8_g260_255\n"
        "s_branch W8_cT\n"
        "W8_s18:\n"
        "s_movk_i32 s34, 20\n"
        "s_call_b64 s[30:31], W8_g0_254\n"
        "s_branch W8_c18\n"
        "W8_s20:\n"
        "s_movk_i32 s34, 22\n"
        "s_call_b64 s[30:31], W8_g0_254\n"
        "s_branch W8_c20\n"
        "W8_s22:\n"
        "s_movk_i32 s34, 24\n"
        "s_call_b64 s[30:31], W8_g0_254\n"
        "s_branch W8_c22\n"
        "W8_s24:\n"
        "s_movk_i32 s34, 26\n"
        "s_call_b64 s[30:31], W8_g0_254\n"
        "s_branch W8_c24\n"
        "W8_s26:\n"
        "s_movk_i32 s34, 28\n"
        "s_call_b64 s[30:31], W8_g0_254\n"
        "s_branch W8_c26\n"
        "W8_end:\n"
        : "+v"(tid)
        : "s"(__builtin_amdgcn_kernarg_segment_ptr()), "s"(blockIdx.x)
        : "v1", "v2", "v3", "v4", "v5", "v6", "v7", "v8", "v9", "v10", "v11", "v12", "v13", "v14", "v15", "v16", "v17", "v18", "v19", "v20", "v21", "v22", "v23", "v24", "v25", "v26", "v27", "v28", "v29", "v30", "v31", "v32", "v33", "v34", "v35", "v36", "v37", "v38", "v39", "v40", "v41", "v42", "v43", "v44", "v45", "v46", "v47", "v48", "v49", "v50", "v51", "v52", "v53", "v54", "v55", "v56", "v57", "v58", "v59", "v60", "v61", "v62", "v63", "v64", "v65", "v66", "v67", "v68", "v69", "v70", "v71", "v72", "v73", "v74", "v75", "v76", "v77", "v78", "v79", "v80", "v81", "v82", "v83", "v84", "v85", "v86", "v87", "v88", "v89", "v90", "v91", "v92", "v93", "v94", "v95", "v96", "v97", "v98", "v99", "v100", "v101", "v102", "v103", "v104", "v105", "v106", "v107", "v108", "v109", "v110", "v111", "v112", "v113", "v114", "v115", "v116", "v117", "v118", "v119", "v120", "v121", "v122", "v123", "v124", "v125", "v126", "v127", "v128", "v129", "v130", "v131", "v132", "v133", "v134", "v135", "v136", "v137", "v138", "v139", "v140", "v141", "v142", "v143", "v144", "v145", "v146", "v147", "v148", "v149", "v150", "v151", "v152", "v153", "v154", "v155", "v156", "v157", "v158", "v159", "v160", "v161", "v162", "v163", "v164", "v165", "v166", "v167", "v168", "v169", "v170", "v171", "v172", "v173", "v174", "v175", "v176", "v177", "v178", "v179", "v180", "v181", "v182", "v183", "v184", "v185", "v186", "v187", "v188", "v189", "v190", "v191", "v192", "v193", "v194", "v195", "v196", "v197", "v198", "v199", "v200", "v201", "v202", "v203", "v204", "v205", "v206", "v207", "v208", "v209", "v210", "v211", "v212", "v213", "v214", "v215", "v216", "v217", "v218", "v219", "v220", "v221", "v222", "v223", "v224", "v225", "v226", "v227", "v228", "v229", "v230", "v231", "v232", "v233", "v234", "v235", "v236", "v237", "v238", "v239", "v240", "v241", "v242", "v243", "v244", "v245", "v246", "v247", "v248", "v249", "v250", "v251", "v252", "v253", "v254", "v255", "s3", "s4", "s5", "s6", "s7", "s8", "s9", "s10", "s11", "s12", "s13", "s14", "s15", "s16", "s17", "s18", "s19", "s20", "s21", "s22", "s23", "s24", "s25", "s26", "s27", "s28", "s29", "s30", "s31", "s33", "s34", "s35", "s36", "s37", "s38", "s39", "s40", "s41", "s42", "s43", "s44", "s45", "s46", "s47", "s48", "s49", "s50", "s51", "s52", "s53", "s54", "s55", "s56", "s57", "s58", "s59", "s60", "s61", "s62", "s63", "s64", "s65", "s66", "s67", "s68", "s69", "s70", "s71", "s72", "s73", "s74", "s75", "s76", "s77", "s78", "s79", "s80", "s81", "s82", "s83", "s84", "s85", "s86", "s87", "s88", "s89", "s90", "s91", "s92", "s93", "s94", "s95", "s96", "s97", "s98", "s99", "vcc", "scc", "memory");
}

extern "C" void kernel_launch(void* const* d_in, const int* in_sizes, int n_in,
                              void* d_out, int out_size, void* d_ws, size_t ws_size,
                              hipStream_t stream) {
    (void)in_sizes; (void)n_in; (void)out_size; (void)ws_size;
    if (ws_size >= (size_t)(128u<<20) + (8u << 20)) d_ws = (char*)d_ws + (128u<<20);
    constexpr int lds_bytes = 155712;
    hipFuncSetAttribute((const void*)w8a8_fused, hipFuncAttributeMaxDynamicSharedMemorySize, lds_bytes);
    w8a8_fused<<<256, 512, lds_bytes, stream>>>(d_in[0], d_in[1], d_ws, d_out, d_in[4], d_in[5], d_in[2], d_in[3]);
}
```
